```python
import torch

def _softmax(x, axis=-1):
    return torch.softmax(x, dim=axis)

def _gelu(x):
    return torch.nn.functional.gelu(x, approximate='tanh')

def _silu(x):
    return torch.nn.functional.silu(x)

def _sigmoid(x):
    return torch.sigmoid(x)

def _layernorm(x, axis=-1, eps=1e-05):
    m = x.mean(dim=axis, keepdim=True)
    v = x.var(dim=axis, keepdim=True, unbiased=False)
    return (x - m) / torch.sqrt(v + eps)

def _scatter_add(tgt, idx, src, axis=0):
    return tgt.clone().index_add_(axis, idx, src)

def _segment_sum(src, seg, n):
    out = torch.zeros((n,) + src.shape[1:], dtype=src.dtype)
    return out.index_add_(0, seg, src)

def _pad(x, pw):
    flat = tuple((v for pair in reversed(pw) for v in pair))
    return torch.nn.functional.pad(x, flat)
kernel_name = 'concat_heads__memory_aug_recurrent_mem_tr__sliding_block__prompt'

def setup_inputs():

    _w = lambda a: (lambda t: t.to(torch.bfloat16) if t.is_floating_point() else t)(torch.as_tensor(a))

    def _A_setup():
        B, S, Dm = (1, 2048, 1024)
        H, Dh = (32, 128)
        M, seg = (8, 32)
        seg = min(seg, S)
        S = S // seg * seg
        return {'x': _w((lambda *s: torch.randn(*s, dtype=torch.bfloat16) * 0.05)(B, S, Dm)), 'mem0': _w((lambda *s: torch.randn(*s, dtype=torch.bfloat16) * 1.6)(B, M, Dm)), 'Wq': _w((lambda *s: torch.randn(*s, dtype=torch.bfloat16) * 0.8)(Dm, H, Dh) * Dm ** (-0.5)), 'Wk': _w((lambda *s: torch.randn(*s, dtype=torch.bfloat16) * 0.8)(Dm, H, Dh) * Dm ** (-0.5)), 'Wv': _w((lambda *s: torch.randn(*s, dtype=torch.bfloat16) * 1.6)(Dm, H, Dh) * Dm ** (-0.5)), 'Wo': _w((lambda *s: torch.randn(*s, dtype=torch.bfloat16) * 1.6)(H, Dh, Dm) * (H * Dh) ** (-0.5)), 'seg_len': seg}
    _A_in = _A_setup()

    def _B_setup():
        B, S, D = (1, 2048, 1024)
        return {'x': _w((lambda *s: torch.randn(*s, dtype=torch.bfloat16) * 0.2)(B, S, D)), 'wqkv': _w((lambda *s: torch.randn(*s, dtype=torch.bfloat16) * 0.2)(D, 3 * D)), 'wo': _w((lambda *s: torch.randn(*s, dtype=torch.bfloat16) * 0.05)(D, D))}
    _B_in = _B_setup()
    return {'A_x': _A_in['x'], 'A_mem0': _A_in['mem0'], 'A_Wq': _A_in['Wq'], 'A_Wk': _A_in['Wk'], 'A_Wv': _A_in['Wv'], 'A_Wo': _A_in['Wo'], 'A_seg_len': _A_in['seg_len'], 'B_x': _B_in['x'], 'B_wqkv': _B_in['wqkv'], 'B_wo': _B_in['wo']}

def reference(A_x, A_mem0, A_Wq, A_Wk, A_Wv, A_Wo, A_seg_len, B_x, B_wqkv, B_wo):

    def _A_ref(x, mem0, Wq, Wk, Wv, Wo, seg_len):
        B, S, Dm = x.shape
        H, Dh = (Wq.shape[1], Wq.shape[2])
        M = mem0.shape[1]
        scale = Dh ** (-0.5)
        n_seg = S // seg_len
        L = M + seg_len
        idx = torch.arange(L)
        mask = (idx[:, None] < M) | (idx[:, None] >= idx[None, :])
        mem = mem0
        outs = []
        for s in range(n_seg):
            xs = x[:, s * seg_len:(s + 1) * seg_len, :].clone()
            h = torch.concatenate([mem, xs], axis=1)
            q = torch.einsum('btd,dhe->bhte', h, Wq)
            k = torch.einsum('btd,dhe->bhte', h, Wk)
            v = torch.einsum('btd,dhe->bhte', h, Wv)
            scores = torch.einsum('bhqe,bhke->bhqk', q, k) * scale
            scores = torch.where(mask[None, None], scores, -1000000000.0)
            p = _softmax(scores, axis=-1)
            o = torch.einsum('bhqk,bhke->bhqe', p, v)
            o = torch.einsum('bhte,hed->btd', o, Wo)
            mem = o[:, :M, :].clone()
            outs.append(o[:, M:, :])
        return torch.concatenate(outs, axis=1)

    def _B_ref(x, wqkv, wo):
        B, S, D = x.shape
        H = 32
        Dh = D // H
        W = 256
        qkv = (lambda x, *d: x.permute(*d))((x @ wqkv).reshape(B, S, 3, H, Dh), 2, 0, 3, 1, 4)
        q, k, v = (qkv[0], qkv[1], qkv[2])
        scores = torch.einsum('bhqd,bhkd->bhqk', q, k) * Dh ** (-0.5)
        idx = torch.arange(S)
        mask = (idx[:, None] - idx[None, :] >= 0) & (idx[:, None] - idx[None, :] < W)
        scores = torch.where(mask[None, None], scores, -1000000000.0)
        p = _softmax(scores, axis=-1)
        o = torch.einsum('bhqk,bhkd->bhqd', p, v)
        return (lambda x, *d: x.permute(*d))(o, 0, 2, 1, 3).reshape(B, S, D) @ wo
    a = _A_ref(A_x, A_mem0, A_Wq, A_Wk, A_Wv, A_Wo, A_seg_len)
    b = _B_ref(B_x, B_wqkv, B_wo)
    return torch.concatenate([a, b], axis=-1)
```

```cpp
#include <hip/hip_runtime.h>
#include <stdint.h>

#define LAS __attribute__((address_space(3)))
#define DI __device__ __forceinline__
typedef unsigned short u16;
typedef __attribute__((ext_vector_type(8))) __bf16 bf16x8;
typedef __attribute__((ext_vector_type(16))) __bf16 bf16x16;
typedef __attribute__((ext_vector_type(2))) __bf16 bf16x2;
typedef __attribute__((ext_vector_type(4))) short s16x4;
typedef __attribute__((ext_vector_type(8))) short s16x8;
typedef __attribute__((ext_vector_type(16))) short s16x16;
typedef __attribute__((ext_vector_type(2))) float f32x2;
typedef __attribute__((ext_vector_type(4))) float f32x4;
typedef __attribute__((ext_vector_type(2))) unsigned u32x2;
typedef __attribute__((ext_vector_type(4))) unsigned u32x4;
typedef LAS unsigned char* ldsp;
typedef __amdgpu_buffer_rsrc_t rsrc_t;

#define D0 3
#define D1 3
#define D2 0
#define PAR0 0

constexpr int S_LEN = 2048, DM = 1024, NH = 32, DH = 128, NSEG = 64;
constexpr int LDX = 3 * NH * DH;
constexpr int LDB3 = 3072;
constexpr unsigned SENT = 0xFFFFFFFFu;
constexpr size_t WS_CTL = 0;
constexpr size_t WS_XCH = 2u << 20;
constexpr int WIN_PART = 0, WIN_MEM = 256, WIN_QKV = 384, NWIN = 384 + 2048;
constexpr size_t XCH_BYTES = (size_t)NWIN * 16384;
constexpr size_t WS_QKVX = 42u << 20;
constexpr size_t WS_QKVB = WS_QKVX + (size_t)S_LEN * LDX * 2;
constexpr size_t WS_OA = WS_QKVB + (size_t)S_LEN * LDB3 * 2;
constexpr size_t WS_OB = WS_OA + (size_t)S_LEN * 4096 * 2;
constexpr size_t WS_END = WS_OB + (size_t)S_LEN * 1024 * 2;

DI unsigned pk_bf16(float a, float b) { f32x2 v = {a, b}; return __builtin_bit_cast(unsigned, __builtin_convertvector(v, bf16x2)); }
DI s16x4 trd(ldsp p) { return __builtin_amdgcn_ds_read_tr16_b64_v4i16((LAS s16x4*)p); }
DI bf16x8 cat8(s16x4 a, s16x4 b) { return __builtin_bit_cast(bf16x8, __builtin_shufflevector(a, b, 0, 1, 2, 3, 4, 5, 6, 7)); }
DI bf16x16 cat16(bf16x8 a, bf16x8 b) { return __builtin_shufflevector(a, b, 0, 1, 2, 3, 4, 5, 6, 7, 8, 9, 10, 11, 12, 13, 14, 15); }
DI bf16x8 lo8(bf16x16 v) { return __builtin_shufflevector(v, v, 0, 1, 2, 3, 4, 5, 6, 7); }
DI bf16x8 hi8(bf16x16 v) { return __builtin_shufflevector(v, v, 8, 9, 10, 11, 12, 13, 14, 15); }
DI bf16x8 asbf(u32x4 v) { return __builtin_bit_cast(bf16x8, v); }
DI f32x4 mma32(bf16x8 a, bf16x8 b, f32x4 c) { return __builtin_amdgcn_mfma_f32_16x16x32_bf16(a, b, c, 0, 0, 0); }
DI f32x4 mma16(u32x2 a, u32x2 b, f32x4 c) { return mma32(asbf((u32x4){a.x, a.y, 0u, 0u}), asbf((u32x4){b.x, b.y, 0u, 0u}), c); }
DI f32x4 smma(u32x4 a, bf16x16 b, f32x4 c, int idx) { return __builtin_amdgcn_smfmac_f32_16x16x64_bf16(asbf(a), b, c, idx, 0, 0); }
DI rsrc_t mkrsrc(const void* p, unsigned bytes) { return __builtin_amdgcn_make_buffer_rsrc((void*)p, 0, bytes, 0x00020000); }
DI u32x4 ld16_sc1(rsrc_t r, unsigned voff, unsigned soff) { return __builtin_amdgcn_raw_buffer_load_b128(r, voff, soff, 16); }
DI void swap16(unsigned& a, unsigned& b) { auto r = __builtin_amdgcn_permlane16_swap(a, b, false, false); a = r[0]; b = r[1]; }
DI void swap32(unsigned& a, unsigned& b) { auto r = __builtin_amdgcn_permlane32_swap(a, b, false, false); a = r[0]; b = r[1]; }
DI void swap16f(float& a, float& b) { unsigned x = __float_as_uint(a), y = __float_as_uint(b); swap16(x, y); a = __uint_as_float(x); b = __uint_as_float(y); }
DI void swap32f(float& a, float& b) { unsigned x = __float_as_uint(a), y = __float_as_uint(b); swap32(x, y); a = __uint_as_float(x); b = __uint_as_float(y); }
DI float rows_max(float v) { float a = v, b = v; swap16f(a, b); v = fmaxf(a, b); a = v; b = v; swap32f(a, b); return fmaxf(a, b); }
DI float rows_sum(float v) { float a = v, b = v; swap16f(a, b); v = a + b; a = v; b = v; swap32f(a, b); return a + b; }
DI unsigned umax4(u32x4 v) { return max(max(v.x, v.y), max(v.z, v.w)); }
#define WAIT_V(n) asm volatile("s_waitcnt vmcnt(" #n ")" ::: "memory")
#define WAIT_L(n) asm volatile("s_waitcnt lgkmcnt(" #n ")" ::: "memory")
#define BAR() __builtin_amdgcn_s_barrier()
#define SCHED() __builtin_amdgcn_sched_barrier(0)
#define LDS_BAR() do { asm volatile("s_waitcnt lgkmcnt(0)" ::: "memory"); __builtin_amdgcn_s_barrier(); asm volatile("" ::: "memory"); } while (0)

constexpr int HTB = 128 * 64 * 2;
DI int lds_byte(int r, int c) { const int st = (r >> 4) * 2 + (c >> 5), rr = r & 15, cc = c & 31, ob = rr * 64 + cc * 2; return st * 1024 + (ob ^ (((ob >> 9) & 1) << 5)); }
DI void stage_rc(int b, int& R, int& C) { const int st = b / 1024, sb = b % 1024, swz = sb ^ (((sb >> 9) & 1) << 5); R = (st >> 1) * 16 + swz / 64; C = (st & 1) * 32 + (swz % 64) / 2; }

struct Unit { const char* A; const char* B; char* C; unsigned lda, ldb, ldc; int nt; };

template <class Sched>
DI void gemm_stream(ldsp lds, const Sched& S) {
    const int tid = threadIdx.x, wid = __builtin_amdgcn_readfirstlane(tid >> 6), lane = tid & 63, wr = wid >> 2, wc = wid & 3, fr = lane & 15, fq = lane >> 4;
    int RA, CA; stage_rc(tid * 16, RA, CA);
    const int kB = 8 * (wid >> 1) + (((lane & 15) >> 1) ^ (((wid >> 1) & 1) << 2));
    const int nB2 = (((wid & 1) * 4 + (lane >> 4)) * 16 + (lane & 1) * 8) * 2;
    const unsigned ldsw = (unsigned)wid * 1024u;
    const int aoff = lds_byte(wr * 64 + fr, fq * 8);
    const int boff0 = fq * 2048 + wc * 512 + (((fr >> 2) ^ ((fq & 1) << 2)) * 32) + (fr & 3) * 8, boff1 = boff0 ^ 128;
#define SA(b, h) (((b) * 2 + (h)) * HTB)
#define SB(b, h) ((4 + (b) * 2 + (h)) * HTB)
#define STAGE(bufoff, gbase, voff, step2) do { \
        __builtin_amdgcn_global_load_lds((const unsigned*)((gbase) + (voff)), (LAS unsigned*)(lds + (bufoff) + ldsw), 16, 0, 0); \
        __builtin_amdgcn_global_load_lds((const unsigned*)((gbase) + (step2) + (voff)), (LAS unsigned*)(lds + (bufoff) + ldsw + 8192), 16, 0, 0); } while (0)
#define LDA(dst, b, h) do { _Pragma("unroll") for (int m = 0; m < 4; ++m) _Pragma("unroll") for (int k = 0; k < 2; ++k) dst[m][k] = *(const LAS bf16x8*)(lds + SA(b, h) + aoff + m * 2048 + k * 1024); } while (0)
#define LDB(dst, b, h) do { _Pragma("unroll") for (int n = 0; n < 2; ++n) _Pragma("unroll") for (int k = 0; k < 2; ++k) \
        dst[n][k] = cat8(trd(lds + SB(b, h) + boff0 + n * 256 + k * 8192), trd(lds + SB(b, h) + boff1 + n * 256 + k * 8192)); } while (0)
#define MMA(ai, bj, At, Bt) do { __builtin_amdgcn_s_setprio(1); _Pragma("unroll") for (int m = 0; m < 4; ++m) _Pragma("unroll") for (int n = 0; n < 2; ++n) _Pragma("unroll") for (int k = 0; k < 2; ++k) \
        acc[ai][bj][m][n] = mma32(Bt[n][k], At[m][k], acc[ai][bj][m][n]); __builtin_amdgcn_s_setprio(0); } while (0)
    Unit cur, nxt; int ui = 0;
    if (!S.next(0, cur)) return;
    f32x4 acc[2][2][4][2];
#pragma unroll
    for (int a = 0; a < 2; ++a)
#pragma unroll
        for (int b = 0; b < 2; ++b)
#pragma unroll
            for (int m = 0; m < 4; ++m)
#pragma unroll
                for (int n = 0; n < 2; ++n) acc[a][b][m][n] = (f32x4){0.f, 0.f, 0.f, 0.f};
    bf16x8 At[4][2], B0[2][2], B1[2][2];
    unsigned vA = (unsigned)RA * cur.lda + (unsigned)CA * 2u, vB = (unsigned)kB * cur.ldb + (unsigned)nB2;
    S.a_ready(cur);
    {
        const size_t a2s = (size_t)64 * cur.lda, ah = (size_t)128 * cur.lda, b2s = (size_t)32 * cur.ldb, bk = (size_t)64 * cur.ldb;
        STAGE(SB(0, 0), cur.B, vB, b2s); STAGE(SB(0, 1), cur.B + 256, vB, b2s); STAGE(SA(0, 0), cur.A, vA, a2s); STAGE(SA(0, 1), cur.A + ah, vA, a2s);
        if (wr == 1) BAR();
        WAIT_V(2); BAR();
        STAGE(SB(1, 0), cur.B + bk, vB, b2s); STAGE(SA(1, 0), cur.A + 128, vA, a2s); STAGE(SB(1, 1), cur.B + bk + 256, vB, b2s);
        WAIT_V(6); BAR();
    }
    for (;;) {
        const bool has_next = S.next(ui + 1, nxt);
        if (!has_next) nxt = cur;
        const int nt = cur.nt;
        for (int t = 0; t < nt; t += 2) {
            const bool last = (t == nt - 2);
            const char* a1 = cur.A + (size_t)(t + 1) * 128 + (size_t)128 * cur.lda;
            const size_t a1s = (size_t)64 * cur.lda; const unsigned vA1 = vA;
            if (last) { if (has_next) S.a_ready(nxt); vA = (unsigned)RA * nxt.lda + (unsigned)CA * 2u; vB = (unsigned)kB * nxt.ldb + (unsigned)nB2; }
            const unsigned lda = last ? nxt.lda : cur.lda, ldb = last ? nxt.ldb : cur.ldb;
            const char* a2 = last ? nxt.A : cur.A + (size_t)(t + 2) * 128;
            const char* b2 = last ? nxt.B : cur.B + (size_t)(t + 2) * 64 * cur.ldb;
            const size_t a2s = (size_t)64 * lda, ah = (size_t)128 * lda, b2s = (size_t)32 * ldb;
            const char* a3 = a2 + 128; const char* b3 = b2 + (size_t)64 * ldb;
            LDB(B0, 0, 0); LDB(B1, 0, 1); SCHED(); LDA(At, 0, 0); STAGE(SA(1, 1), a1, vA1, a1s);
            WAIT_V(8); WAIT_L(0); BAR(); MMA(0, 0, At, B0); MMA(0, 1, At, B1); BAR(); SCHED();
            LDA(At, 0, 1); STAGE(SB(0, 0), b2, vB, b2s); STAGE(SB(0, 1), b2 + 256, vB, b2s); STAGE(SA(0, 0), a2, vA, a2s);
            WAIT_V(8); WAIT_L(0); BAR(); MMA(1, 0, At, B0); MMA(1, 1, At, B1); BAR(); SCHED();
            LDB(B0, 1, 0); LDB(B1, 1, 1); SCHED(); LDA(At, 1, 0); STAGE(SA(0, 1), a2 + ah, vA, a2s);
            WAIT_V(8); WAIT_L(0); BAR(); MMA(0, 0, At, B0); MMA(0, 1, At, B1); BAR(); SCHED();
            LDA(At, 1, 1); STAGE(SB(1, 0), b3, vB, b2s); STAGE(SB(1, 1), b3 + 256, vB, b2s); STAGE(SA(1, 0), a3, vA, a2s);
            WAIT_V(8); WAIT_L(0); BAR(); MMA(1, 0, At, B0); MMA(1, 1, At, B1); BAR(); SCHED();
        }
        if (wr == 0) BAR();
        {
            char* cp = cur.C + (size_t)(wr * 64 + fr) * cur.ldc + (size_t)(wc * 32 + (fq & 1) * 16 + (fq >> 1) * 8) * 2;
#pragma unroll
            for (int ai = 0; ai < 2; ++ai)
#pragma unroll
                for (int m = 0; m < 4; ++m)
#pragma unroll
                    for (int bj = 0; bj < 2; ++bj) {
                        const f32x4 v0 = acc[ai][bj][m][0], v1 = acc[ai][bj][m][1];
                        unsigned ax = pk_bf16(v0[0], v0[1]), ay = pk_bf16(v0[2], v0[3]), bx = pk_bf16(v1[0], v1[1]), by = pk_bf16(v1[2], v1[3]);
                        swap16(ax, bx); swap16(ay, by);
                        *(u32x4*)(cp + (size_t)(ai * 128 + m * 16) * cur.ldc + bj * 256) = (u32x4){ax, ay, bx, by};
                    }
        }
        S.done(cur);
        if (!has_next) break;
#pragma unroll
        for (int a = 0; a < 2; ++a)
#pragma unroll
            for (int b = 0; b < 2; ++b)
#pragma unroll
                for (int m = 0; m < 4; ++m)
#pragma unroll
                    for (int n = 0; n < 2; ++n) acc[a][b][m][n] = (f32x4){0.f, 0.f, 0.f, 0.f};
        cur = nxt; ++ui;
        if (wr == 1) BAR();
    }
    WAIT_V(0);
    BAR();
#undef SA
#undef SB
#undef STAGE
#undef LDA
#undef LDB
#undef MMA
}

struct Params {
    const u16 *ax, *mem0, *wq, *wk, *wv, *wo, *bx, *bwqkv, *bwo;
    u16* out; char* ws;
};

struct SchedP {
    Params p; int phase, G, c;
    DI bool next(int i, Unit& u) const {
        const int L = i * G + c;
        if (phase == 0) {
            if (L >= 480) return false;
            if (L < 384) { const int pm = L & 7, pn = L >> 3, mat = pn >> 4; const u16 *w0 = p.wq, *w1 = p.wk, *w2 = p.wv; const u16* W = mat == 0 ? w0 : (mat == 1 ? w1 : w2);
                u.A = (const char*)(p.ax + (size_t)pm * 256 * DM); u.lda = DM * 2; u.B = (const char*)(W + (pn & 15) * 256); u.ldb = 4096 * 2; u.nt = 16;
                u.C = p.ws + WS_QKVX + ((size_t)pm * 256 * LDX + (size_t)pn * 256) * 2; u.ldc = LDX * 2; }
            else { const int q = L - 384, pm = q & 7, pn = q >> 3;
                u.A = (const char*)(p.bx + (size_t)pm * 256 * DM); u.lda = DM * 2; u.B = (const char*)(p.bwqkv + pn * 256); u.ldb = LDB3 * 2; u.nt = 16;
                u.C = p.ws + WS_QKVB + ((size_t)pm * 256 * LDB3 + (size_t)pn * 256) * 2; u.ldc = LDB3 * 2; }
        } else {
            if (L >= 64) return false;
            if (L < 32) { const int pm = L & 7, pn = L >> 3;
                u.A = p.ws + WS_OA + (size_t)pm * 256 * 4096 * 2; u.lda = 4096 * 2; u.B = (const char*)(p.wo + pn * 256); u.ldb = DM * 2; u.nt = 64;
                u.C = (char*)(p.out + (size_t)pm * 256 * 2048 + pn * 256); u.ldc = 2048 * 2; }
            else { const int q = L - 32, pm = q & 7, pn = q >> 3;
                u.A = p.ws + WS_OB + (size_t)pm * 256 * 1024 * 2; u.lda = 1024 * 2; u.B = (const char*)(p.bwo + pn * 256); u.ldb = DM * 2; u.nt = 16;
                u.C = (char*)(p.out + (size_t)pm * 256 * 2048 + 1024 + pn * 256); u.ldc = 2048 * 2; }
        }
        return true;
    }
    DI void a_ready(const Unit&) const {}
    DI void done(const Unit&) const {}
};

__global__ __launch_bounds__(512, 2) void k_gemm(Params p, int phase) {
    extern __shared__ __attribute__((aligned(16))) unsigned char shm[];
    SchedP S{p, phase, (int)gridDim.x, (int)blockIdx.x};
    gemm_stream((ldsp)shm, S);
}

DI unsigned win_off(int idx) { return (unsigned)idx * 16384u + ((((unsigned)idx >> 7) ^ PAR0) & 1u) * 8192u; }
DI bool spin_out(unsigned& spins, unsigned* tmo) {
    if ((++spins & 63u) == 0u) {
        if (spins > (1u << 18)) { __hip_atomic_store(tmo, 1u, __ATOMIC_RELAXED, __HIP_MEMORY_SCOPE_AGENT); return true; }
        if (__builtin_amdgcn_readfirstlane(__hip_atomic_load(tmo, __ATOMIC_RELAXED, __HIP_MEMORY_SCOPE_AGENT))) return true;
    }
    return false;
}
constexpr int L_RED = 0, L_PX = 30720, L_PM = L_PX + 1024, L_VX = 32768;

DI void chain_role(const Params& p, int rank, ldsp lds, unsigned long long* stamps) {
    const int tid = threadIdx.x, w = __builtin_amdgcn_readfirstlane(tid >> 6), lane = tid & 63, g = lane >> 4, n = lane & 15;
    const int h = rank >> 2, j = rank & 3;
    unsigned* tmo = (unsigned*)(p.ws + WS_CTL);
    const rsrc_t xr = mkrsrc(p.ws + WS_XCH, (unsigned)XCH_BYTES);
    const rsrc_t qr = mkrsrc(p.ws + WS_QKVX, (unsigned)((size_t)S_LEN * LDX * 2));
    if (w == 7 && lane < 32) {
        const int G = rank >> 1, m = 4 * (rank & 1) + (n >> 2), col = 16 * G + 4 * (n & 3) + 2 * g;
        const unsigned v = *(const unsigned*)(p.mem0 + m * DM + col);
        __builtin_amdgcn_raw_buffer_store_b32(min(v, SENT - 1), xr, (unsigned)lane * 4u + (unsigned)(rank & 63) * 128u, win_off(WIN_MEM + (rank >> 6)), 16);
    }
    bf16x16 W[6][2], WO[2][2];
    {
        ldsp my = lds + w * 16384;
        const int rrow = lane >> 2, rc = (lane & 3) * 8;
        const int toff = (8 * g + (n >> 2)) * 64 + (n & 3) * 8;
#define FRAG(base) cat16(cat8(trd((base)), trd((base) + 256)), cat8(trd((base) + 2048), trd((base) + 2048 + 256)))
#pragma unroll
        for (int i = 0; i < 8; ++i) {
            __builtin_amdgcn_global_load_lds((const unsigned*)(p.wq + (size_t)(128 * w + 16 * i + rrow) * 4096 + h * 128 + 32 * j + rc), (LAS unsigned*)(my + i * 1024), 16, 0, 0);
            __builtin_amdgcn_global_load_lds((const unsigned*)(p.wk + (size_t)(128 * w + 16 * i + rrow) * 4096 + h * 128 + 32 * j + rc), (LAS unsigned*)(my + 8192 + i * 1024), 16, 0, 0);
        }
        WAIT_V(0);
#pragma unroll
        for (int T = 0; T < 4; ++T)
#pragma unroll
            for (int t = 0; t < 2; ++t) W[T][t] = FRAG(my + (T >> 1) * 8192 + t * 4096 + (T & 1) * 32 + toff);
#pragma unroll
        for (int T = 0; T < 4; ++T) asm volatile("" : "+v"(W[T][0]), "+v"(W[T][1]));
        WAIT_L(0);
#pragma unroll
        for (int i = 0; i < 8; ++i) {
            __builtin_amdgcn_global_load_lds((const unsigned*)(p.wv + (size_t)(128 * w + 16 * i + rrow) * 4096 + h * 128 + 32 * j + rc), (LAS unsigned*)(my + i * 1024), 16, 0, 0);
            __builtin_amdgcn_global_load_lds((const unsigned*)(p.wo + (size_t)(h * 128 + 16 * i + rrow) * 1024 + 256 * j + 32 * w + rc), (LAS unsigned*)(my + 8192 + i * 1024), 16, 0, 0);
        }
        WAIT_V(0);
#pragma unroll
        for (int T = 0; T < 2; ++T)
#pragma unroll
            for (int t = 0; t < 2; ++t) { W[4 + T][t] = FRAG(my + t * 4096 + T * 32 + toff); WO[T][t] = FRAG(my + 8192 + t * 4096 + T * 32 + toff); }
#pragma unroll
        for (int T = 0; T < 2; ++T) asm volatile("" : "+v"(W[4 + T][0]), "+v"(W[4 + T][1]), "+v"(WO[T][0]), "+v"(WO[T][1]));
        WAIT_L(0);
#undef FRAG
    }
    BAR();
    const int sidx = ((n >> 2) & 1) ? 0xEEEE : 0x4444;
    const unsigned l16 = (unsigned)lane * 16u;
    const int redw = (L_RED / 4 + w * 960 + ((g & 1) * 16 + n) * 8 + (((g & 1) * 16 + n) >> 2) * 8 + 4 * (g >> 1)) * 4;
    int redr;
    { int mat, m, col;
      if (w < 4) { const int r = lane >> 2; mat = r >> 3; m = r & 7; col = 8 * w + 2 * (lane & 3); }
      else { const int r = lane >> 2; mat = 2; m = 4 * (r >> 3) + (r & 3); col = 16 * ((w - 4) & 1) + 4 * (lane & 3) + 2 * ((r >> 2) & 1); }
      redr = (mat * 320 + col * 8 + (col >> 2) * 8 + m) * 4; }
    const unsigned vg_off = (unsigned)(g >> 1) * 1536u + 1024u + (unsigned)(lane & 31) * 16u;
    const unsigned pst_off = (unsigned)(2 * (16 * j + 2 * w) + (n >> 2)) * 16384u + (unsigned)h * 256u + (unsigned)(n & 3) * 64u + (unsigned)g * 16u;
    const unsigned kx_off = (unsigned)n * (LDX * 2) + (unsigned)(4096 + h * 128 + g * 8) * 2u;
    const int vxr = L_VX + n * 256;
    const float csc = 0.08838834764831845f * 1.4426950408889634f;

    u32x4 TR[8];
    u32x4 VWX[2];
    const unsigned vx_src = (unsigned)(8 * (w & 3) + g) * (LDX * 2) + (unsigned)(8192 + h * 128) * 2u;
    auto dma_vx = [&](int s) {
        if (w < 4) {
#pragma unroll
            for (int i = 0; i < 2; ++i)
                __builtin_amdgcn_raw_ptr_buffer_load_lds(qr, (LAS void*)(lds + L_VX + (s & 1) * 8192 + (w * 2 + i) * 1024), 16,
                                                         vx_src + (unsigned)((n ^ ((8 * w + 4 * i + g) & 15)) * 16), (unsigned)(32 * s + 4 * i) * (LDX * 2), 0, 16);
        }
    };
    auto load_kx = [&](int s) {
#pragma unroll
        for (int kt = 0; kt < 2; ++kt)
#pragma unroll
            for (int kk = 0; kk < 4; ++kk) TR[kt * 4 + kk] = ld16_sc1(qr, kx_off + kk * 64, (unsigned)(32 * s + 16 * kt) * (LDX * 2));
    };
    auto vwx_kt = [&](int s, int kt) {
        bf16x8 VA[4];
#pragma unroll
        for (int kk = 0; kk < 4; ++kk) VA[kk] = *(const LAS bf16x8*)(lds + vxr + (s & 1) * 8192 + kt * 4096 + (((4 * kk + g) ^ n) * 16));
#pragma unroll
        for (int ct = 0; ct < 2; ++ct) {
            f32x4 d = {0.f, 0.f, 0.f, 0.f};
#pragma unroll
            for (int kk = 0; kk < 4; ++kk) d = mma32(VA[kk], (kk & 1) ? hi8(WO[ct][kk >> 1]) : lo8(WO[ct][kk >> 1]), d);
            VWX[ct][2 * kt] = pk_bf16(d[0], d[1]); VWX[ct][2 * kt + 1] = pk_bf16(d[2], d[3]);
        }
    };
    dma_vx(0); dma_vx(1);
    if (w == 6) load_kx(0);
    WAIT_V(0); BAR();
    vwx_kt(0, 0); vwx_kt(0, 1);

    unsigned spins = 0;
#define STAMP(x)
    for (int s = 0; s < NSEG; ++s) {
        for (;;) {
            const unsigned so = win_off(WIN_MEM + 2 * s + (w >> 2)) + (unsigned)(w & 3) * 2048u;
            asm volatile("" ::: "memory");
            const u32x4 a0 = ld16_sc1(xr, l16, so), a1 = ld16_sc1(xr, l16 + 1024, so);
#pragma unroll
            for (int mt = 0; mt < 3; ++mt) {
                f32x4 ca = {0.f, 0.f, 0.f, 0.f}, cb = {0.f, 0.f, 0.f, 0.f};
                ca = smma(a0, W[2 * mt][0], ca, sidx); cb = smma(a0, W[2 * mt + 1][0], cb, sidx);
                ca = smma(a1, W[2 * mt][1], ca, sidx); cb = smma(a1, W[2 * mt + 1][1], cb, sidx);
                f32x4 r;
#pragma unroll
                for (int i = 0; i < 4; ++i) { float x = ca[i], y = cb[i]; swap16f(x, y); r[i] = x + y; }
                *(LAS f32x4*)(lds + redw + mt * 1280) = r;
            }
            const bool ok = max(umax4(a0), umax4(a1)) != SENT;
            if (__all(ok)) break;
            if (spin_out(spins, tmo)) break;
        }
        STAMP(T1);
        LDS_BAR();
        const unsigned qwin = win_off(WIN_QKV + 32 * s + h);
        if (w < 6) {
            f32x2 v[8];
#pragma unroll
            for (int ww = 0; ww < 8; ++ww) { const LAS float* q = (const LAS float*)(lds + redr + ww * 3840); v[ww] = (f32x2){q[0], q[8]}; }
            const f32x2 t = ((v[0] + v[1]) + (v[2] + v[3])) + ((v[4] + v[5]) + (v[6] + v[7]));
            __builtin_amdgcn_raw_buffer_store_b32(min(pk_bf16(t.x, t.y), SENT - 1), xr, (unsigned)lane * 4u, qwin + (unsigned)j * 1536u + (unsigned)w * 256u, 16);
        } else if (w == 7 && s > 0) {
            const u32x4 sv = {SENT, SENT, SENT, SENT};
            const unsigned so = win_off(WIN_PART + ((s - 1) & 1) * 128 + rank);
#pragma unroll
            for (int i = 0; i < 8; ++i) __builtin_amdgcn_raw_buffer_store_b128(sv, xr, l16 + (i & 3) * 1024, so + (i >> 2) * 4096, 16);
        }
        STAMP(T2);
        if (s == NSEG - 1) break;
        if (D1) __builtin_amdgcn_s_sleep(D1);
        u32x2 VWM[2];
        for (;;) {
            asm volatile("" ::: "memory");
            u32x4 X[4];
            if (w == 6) {
#pragma unroll
                for (int k = 0; k < 4; ++k) X[k] = ld16_sc1(xr, l16 + (k & 1) * 1536, qwin + (k >> 1) * 3072);
            }
            const u32x4 v0 = ld16_sc1(xr, vg_off, qwin), v1 = ld16_sc1(xr, vg_off, qwin + 3072);
            unsigned mx4 = 0;
            if (w == 6) {
                f32x4 sm = {0.f, 0.f, 0.f, 0.f}, s0 = sm, s1 = sm;
#pragma unroll
                for (int k = 0; k < 4; ++k) { s0 = mma32(asbf(TR[k]), asbf(X[k]), s0); s1 = mma32(asbf(TR[4 + k]), asbf(X[k]), s1); sm = mma32(asbf(X[k]), asbf(X[k]), sm); }
                mx4 = max(max(umax4(X[0]), umax4(X[1])), max(umax4(X[2]), umax4(X[3])));
                const float ninf = -__builtin_inff();
                if (g < 2) sm = (f32x4){ninf, ninf, ninf, ninf};
                float m = fmaxf(fmaxf(fmaxf(s0[0], s0[1]), fmaxf(s0[2], s0[3])), fmaxf(fmaxf(s1[0], s1[1]), fmaxf(s1[2], s1[3])));
                m = fmaxf(m, fmaxf(fmaxf(sm[0], sm[1]), fmaxf(sm[2], sm[3])));
                m = rows_max(m);
                const float mc = -m * csc;
                float sum = 0.f;
#pragma unroll
                for (int i = 0; i < 4; ++i) { s0[i] = __builtin_amdgcn_exp2f(fmaf(s0[i], csc, mc)); s1[i] = __builtin_amdgcn_exp2f(fmaf(s1[i], csc, mc)); sm[i] = __builtin_amdgcn_exp2f(fmaf(sm[i], csc, mc));
                    sum += (s0[i] + s1[i]) + sm[i]; }
                sum = rows_sum(sum);
                const float inv = __builtin_amdgcn_rcpf(sum);
                *(LAS u32x4*)(lds + L_PX + lane * 16) = (u32x4){pk_bf16(s0[0] * inv, s0[1] * inv), pk_bf16(s0[2] * inv, s0[3] * inv), pk_bf16(s1[0] * inv, s1[1] * inv), pk_bf16(s1[2] * inv, s1[3] * inv)};
                *(LAS u32x2*)(lds + L_PM + lane * 8) = (u32x2){pk_bf16(sm[0] * inv, sm[1] * inv), pk_bf16(sm[2] * inv, sm[3] * inv)};
            }
#pragma unroll
            for (int ct = 0; ct < 2; ++ct) {
                f32x4 d = {0.f, 0.f, 0.f, 0.f};
                d = smma(v0, WO[ct][0], d, sidx); d = smma(v1, WO[ct][1], d, sidx);
                VWM[ct] = (u32x2){pk_bf16(d[0], d[1]), pk_bf16(d[2], d[3])};
            }
            const bool ok = max(mx4, max(umax4(v0), umax4(v1))) != SENT;
            if (__all(ok)) break;
            if (spin_out(spins, tmo)) break;
        }
        STAMP(T3);
        LDS_BAR();
        {
            const u32x4 px = *(const LAS u32x4*)(lds + L_PX + lane * 16);
            const u32x2 pm = *(const LAS u32x2*)(lds + L_PM + ((2 + (g >> 1)) * 16 + n) * 8);
            const unsigned so = win_off(WIN_PART + (s & 1) * 128);
#pragma unroll
            for (int ct = 0; ct < 2; ++ct) {
                f32x4 d = {0.f, 0.f, 0.f, 0.f};
                d = mma32(asbf(VWX[ct]), asbf(px), d); d = mma16(VWM[ct], pm, d);
                if (n < 8) __builtin_amdgcn_raw_buffer_store_b128(__builtin_bit_cast(u32x4, d), xr, pst_off, so + ct * 32768, 16);
            }
        }
        STAMP(T4);
        vwx_kt(s + 1, 0);
        if (D2) __builtin_amdgcn_s_sleep(D2);
        if (w == 7) {
            const unsigned so = win_off(WIN_PART + (s & 1) * 128 + rank);
            bool first = true;
            for (;;) {
                asm volatile("" ::: "memory");
                u32x4 (&q)[8] = TR;
#pragma unroll
                for (int i = 0; i < 8; ++i) q[i] = ld16_sc1(xr, l16 + (i & 3) * 1024, so + (i >> 2) * 4096);
                if (first) vwx_kt(s + 1, 1);
                first = false;
                f32x4 t = ((__builtin_bit_cast(f32x4, q[0]) + __builtin_bit_cast(f32x4, q[1])) + (__builtin_bit_cast(f32x4, q[2]) + __builtin_bit_cast(f32x4, q[3]))) +
                          ((__builtin_bit_cast(f32x4, q[4]) + __builtin_bit_cast(f32x4, q[5])) + (__builtin_bit_cast(f32x4, q[6]) + __builtin_bit_cast(f32x4, q[7])));
                float x = t[0], z = t[2], y = t[1], u = t[3];
                swap16f(x, z); swap16f(y, u);
                x += z; y += u;
                float x2 = x, y2 = y; swap32f(x, x2); swap32f(y, y2);
                x += x2; y += y2;
                bool fin = !__any((x != x) | (y != y));
                if (!fin) {
                    unsigned mx = 0;
#pragma unroll
                    for (int i = 0; i < 8; ++i) mx = max(mx, umax4(q[i]));
                    fin = __all(mx != SENT);
                    if (!fin) fin = spin_out(spins, tmo);
                }
                if (fin) {
                    if (lane < 32) __builtin_amdgcn_raw_buffer_store_b32(min(pk_bf16(x, y), SENT - 1), xr, (unsigned)lane * 4u + (unsigned)(rank & 63) * 128u, win_off(WIN_MEM + 2 * (s + 1) + (rank >> 6)), 16);
                    break;
                }
            }
        } else {
            vwx_kt(s + 1, 1);
            if (s + 2 < NSEG - 1) dma_vx(s + 2);
            if (w == 6) load_kx(min(s + 1, NSEG - 2));
        }
        STAMP(T5);
        BAR();
        if (D0) __builtin_amdgcn_s_sleep(D0);
    }
}

__global__ __launch_bounds__(512, 2) void k_chain(Params p) {
    extern __shared__ __attribute__((aligned(16))) unsigned char shm[];
    const int b = blockIdx.x;
    if ((b & 7) >= 4) return;
    chain_role(p, (b & 3) * 32 + (b >> 3), (ldsp)shm, (unsigned long long*)(p.ws + WS_END));
}

DI void attn_a_item(const Params& p, int s, int h, ldsp my, int lane) {
    const int g = lane >> 4, n = lane & 15;
    const char* X = p.ws + WS_QKVX;
    const rsrc_t xr = mkrsrc(p.ws + WS_XCH, (unsigned)XCH_BYTES);
#pragma unroll
    for (int i = 0; i < 8; ++i) { const int row = 4 * i + g;
        __builtin_amdgcn_global_load_lds((const unsigned*)(X + ((size_t)(32 * s + row) * LDX + 8192 + h * 128 + ((n ^ ((row & 7) << 1)) * 8)) * 2), (LAS unsigned*)(my + i * 1024), 16, 0, 0); }
    u32x4 Q[2][4], K[2][4], XM[4];
    const char* rp = X + ((size_t)(32 * s + n) * LDX + h * 128 + g * 8) * 2;
#pragma unroll
    for (int t = 0; t < 2; ++t)
#pragma unroll
        for (int k = 0; k < 4; ++k) { Q[t][k] = *(const u32x4*)(rp + (size_t)t * 16 * LDX * 2 + k * 64); K[t][k] = *(const u32x4*)(rp + (size_t)t * 16 * LDX * 2 + 8192 + k * 64); }
    const unsigned qwin = win_off(WIN_QKV + 32 * s + h);
#pragma unroll
    for (int k = 0; k < 4; ++k) XM[k] = ld16_sc1(xr, (unsigned)lane * 16u + k * 1536, qwin);
#pragma unroll
    for (int ts = 0; ts < 2; ++ts) {
        const u32x4 v = ld16_sc1(xr, (unsigned)(g >> 1) * 1536u + 1024u + (unsigned)(lane & 31) * 16u + ts * 3072, qwin);
        const int m = 4 * (n >> 3) + (n & 3), hf = (n >> 2) & 1, row = 32 + m;
#pragma unroll
        for (int i = 0; i < 4; ++i) { const int e = 64 * ts + 16 * g + 4 * i + 2 * hf;
            *(LAS unsigned*)(my + row * 256 + (((e >> 3) ^ ((row & 7) << 1)) * 16) + (e & 7) * 2) = v[i]; }
    }
    const float csc = 0.08838834764831845f * 1.4426950408889634f, ninf = -__builtin_inff();
    u32x4 PX[2]; u32x2 PM[2];
#pragma unroll
    for (int qt = 0; qt < 2; ++qt) {
        f32x4 sm = {0.f, 0.f, 0.f, 0.f}, s0 = sm, s1 = sm;
#pragma unroll
        for (int k = 0; k < 4; ++k) { sm = mma32(asbf(XM[k]), asbf(Q[qt][k]), sm); s0 = mma32(asbf(K[0][k]), asbf(Q[qt][k]), s0); if (qt == 1) s1 = mma32(asbf(K[1][k]), asbf(Q[qt][k]), s1); }
#pragma unroll
        for (int i = 0; i < 4; ++i) {
            if (g < 2) sm[i] = ninf;
            if (qt == 0) { if (4 * g + i > n) s0[i] = ninf; s1[i] = ninf; }
            else if (4 * g + i > n) s1[i] = ninf;
        }
        float m = fmaxf(fmaxf(fmaxf(s0[0], s0[1]), fmaxf(s0[2], s0[3])), fmaxf(fmaxf(s1[0], s1[1]), fmaxf(s1[2], s1[3])));
        m = fmaxf(m, fmaxf(fmaxf(sm[0], sm[1]), fmaxf(sm[2], sm[3])));
        m = rows_max(m);
        const float mc = -m * csc; float sum = 0.f;
#pragma unroll
        for (int i = 0; i < 4; ++i) { s0[i] = __builtin_amdgcn_exp2f(fmaf(s0[i], csc, mc)); s1[i] = __builtin_amdgcn_exp2f(fmaf(s1[i], csc, mc)); sm[i] = __builtin_amdgcn_exp2f(fmaf(sm[i], csc, mc));
            sum += (s0[i] + s1[i]) + sm[i]; }
        sum = rows_sum(sum);
        const float inv = __builtin_amdgcn_rcpf(sum);
        PX[qt] = (u32x4){pk_bf16(s0[0] * inv, s0[1] * inv), pk_bf16(s0[2] * inv, s0[3] * inv), pk_bf16(s1[0] * inv, s1[1] * inv), pk_bf16(s1[2] * inv, s1[3] * inv)};
        PM[qt] = (u32x2){pk_bf16(sm[0] * inv, sm[1] * inv), pk_bf16(sm[2] * inv, sm[3] * inv)};
    }
    asm volatile("s_waitcnt vmcnt(0) lgkmcnt(0)" ::: "memory");
    const int q4 = n >> 2, p4 = n & 3;
    const int r0 = 4 * g + q4, rm = 32 + 4 * (g & 1) + q4;
    char* op = p.ws + WS_OA + ((size_t)(32 * s + n) * 4096 + h * 128 + (g & 1) * 16 + (g >> 1) * 8) * 2;
#pragma unroll
    for (int ep = 0; ep < 4; ++ep) {
        f32x4 d[2][2];
#pragma unroll
        for (int e2 = 0; e2 < 2; ++e2) {
            const int ch = (2 * ep + e2) * 2 + (p4 >> 1);
            const bf16x8 va = cat8(trd(my + r0 * 256 + ((ch ^ ((r0 & 7) << 1)) * 16) + (p4 & 1) * 8), trd(my + (r0 + 16) * 256 + ((ch ^ ((r0 & 7) << 1)) * 16) + (p4 & 1) * 8));
            const s16x4 vm = trd(my + rm * 256 + ((ch ^ ((rm & 7) << 1)) * 16) + (p4 & 1) * 8);
#pragma unroll
            for (int qt = 0; qt < 2; ++qt) { f32x4 c = {0.f, 0.f, 0.f, 0.f}; c = mma32(va, asbf(PX[qt]), c); d[qt][e2] = mma16(__builtin_bit_cast(u32x2, vm), PM[qt], c); }
        }
#pragma unroll
        for (int qt = 0; qt < 2; ++qt) {
            unsigned ax = pk_bf16(d[qt][0][0], d[qt][0][1]), ay = pk_bf16(d[qt][0][2], d[qt][0][3]), bx = pk_bf16(d[qt][1][0], d[qt][1][1]), by = pk_bf16(d[qt][1][2], d[qt][1][3]);
            swap16(ax, bx); swap16(ay, by);
            *(u32x4*)(op + (size_t)qt * 16 * 4096 * 2 + ep * 64) = (u32x4){ax, ay, bx, by};
        }
    }
    asm volatile("s_waitcnt lgkmcnt(0)" ::: "memory");
}

DI void attn_b_item(const Params& p, int hb, int qb, ldsp lds) {
    const int tid = threadIdx.x, w = __builtin_amdgcn_readfirstlane(tid >> 6), lane = tid & 63, g = lane >> 4, n = lane & 15;
    const char* X = p.ws + WS_QKVB;
    const int q0 = qb * 128;
#pragma unroll
    for (int i = 0; i < 3; ++i) { const int pc = w * 3 + i, r = pc * 16 + (lane >> 2), c = lane & 3; const int key = max(q0 - 256 + r, 0);
        const char* rp = X + ((size_t)key * LDB3 + hb * 32) * 2;
        __builtin_amdgcn_global_load_lds((const unsigned*)(rp + 2048 + ((c ^ (((r >> 3) & 1) * 3)) * 16)), (LAS unsigned*)(lds + pc * 1024), 16, 0, 0);
        __builtin_amdgcn_global_load_lds((const unsigned*)(rp + 4096 + ((c ^ (((r >> 2) & 1) << 1)) * 16)), (LAS unsigned*)(lds + 24576 + pc * 1024), 16, 0, 0); }
    const u32x4 Q = *(const u32x4*)(X + ((size_t)(q0 + 16 * w + n) * LDB3 + hb * 32 + g * 8) * 2);
    asm volatile("s_waitcnt vmcnt(0)" ::: "memory"); BAR(); asm volatile("" ::: "memory");
    f32x4 sc[17];
    const ldsp kp = lds + (16 * w + n) * 64 + ((g ^ (((n >> 3) & 1) * 3)) * 16);
#pragma unroll
    for (int t = 0; t < 17; ++t) sc[t] = mma32(*(const LAS bf16x8*)(kp + t * 1024), asbf(Q), (f32x4){0.f, 0.f, 0.f, 0.f});
    const float ninf = -__builtin_inff();
    const int tmin = (256 - q0 - 16 * w) >> 4;
#pragma unroll
    for (int i = 0; i < 4; ++i) { if (4 * g + i <= n) sc[0][i] = ninf; if (4 * g + i > n) sc[16][i] = ninf; }
    if (tmin > 0) {
#pragma unroll
        for (int t = 0; t < 16; ++t) if (t < tmin) sc[t] = (f32x4){ninf, ninf, ninf, ninf};
    }
    float m = ninf;
#pragma unroll
    for (int t = 0; t < 17; ++t) m = fmaxf(m, fmaxf(fmaxf(sc[t][0], sc[t][1]), fmaxf(sc[t][2], sc[t][3])));
    m = rows_max(m);
    const float csc = 0.17677669529663687f * 1.4426950408889634f, mc = -m * csc;
    float sum = 0.f;
#pragma unroll
    for (int t = 0; t < 17; ++t)
#pragma unroll
        for (int i = 0; i < 4; ++i) { sc[t][i] = __builtin_amdgcn_exp2f(fmaf(sc[t][i], csc, mc)); sum += sc[t][i]; }
    sum = rows_sum(sum);
    const float inv = __builtin_amdgcn_rcpf(sum);
    const int q4 = n >> 2, p4 = n & 3, r0 = 16 * w + 4 * g + q4;
    f32x4 d[2] = {{0.f, 0.f, 0.f, 0.f}, {0.f, 0.f, 0.f, 0.f}};
    const ldsp vp = lds + 24576 + r0 * 64 + (p4 & 1) * 8;
    const int sw = (g & 1) << 1;
#pragma unroll
    for (int tp = 0; tp < 8; ++tp) {
        const u32x4 P = {pk_bf16(sc[2 * tp][0] * inv, sc[2 * tp][1] * inv), pk_bf16(sc[2 * tp][2] * inv, sc[2 * tp][3] * inv),
                         pk_bf16(sc[2 * tp + 1][0] * inv, sc[2 * tp + 1][1] * inv), pk_bf16(sc[2 * tp + 1][2] * inv, sc[2 * tp + 1][3] * inv)};
#pragma unroll
        for (int et = 0; et < 2; ++et) { const int ch = ((2 * et + (p4 >> 1)) ^ sw) * 16;
            d[et] = mma32(cat8(trd(vp + tp * 2048 + ch), trd(vp + tp * 2048 + 1024 + ch)), asbf(P), d[et]); }
    }
    { const u32x2 P = {pk_bf16(sc[16][0] * inv, sc[16][1] * inv), pk_bf16(sc[16][2] * inv, sc[16][3] * inv)};
#pragma unroll
      for (int et = 0; et < 2; ++et) { const int ch = ((2 * et + (p4 >> 1)) ^ sw) * 16; d[et] = mma16(__builtin_bit_cast(u32x2, trd(vp + 16384 + ch)), P, d[et]); } }
    unsigned ax = pk_bf16(d[0][0], d[0][1]), ay = pk_bf16(d[0][2], d[0][3]), bx = pk_bf16(d[1][0], d[1][1]), by = pk_bf16(d[1][2], d[1][3]);
    swap16(ax, bx); swap16(ay, by);
    *(u32x4*)(p.ws + WS_OB + ((size_t)(q0 + 16 * w + n) * 1024 + hb * 32 + (g & 1) * 16 + (g >> 1) * 8) * 2) = (u32x4){ax, ay, bx, by};
    LDS_BAR();
}

__global__ __launch_bounds__(512, 2) void k_attn(Params p) {
    extern __shared__ __attribute__((aligned(16))) unsigned char shm[];
    const int b = blockIdx.x, w = __builtin_amdgcn_readfirstlane(threadIdx.x >> 6);
    if (b < 256) { const int it = b * 8 + w; attn_a_item(p, it >> 5, it & 31, (ldsp)shm + w * 10240, threadIdx.x & 63); }
    else { const int it = b - 256; attn_b_item(p, it & 31, it >> 5, (ldsp)shm); }
}

extern "C" void kernel_launch(void* const* d_in, const int* in_sizes, int n_in, void* d_out, int out_size, void* d_ws, size_t ws_size, hipStream_t stream) {
    Params p;
    p.ax = (const u16*)d_in[0]; p.mem0 = (const u16*)d_in[1]; p.wq = (const u16*)d_in[2]; p.wk = (const u16*)d_in[3]; p.wv = (const u16*)d_in[4]; p.wo = (const u16*)d_in[5];
    p.bx = (const u16*)d_in[7]; p.bwqkv = (const u16*)d_in[8]; p.bwo = (const u16*)d_in[9];
    p.out = (u16*)d_out; p.ws = (char*)d_ws;
    hipFuncSetAttribute((const void*)k_gemm, hipFuncAttributeMaxDynamicSharedMemorySize, 131072);
    hipFuncSetAttribute((const void*)k_chain, hipFuncAttributeMaxDynamicSharedMemorySize, 131072);
    hipFuncSetAttribute((const void*)k_attn, hipFuncAttributeMaxDynamicSharedMemorySize, 81920);
    hipMemsetAsync(p.ws + WS_CTL, 0, 65536, stream);
    hipMemsetAsync(p.ws + WS_XCH, 0xFF, XCH_BYTES, stream);
    k_gemm<<<256, 512, 131072, stream>>>(p, 0);
    k_chain<<<256, 512, 131072, stream>>>(p);
    k_attn<<<256 + 512, 512, 81920, stream>>>(p);
    k_gemm<<<64, 512, 131072, stream>>>(p, 1);
}
```

```cpp
#include <hip/hip_runtime.h>
#include <stdint.h>
#include <stdlib.h>

#define LAS __attribute__((address_space(3)))
#define DI __device__ __forceinline__
typedef unsigned short u16;
typedef __attribute__((ext_vector_type(8))) __bf16 bf16x8;
typedef __attribute__((ext_vector_type(16))) __bf16 bf16x16;
typedef __attribute__((ext_vector_type(2))) __bf16 bf16x2;
typedef __attribute__((ext_vector_type(4))) short s16x4;
typedef __attribute__((ext_vector_type(8))) short s16x8;
typedef __attribute__((ext_vector_type(16))) short s16x16;
typedef __attribute__((ext_vector_type(2))) float f32x2;
typedef __attribute__((ext_vector_type(4))) float f32x4;
typedef __attribute__((ext_vector_type(2))) unsigned u32x2;
typedef __attribute__((ext_vector_type(4))) unsigned u32x4;
typedef LAS unsigned char* ldsp;
typedef __amdgpu_buffer_rsrc_t rsrc_t;

constexpr int S_LEN = 2048, DM = 1024, NH = 32, DH = 128, NSEG = 64;
constexpr int LDX = 3 * NH * DH;
constexpr int LDB3 = 3072;
constexpr unsigned SENT = 0xFFFFFFFFu;
constexpr size_t WS_XCH = 2u << 20;
constexpr int NWIN = 320 + 63 * 32, NWIN_INIT = 104;
constexpr size_t XCH_BYTES = (size_t)NWIN * 16384;
constexpr size_t WS_CTL = WS_XCH + 100 * 16384;
constexpr int C_TMO = 0, C_PAR = 64, C_HEAD = 128, C_PROG = 192, C_Q = 256, C_B = C_Q + 8 * 32, C_OA = C_B + 8 * 32, C_OB = C_OA + 16 * 32, C_KV = 4096, C_PROBE = 12288;
constexpr size_t WS_QKVX = 40u << 20;
constexpr size_t WS_QKVB = WS_QKVX + (size_t)S_LEN * LDX * 2;
constexpr size_t WS_OA = WS_QKVB + (size_t)S_LEN * LDB3 * 2;
constexpr size_t WS_OB = WS_OA + (size_t)S_LEN * 4096 * 2;
constexpr size_t WS_END = WS_OB + (size_t)S_LEN * 1024 * 2;
constexpr int LDS_BYTES = 131072 + 1024;

DI unsigned pk_bf16(float a, float b) { f32x2 v = {a, b}; return __builtin_bit_cast(unsigned, __builtin_convertvector(v, bf16x2)); }
DI s16x4 trd(ldsp p) { return __builtin_amdgcn_ds_read_tr16_b64_v4i16((LAS s16x4*)p); }
DI bf16x8 cat8(s16x4 a, s16x4 b) { return __builtin_bit_cast(bf16x8, __builtin_shufflevector(a, b, 0, 1, 2, 3, 4, 5, 6, 7)); }
DI bf16x16 cat16(bf16x8 a, bf16x8 b) { return __builtin_shufflevector(a, b, 0, 1, 2, 3, 4, 5, 6, 7, 8, 9, 10, 11, 12, 13, 14, 15); }
DI bf16x8 lo8(bf16x16 v) { return __builtin_shufflevector(v, v, 0, 1, 2, 3, 4, 5, 6, 7); }
DI bf16x8 hi8(bf16x16 v) { return __builtin_shufflevector(v, v, 8, 9, 10, 11, 12, 13, 14, 15); }
DI bf16x8 asbf(u32x4 v) { return __builtin_bit_cast(bf16x8, v); }
DI f32x4 mma32(bf16x8 a, bf16x8 b, f32x4 c) { return __builtin_amdgcn_mfma_f32_16x16x32_bf16(a, b, c, 0, 0, 0); }
DI f32x4 mma16(u32x2 a, u32x2 b, f32x4 c) { return mma32(asbf((u32x4){a.x, a.y, 0u, 0u}), asbf((u32x4){b.x, b.y, 0u, 0u}), c); }
DI f32x4 smma(u32x4 a, bf16x16 b, f32x4 c, int idx) { return __builtin_amdgcn_smfmac_f32_16x16x64_bf16(asbf(a), b, c, idx, 0, 0); }
DI rsrc_t mkrsrc(const void* p, unsigned bytes) { return __builtin_amdgcn_make_buffer_rsrc((void*)p, 0, bytes, 0x00020000); }
DI u32x4 ld16_sc1(rsrc_t r, unsigned voff, unsigned soff) { return __builtin_amdgcn_raw_buffer_load_b128(r, voff, soff, 16); }
DI void swap16(unsigned& a, unsigned& b) { auto r = __builtin_amdgcn_permlane16_swap(a, b, false, false); a = r[0]; b = r[1]; }
DI void swap32(unsigned& a, unsigned& b) { auto r = __builtin_amdgcn_permlane32_swap(a, b, false, false); a = r[0]; b = r[1]; }
DI void swap16f(float& a, float& b) { unsigned x = __float_as_uint(a), y = __float_as_uint(b); swap16(x, y); a = __uint_as_float(x); b = __uint_as_float(y); }
DI void swap32f(float& a, float& b) { unsigned x = __float_as_uint(a), y = __float_as_uint(b); swap32(x, y); a = __uint_as_float(x); b = __uint_as_float(y); }
DI float rows_max(float v) { float a = v, b = v; swap16f(a, b); v = fmaxf(a, b); a = v; b = v; swap32f(a, b); return fmaxf(a, b); }
DI float rows_sum(float v) { float a = v, b = v; swap16f(a, b); v = a + b; a = v; b = v; swap32f(a, b); return a + b; }
DI unsigned umax4(u32x4 v) { return max(max(v.x, v.y), max(v.z, v.w)); }
#define WAIT_V(n) asm volatile("s_waitcnt vmcnt(" #n ")" ::: "memory")
#define WAIT_L(n) asm volatile("s_waitcnt lgkmcnt(" #n ")" ::: "memory")
#define BAR() __builtin_amdgcn_s_barrier()
#define SCHED() __builtin_amdgcn_sched_barrier(0)
#define LDS_BAR() do { asm volatile("s_waitcnt lgkmcnt(0)" ::: "memory"); __builtin_amdgcn_s_barrier(); asm volatile("" ::: "memory"); } while (0)

constexpr int HTB = 128 * 64 * 2;
DI int lds_byte(int r, int c) { const int st = (r >> 4) * 2 + (c >> 5), rr = r & 15, cc = c & 31, ob = rr * 64 + cc * 2; return st * 1024 + (ob ^ (((ob >> 9) & 1) << 5)); }
DI void stage_rc(int b, int& R, int& C) { const int st = b / 1024, sb = b % 1024, swz = sb ^ (((sb >> 9) & 1) << 5); R = (st >> 1) * 16 + swz / 64; C = (st & 1) * 32 + (swz % 64) / 2; }

struct Params {
    const u16 *ax, *mem0, *wq, *wk, *wv, *wo, *bx, *bwqkv, *bwo;
    u16* out; char* ws; unsigned d0, d1, d2, d3;
};
#define RLX_AGENT __ATOMIC_RELAXED, __HIP_MEMORY_SCOPE_AGENT
DI bool spin_out(unsigned& spins, unsigned* ctl) {
    spins = __builtin_amdgcn_readfirstlane(spins) + 1u;
    if ((spins & 63u) == 0u) {
        if (spins > (1u << 18)) { __hip_atomic_store(ctl + C_TMO, 1u, RLX_AGENT); return true; }
        if (__builtin_amdgcn_readfirstlane(__hip_atomic_load(ctl + C_TMO, RLX_AGENT)) != SENT) return true;
    }
    return false;
}
DI void wait_count(unsigned* ctl, int word, unsigned need) {
    unsigned spins = 0;
    while (__builtin_amdgcn_readfirstlane(__hip_atomic_load(ctl + word, RLX_AGENT)) + 1u < need) { __builtin_amdgcn_s_sleep(8); if (spin_out(spins, ctl)) break; }
}
DI void wg_wait(unsigned* ctl, int word0, unsigned need0, int word1, unsigned need1) {
    if (threadIdx.x < 64) {
        wait_count(ctl, word0, need0); wait_count(ctl, word1, need1);
        __builtin_amdgcn_fence(__ATOMIC_ACQUIRE, "agent");
        asm volatile("s_waitcnt vmcnt(0)" ::: "memory");
    }
    asm volatile("" ::: "memory"); BAR(); asm volatile("" ::: "memory");
}
struct Unit { const char* A; const char* B; char* C; unsigned lda, ldb, ldc; int nt, sig, nsig; };
DI int fresh_tid() { int t = threadIdx.x; asm volatile("" : "+v"(t)); return t; }
DI void signal(unsigned* ctl, int sig, int nsig) { const int lane = fresh_tid() & 63; if (lane < nsig) __hip_atomic_fetch_add(ctl + sig + (lane & 1) * 16 + (lane >> 1) * 512, 1u, RLX_AGENT); }
DI void store_pair(rsrc_t cr, unsigned voff, unsigned soff, f32x4 v0, f32x4 v1, bool wt) {
    unsigned ax = pk_bf16(v0[0], v0[1]), ay = pk_bf16(v0[2], v0[3]), bx = pk_bf16(v1[0], v1[1]), by = pk_bf16(v1[2], v1[3]);
    swap16(ax, bx); swap16(ay, by);
    if (wt) __builtin_amdgcn_raw_buffer_store_b128((u32x4){ax, ay, bx, by}, cr, voff, soff, 16);
    else __builtin_amdgcn_raw_buffer_store_b128((u32x4){ax, ay, bx, by}, cr, voff, soff, 0);
}
#define STAGE(bufoff, gbase, voff, step2) do { \
        __builtin_amdgcn_global_load_lds((const unsigned*)((gbase) + (voff)), (LAS unsigned*)(lds + (bufoff) + ldsw), 16, 0, 0); \
        __builtin_amdgcn_global_load_lds((const unsigned*)((gbase) + (step2) + (voff)), (LAS unsigned*)(lds + (bufoff) + ldsw + 8192), 16, 0, 0); } while (0)
#define LDA(dst, off) do { _Pragma("unroll") for (int m = 0; m < 4; ++m) _Pragma("unroll") for (int k = 0; k < 2; ++k) dst[m][k] = *(const LAS bf16x8*)(lds + (off) + aoff + m * 2048 + k * 1024); } while (0)
#define TRD(dst, base, off) asm volatile("ds_read_b64_tr_b16 %0, %1 offset:%2" : "=v"(dst) : "v"(base), "i"(off) : "memory")
#define LDB(dst, v0, v1, off) do { _Pragma("unroll") for (int n = 0; n < 2; ++n) _Pragma("unroll") for (int k = 0; k < 2; ++k) { s16x4 lo_, hi_; \
        if (n == 0 && k == 0) { TRD(lo_, v0, (off)); TRD(hi_, v1, (off)); } else if (n == 0) { TRD(lo_, v0, (off) + 8192); TRD(hi_, v1, (off) + 8192); } \
        else if (k == 0) { TRD(lo_, v0, (off) + 256); TRD(hi_, v1, (off) + 256); } else { TRD(lo_, v0, (off) + 8448); TRD(hi_, v1, (off) + 8448); } \
        dst[n][k] = cat8(lo_, hi_); } } while (0)
#define MMA(accv, At, Bt) do { SCHED(); __builtin_amdgcn_s_setprio(1); _Pragma("unroll") for (int m = 0; m < 4; ++m) _Pragma("unroll") for (int n = 0; n < 2; ++n) _Pragma("unroll") for (int k = 0; k < 2; ++k) \
        accv[m][n] = mma32(Bt[n][k], At[m][k], accv[m][n]); __builtin_amdgcn_s_setprio(0); } while (0)
#define GEMM_LANE_SETUP() \
    const int tid = fresh_tid(); \
    const int wid = __builtin_amdgcn_readfirstlane(tid >> 6), lane = tid & 63, wr = wid >> 2, wc = wid & 3, fr = lane & 15, fq = lane >> 4; \
    int RA, CA; stage_rc(tid * 16, RA, CA); \
    const int kB = 8 * (wid >> 1) + (((lane & 15) >> 1) ^ (((wid >> 1) & 1) << 2)); \
    const int nB2 = (((wid & 1) * 4 + (lane >> 4)) * 16 + (lane & 1) * 8) * 2; \
    const unsigned ldsw = (unsigned)wid * 1024u; \
    const int aoff = lds_byte(wr * 64 + fr, fq * 8); \
    const int boff0 = fq * 2048 + wc * 512 + (((fr >> 2) ^ ((fq & 1) << 2)) * 32) + (fr & 3) * 8, boff1 = boff0 ^ 128;

template <class Sched>
DI void gemm_stream(ldsp lds, const Sched& S, unsigned* ctl) {
    GEMM_LANE_SETUP();
#define SA(b, h) (((b) * 2 + (h)) * HTB)
#define SB(b, h) ((4 + (b) * 2 + (h)) * HTB)
    const unsigned vb0 = (unsigned)(size_t)lds + 65536u + (unsigned)boff0, vb1 = (unsigned)(size_t)lds + 65536u + (unsigned)boff1;
    Unit cur, nxt; int ui = 0;
    if (!S.next(0, cur)) return;
    f32x4 acc[2][2][4][2];
#pragma unroll
    for (int a = 0; a < 2; ++a)
#pragma unroll
        for (int b = 0; b < 2; ++b)
#pragma unroll
            for (int m = 0; m < 4; ++m)
#pragma unroll
                for (int n = 0; n < 2; ++n) acc[a][b][m][n] = (f32x4){0.f, 0.f, 0.f, 0.f};
    bf16x8 At[4][2], B0[2][2], B1[2][2];
    unsigned vA = (unsigned)RA * cur.lda + (unsigned)CA * 2u, vB = (unsigned)kB * cur.ldb + (unsigned)nB2;
    {
        const size_t a2s = (size_t)64 * cur.lda, ah = (size_t)128 * cur.lda, b2s = (size_t)32 * cur.ldb, bk = (size_t)64 * cur.ldb;
        STAGE(SB(0, 0), cur.B, vB, b2s); STAGE(SB(0, 1), cur.B + 256, vB, b2s); STAGE(SA(0, 0), cur.A, vA, a2s); STAGE(SA(0, 1), cur.A + ah, vA, a2s);
        if (wr == 1) BAR();
        WAIT_V(2); BAR();
        STAGE(SB(1, 0), cur.B + bk, vB, b2s); STAGE(SA(1, 0), cur.A + 128, vA, a2s); STAGE(SB(1, 1), cur.B + bk + 256, vB, b2s);
        WAIT_V(6); BAR();
    }
    int psig = 0, pn = 0;
    for (;;) {
        const bool has_next = S.next(ui + 1, nxt);
        if (!has_next) nxt = cur;
        int nt = cur.nt; asm volatile("" : "+s"(nt));
        for (int t = 0; t < nt; t += 2) {
            const bool last = (t == nt - 2);
            const char* a1 = cur.A + (size_t)(t + 1) * 128 + (size_t)128 * cur.lda;
            const size_t a1s = (size_t)64 * cur.lda; const unsigned vA1 = vA;
            if (last) { const int tz = fresh_tid(); int R2, C2; stage_rc(tz * 16, R2, C2);
                vA = (unsigned)R2 * nxt.lda + (unsigned)C2 * 2u;
                vB = (unsigned)(8 * (wid >> 1) + (((tz & 15) >> 1) ^ (((wid >> 1) & 1) << 2))) * nxt.ldb + (unsigned)((((wid & 1) * 4 + ((tz >> 4) & 3)) * 16 + (tz & 1) * 8) * 2); }
            const unsigned lda = last ? nxt.lda : cur.lda, ldb = last ? nxt.ldb : cur.ldb;
            const char* a2 = last ? nxt.A : cur.A + (size_t)(t + 2) * 128;
            const char* b2 = last ? nxt.B : cur.B + (size_t)(t + 2) * 64 * cur.ldb;
            const size_t a2s = (size_t)64 * lda, ah = (size_t)128 * lda, b2s = (size_t)32 * ldb;
            const char* a3 = a2 + 128; const char* b3 = b2 + (size_t)64 * ldb;
            LDB(B0, vb0, vb1, 0); LDB(B1, vb0, vb1, HTB); SCHED(); LDA(At, SA(0, 0)); STAGE(SA(1, 1), a1, vA1, a1s);
            WAIT_V(8); WAIT_L(0); BAR(); MMA(acc[0][0], At, B0); MMA(acc[0][1], At, B1); BAR(); SCHED();
            LDA(At, SA(0, 1)); STAGE(SB(0, 0), b2, vB, b2s); STAGE(SB(0, 1), b2 + 256, vB, b2s); STAGE(SA(0, 0), a2, vA, a2s);
            WAIT_V(8);
            asm volatile("" : "+s"(pn));
            if (pn) { signal(ctl, psig, pn); pn = 0; }
            WAIT_L(0); BAR(); MMA(acc[1][0], At, B0); MMA(acc[1][1], At, B1); BAR(); SCHED();
            LDB(B0, vb0, vb1, 2 * HTB); LDB(B1, vb0, vb1, 3 * HTB); SCHED(); LDA(At, SA(1, 0)); STAGE(SA(0, 1), a2 + ah, vA, a2s);
            WAIT_V(8); WAIT_L(0); BAR(); MMA(acc[0][0], At, B0); MMA(acc[0][1], At, B1); BAR(); SCHED();
            LDA(At, SA(1, 1)); STAGE(SB(1, 0), b3, vB, b2s); STAGE(SB(1, 1), b3 + 256, vB, b2s); STAGE(SA(1, 0), a3, vA, a2s);
            WAIT_V(8); WAIT_L(0); BAR(); MMA(acc[1][0], At, B0); MMA(acc[1][1], At, B1); BAR(); SCHED();
        }
        if (wr == 0) BAR();
        {
            const rsrc_t cr = mkrsrc(cur.C, 256u * cur.ldc);
            const int tz = fresh_tid();
            const unsigned co = (unsigned)(wr * 64 + (tz & 15)) * cur.ldc + (unsigned)(wc * 32 + ((tz >> 4) & 1) * 16 + ((tz >> 5) & 1) * 8) * 2u;
#pragma unroll
            for (int ai = 0; ai < 2; ++ai)
#pragma unroll
                for (int m = 0; m < 4; ++m)
#pragma unroll
                    for (int bj = 0; bj < 2; ++bj) store_pair(cr, co + bj * 256, (unsigned)(ai * 128 + m * 16) * cur.ldc, acc[ai][bj][m][0], acc[ai][bj][m][1], true);
        }
        psig = cur.sig; pn = cur.nsig;
        if (!has_next) break;
#pragma unroll
        for (int a = 0; a < 2; ++a)
#pragma unroll
            for (int b = 0; b < 2; ++b)
#pragma unroll
                for (int m = 0; m < 4; ++m)
#pragma unroll
                    for (int n = 0; n < 2; ++n) acc[a][b][m][n] = (f32x4){0.f, 0.f, 0.f, 0.f};
        cur = nxt; ++ui;
        if (wr == 1) BAR();
    }
    WAIT_V(0);
    signal(ctl, psig, pn);
    BAR();
#undef SA
#undef SB
}

DI void gemm_small(ldsp lds, const Unit u, unsigned* ctl) {
    GEMM_LANE_SETUP();
    const unsigned vA = (unsigned)RA * u.lda + (unsigned)CA * 2u, vB = (unsigned)kB * u.ldb + (unsigned)nB2;
    const size_t a2s = (size_t)64 * u.lda, b2s = (size_t)32 * u.ldb, bk = (size_t)64 * u.ldb;
    f32x4 acc[4][2];
#pragma unroll
    for (int m = 0; m < 4; ++m)
#pragma unroll
        for (int n = 0; n < 2; ++n) acc[m][n] = (f32x4){0.f, 0.f, 0.f, 0.f};
    int nt = u.nt; asm volatile("" : "+s"(nt));
    const unsigned vb0 = (unsigned)(size_t)lds + (unsigned)boff0, vb1 = (unsigned)(size_t)lds + (unsigned)boff1, vb2 = vb0 + 65536u, vb3 = vb1 + 65536u;
#pragma unroll
    for (int t = 0; t < 3; ++t) { STAGE(t * 32768, u.A + t * 128, vA, a2s); STAGE(t * 32768 + 16384, u.B + t * bk, vB, b2s); }
    for (int t0 = 0; t0 < nt; t0 += 4) {
#pragma unroll
        for (int i = 0; i < 4; ++i) {
            const int tn = min(t0 + i + 3, nt - 1);
            WAIT_V(8); BAR();
            bf16x8 At[4][2], Bt[2][2];
            if (i == 0) LDB(Bt, vb0, vb1, 16384); else if (i == 1) LDB(Bt, vb0, vb1, 49152); else if (i == 2) LDB(Bt, vb2, vb3, 16384); else LDB(Bt, vb2, vb3, 49152);
            LDA(At, i * 32768);
            STAGE(((i + 3) & 3) * 32768, u.A + (size_t)tn * 128, vA, a2s); STAGE(((i + 3) & 3) * 32768 + 16384, u.B + (size_t)tn * bk, vB, b2s);
            WAIT_L(0); SCHED();
            MMA(acc, At, Bt);
            SCHED();
        }
    }
    const rsrc_t cr = mkrsrc(u.C, 128u * u.ldc);
    const unsigned co = (unsigned)(wr * 64 + fr) * u.ldc + (unsigned)(wc * 32 + (fq & 1) * 16 + (fq >> 1) * 8) * 2u;
#pragma unroll
    for (int m = 0; m < 4; ++m) store_pair(cr, co, (unsigned)(m * 16) * u.ldc, acc[m][0], acc[m][1], u.nsig != 0);
    WAIT_V(0);
    signal(ctl, u.sig, u.nsig);
    BAR();
}

#define win_off(idx) ((unsigned)(idx) * 16384u + ((((unsigned)(idx) >> 7) ^ par) & 1u) * 8192u)
#define W_PART(slot) ((slot) * 128)
#define W_MEM(s) ((s) < 2 ? 64 + 2 * (s) : 188 + 2 * (s))
#define W_QKV(s) ((s) == 0 ? 68 : 288 + 32 * (s))
DI void dly(unsigned n) { for (unsigned i = 0; i < n; ++i) __builtin_amdgcn_s_sleep(1); }
DI bool all_set(unsigned mx) { return __builtin_amdgcn_ballot_w64(mx == SENT) == 0ull; }
DI u32x4 rows_gather(unsigned v) { unsigned a = v, b = v; swap16(a, b); unsigned c = a, d = b; swap32(a, c); swap32(b, d); return (u32x4){a, b, c, d}; }
DI float max3f(float a, float b, float c) { return fmaxf(fmaxf(a, b), c); }
constexpr int RED_M = 144;
constexpr int L_RED = 0, L_PX = 32768, L_PM = L_PX + 1024, L_ML = L_PM + 512, L_FLAG = L_ML + 256, L_VM = 34816, L_X = L_VM + 2048  ;

#define RETRY(i)
#define SEG(i)

template <int ROLE>
DI void chain_loop(const Params& p, const unsigned par, const int rank, const int w, ldsp lds, const bf16x16 (&W)[6][2], const bf16x16 (&WO)[2][2], unsigned long long* stamps) {
    const int lane = fresh_tid() & 63, g = lane >> 4, n = lane & 15;
    const int h = rank >> 2, j = rank & 3;
    unsigned* ctl = (unsigned*)(p.ws + WS_CTL);
    const rsrc_t xr = mkrsrc(p.ws + WS_XCH, (unsigned)XCH_BYTES);
    const rsrc_t qr = mkrsrc(p.ws + WS_QKVX, (unsigned)((size_t)S_LEN * LDX * 2));
    const int sidx = (n >> 3) ? 0xEEEE : 0x4444;
    const unsigned l16 = (unsigned)lane * 16u;
    const int redw = (w * 1024 + (n & 1) * 512 + ((g >> 1) * 8 + (n >> 1)) * 8 + 4 * (g & 1)) * 4;
    const int redr = (w < 4 ? (n >> 3) * RED_M + (4 * w + g) * 8 + (n & 7) : 2 * RED_M + (8 * (w & 1) + 2 * g + (n >> 3)) * 8 + (n & 7)) * 4;
    const unsigned vg_off = (unsigned)(g >> 1) * 1536u + 1024u + (unsigned)(lane & 31) * 16u;
    const int rho = 4 * (8 * j + w) + g;
    const unsigned pst_off = (unsigned)(rho >> 1) * 16384u + (unsigned)(rho & 1) * 4096u + (unsigned)((h ^ rho) & 31) * 128u + (unsigned)(n & 7) * 16u;
    const int vxr = L_X + n * 256;
    const float csc = 0.08838834764831845f * 1.4426950408889634f;
    float ninf = -__builtin_inff(); asm volatile("" : "+v"(ninf));
    u32x4 KX[8], VWX[2];
    const unsigned x_src = (unsigned)(8 * (w & 3) + g) * (LDX * 2) + (unsigned)(4096 + h * 128) * 2u;
    auto dma_x = [&](int s) {
        if (ROLE == 0 && w < 4) {
            if ((s & 3) == 0) { wait_count(ctl, C_KV + (s >> 2) * 512 + h * 16, 16u); asm volatile("" ::: "memory"); }
#pragma unroll
            for (int i = 0; i < 2; ++i) {
                const unsigned vo = x_src + (unsigned)((n ^ ((8 * w + 4 * i + g) & 15)) * 16), so = (unsigned)(32 * s + 4 * i) * (LDX * 2);
                __builtin_amdgcn_raw_ptr_buffer_load_lds(qr, (LAS void*)(lds + L_X + (s & 1) * 16384 + (w * 2 + i) * 1024), 16, vo, so + 8192u, 0, 16);
                __builtin_amdgcn_raw_ptr_buffer_load_lds(qr, (LAS void*)(lds + L_X + (s & 1) * 16384 + 8192 + (w * 2 + i) * 1024), 16, vo, so, 0, 16);
            }
        }
    };
    auto load_kx = [&](int s) {
#pragma unroll
        for (int kt = 0; kt < 2; ++kt)
#pragma unroll
            for (int kk = 0; kk < 4; ++kk) KX[kt * 4 + kk] = *(const LAS u32x4*)(lds + vxr + (s & 1) * 16384 + 8192 + kt * 4096 + (((4 * kk + g) ^ n) * 16));
    };
    auto vwx_kt = [&](int s, int kt) {
        bf16x8 VA[4];
#pragma unroll
        for (int kk = 0; kk < 4; ++kk) VA[kk] = *(const LAS bf16x8*)(lds + vxr + (s & 1) * 16384 + kt * 4096 + (((4 * kk + g) ^ n) * 16));
#pragma unroll
        for (int ct = 0; ct < 2; ++ct) {
            f32x4 d = {0.f, 0.f, 0.f, 0.f};
#pragma unroll
            for (int kk = 0; kk < 4; ++kk) d = mma32(VA[kk], (kk & 1) ? hi8(WO[ct][kk >> 1]) : lo8(WO[ct][kk >> 1]), d);
            VWX[ct][2 * kt] = pk_bf16(d[0], d[1]); VWX[ct][2 * kt + 1] = pk_bf16(d[2], d[3]);
        }
    };
    auto rmax = [&](float v) { float a = v, b = v; swap16f(a, b); v = max3f(a, b, ninf); a = v; b = v; swap32f(a, b); return max3f(a, b, ninf); };
    dma_x(0); dma_x(1);
    if (ROLE == 1 && lane == 0) *(volatile LAS unsigned*)(lds + L_FLAG) = 0u;
    asm volatile("s_waitcnt vmcnt(0) lgkmcnt(0)" ::: "memory"); BAR(); asm volatile("" ::: "memory");
    vwx_kt(0, 0); vwx_kt(0, 1);
    if (ROLE == 2) load_kx(0);
    if (ROLE >= 1) __builtin_amdgcn_s_setprio(2);

    unsigned spins = 0;
    for (int s = 0; s < NSEG; ++s) {
        f32x4 c[6];
#pragma unroll
        for (int i = 0; i < 6; ++i) { c[i] = (f32x4){0.f, 0.f, 0.f, 0.f}; asm volatile("" : "+v"(c[i])); }
        const unsigned qwin = win_off(W_QKV(s) + h);
        u32x4 a0, a1;
        {
            const unsigned so = win_off(W_MEM(s) + (w >> 2)) + (unsigned)(w & 3) * 2048u;
            for (;;) {
                asm volatile("" ::: "memory");
                a0 = ld16_sc1(xr, l16, so); a1 = ld16_sc1(xr, l16 + 1024, so);
                if (__builtin_expect(all_set(max(umax4(a0), umax4(a1))), 1)) break;
                RETRY(0);
                if (spin_out(spins, ctl)) break;
            }
        }
        SEG(0);
        auto fold = [&](int mt) {
            f32x4 r;
#pragma unroll
            for (int i = 0; i < 4; ++i) { float x = c[2 * mt][i], y = c[2 * mt + 1][i]; swap32f(x, y); r[i] = x + y; }
            *(LAS f32x4*)(lds + redw + mt * (RED_M * 4)) = r;
        };
        c[0] = smma(a0, W[0][0], c[0], sidx); c[1] = smma(a0, W[1][0], c[1], sidx); c[0] = smma(a1, W[0][1], c[0], sidx); c[1] = smma(a1, W[1][1], c[1], sidx);
        c[2] = smma(a0, W[2][0], c[2], sidx); c[3] = smma(a0, W[3][0], c[3], sidx);
        SCHED();
        c[2] = smma(a1, W[2][1], c[2], sidx); c[3] = smma(a1, W[3][1], c[3], sidx); c[4] = smma(a0, W[4][0], c[4], sidx); c[5] = smma(a0, W[5][0], c[5], sidx);
        fold(0);
        SCHED();
        c[4] = smma(a1, W[4][1], c[4], sidx); c[5] = smma(a1, W[5][1], c[5], sidx);
        fold(1);
        SCHED();
        fold(2);
        SEG(1);
        LDS_BAR();
        SEG(2);
        if (ROLE <= 1) {
            f32x2 v[8];
#pragma unroll
            for (int ww = 0; ww < 8; ++ww) { const LAS float* q = (const LAS float*)(lds + redr + ww * 4096); v[ww] = (f32x2){q[0], q[512]}; }
            const f32x2 t = ((v[0] + v[1]) + (v[2] + v[3])) + ((v[4] + v[5]) + (v[6] + v[7]));
            const u32x4 o = rows_gather(min(pk_bf16(t.x, t.y), SENT - 1));
            if (lane < 16) __builtin_amdgcn_raw_buffer_store_b128(o, xr, l16, qwin + (unsigned)j * 1536u + (unsigned)w * 256u, 16);
        } else if (ROLE == 3) {
            if (rank == 0 && lane == 0) __hip_atomic_store(ctl + C_PROG, (unsigned)s, RLX_AGENT);
        } else if (ROLE == 2 && s > 0) {
            const u32x4 sv = {SENT, SENT, SENT, SENT};
            const unsigned so = win_off(W_PART((s - 1) & 1) + (rank >> 1)) + (unsigned)(rank & 1) * 4096u;
#pragma unroll
            for (int i = 0; i < 4; ++i) __builtin_amdgcn_raw_buffer_store_b128(sv, xr, l16 + i * 1024, so, 16);
        }
        SEG(3);
        if (s == NSEG - 1) break;
        f32x4 dv[2];
#pragma unroll
        for (int i = 0; i < 2; ++i) { dv[i] = (f32x4){0.f, 0.f, 0.f, 0.f}; asm volatile("" : "+v"(dv[i])); }
        if (ROLE >= 1) dly(ROLE >= 2 ? p.d3 : p.d1);
        SEG(11);
        u32x4 X[4], v0, v1;
        if (ROLE >= 1) {
            for (;;) {
                asm volatile("" ::: "memory");
                unsigned mx = 0;
                if (ROLE >= 2) {
#pragma unroll
                    for (int k = 0; k < 4; ++k) X[k] = ld16_sc1(xr, (ROLE == 2 ? (l16 & ~128u) : l16) + (k & 1) * 1536, qwin + (k >> 1) * 3072);
                }
                v0 = ld16_sc1(xr, vg_off, qwin); v1 = ld16_sc1(xr, vg_off, qwin + 3072);
                if (ROLE >= 2) mx = max(max(umax4(X[0]), umax4(X[1])), max(umax4(X[2]), umax4(X[3])));
                if (__builtin_expect(all_set(max(mx, max(umax4(v0), umax4(v1)))), 1)) break;
                RETRY(1);
                if (spin_out(spins, ctl)) break;
            }
        }
        SEG(4);
        if (ROLE == 1) {
            *(LAS u32x4*)(lds + L_VM + lane * 16) = v0; *(LAS u32x4*)(lds + L_VM + 1024 + lane * 16) = v1;
            asm volatile("" ::: "memory");
            *(volatile LAS unsigned*)(lds + L_FLAG) = (unsigned)s + 1u;
        } else if (ROLE == 0) {
            while (__builtin_amdgcn_readfirstlane(*(volatile LAS unsigned*)(lds + L_FLAG)) != (unsigned)s + 1u) __builtin_amdgcn_s_sleep(1);
            asm volatile("" ::: "memory");
            v0 = *(const LAS u32x4*)(lds + L_VM + lane * 16); v1 = *(const LAS u32x4*)(lds + L_VM + 1024 + lane * 16);
        }
        if (ROLE == 2) {
            f32x4 s0 = {0.f, 0.f, 0.f, 0.f}, s1 = s0;
#pragma unroll
            for (int k = 0; k < 4; ++k) { s0 = mma32(asbf(KX[k]), asbf(X[k]), s0); s1 = mma32(asbf(KX[4 + k]), asbf(X[k]), s1); }
            dv[0] = smma(v0, WO[0][0], dv[0], sidx);
            SCHED();
            const float m = rmax(max3f(max3f(s0[0], s0[1], s0[2]), max3f(s0[3], s1[0], s1[1]), max3f(s1[2], s1[3], ninf)));
            dv[1] = smma(v0, WO[1][0], dv[1], sidx);
            dv[0] = smma(v1, WO[0][1], dv[0], sidx);
            SCHED();
            const float mc = -m * csc;
#pragma unroll
            for (int i = 0; i < 4; ++i) { s0[i] = __builtin_amdgcn_exp2f(fmaf(s0[i], csc, mc)); s1[i] = __builtin_amdgcn_exp2f(fmaf(s1[i], csc, mc)); }
            dv[1] = smma(v1, WO[1][1], dv[1], sidx);
            *(LAS u32x4*)(lds + L_PX + lane * 16) = (u32x4){pk_bf16(s0[0], s0[1]), pk_bf16(s0[2], s0[3]), pk_bf16(s1[0], s1[1]), pk_bf16(s1[2], s1[3])};
            const f32x4 t = s0 + s1;
            const float l = rows_sum((t[0] + t[1]) + (t[2] + t[3]));
            *(LAS f32x2*)(lds + L_ML + n * 16) = (f32x2){mc, l};
        } else if (ROLE == 3) {
            f32x4 sm = {0.f, 0.f, 0.f, 0.f};
#pragma unroll
            for (int k = 0; k < 4; ++k) sm = mma32(asbf(X[k]), asbf(X[k]), sm);
#pragma unroll
            for (int ct = 0; ct < 2; ++ct) { dv[ct] = smma(v0, WO[ct][0], dv[ct], sidx); dv[ct] = smma(v1, WO[ct][1], dv[ct], sidx); }
            if (g < 2) sm = (f32x4){ninf, ninf, ninf, ninf};
            const float m = rmax(max3f(max3f(sm[0], sm[1], sm[2]), sm[3], ninf));
            const float mc = -m * csc;
#pragma unroll
            for (int i = 0; i < 4; ++i) sm[i] = __builtin_amdgcn_exp2f(fmaf(sm[i], csc, mc));
            *(LAS u32x2*)(lds + L_PM + lane * 8) = (u32x2){pk_bf16(sm[0], sm[1]), pk_bf16(sm[2], sm[3])};
            const float l = rows_sum((sm[0] + sm[1]) + (sm[2] + sm[3]));
            *(LAS f32x2*)(lds + L_ML + n * 16 + 8) = (f32x2){mc, l};
        } else {
#pragma unroll
            for (int ct = 0; ct < 2; ++ct) { dv[ct] = smma(v0, WO[ct][0], dv[ct], sidx); dv[ct] = smma(v1, WO[ct][1], dv[ct], sidx); }
        }
        u32x4 VWM[2];
#pragma unroll
        for (int ct = 0; ct < 2; ++ct) { VWM[ct] = (u32x4){pk_bf16(dv[ct][0], dv[ct][1]), pk_bf16(dv[ct][2], dv[ct][3]), 0u, 0u}; asm volatile("" : "+v"(VWM[ct])); }
        unsigned zz0 = 0u, zz1 = 0u; asm volatile("" : "+v"(zz0), "+v"(zz1));
        const unsigned pso = win_off(W_PART(s & 1));
        SEG(5);
        LDS_BAR();
        SEG(6);
        {
            const f32x4 ml = *(const LAS f32x4*)(lds + L_ML + n * 16);
            const u32x4 px = *(const LAS u32x4*)(lds + L_PX + lane * 16);
            const u32x2 pm2 = *(const LAS u32x2*)(lds + L_PM + ((2 + (g & 1)) * 16 + n) * 8);
            const u32x4 pm = {pm2.x, pm2.y, zz0, zz1};
            f32x4 dx[2], dm[2];
#pragma unroll
            for (int ct = 0; ct < 2; ++ct) dx[ct] = mma32(asbf(VWX[ct]), asbf(px), (f32x4){0.f, 0.f, 0.f, 0.f});
#pragma unroll
            for (int ct = 0; ct < 2; ++ct) dm[ct] = mma32(asbf(VWM[ct]), asbf(pm), (f32x4){0.f, 0.f, 0.f, 0.f});
            const float dl = ml[0] - ml[2];
            const float e = __builtin_amdgcn_exp2f(-__builtin_fabsf(dl));
            const float ex = dl > 0.f ? e : 1.f, em = dl > 0.f ? 1.f : e;
            const float inv = __builtin_amdgcn_rcpf(fmaf(ex, ml[1], em * ml[3]));
            float ax = ex * inv, am = em * inv;
            asm volatile("" : "+v"(ax), "+v"(am));
            const f32x4 d0 = dx[0] * ax + dm[0] * am, d1 = dx[1] * ax + dm[1] * am;
            u32x4 o = {min(pk_bf16(d0[0], d0[1]), SENT - 1), min(pk_bf16(d0[2], d0[3]), SENT - 1), min(pk_bf16(d1[0], d1[1]), SENT - 1), min(pk_bf16(d1[2], d1[3]), SENT - 1)};
            asm volatile("" : "+v"(o));
            if (n < 8) __builtin_amdgcn_raw_buffer_store_b128(o, xr, pst_off, pso, 16);
        }
        SEG(7);
        SCHED();
        if (s + 2 < NSEG - 1) dma_x(s + 2);
        vwx_kt(s + 1, 0);
        SCHED();
        SEG(8);
        if (ROLE == 3) {
            u32x4 SEL;
            { const unsigned one = (n & 1) ? 0x3f800000u : 0x00003f80u; const int dw = (n & 7) >> 1; SEL = (u32x4){dw == 0 ? one : 0u, dw == 1 ? one : 0u, dw == 2 ? one : 0u, dw == 3 ? one : 0u}; }
            vwx_kt(s + 1, 1);
            SCHED();
            dly(p.d2);
            SEG(12);
            const unsigned so = win_off(W_PART(s & 1) + (rank >> 1)) + (unsigned)(rank & 1) * 4096u;
            const unsigned mo = win_off(W_MEM(s + 1) + (rank >> 6)) + (unsigned)(rank & 63) * 128u;
            u32x4 q[4];
            for (;;) {
                asm volatile("" ::: "memory");
#pragma unroll
                for (int i = 0; i < 4; ++i) q[i] = ld16_sc1(xr, l16 + i * 1024, so);
                if (__builtin_expect(all_set(max(max(umax4(q[0]), umax4(q[1])), max(umax4(q[2]), umax4(q[3])))), 1)) break;
                RETRY(2);
                if (spin_out(spins, ctl)) break;
            }
            f32x4 t = {0.f, 0.f, 0.f, 0.f};
#pragma unroll
            for (int i = 0; i < 4; ++i) t = mma32(asbf(SEL), asbf(q[i]), t);
#pragma unroll
            for (int i = 0; i < 4; ++i) t[i] += __uint_as_float(__builtin_amdgcn_update_dpp(0, __float_as_uint(t[i]), 0x128, 0xf, 0xf, false));
            unsigned a0 = min(pk_bf16(t[0], t[1]), SENT - 1), a1 = min(pk_bf16(t[2], t[3]), SENT - 1);
            unsigned b0 = a0, b1 = a1; swap16(a0, b0); swap16(a1, b1);
            if (lane < 8) __builtin_amdgcn_raw_buffer_store_b128((u32x4){a0, a1, b0, b1}, xr, l16, mo, 16);
        } else {
            vwx_kt(s + 1, 1);
            if (ROLE == 2) load_kx(s + 1);
        }
        SEG(9);
        LDS_BAR();
        dly(p.d0);
        SEG(10);
    }
    if (ROLE == 1 && rank == 0 && lane == 0) __hip_atomic_store(ctl + C_PROG, (unsigned)NSEG, RLX_AGENT);
    __builtin_amdgcn_s_setprio(0);
}

DI void chain_role(const Params& p, int rank, ldsp lds, unsigned long long* stamps) {
    const int tid = fresh_tid(), w = __builtin_amdgcn_readfirstlane(tid >> 6), lane = tid & 63, g = lane >> 4, n = lane & 15;
    const int h = rank >> 2, j = rank & 3;
    const rsrc_t xr = mkrsrc(p.ws + WS_XCH, (unsigned)XCH_BYTES);
    unsigned* ctl = (unsigned*)(p.ws + WS_CTL);
    if (rank == 0 && w == 7) {
        unsigned best0 = ~0u, best1 = ~0u;
        for (int i = 0; i < 12; ++i) {
            const unsigned long long t0 = __builtin_amdgcn_s_memtime();
            __builtin_amdgcn_raw_buffer_store_b32(0u, xr, (unsigned)(100 * 16384 + C_PROBE * 4) + (unsigned)(i & 1) * 8192u + (unsigned)(i >> 1) * 256u, 0, 16);
            asm volatile("s_waitcnt vmcnt(0)" ::: "memory");
            const unsigned dt = (unsigned)(__builtin_amdgcn_s_memtime() - t0);
            if (i & 1) best1 = min(best1, dt); else best0 = min(best0, dt);
        }
        if (lane == 0) __hip_atomic_store(ctl + C_PAR, best1 < best0 ? 1u : 0u, RLX_AGENT);
    }
    bf16x16 W[6][2], WO[2][2];
    {
        ldsp my = lds + w * 16384;
        const int rrow = lane >> 2, rc = (lane & 3) * 8, q4 = n >> 2;
        const int toffo = (8 * g + q4) * 64 + (n & 3) * 8;
        const int toffq = (16 * (g & 1) + 8 * (g >> 1) + 4 * (q4 >> 1) + (q4 & 1)) * 64 + (n & 3) * 8;
#define FRAG(base, st) cat16(cat8(trd((base)), trd((base) + (st))), cat8(trd((base) + 2048), trd((base) + 2048 + (st))))
#pragma unroll
        for (int i = 0; i < 8; ++i) {
            __builtin_amdgcn_global_load_lds((const unsigned*)(p.wq + (size_t)(128 * w + 16 * i + rrow) * 4096 + h * 128 + 32 * j + rc), (LAS unsigned*)(my + i * 1024), 16, 0, 0);
            __builtin_amdgcn_global_load_lds((const unsigned*)(p.wk + (size_t)(128 * w + 16 * i + rrow) * 4096 + h * 128 + 32 * j + rc), (LAS unsigned*)(my + 8192 + i * 1024), 16, 0, 0);
        }
        WAIT_V(0);
#pragma unroll
        for (int T = 0; T < 4; ++T)
#pragma unroll
            for (int t = 0; t < 2; ++t) W[T][t] = FRAG(my + (T >> 1) * 8192 + t * 4096 + (T & 1) * 32 + toffq, 128);
#pragma unroll
        for (int T = 0; T < 4; ++T) asm volatile("" : "+v"(W[T][0]), "+v"(W[T][1]));
        WAIT_L(0);
#pragma unroll
        for (int i = 0; i < 8; ++i) {
            __builtin_amdgcn_global_load_lds((const unsigned*)(p.wv + (size_t)(128 * w + 16 * i + rrow) * 4096 + h * 128 + 32 * j + rc), (LAS unsigned*)(my + i * 1024), 16, 0, 0);
            __builtin_amdgcn_global_load_lds((const unsigned*)(p.wo + (size_t)(h * 128 + 16 * i + rrow) * 1024 + 256 * j + 32 * w + rc), (LAS unsigned*)(my + 8192 + i * 1024), 16, 0, 0);
        }
        WAIT_V(0);
#pragma unroll
        for (int T = 0; T < 2; ++T)
#pragma unroll
            for (int t = 0; t < 2; ++t) { W[4 + T][t] = FRAG(my + t * 4096 + T * 32 + toffq, 128); WO[T][t] = FRAG(my + 8192 + t * 4096 + T * 32 + toffo, 256); }
#pragma unroll
        for (int T = 0; T < 2; ++T) asm volatile("" : "+v"(W[4 + T][0]), "+v"(W[4 + T][1]), "+v"(WO[T][0]), "+v"(WO[T][1]));
        WAIT_L(0);
#undef FRAG
    }
    wait_count(ctl, C_PAR, 1u);
    const unsigned par = __builtin_amdgcn_readfirstlane(__hip_atomic_load(ctl + C_PAR, RLX_AGENT)) & 1u;
    if (w == 7 && lane < 8) {
        const unsigned* src = (const unsigned*)(p.mem0 + lane * DM + 32 * (rank >> 2) + 4 * (rank & 3));
        const u32x4 v = {min(src[0], SENT - 1), min(src[1], SENT - 1), min(src[8], SENT - 1), min(src[9], SENT - 1)};
        __builtin_amdgcn_raw_buffer_store_b128(v, xr, (unsigned)lane * 16u + (unsigned)(rank & 63) * 128u, win_off(W_MEM(0) + (rank >> 6)), 16);
    }
    {
        const u32x4 sv = {SENT, SENT, SENT, SENT};
        for (int s = 1 + w; s < NSEG; s += 8) {
            __builtin_amdgcn_raw_buffer_store_b128(sv, xr, (unsigned)lane * 16u, win_off(W_QKV(s) + h) + (unsigned)j * 1536u, 16);
            if (lane < 32) __builtin_amdgcn_raw_buffer_store_b128(sv, xr, (unsigned)lane * 16u, win_off(W_QKV(s) + h) + (unsigned)j * 1536u + 1024u, 16);
            if (lane < 8 && s > 1) __builtin_amdgcn_raw_buffer_store_b128(sv, xr, (unsigned)lane * 16u + (unsigned)(rank & 63) * 128u, win_off(W_MEM(s) + (rank >> 6)), 16);
        }
        if (w < 4) __builtin_amdgcn_raw_buffer_store_b128(sv, xr, (unsigned)lane * 16u + (unsigned)w * 1024u, win_off(W_PART(1) + (rank >> 1)) + (unsigned)(rank & 1) * 4096u, 16);
    }
    BAR();
    if (w < 5) chain_loop<0>(p, par, rank, w, lds, W, WO, stamps);
    else if (w == 5) chain_loop<1>(p, par, rank, 5, lds, W, WO, stamps);
    else if (w == 6) chain_loop<2>(p, par, rank, 6, lds, W, WO, stamps);
    else chain_loop<3>(p, par, rank, 7, lds, W, WO, stamps);
}

DI void attn_a_item(const Params& p, unsigned par, int s, int h, ldsp my, int lane) {
    const int g = lane >> 4, n = lane & 15;
    const char* X = p.ws + WS_QKVX;
    const rsrc_t xr = mkrsrc(p.ws + WS_XCH, (unsigned)XCH_BYTES);
#pragma unroll
    for (int i = 0; i < 8; ++i) { const int row = 4 * i + g;
        __builtin_amdgcn_global_load_lds((const unsigned*)(X + ((size_t)(32 * s + row) * LDX + 8192 + h * 128 + ((n ^ ((row & 7) << 1)) * 8)) * 2), (LAS unsigned*)(my + i * 1024), 16, 0, 0); }
    u32x4 Q[2][4], K[2][4], XM[4];
    const char* rp = X + ((size_t)(32 * s + n) * LDX + h * 128 + g * 8) * 2;
#pragma unroll
    for (int t = 0; t < 2; ++t)
#pragma unroll
        for (int k = 0; k < 4; ++k) { Q[t][k] = *(const u32x4*)(rp + (size_t)t * 16 * LDX * 2 + k * 64); K[t][k] = *(const u32x4*)(rp + (size_t)t * 16 * LDX * 2 + 8192 + k * 64); }
    const unsigned qwin = win_off(W_QKV(s) + h);
    u32x4 VM[2];
    for (unsigned spins = 0;;) {
        asm volatile("" ::: "memory");
#pragma unroll
        for (int k = 0; k < 4; ++k) XM[k] = ld16_sc1(xr, (unsigned)lane * 16u + (k & 1) * 1536, qwin + (k >> 1) * 3072);
#pragma unroll
        for (int ts = 0; ts < 2; ++ts) VM[ts] = ld16_sc1(xr, (unsigned)(g >> 1) * 1536u + 1024u + (unsigned)(lane & 31) * 16u, qwin + ts * 3072);
        if (all_set(max(max(max(umax4(XM[0]), umax4(XM[1])), max(umax4(XM[2]), umax4(XM[3]))), max(umax4(VM[0]), umax4(VM[1]))))) break;
        __builtin_amdgcn_s_sleep(4);
        if (spin_out(spins, (unsigned*)(p.ws + WS_CTL))) break;
    }
#pragma unroll
    for (int ts = 0; ts < 2; ++ts) {
        const u32x4 v = VM[ts];
        const int m = n & 7, hf = n >> 3, row = 32 + m;
#pragma unroll
        for (int i = 0; i < 4; ++i) { const int e = 64 * ts + 16 * g + 4 * i + 2 * hf;
            *(LAS unsigned*)(my + row * 256 + (((e >> 3) ^ ((row & 7) << 1)) * 16) + (e & 7) * 2) = v[i]; }
    }
    const float csc = 0.08838834764831845f * 1.4426950408889634f, ninf = -__builtin_inff();
    u32x4 PX[2]; u32x2 PM[2];
#pragma unroll
    for (int qt = 0; qt < 2; ++qt) {
        f32x4 sm = {0.f, 0.f, 0.f, 0.f}, s0 = sm, s1 = sm;
#pragma unroll
        for (int k = 0; k < 4; ++k) { sm = mma32(asbf(XM[k]), asbf(Q[qt][k]), sm); s0 = mma32(asbf(K[0][k]), asbf(Q[qt][k]), s0); if (qt == 1) s1 = mma32(asbf(K[1][k]), asbf(Q[qt][k]), s1); }
#pragma unroll
        for (int i = 0; i < 4; ++i) {
            if (g < 2) sm[i] = ninf;
            if (qt == 0) { if (4 * g + i > n) s0[i] = ninf; s1[i] = ninf; }
            else if (4 * g + i > n) s1[i] = ninf;
        }
        float m = fmaxf(fmaxf(fmaxf(s0[0], s0[1]), fmaxf(s0[2], s0[3])), fmaxf(fmaxf(s1[0], s1[1]), fmaxf(s1[2], s1[3])));
        m = fmaxf(m, fmaxf(fmaxf(sm[0], sm[1]), fmaxf(sm[2], sm[3])));
        m = rows_max(m);
        const float mc = -m * csc; float sum = 0.f;
#pragma unroll
        for (int i = 0; i < 4; ++i) { s0[i] = __builtin_amdgcn_exp2f(fmaf(s0[i], csc, mc)); s1[i] = __builtin_amdgcn_exp2f(fmaf(s1[i], csc, mc)); sm[i] = __builtin_amdgcn_exp2f(fmaf(sm[i], csc, mc));
            sum += (s0[i] + s1[i]) + sm[i]; }
        sum = rows_sum(sum);
        const float inv = __builtin_amdgcn_rcpf(sum);
        PX[qt] = (u32x4){pk_bf16(s0[0] * inv, s0[1] * inv), pk_bf16(s0[2] * inv, s0[3] * inv), pk_bf16(s1[0] * inv, s1[1] * inv), pk_bf16(s1[2] * inv, s1[3] * inv)};
        PM[qt] = (u32x2){pk_bf16(sm[0] * inv, sm[1] * inv), pk_bf16(sm[2] * inv, sm[3] * inv)};
    }
    asm volatile("s_waitcnt vmcnt(0) lgkmcnt(0)" ::: "memory");
    const int q4 = n >> 2, p4 = n & 3;
    const int r0 = 4 * g + q4, rm = 32 + 4 * (g & 1) + q4;
    const rsrc_t orr = mkrsrc(p.ws + WS_OA + (size_t)32 * s * 4096 * 2, 32u * 4096u * 2u);
    const unsigned oo = (unsigned)(n * 4096 + h * 128 + (g & 1) * 16 + (g >> 1) * 8) * 2u;
#pragma unroll
    for (int ep = 0; ep < 4; ++ep) {
        f32x4 d[2][2];
#pragma unroll
        for (int e2 = 0; e2 < 2; ++e2) {
            const int ch = (2 * ep + e2) * 2 + (p4 >> 1);
            const bf16x8 va = cat8(trd(my + r0 * 256 + ((ch ^ ((r0 & 7) << 1)) * 16) + (p4 & 1) * 8), trd(my + (r0 + 16) * 256 + ((ch ^ ((r0 & 7) << 1)) * 16) + (p4 & 1) * 8));
            const s16x4 vm = trd(my + rm * 256 + ((ch ^ ((rm & 7) << 1)) * 16) + (p4 & 1) * 8);
#pragma unroll
            for (int qt = 0; qt < 2; ++qt) { f32x4 c = {0.f, 0.f, 0.f, 0.f}; c = mma32(va, asbf(PX[qt]), c); d[qt][e2] = mma16(__builtin_bit_cast(u32x2, vm), PM[qt], c); }
        }
#pragma unroll
        for (int qt = 0; qt < 2; ++qt) store_pair(orr, oo + ep * 64, (unsigned)qt * 16u * 4096u * 2u, d[qt][0], d[qt][1], true);
    }
    asm volatile("s_waitcnt vmcnt(0) lgkmcnt(0)" ::: "memory");
    signal((unsigned*)(p.ws + WS_CTL), C_OA + (s >> 2) * 32, 1);
}

DI void attn_b_item(const Params& p, int hb, int qb, ldsp lds) {
    const int tid = fresh_tid(), w = __builtin_amdgcn_readfirstlane(tid >> 6), lane = tid & 63, g = lane >> 4, n = lane & 15;
    const char* X = p.ws + WS_QKVB;
    const int q0 = qb * 128;
#pragma unroll
    for (int i = 0; i < 3; ++i) { const int pc = w * 3 + i, r = pc * 16 + (lane >> 2), c = lane & 3; const int key = max(q0 - 256 + r, 0);
        const char* rp = X + ((size_t)key * LDB3 + hb * 32) * 2;
        __builtin_amdgcn_global_load_lds((const unsigned*)(rp + 2048 + ((c ^ (((r >> 3) & 1) * 3)) * 16)), (LAS unsigned*)(lds + pc * 1024), 16, 0, 0);
        __builtin_amdgcn_global_load_lds((const unsigned*)(rp + 4096 + ((c ^ (((r >> 2) & 1) << 1)) * 16)), (LAS unsigned*)(lds + 24576 + pc * 1024), 16, 0, 0); }
    const u32x4 Q = *(const u32x4*)(X + ((size_t)(q0 + 16 * w + n) * LDB3 + hb * 32 + g * 8) * 2);
    asm volatile("s_waitcnt vmcnt(0)" ::: "memory"); BAR(); asm volatile("" ::: "memory");
    f32x4 sc[17];
    const ldsp kp = lds + (16 * w + n) * 64 + ((g ^ (((n >> 3) & 1) * 3)) * 16);
#pragma unroll
    for (int t = 0; t < 17; ++t) sc[t] = mma32(*(const LAS bf16x8*)(kp + t * 1024), asbf(Q), (f32x4){0.f, 0.f, 0.f, 0.f});
    const float ninf = -__builtin_inff();
    const int tmin = (256 - q0 - 16 * w) >> 4;
#pragma unroll
    for (int i = 0; i < 4; ++i) { if (4 * g + i <= n) sc[0][i] = ninf; if (4 * g + i > n) sc[16][i] = ninf; }
    if (tmin > 0) {
#pragma unroll
        for (int t = 0; t < 16; ++t) if (t < tmin) sc[t] = (f32x4){ninf, ninf, ninf, ninf};
    }
    float m = ninf;
#pragma unroll
    for (int t = 0; t < 17; ++t) m = fmaxf(m, fmaxf(fmaxf(sc[t][0], sc[t][1]), fmaxf(sc[t][2], sc[t][3])));
    m = rows_max(m);
    const float csc = 0.17677669529663687f * 1.4426950408889634f, mc = -m * csc;
    float sum = 0.f;
#pragma unroll
    for (int t = 0; t < 17; ++t)
#pragma unroll
        for (int i = 0; i < 4; ++i) { sc[t][i] = __builtin_amdgcn_exp2f(fmaf(sc[t][i], csc, mc)); sum += sc[t][i]; }
    sum = rows_sum(sum);
    const float inv = __builtin_amdgcn_rcpf(sum);
    const int q4 = n >> 2, p4 = n & 3, r0 = 16 * w + 4 * g + q4;
    f32x4 d[2] = {{0.f, 0.f, 0.f, 0.f}, {0.f, 0.f, 0.f, 0.f}};
    const ldsp vp = lds + 24576 + r0 * 64 + (p4 & 1) * 8;
    const int sw = (g & 1) << 1;
#pragma unroll
    for (int tp = 0; tp < 8; ++tp) {
        const u32x4 P = {pk_bf16(sc[2 * tp][0] * inv, sc[2 * tp][1] * inv), pk_bf16(sc[2 * tp][2] * inv, sc[2 * tp][3] * inv),
                         pk_bf16(sc[2 * tp + 1][0] * inv, sc[2 * tp + 1][1] * inv), pk_bf16(sc[2 * tp + 1][2] * inv, sc[2 * tp + 1][3] * inv)};
#pragma unroll
        for (int et = 0; et < 2; ++et) { const int ch = ((2 * et + (p4 >> 1)) ^ sw) * 16;
            d[et] = mma32(cat8(trd(vp + tp * 2048 + ch), trd(vp + tp * 2048 + 1024 + ch)), asbf(P), d[et]); }
    }
    { const u32x2 P = {pk_bf16(sc[16][0] * inv, sc[16][1] * inv), pk_bf16(sc[16][2] * inv, sc[16][3] * inv)};
#pragma unroll
      for (int et = 0; et < 2; ++et) { const int ch = ((2 * et + (p4 >> 1)) ^ sw) * 16; d[et] = mma16(__builtin_bit_cast(u32x2, trd(vp + 16384 + ch)), P, d[et]); } }
    store_pair(mkrsrc(p.ws + WS_OB + (size_t)q0 * 1024 * 2, 128u * 1024u * 2u), (unsigned)((16 * w + n) * 1024 + hb * 32 + (g & 1) * 16 + (g >> 1) * 8) * 2u, 0u, d[0], d[1], true);
    asm volatile("s_waitcnt vmcnt(0) lgkmcnt(0)" ::: "memory");
    signal((unsigned*)(p.ws + WS_CTL), C_OB + qb * 32, 1);
    BAR();
}

struct BigSched {
    const u16 *ax, *wq, *wk, *wv, *bx, *bw; char* ws; int c;
    DI bool next(int i, Unit& u) const {
        const int L = i * 128 + c;
        if (L >= 416) return false;
        u.lda = DM * 2; u.nt = 16;
        if (L < 192 + 96 || L >= 384) {
            int pm, pn;
            if (L < 192) { pm = 2 + (L >> 5); pn = 16 + (L & 31); } else if (L < 288) { pm = (L - 192) >> 4; pn = (L - 192) & 15; } else { pm = 6 + ((L - 384) >> 4); pn = (L - 384) & 15; }
            const u16 *w0 = wq, *w1 = wk, *w2 = wv; const u16* W = pn < 16 ? w0 : (pn < 32 ? w1 : w2);
            u.A = (const char*)(ax + (size_t)pm * 256 * DM); u.B = (const char*)(W + (pn & 15) * 256); u.ldb = 4096 * 2;
            u.C = ws + WS_QKVX + ((size_t)pm * 256 * LDX + (size_t)pn * 256) * 2; u.ldc = LDX * 2;
            if (pn < 16) { u.sig = C_Q + pm * 32; u.nsig = 1; } else { u.sig = C_KV + 2 * pm * 512 + (pn & 15) * 32; u.nsig = 4; }
        } else {
            const int q = L - 288, pm = q / 12, pn = q - pm * 12;
            u.A = (const char*)(bx + (size_t)pm * 256 * DM); u.B = (const char*)(bw + pn * 256); u.ldb = LDB3 * 2;
            u.C = ws + WS_QKVB + ((size_t)pm * 256 * LDB3 + (size_t)pn * 256) * 2; u.ldc = LDB3 * 2; u.sig = C_B + pm * 32; u.nsig = 1;
        }
        return true;
    }
};

__global__ __launch_bounds__(512, 2) void k_fused(Params p) {
    extern __shared__ __attribute__((aligned(16))) unsigned char shm[];
    ldsp lds = (ldsp)shm;
    unsigned* ctl = (unsigned*)(p.ws + WS_CTL);
    const int b = blockIdx.x, id = (b & 3) * 32 + (b >> 3);
#define WSTAMP(code)
    WSTAMP(1);
    if ((b & 7) < 4) {
        chain_role(p, id, lds, (unsigned long long*)(p.ws + WS_END));
        BAR();
        WSTAMP(2);
    } else {
#pragma unroll 1
        for (int r = 0; r < 2; ++r) {
            const int rb = 2 * r + (id >> 6), ct = id & 63;
            const u16 *w1 = p.wk, *w2 = p.wv;
            Unit u; u.A = (const char*)(p.ax + (size_t)rb * 128 * DM); u.lda = DM * 2; u.B = (const char*)((ct < 32 ? w1 : w2) + (ct & 31) * 128); u.ldb = 4096 * 2; u.nt = 16;
            u.C = p.ws + WS_QKVX + ((size_t)rb * 128 * LDX + 4096 + (size_t)ct * 128) * 2; u.ldc = LDX * 2; u.sig = C_KV + rb * 512 + (ct & 31) * 16; u.nsig = 1;
            gemm_small(lds, u, ctl);
            WSTAMP(3);
        }
        BigSched S{p.ax, p.wq, p.wk, p.wv, p.bx, p.bwqkv, p.ws, id};
        gemm_stream(lds, S, ctl);
        WSTAMP(4);
    }
    volatile LAS unsigned* qw = (volatile LAS unsigned*)(lds + 131072);
    const int w = __builtin_amdgcn_readfirstlane(threadIdx.x >> 6);
    unsigned par = 0;
#pragma unroll 1
    for (bool first = true;; first = false) {
        if (threadIdx.x < 64) {
            if (first) wait_count(ctl, C_PAR, 1u);
            if (threadIdx.x == 0) { qw[0] = __hip_atomic_fetch_add(ctl + C_HEAD, 1u, RLX_AGENT) + 1u; if (first) qw[1] = __hip_atomic_load(ctl + C_PAR, RLX_AGENT); }
        }
        LDS_BAR();
        int it = (int)__builtin_amdgcn_readfirstlane(qw[0]);
        par = __builtin_amdgcn_readfirstlane(qw[1]) & 1u;
        LDS_BAR();
        int kind, x;
        if (it < 128) { kind = 0; x = it; }
        else if (it < 640) { kind = 1; x = it - 128; }
        else if (it < 704) { kind = 2; x = it - 640; }
        else if (it < 832) { kind = 3; x = it - 704; }
        else if (it < 848) { kind = 0; x = 128 + it - 832; }
        else if (it < 848 + 7 * 24) { const int q = it - 848, blk = q / 24, r = q - blk * 24; if (r < 16) { kind = 0; x = (9 + blk) * 16 + r; } else { kind = 2; x = (8 + blk) * 8 + r - 16; } }
        else if (it < 1024) { kind = 2; x = 120 + it - 1016; }
        else break;
        if (kind == 0) {
            const int s = x >> 2;
            wg_wait(ctl, C_Q + (s >> 3) * 32, 128u, C_PROG, (unsigned)s + 2u);
            attn_a_item(p, par, s, (x & 3) * 8 + w, lds + w * 10240, fresh_tid() & 63);
            BAR();
        } else if (kind == 1) {
            const int qb = x >> 5;
            wg_wait(ctl, C_B + (qb >> 1) * 32, 96u, C_B + max((qb >> 1) - 1, 0) * 32, 96u);
            attn_b_item(p, x & 31, qb, lds);
        } else {
            const int rb = x >> 3, ct = x & 7;
            Unit u; u.ldb = DM * 2; u.ldc = 2048 * 2; u.sig = 0; u.nsig = 0;
            if (kind == 2) { u.A = p.ws + WS_OA + (size_t)rb * 128 * 4096 * 2; u.lda = 4096 * 2; u.B = (const char*)(p.wo + ct * 128); u.nt = 64; u.C = (char*)(p.out + (size_t)rb * 128 * 2048 + ct * 128); }
            else { u.A = p.ws + WS_OB + (size_t)rb * 128 * 1024 * 2; u.lda = 1024 * 2; u.B = (const char*)(p.bwo + ct * 128); u.nt = 16; u.C = (char*)(p.out + (size_t)rb * 128 * 2048 + 1024 + ct * 128); }
            const int word = kind == 2 ? C_OA + rb * 32 : C_OB + rb * 32; const unsigned need = kind == 2 ? 128u : 256u;
            wg_wait(ctl, word, need, word, need);
            gemm_small(lds, u, ctl);
        }
        WSTAMP(16 + kind + (it << 8));
    }
}

extern "C" void kernel_launch(void* const* d_in, const int* in_sizes, int n_in, void* d_out, int out_size, void* d_ws, size_t ws_size, hipStream_t stream) {
    Params p;
    p.ax = (const u16*)d_in[0]; p.mem0 = (const u16*)d_in[1]; p.wq = (const u16*)d_in[2]; p.wk = (const u16*)d_in[3]; p.wv = (const u16*)d_in[4]; p.wo = (const u16*)d_in[5];
    p.bx = (const u16*)d_in[7]; p.bwqkv = (const u16*)d_in[8]; p.bwo = (const u16*)d_in[9];
    p.out = (u16*)d_out; p.ws = (char*)d_ws + ((0x200000u - ((uintptr_t)d_ws & 0x1FFFFFu)) & 0x1FFFFFu);
    p.d0 = 8; p.d1 = 4; p.d2 = 6; p.d3 = 10;
    (void)hipFuncSetAttribute((const void*)k_fused, hipFuncAttributeMaxDynamicSharedMemorySize, LDS_BYTES);
    (void)hipMemsetAsync(p.ws + WS_XCH, 0xFF, (size_t)NWIN_INIT * 16384, stream);
    k_fused<<<256, 512, LDS_BYTES, stream>>>(p);
}
```

```cpp
#include <hip/hip_runtime.h>
#include <stdint.h>
#include <stdlib.h>

#define LAS __attribute__((address_space(3)))
#define DI __device__ __forceinline__
typedef unsigned short u16;
typedef __attribute__((ext_vector_type(8))) __bf16 bf16x8;
typedef __attribute__((ext_vector_type(16))) __bf16 bf16x16;
typedef __attribute__((ext_vector_type(2))) __bf16 bf16x2;
typedef __attribute__((ext_vector_type(4))) short s16x4;
typedef __attribute__((ext_vector_type(8))) short s16x8;
typedef __attribute__((ext_vector_type(16))) short s16x16;
typedef __attribute__((ext_vector_type(2))) float f32x2;
typedef __attribute__((ext_vector_type(4))) float f32x4;
typedef __attribute__((ext_vector_type(2))) unsigned u32x2;
typedef __attribute__((ext_vector_type(4))) unsigned u32x4;
typedef LAS unsigned char* ldsp;
typedef __amdgpu_buffer_rsrc_t rsrc_t;

constexpr int S_LEN = 2048, DM = 1024, NH = 32, DH = 128, NSEG = 64;
constexpr int LDB3 = 3072;
constexpr unsigned XH = S_LEN * 256, BH = S_LEN * 64;
constexpr unsigned SENT = 0xFFFFFFFFu;
constexpr size_t WS_XCH = 2u << 20;
constexpr int NWIN = 640, NWIN_INIT = 6;
constexpr unsigned XCH_KM = NWIN * 16384u;
constexpr size_t XCH_BYTES = (size_t)XCH_KM + 64u * 128u * 1536u;
constexpr size_t WS_CTL = WS_XCH + 2 * 16384;
constexpr int C_TMO = 0, C_PAR = 64, C_HEAD = 128, C_PROG = 192, C_Q = 256, C_B = C_Q + 8 * 32, C_OA = C_B + 8 * 32, C_OB = C_OA + 16 * 32, C_OS = C_OB + 16 * 32, C_DONE = C_OS + 12 * 32, C_KV = 4096, C_PROBE = 12288;
constexpr size_t WS_QKVX = 26u << 20;
constexpr size_t WS_QKVB = WS_QKVX + (size_t)96 * XH;
constexpr size_t WS_OA = WS_QKVB + (size_t)96 * BH;
constexpr size_t WS_OB = WS_OA + (size_t)S_LEN * 4096 * 2;
constexpr size_t WS_END = WS_OB + (size_t)S_LEN * 1024 * 2;
constexpr int LDS_BYTES = 131072 + 1024;

DI unsigned pk_bf16(float a, float b) { f32x2 v = {a, b}; return __builtin_bit_cast(unsigned, __builtin_convertvector(v, bf16x2)); }
DI s16x4 trd(ldsp p) { return __builtin_amdgcn_ds_read_tr16_b64_v4i16((LAS s16x4*)p); }
DI bf16x8 cat8(s16x4 a, s16x4 b) { return __builtin_bit_cast(bf16x8, __builtin_shufflevector(a, b, 0, 1, 2, 3, 4, 5, 6, 7)); }
DI bf16x16 cat16(bf16x8 a, bf16x8 b) { return __builtin_shufflevector(a, b, 0, 1, 2, 3, 4, 5, 6, 7, 8, 9, 10, 11, 12, 13, 14, 15); }
DI bf16x8 lo8(bf16x16 v) { return __builtin_shufflevector(v, v, 0, 1, 2, 3, 4, 5, 6, 7); }
DI bf16x8 hi8(bf16x16 v) { return __builtin_shufflevector(v, v, 8, 9, 10, 11, 12, 13, 14, 15); }
DI bf16x8 asbf(u32x4 v) { return __builtin_bit_cast(bf16x8, v); }
DI f32x4 mma32(bf16x8 a, bf16x8 b, f32x4 c) { return __builtin_amdgcn_mfma_f32_16x16x32_bf16(a, b, c, 0, 0, 0); }
DI f32x4 mma16(u32x2 a, u32x2 b, f32x4 c) { return mma32(asbf((u32x4){a.x, a.y, 0u, 0u}), asbf((u32x4){b.x, b.y, 0u, 0u}), c); }
DI f32x4 smma(u32x4 a, bf16x16 b, f32x4 c, int idx) { return __builtin_amdgcn_smfmac_f32_16x16x64_bf16(asbf(a), b, c, idx, 0, 0); }
DI rsrc_t mkrsrc(const void* p, unsigned bytes) { return __builtin_amdgcn_make_buffer_rsrc((void*)p, 0, bytes, 0x00020000); }
DI u32x4 ld16_sc1(rsrc_t r, unsigned voff, unsigned soff) { return __builtin_amdgcn_raw_buffer_load_b128(r, voff, soff, 16); }
DI void swap16(unsigned& a, unsigned& b) { auto r = __builtin_amdgcn_permlane16_swap(a, b, false, false); a = r[0]; b = r[1]; }
DI void swap32(unsigned& a, unsigned& b) { auto r = __builtin_amdgcn_permlane32_swap(a, b, false, false); a = r[0]; b = r[1]; }
DI void swap16f(float& a, float& b) { unsigned x = __float_as_uint(a), y = __float_as_uint(b); swap16(x, y); a = __uint_as_float(x); b = __uint_as_float(y); }
DI void swap32f(float& a, float& b) { unsigned x = __float_as_uint(a), y = __float_as_uint(b); swap32(x, y); a = __uint_as_float(x); b = __uint_as_float(y); }
DI float rows_max(float v) { float a = v, b = v; swap16f(a, b); v = fmaxf(a, b); a = v; b = v; swap32f(a, b); return fmaxf(a, b); }
DI float rows_sum(float v) { float a = v, b = v; swap16f(a, b); v = a + b; a = v; b = v; swap32f(a, b); return a + b; }
DI unsigned umax4(u32x4 v) { return max(max(v.x, v.y), max(v.z, v.w)); }
#define WAIT_V(n) asm volatile("s_waitcnt vmcnt(" #n ")" ::: "memory")
#define WAIT_L(n) asm volatile("s_waitcnt lgkmcnt(" #n ")" ::: "memory")
#define BAR() __builtin_amdgcn_s_barrier()
#define SCHED() __builtin_amdgcn_sched_barrier(0)
#define LDS_BAR() do { asm volatile("s_waitcnt lgkmcnt(0)" ::: "memory"); __builtin_amdgcn_s_barrier(); asm volatile("" ::: "memory"); } while (0)

constexpr int HTB = 128 * 64 * 2;
DI int lds_byte(int r, int c) { const int st = (r >> 4) * 2 + (c >> 5), rr = r & 15, cc = c & 31, ob = rr * 64 + cc * 2; return st * 1024 + (ob ^ (((ob >> 9) & 1) << 5)); }
DI void stage_rc(int b, int& R, int& C) { const int st = b / 1024, sb = b % 1024, swz = sb ^ (((sb >> 9) & 1) << 5); R = (st >> 1) * 16 + swz / 64; C = (st & 1) * 32 + (swz % 64) / 2; }

struct Params {
    const u16 *ax, *mem0, *wq, *wk, *wv, *wo, *bx, *bwqkv, *bwo;
    u16* out; char* ws; unsigned d0, d1, d2, d3;
};
#define RLX_AGENT __ATOMIC_RELAXED, __HIP_MEMORY_SCOPE_AGENT
DI bool spin_out(unsigned& spins, unsigned* ctl) {
    spins = __builtin_amdgcn_readfirstlane(spins) + 1u;
    if ((spins & 63u) == 0u) {
        if (spins > (1u << 18)) { __hip_atomic_store(ctl + C_TMO, 1u, RLX_AGENT); return true; }
        if (__builtin_amdgcn_readfirstlane(__hip_atomic_load(ctl + C_TMO, RLX_AGENT)) != SENT) return true;
    }
    return false;
}
DI void wait_count(unsigned* ctl, int word, unsigned need) {
    unsigned spins = 0;
    while (__builtin_amdgcn_readfirstlane(__hip_atomic_load(ctl + word, RLX_AGENT)) + 1u < need) { __builtin_amdgcn_s_sleep(8); if (spin_out(spins, ctl)) break; }
}
DI void wg_wait(unsigned* ctl, int word0, unsigned need0, int word1, unsigned need1) {
    if (threadIdx.x < 64) {
        wait_count(ctl, word0, need0); wait_count(ctl, word1, need1);
        __builtin_amdgcn_fence(__ATOMIC_ACQUIRE, "agent");
        asm volatile("s_waitcnt vmcnt(0)" ::: "memory");
    }
    asm volatile("" ::: "memory"); BAR(); asm volatile("" ::: "memory");
}
struct Unit { const char* A; const char* B; char* C; unsigned lda, ldb, ldc, cw, ch, ak, ah; int nt, sig, nsig; };
DI int fresh_tid() { int t = threadIdx.x; asm volatile("" : "+v"(t)); return t; }
DI void signal(unsigned* ctl, int sig, int nsig) { const int lane = fresh_tid() & 63; if (lane < nsig) __hip_atomic_fetch_add(ctl + sig + (lane & 1) * 16 + (lane >> 1) * 512, 1u, RLX_AGENT); }
DI void store_pair(rsrc_t cr, unsigned voff, unsigned soff, f32x4 v0, f32x4 v1, bool wt) {
    unsigned ax = pk_bf16(v0[0], v0[1]), ay = pk_bf16(v0[2], v0[3]), bx = pk_bf16(v1[0], v1[1]), by = pk_bf16(v1[2], v1[3]);
    swap16(ax, bx); swap16(ay, by);
    if (wt) __builtin_amdgcn_raw_buffer_store_b128((u32x4){ax, ay, bx, by}, cr, voff, soff, 16);
    else __builtin_amdgcn_raw_buffer_store_b128((u32x4){ax, ay, bx, by}, cr, voff, soff, 0);
}
#define STAGE(bufoff, gbase, voff, step2) do { \
        __builtin_amdgcn_global_load_lds((const unsigned*)((gbase) + (voff)), (LAS unsigned*)(lds + (bufoff) + ldsw), 16, 0, 0); \
        __builtin_amdgcn_global_load_lds((const unsigned*)((gbase) + (step2) + (voff)), (LAS unsigned*)(lds + (bufoff) + ldsw + 8192), 16, 0, 0); } while (0)
#define LDA(dst, off) do { _Pragma("unroll") for (int m = 0; m < 4; ++m) _Pragma("unroll") for (int k = 0; k < 2; ++k) dst[m][k] = *(const LAS bf16x8*)(lds + (off) + aoff + m * 2048 + k * 1024); } while (0)
#define TRD(dst, base, off) asm volatile("ds_read_b64_tr_b16 %0, %1 offset:%2" : "=v"(dst) : "v"(base), "i"(off) : "memory")
#define LDB(dst, v0, v1, off) do { _Pragma("unroll") for (int n = 0; n < 2; ++n) _Pragma("unroll") for (int k = 0; k < 2; ++k) { s16x4 lo_, hi_; \
        if (n == 0 && k == 0) { TRD(lo_, v0, (off)); TRD(hi_, v1, (off)); } else if (n == 0) { TRD(lo_, v0, (off) + 8192); TRD(hi_, v1, (off) + 8192); } \
        else if (k == 0) { TRD(lo_, v0, (off) + 256); TRD(hi_, v1, (off) + 256); } else { TRD(lo_, v0, (off) + 8448); TRD(hi_, v1, (off) + 8448); } \
        dst[n][k] = cat8(lo_, hi_); } } while (0)
#define MMA(accv, At, Bt) do { SCHED(); __builtin_amdgcn_s_setprio(1); _Pragma("unroll") for (int m = 0; m < 4; ++m) _Pragma("unroll") for (int n = 0; n < 2; ++n) _Pragma("unroll") for (int k = 0; k < 2; ++k) \
        accv[m][n] = mma32(Bt[n][k], At[m][k], accv[m][n]); __builtin_amdgcn_s_setprio(0); } while (0)
#define GEMM_LANE_SETUP() \
    const int tid = fresh_tid(); \
    const int wid = __builtin_amdgcn_readfirstlane(tid >> 6), lane = tid & 63, wr = wid >> 2, wc = wid & 3, fr = lane & 15, fq = lane >> 4; \
    int RA, CA; stage_rc(tid * 16, RA, CA); \
    const int kB = 8 * (wid >> 1) + (((lane & 15) >> 1) ^ (((wid >> 1) & 1) << 2)); \
    const int nB2 = (((wid & 1) * 4 + (lane >> 4)) * 16 + (lane & 1) * 8) * 2; \
    const unsigned ldsw = (unsigned)wid * 1024u; \
    const int aoff = lds_byte(wr * 64 + fr, fq * 8); \
    const int boff0 = fq * 2048 + wc * 512 + (((fr >> 2) ^ ((fq & 1) << 2)) * 32) + (fr & 3) * 8, boff1 = boff0 ^ 128;

template <class Sched>
DI void gemm_stream(ldsp lds, const Sched& S, unsigned* ctl) {
    GEMM_LANE_SETUP();
#define SA(b, h) (((b) * 2 + (h)) * HTB)
#define SB(b, h) ((4 + (b) * 2 + (h)) * HTB)
    const unsigned vb0 = (unsigned)(size_t)lds + 65536u + (unsigned)boff0, vb1 = (unsigned)(size_t)lds + 65536u + (unsigned)boff1;
    Unit cur, nxt; int ui = 0;
    if (!S.next(0, cur)) return;
    f32x4 acc[2][2][4][2];
#pragma unroll
    for (int a = 0; a < 2; ++a)
#pragma unroll
        for (int b = 0; b < 2; ++b)
#pragma unroll
            for (int m = 0; m < 4; ++m)
#pragma unroll
                for (int n = 0; n < 2; ++n) acc[a][b][m][n] = (f32x4){0.f, 0.f, 0.f, 0.f};
    bf16x8 At[4][2], B0[2][2], B1[2][2];
    unsigned vA = (unsigned)RA * cur.lda + (unsigned)CA * 2u, vB = (unsigned)kB * cur.ldb + (unsigned)nB2;
    {
        const size_t a2s = (size_t)64 * cur.lda, ah = cur.ah, b2s = (size_t)32 * cur.ldb, bk = (size_t)64 * cur.ldb;
        STAGE(SB(0, 0), cur.B, vB, b2s); STAGE(SB(0, 1), cur.B + 256, vB, b2s); STAGE(SA(0, 0), cur.A, vA, a2s); STAGE(SA(0, 1), cur.A + ah, vA, a2s);
        if (wr == 1) BAR();
        WAIT_V(2); BAR();
        STAGE(SB(1, 0), cur.B + bk, vB, b2s); STAGE(SA(1, 0), cur.A + cur.ak, vA, a2s); STAGE(SB(1, 1), cur.B + bk + 256, vB, b2s);
        WAIT_V(6); BAR();
    }
    int psig = 0, pn = 0;
    for (;;) {
        const bool has_next = S.next(ui + 1, nxt);
        if (!has_next) nxt = cur;
        int nt = cur.nt; asm volatile("" : "+s"(nt));
        for (int t = 0; t < nt; t += 2) {
            const bool last = (t == nt - 2);
            const char* a1 = cur.A + (size_t)(t + 1) * cur.ak + cur.ah;
            const size_t a1s = (size_t)64 * cur.lda; const unsigned vA1 = vA;
            if (last) { const int tz = fresh_tid(); int R2, C2; stage_rc(tz * 16, R2, C2);
                vA = (unsigned)R2 * nxt.lda + (unsigned)C2 * 2u;
                vB = (unsigned)(8 * (wid >> 1) + (((tz & 15) >> 1) ^ (((wid >> 1) & 1) << 2))) * nxt.ldb + (unsigned)((((wid & 1) * 4 + ((tz >> 4) & 3)) * 16 + (tz & 1) * 8) * 2); }
            const unsigned lda = last ? nxt.lda : cur.lda, ldb = last ? nxt.ldb : cur.ldb;
            const char* a2 = last ? nxt.A : cur.A + (size_t)(t + 2) * cur.ak;
            const char* b2 = last ? nxt.B : cur.B + (size_t)(t + 2) * 64 * cur.ldb;
            const size_t a2s = (size_t)64 * lda, ah = last ? nxt.ah : cur.ah, b2s = (size_t)32 * ldb;
            const char* a3 = a2 + (last ? nxt.ak : cur.ak); const char* b3 = b2 + (size_t)64 * ldb;
            LDB(B0, vb0, vb1, 0); LDB(B1, vb0, vb1, HTB); SCHED(); LDA(At, SA(0, 0)); STAGE(SA(1, 1), a1, vA1, a1s);
            WAIT_V(8); WAIT_L(0); BAR(); MMA(acc[0][0], At, B0); MMA(acc[0][1], At, B1); BAR(); SCHED();
            LDA(At, SA(0, 1)); STAGE(SB(0, 0), b2, vB, b2s); STAGE(SB(0, 1), b2 + 256, vB, b2s); STAGE(SA(0, 0), a2, vA, a2s);
            WAIT_V(8);
            asm volatile("" : "+s"(pn));
            if (pn) { signal(ctl, psig, pn); pn = 0; }
            WAIT_L(0); BAR(); MMA(acc[1][0], At, B0); MMA(acc[1][1], At, B1); BAR(); SCHED();
            LDB(B0, vb0, vb1, 2 * HTB); LDB(B1, vb0, vb1, 3 * HTB); SCHED(); LDA(At, SA(1, 0)); STAGE(SA(0, 1), a2 + ah, vA, a2s);
            WAIT_V(8); WAIT_L(0); BAR(); MMA(acc[0][0], At, B0); MMA(acc[0][1], At, B1); BAR(); SCHED();
            LDA(At, SA(1, 1)); STAGE(SB(1, 0), b3, vB, b2s); STAGE(SB(1, 1), b3 + 256, vB, b2s); STAGE(SA(1, 0), a3, vA, a2s);
            WAIT_V(8); WAIT_L(0); BAR(); MMA(acc[1][0], At, B0); MMA(acc[1][1], At, B1); BAR(); SCHED();
        }
        if (wr == 0) BAR();
        {
            const rsrc_t cr = mkrsrc(cur.C, 0x1000000u);
            const int tz = fresh_tid();
            const unsigned co = (unsigned)(wr * 64 + (tz & 15)) * cur.ldc + (unsigned)wc * cur.cw + (unsigned)(((tz >> 4) & 1) * 32 + ((tz >> 5) & 1) * 16);
#pragma unroll
            for (int ai = 0; ai < 2; ++ai)
#pragma unroll
                for (int m = 0; m < 4; ++m)
#pragma unroll
                    for (int bj = 0; bj < 2; ++bj) store_pair(cr, co, (unsigned)(ai * 128 + m * 16) * cur.ldc + (unsigned)bj * cur.ch, acc[ai][bj][m][0], acc[ai][bj][m][1], true);
        }
        psig = cur.sig; pn = cur.nsig;
        if (!has_next) break;
#pragma unroll
        for (int a = 0; a < 2; ++a)
#pragma unroll
            for (int b = 0; b < 2; ++b)
#pragma unroll
                for (int m = 0; m < 4; ++m)
#pragma unroll
                    for (int n = 0; n < 2; ++n) acc[a][b][m][n] = (f32x4){0.f, 0.f, 0.f, 0.f};
        cur = nxt; ++ui;
        if (wr == 1) BAR();
    }
    WAIT_V(0);
    signal(ctl, psig, pn);
    BAR();
#undef SA
#undef SB
}

DI void gemm_small(ldsp lds, const Unit u, unsigned* ctl) {
    GEMM_LANE_SETUP();
    const unsigned vA = (unsigned)RA * u.lda + (unsigned)CA * 2u, vB = (unsigned)kB * u.ldb + (unsigned)nB2;
    const size_t a2s = (size_t)64 * u.lda, b2s = (size_t)32 * u.ldb, bk = (size_t)64 * u.ldb;
    f32x4 acc[4][2];
#pragma unroll
    for (int m = 0; m < 4; ++m)
#pragma unroll
        for (int n = 0; n < 2; ++n) acc[m][n] = (f32x4){0.f, 0.f, 0.f, 0.f};
    int nt = u.nt; asm volatile("" : "+s"(nt));
    const unsigned vb0 = (unsigned)(size_t)lds + (unsigned)boff0, vb1 = (unsigned)(size_t)lds + (unsigned)boff1, vb2 = vb0 + 65536u, vb3 = vb1 + 65536u;
#pragma unroll
    for (int t = 0; t < 3; ++t) { STAGE(t * 32768, u.A + (size_t)t * u.ak, vA, a2s); STAGE(t * 32768 + 16384, u.B + t * bk, vB, b2s); }
    for (int t0 = 0; t0 < nt; t0 += 4) {
#pragma unroll
        for (int i = 0; i < 4; ++i) {
            const int tn = min(t0 + i + 3, nt - 1);
            WAIT_V(8); BAR();
            bf16x8 At[4][2], Bt[2][2];
            if (i == 0) LDB(Bt, vb0, vb1, 16384); else if (i == 1) LDB(Bt, vb0, vb1, 49152); else if (i == 2) LDB(Bt, vb2, vb3, 16384); else LDB(Bt, vb2, vb3, 49152);
            LDA(At, i * 32768);
            STAGE(((i + 3) & 3) * 32768, u.A + (size_t)tn * u.ak, vA, a2s); STAGE(((i + 3) & 3) * 32768 + 16384, u.B + (size_t)tn * bk, vB, b2s);
            WAIT_L(0); SCHED();
            MMA(acc, At, Bt);
            SCHED();
        }
    }
    const rsrc_t cr = mkrsrc(u.C, 128u * u.ldc);
    const unsigned co = (unsigned)(wr * 64 + fr) * u.ldc + (unsigned)(wc * 32 + (fq & 1) * 16 + (fq >> 1) * 8) * 2u;
#pragma unroll
    for (int m = 0; m < 4; ++m) store_pair(cr, co, (unsigned)(m * 16) * u.ldc, acc[m][0], acc[m][1], true);
    WAIT_V(0);
    signal(ctl, u.sig, u.nsig);
    BAR();
}

#define win_off(idx) ((unsigned)(idx) * 16384u + ((((unsigned)(idx) >> 7) ^ par) & 1u) * 8192u)
#define W_MEM(s) ((s) == 0 ? 0 : 6 + 2 * (s))
#define W_PART(slot) (192 + (slot) * 64)
#define W_X(slot) (384 + (slot) * 128)
DI void dly(unsigned n) { for (unsigned i = 0; i < n; ++i) __builtin_amdgcn_s_sleep(1); }
DI bool all_set(unsigned mx) { return __builtin_amdgcn_ballot_w64(mx == SENT) == 0ull; }
DI u32x4 rows_gather(unsigned v) { unsigned a = v, b = v; swap16(a, b); unsigned c = a, d = b; swap32(a, c); swap32(b, d); return (u32x4){a, b, c, d}; }
DI float max3f(float a, float b, float c) { return fmaxf(fmaxf(a, b), c); }
constexpr int RED_M = 144;
constexpr int L_RED = 0, L_PX = 32768, L_PM = L_PX + 1024, L_ML = L_PM + 512, L_FLAG = L_ML + 256, L_VM = 34816, L_OWN = L_VM + 2048, L_X = L_OWN + 1536  ;

#define RETRY(i)
#define PH0()
#define PH1(e)
#define SEG(i)

template <int ROLE>
DI void chain_loop(const Params& p, const unsigned par, const int rank, const int w, ldsp lds, const bf16x16 (&W)[6][2], const bf16x16 (&WO)[2][2], unsigned long long* stamps) {
    const int lane = fresh_tid() & 63, g = lane >> 4, n = lane & 15;
    const int h = rank >> 2, j = rank & 3;
    unsigned* ctl = (unsigned*)(p.ws + WS_CTL);
    const rsrc_t xr = mkrsrc(p.ws + WS_XCH, (unsigned)XCH_BYTES);
    const rsrc_t qr = mkrsrc(p.ws + WS_QKVX + (size_t)(32 + h) * XH, 33u * XH);
    const int sidx = (n >> 3) ? 0xEEEE : 0x4444;
    const unsigned l16 = (unsigned)lane * 16u;
    const int redw = (w * 1024 + (n & 1) * 512 + ((g >> 1) * 8 + (n >> 1)) * 8 + 4 * (g & 1)) * 4;
    const int redr = (w < 4 ? (n >> 3) * RED_M + (4 * w + g) * 8 + (n & 7) : 2 * RED_M + (8 * (w & 1) + 2 * g + (n >> 3)) * 8 + (n & 7)) * 4;
    const bool vown = (g >> 1) == (j & 1);
    const unsigned vg1 = (unsigned)(((j & 2) ^ 2) + (g >> 1)) * 1536u + 1024u + (unsigned)(lane & 31) * 16u;
    const unsigned vg0 = vown ? 0x80000000u : (vg1 + (unsigned)(j & 2) * 3072u - 3072u);
    const unsigned pub_off = (g == j ? XCH_KM + (unsigned)rank * 1536u : (unsigned)(4 * h + g) * 16384u + (unsigned)j * 1536u) + (unsigned)w * 256u + (unsigned)n * 16u;
    const int red = ((j + 1) & 3) * 32 + h;
    const int rho = 4 * (8 * j + w) + g;
    const unsigned pst_off = (unsigned)(rho >> 1) * 16384u + (unsigned)(rho & 1) * 4096u + (unsigned)((h ^ rho) & 31) * 128u + (unsigned)(n & 7) * 16u;
    const int vxr = L_X + n * 256;
    const float csc = 0.08838834764831845f * 1.4426950408889634f;
    float ninf = -__builtin_inff(); asm volatile("" : "+v"(ninf));
    u32x4 KX[8], VWX[2];
    const unsigned x_src = (unsigned)(8 * (w & 3) + g) * 256u;
    auto dma_x = [&](int s) {
        if (ROLE == 0 && w < 4) {
            if ((s & 3) == 0) { wait_count(ctl, C_KV + (s >> 2) * 512 + h * 16, 16u); asm volatile("" ::: "memory"); }
#pragma unroll
            for (int i = 0; i < 2; ++i) {
                const unsigned vo = x_src + (unsigned)((n ^ ((8 * w + 4 * i + g) & 15)) * 16), so = (unsigned)(32 * s + 4 * i) * 256u;
                __builtin_amdgcn_raw_ptr_buffer_load_lds(qr, (LAS void*)(lds + L_X + (s & 1) * 16384 + (w * 2 + i) * 1024), 16, vo, so + 32u * XH, 0, 16);
                __builtin_amdgcn_raw_ptr_buffer_load_lds(qr, (LAS void*)(lds + L_X + (s & 1) * 16384 + 8192 + (w * 2 + i) * 1024), 16, vo, so, 0, 16);
            }
        }
    };
    auto load_kx = [&](int s) {
#pragma unroll
        for (int kt = 0; kt < 2; ++kt)
#pragma unroll
            for (int kk = 0; kk < 4; ++kk) KX[kt * 4 + kk] = *(const LAS u32x4*)(lds + vxr + (s & 1) * 16384 + 8192 + kt * 4096 + (((4 * ((j + kk) & 3) + g) ^ n) * 16));
    };
    auto vwx_kt = [&](int s, int kt) {
        bf16x8 VA[4];
#pragma unroll
        for (int kk = 0; kk < 4; ++kk) VA[kk] = *(const LAS bf16x8*)(lds + vxr + (s & 1) * 16384 + kt * 4096 + (((4 * (kk ^ (j & 2)) + g) ^ n) * 16));
#pragma unroll
        for (int ct = 0; ct < 2; ++ct) {
            f32x4 d = {0.f, 0.f, 0.f, 0.f};
#pragma unroll
            for (int kk = 0; kk < 4; ++kk) d = mma32(VA[kk], (kk & 1) ? hi8(WO[ct][kk >> 1]) : lo8(WO[ct][kk >> 1]), d);
            VWX[ct][2 * kt] = pk_bf16(d[0], d[1]); VWX[ct][2 * kt + 1] = pk_bf16(d[2], d[3]);
        }
    };
    auto rmax = [&](float v) { float a = v, b = v; swap16f(a, b); v = max3f(a, b, ninf); a = v; b = v; swap32f(a, b); return max3f(a, b, ninf); };
    dma_x(0); dma_x(1);
    if (ROLE == 1 && lane == 0) *(volatile LAS unsigned*)(lds + L_FLAG) = 0u;
    asm volatile("s_waitcnt vmcnt(0) lgkmcnt(0)" ::: "memory"); BAR(); asm volatile("" ::: "memory");
    vwx_kt(0, 0); vwx_kt(0, 1);
    if (ROLE == 2) load_kx(0);
    if (ROLE >= 1) __builtin_amdgcn_s_setprio(2);

    unsigned spins = 0;
    for (int s = 0; s < NSEG; ++s) {
        f32x4 c[6];
#pragma unroll
        for (int i = 0; i < 6; ++i) { c[i] = (f32x4){0.f, 0.f, 0.f, 0.f}; asm volatile("" : "+v"(c[i])); }
        const unsigned xw = win_off(W_X(s & 1));
        unsigned pub = pub_off + (g == j ? (unsigned)s * (128u * 1536u) : xw); asm volatile("" : "+v"(pub));
        u32x4 a0, a1;
        {
            PH0();
            const unsigned so = win_off(W_MEM(s) + (w >> 2)) + (unsigned)(w & 3) * 2048u;
            for (;;) {
                asm volatile("" ::: "memory");
                a0 = ld16_sc1(xr, l16, so); a1 = ld16_sc1(xr, l16 + 1024, so);
                if (__builtin_expect(all_set(max(umax4(a0), umax4(a1))), 1)) break;
                RETRY(0);
                if (spin_out(spins, ctl)) break;
            }
            PH1(0);
        }
        SEG(0);
        if (s > 0) {
            const u32x4 sv = {SENT, SENT, SENT, SENT};
            if (ROLE <= 1) __builtin_amdgcn_raw_buffer_store_b128(sv, xr, l16, win_off(W_X((s - 1) & 1)) + (unsigned)rank * 16384u + (unsigned)w * 1024u, 16);
            else {
                const unsigned so = win_off(W_PART((s - 1) & 1)) + (unsigned)(red >> 1) * 16384u + (unsigned)(red & 1) * 4096u + (unsigned)(w & 1) * 2048u;
                __builtin_amdgcn_raw_buffer_store_b128(sv, xr, l16, so, 16); __builtin_amdgcn_raw_buffer_store_b128(sv, xr, l16 + 1024, so, 16);
            }
        }
        auto fold = [&](int mt) {
            f32x4 r;
#pragma unroll
            for (int i = 0; i < 4; ++i) { float x = c[2 * mt][i], y = c[2 * mt + 1][i]; swap32f(x, y); r[i] = x + y; }
            *(LAS f32x4*)(lds + redw + mt * (RED_M * 4)) = r;
        };
        c[0] = smma(a0, W[0][0], c[0], sidx); c[1] = smma(a0, W[1][0], c[1], sidx); c[0] = smma(a1, W[0][1], c[0], sidx); c[1] = smma(a1, W[1][1], c[1], sidx);
        c[2] = smma(a0, W[2][0], c[2], sidx); c[3] = smma(a0, W[3][0], c[3], sidx);
        SCHED();
        c[2] = smma(a1, W[2][1], c[2], sidx); c[3] = smma(a1, W[3][1], c[3], sidx); c[4] = smma(a0, W[4][0], c[4], sidx); c[5] = smma(a0, W[5][0], c[5], sidx);
        fold(0);
        SCHED();
        c[4] = smma(a1, W[4][1], c[4], sidx); c[5] = smma(a1, W[5][1], c[5], sidx);
        fold(1);
        SCHED();
        fold(2);
        SEG(1);
        LDS_BAR();
        SEG(2);
        if (ROLE <= 1) {
            f32x2 v[8];
#pragma unroll
            for (int ww = 0; ww < 8; ++ww) { const LAS float* q = (const LAS float*)(lds + redr + ww * 4096); v[ww] = (f32x2){q[0], q[512]}; }
            const f32x2 t = ((v[0] + v[1]) + (v[2] + v[3])) + ((v[4] + v[5]) + (v[6] + v[7]));
            const u32x4 o = rows_gather(min(pk_bf16(t.x, t.y), SENT - 1));
            __builtin_amdgcn_raw_buffer_store_b128(o, xr, pub, 0, 16);
            if (lane < 16) *(LAS u32x4*)(lds + L_OWN + w * 256 + lane * 16) = o;
        } else if (ROLE == 3) {
            if (rank == 0 && lane == 0) __hip_atomic_store(ctl + C_PROG, (unsigned)s, RLX_AGENT);
        }
        SEG(3);
        if (s == NSEG - 1) break;
        f32x4 dv[2];
#pragma unroll
        for (int i = 0; i < 2; ++i) { dv[i] = (f32x4){0.f, 0.f, 0.f, 0.f}; asm volatile("" : "+v"(dv[i])); }
        LDS_BAR();
        if (ROLE >= 1) dly(p.d1);
        SEG(11);
        u32x4 X[4], v0, v1;
        if (ROLE >= 1) {
            PH0();
            const unsigned cw = xw + (unsigned)rank * 16384u;
            const u32x4 vo = *(const LAS u32x4*)(lds + L_OWN + 1024 + (lane & 31) * 16);
            if (ROLE >= 2) X[0] = *(const LAS u32x4*)(lds + L_OWN + (ROLE == 2 ? (l16 & ~128u) : l16));
            for (;;) {
                asm volatile("" ::: "memory");
                unsigned mx = 0;
                if (ROLE >= 2) {
#pragma unroll
                    for (int k = 1; k < 4; ++k) X[k] = ld16_sc1(xr, ROLE == 2 ? (l16 & ~128u) : l16, cw + (unsigned)((j + k) & 3) * 1536u);
                }
                v0 = ld16_sc1(xr, vg0, cw); v1 = ld16_sc1(xr, vg1, cw);
                if (ROLE >= 2) mx = max(umax4(X[1]), max(umax4(X[2]), umax4(X[3])));
                if (__builtin_expect(all_set(max(mx, max(umax4(v0), umax4(v1)))), 1)) break;
                RETRY(1);
                if (spin_out(spins, ctl)) break;
            }
            PH1(1);
            if (vown) v0 = vo;
        }
        SEG(4);
        if (ROLE == 1) {
            *(LAS u32x4*)(lds + L_VM + lane * 16) = v0; *(LAS u32x4*)(lds + L_VM + 1024 + lane * 16) = v1;
            asm volatile("" ::: "memory");
            *(volatile LAS unsigned*)(lds + L_FLAG) = (unsigned)s + 1u;
        } else if (ROLE == 0) {
            while (__builtin_amdgcn_readfirstlane(*(volatile LAS unsigned*)(lds + L_FLAG)) != (unsigned)s + 1u) __builtin_amdgcn_s_sleep(1);
            asm volatile("" ::: "memory");
            v0 = *(const LAS u32x4*)(lds + L_VM + lane * 16); v1 = *(const LAS u32x4*)(lds + L_VM + 1024 + lane * 16);
        }
        if (ROLE == 2) {
            f32x4 s0 = {0.f, 0.f, 0.f, 0.f}, s1 = s0;
#pragma unroll
            for (int k = 0; k < 4; ++k) { s0 = mma32(asbf(KX[k]), asbf(X[k]), s0); s1 = mma32(asbf(KX[4 + k]), asbf(X[k]), s1); }
            dv[0] = smma(v0, WO[0][0], dv[0], sidx);
            SCHED();
            const float m = rmax(max3f(max3f(s0[0], s0[1], s0[2]), max3f(s0[3], s1[0], s1[1]), max3f(s1[2], s1[3], ninf)));
            dv[1] = smma(v0, WO[1][0], dv[1], sidx);
            dv[0] = smma(v1, WO[0][1], dv[0], sidx);
            SCHED();
            const float mc = -m * csc;
#pragma unroll
            for (int i = 0; i < 4; ++i) { s0[i] = __builtin_amdgcn_exp2f(fmaf(s0[i], csc, mc)); s1[i] = __builtin_amdgcn_exp2f(fmaf(s1[i], csc, mc)); }
            dv[1] = smma(v1, WO[1][1], dv[1], sidx);
            *(LAS u32x4*)(lds + L_PX + lane * 16) = (u32x4){pk_bf16(s0[0], s0[1]), pk_bf16(s0[2], s0[3]), pk_bf16(s1[0], s1[1]), pk_bf16(s1[2], s1[3])};
            const f32x4 t = s0 + s1;
            const float l = rows_sum((t[0] + t[1]) + (t[2] + t[3]));
            *(LAS f32x2*)(lds + L_ML + n * 16) = (f32x2){mc, l};
        } else if (ROLE == 3) {
            f32x4 sm = {0.f, 0.f, 0.f, 0.f};
#pragma unroll
            for (int k = 0; k < 4; ++k) sm = mma32(asbf(X[k]), asbf(X[k]), sm);
#pragma unroll
            for (int ct = 0; ct < 2; ++ct) { dv[ct] = smma(v0, WO[ct][0], dv[ct], sidx); dv[ct] = smma(v1, WO[ct][1], dv[ct], sidx); }
            if (g < 2) sm = (f32x4){ninf, ninf, ninf, ninf};
            const float m = rmax(max3f(max3f(sm[0], sm[1], sm[2]), sm[3], ninf));
            const float mc = -m * csc;
#pragma unroll
            for (int i = 0; i < 4; ++i) sm[i] = __builtin_amdgcn_exp2f(fmaf(sm[i], csc, mc));
            *(LAS u32x2*)(lds + L_PM + lane * 8) = (u32x2){pk_bf16(sm[0], sm[1]), pk_bf16(sm[2], sm[3])};
            const float l = rows_sum((sm[0] + sm[1]) + (sm[2] + sm[3]));
            *(LAS f32x2*)(lds + L_ML + n * 16 + 8) = (f32x2){mc, l};
        } else {
#pragma unroll
            for (int ct = 0; ct < 2; ++ct) { dv[ct] = smma(v0, WO[ct][0], dv[ct], sidx); dv[ct] = smma(v1, WO[ct][1], dv[ct], sidx); }
        }
        u32x4 VWM[2];
#pragma unroll
        for (int ct = 0; ct < 2; ++ct) { VWM[ct] = (u32x4){pk_bf16(dv[ct][0], dv[ct][1]), pk_bf16(dv[ct][2], dv[ct][3]), 0u, 0u}; asm volatile("" : "+v"(VWM[ct])); }
        unsigned zz0 = 0u, zz1 = 0u; asm volatile("" : "+v"(zz0), "+v"(zz1));
        const unsigned pso = win_off(W_PART(s & 1));
        if (ROLE == 0) WAIT_V(0);
        SEG(5);
        LDS_BAR();
        SEG(6);
        {
            const f32x4 ml = *(const LAS f32x4*)(lds + L_ML + n * 16);
            const u32x4 px = *(const LAS u32x4*)(lds + L_PX + lane * 16);
            const u32x2 pm2 = *(const LAS u32x2*)(lds + L_PM + ((2 + (g & 1)) * 16 + n) * 8);
            const u32x4 pm = {pm2.x, pm2.y, zz0, zz1};
            f32x4 dx[2], dm[2];
#pragma unroll
            for (int ct = 0; ct < 2; ++ct) dx[ct] = mma32(asbf(VWX[ct]), asbf(px), (f32x4){0.f, 0.f, 0.f, 0.f});
#pragma unroll
            for (int ct = 0; ct < 2; ++ct) dm[ct] = mma32(asbf(VWM[ct]), asbf(pm), (f32x4){0.f, 0.f, 0.f, 0.f});
            const float dl = ml[0] - ml[2];
            const float e = __builtin_amdgcn_exp2f(-__builtin_fabsf(dl));
            const float ex = dl > 0.f ? e : 1.f, em = dl > 0.f ? 1.f : e;
            const float inv = __builtin_amdgcn_rcpf(fmaf(ex, ml[1], em * ml[3]));
            float ax = ex * inv, am = em * inv;
            asm volatile("" : "+v"(ax), "+v"(am));
            const f32x4 d0 = dx[0] * ax + dm[0] * am, d1 = dx[1] * ax + dm[1] * am;
            u32x4 o = {min(pk_bf16(d0[0], d0[1]), SENT - 1), min(pk_bf16(d0[2], d0[3]), SENT - 1), min(pk_bf16(d1[0], d1[1]), SENT - 1), min(pk_bf16(d1[2], d1[3]), SENT - 1)};
            asm volatile("" : "+v"(o));
            if (n < 8) __builtin_amdgcn_raw_buffer_store_b128(o, xr, pst_off, pso, 16);
        }
        SEG(7);
        SCHED();
        if (s + 2 < NSEG - 1) dma_x(s + 2);
        vwx_kt(s + 1, 0);
        SCHED();
        SEG(8);
        if (ROLE == 3) {
            u32x4 SEL;
            { const unsigned one = (n & 1) ? 0x3f800000u : 0x00003f80u; const int dw = (n & 7) >> 1; SEL = (u32x4){dw == 0 ? one : 0u, dw == 1 ? one : 0u, dw == 2 ? one : 0u, dw == 3 ? one : 0u}; }
            vwx_kt(s + 1, 1);
            SCHED();
            dly(p.d2);
            SEG(12);
            const unsigned so = pso + (unsigned)(red >> 1) * 16384u + (unsigned)(red & 1) * 4096u;
            const unsigned mo = win_off(W_MEM(s + 1) + (red >> 6)) + (unsigned)(red & 63) * 128u;
            u32x4 q[4];
            PH0();
            for (;;) {
                asm volatile("" ::: "memory");
#pragma unroll
                for (int i = 0; i < 4; ++i) q[i] = ld16_sc1(xr, l16 + i * 1024, so);
                if (__builtin_expect(all_set(max(max(umax4(q[0]), umax4(q[1])), max(umax4(q[2]), umax4(q[3])))), 1)) break;
                RETRY(2);
                if (spin_out(spins, ctl)) break;
            }
            PH1(2);
            f32x4 t = {0.f, 0.f, 0.f, 0.f};
#pragma unroll
            for (int i = 0; i < 4; ++i) t = mma32(asbf(SEL), asbf(q[i]), t);
#pragma unroll
            for (int i = 0; i < 4; ++i) t[i] += __uint_as_float(__builtin_amdgcn_update_dpp(0, __float_as_uint(t[i]), 0x128, 0xf, 0xf, false));
            unsigned a0 = min(pk_bf16(t[0], t[1]), SENT - 1), a1 = min(pk_bf16(t[2], t[3]), SENT - 1);
            unsigned b0 = a0, b1 = a1; swap16(a0, b0); swap16(a1, b1);
            if (lane < 8) __builtin_amdgcn_raw_buffer_store_b128((u32x4){a0, a1, b0, b1}, xr, l16, mo, 16);
        } else {
            vwx_kt(s + 1, 1);
            if (ROLE == 2) load_kx(s + 1);
        }
        SEG(9);
        LDS_BAR();
        dly(p.d0);
        SEG(10);
    }
    WAIT_V(0);
    __builtin_amdgcn_s_setprio(0);
}

DI void chain_role(const Params& p, int rank, ldsp lds, unsigned long long* stamps) {
    const int tid = fresh_tid(), w = __builtin_amdgcn_readfirstlane(tid >> 6), lane = tid & 63, g = lane >> 4, n = lane & 15;
    const int h = rank >> 2, j = rank & 3;
    const rsrc_t xr = mkrsrc(p.ws + WS_XCH, (unsigned)XCH_BYTES);
    unsigned* ctl = (unsigned*)(p.ws + WS_CTL);
    if (rank == 0 && w == 7) {
        unsigned best0 = ~0u, best1 = ~0u;
        for (int i = 0; i < 12; ++i) {
            const unsigned long long t0 = __builtin_amdgcn_s_memtime();
            __builtin_amdgcn_raw_buffer_store_b32(0u, xr, (unsigned)(2 * 16384 + C_PROBE * 4) + (unsigned)(i & 1) * 8192u + (unsigned)(i >> 1) * 256u, 0, 16);
            asm volatile("s_waitcnt vmcnt(0)" ::: "memory");
            const unsigned dt = (unsigned)(__builtin_amdgcn_s_memtime() - t0);
            if (i & 1) best1 = min(best1, dt); else best0 = min(best0, dt);
        }
        if (lane == 0) __hip_atomic_store(ctl + C_PAR, best1 < best0 ? 1u : 0u, RLX_AGENT);
    }
    bf16x16 W[6][2], WO[2][2];
    {
        ldsp my = lds + w * 16384;
        const int rrow = lane >> 2, rc = (lane & 3) * 8, q4 = n >> 2;
        const int toffo = (8 * g + q4) * 64 + (n & 3) * 8;
        const int toffq = (16 * (g & 1) + 8 * (g >> 1) + 4 * (q4 >> 1) + (q4 & 1)) * 64 + (n & 3) * 8;
#define FRAG(base, st) cat16(cat8(trd((base)), trd((base) + (st))), cat8(trd((base) + 2048), trd((base) + 2048 + (st))))
#pragma unroll
        for (int i = 0; i < 8; ++i) {
            __builtin_amdgcn_global_load_lds((const unsigned*)(p.wq + (size_t)(128 * w + 16 * i + rrow) * 4096 + h * 128 + 32 * j + rc), (LAS unsigned*)(my + i * 1024), 16, 0, 0);
            __builtin_amdgcn_global_load_lds((const unsigned*)(p.wk + (size_t)(128 * w + 16 * i + rrow) * 4096 + h * 128 + 32 * j + rc), (LAS unsigned*)(my + 8192 + i * 1024), 16, 0, 0);
        }
        WAIT_V(0);
#pragma unroll
        for (int T = 0; T < 4; ++T)
#pragma unroll
            for (int t = 0; t < 2; ++t) W[T][t] = FRAG(my + (T >> 1) * 8192 + t * 4096 + (T & 1) * 32 + toffq, 128);
#pragma unroll
        for (int T = 0; T < 4; ++T) asm volatile("" : "+v"(W[T][0]), "+v"(W[T][1]));
        WAIT_L(0);
#pragma unroll
        for (int i = 0; i < 8; ++i) {
            __builtin_amdgcn_global_load_lds((const unsigned*)(p.wv + (size_t)(128 * w + 16 * i + rrow) * 4096 + h * 128 + 32 * j + rc), (LAS unsigned*)(my + i * 1024), 16, 0, 0);
            __builtin_amdgcn_global_load_lds((const unsigned*)(p.wo + (size_t)(h * 128 + 16 * i + rrow) * 1024 + 256 * j + 32 * w + rc), (LAS unsigned*)(my + 8192 + i * 1024), 16, 0, 0);
        }
        WAIT_V(0);
#pragma unroll
        for (int T = 0; T < 2; ++T)
#pragma unroll
            for (int t = 0; t < 2; ++t) { W[4 + T][t] = FRAG(my + t * 4096 + T * 32 + toffq, 128); WO[T][t] = FRAG(my + 8192 + (t ^ (j >> 1)) * 4096 + T * 32 + toffo, 256); }
#pragma unroll
        for (int T = 0; T < 2; ++T) asm volatile("" : "+v"(W[4 + T][0]), "+v"(W[4 + T][1]), "+v"(WO[T][0]), "+v"(WO[T][1]));
        WAIT_L(0);
#undef FRAG
    }
    wait_count(ctl, C_PAR, 1u);
    const unsigned par = __builtin_amdgcn_readfirstlane(__hip_atomic_load(ctl + C_PAR, RLX_AGENT)) & 1u;
    const int red = ((j + 1) & 3) * 32 + h;
    {
        const u32x4 sv = {SENT, SENT, SENT, SENT};
        if (w < 6) {
            __builtin_amdgcn_raw_buffer_store_b128(sv, xr, (unsigned)lane * 16u, win_off(W_X(0)) + (unsigned)rank * 16384u + (unsigned)w * 1024u, 16);
            __builtin_amdgcn_raw_buffer_store_b128(sv, xr, (unsigned)lane * 16u, win_off(W_X(1)) + (unsigned)rank * 16384u + (unsigned)w * 1024u, 16);
        }
        __builtin_amdgcn_raw_buffer_store_b128(sv, xr, (unsigned)lane * 16u, win_off(W_PART(w >> 2)) + (unsigned)(red >> 1) * 16384u + (unsigned)(red & 1) * 4096u + (unsigned)(w & 3) * 1024u, 16);
        const int s = 8 * w + (lane >> 3);
        if (s > 0) __builtin_amdgcn_raw_buffer_store_b128(sv, xr, win_off(W_MEM(s) + (red >> 6)) + (unsigned)(red & 63) * 128u + (unsigned)(lane & 7) * 16u, 0, 16);
    }
    WAIT_V(0);
    BAR();
    if (w == 7 && lane < 8) {
        const unsigned* src = (const unsigned*)(p.mem0 + lane * DM + 32 * (red >> 2) + 4 * (red & 3));
        const u32x4 v = {min(src[0], SENT - 1), min(src[1], SENT - 1), min(src[8], SENT - 1), min(src[9], SENT - 1)};
        __builtin_amdgcn_raw_buffer_store_b128(v, xr, (unsigned)lane * 16u + (unsigned)(red & 63) * 128u, win_off(W_MEM(0) + (red >> 6)), 16);
    }
    if (w < 5) chain_loop<0>(p, par, rank, w, lds, W, WO, stamps);
    else if (w == 5) chain_loop<1>(p, par, rank, 5, lds, W, WO, stamps);
    else if (w == 6) chain_loop<2>(p, par, rank, 6, lds, W, WO, stamps);
    else chain_loop<3>(p, par, rank, 7, lds, W, WO, stamps);
}

template <class Wait>
DI void attn_a_item(const Params& p, int s, int h, ldsp my, int lane, const Wait& wait_mem) {
    const int g = lane >> 4, n = lane & 15;
    const rsrc_t xr = mkrsrc(p.ws + WS_XCH, (unsigned)XCH_BYTES);
    const rsrc_t qr = mkrsrc(p.ws + WS_QKVX + (size_t)h * XH + (size_t)s * 8192, 65u * XH);
#pragma unroll
    for (int i = 0; i < 8; ++i)
        __builtin_amdgcn_raw_ptr_buffer_load_lds(qr, (LAS void*)(my + i * 1024), 16, (unsigned)g * 256u + (unsigned)((n ^ (((4 * (i & 1) + g) & 7) << 1)) * 16), 64u * XH + (unsigned)i * 1024u, 0, 16);
    u32x4 Q[2][4], K[2][4], XM[4];
    const unsigned ro = (unsigned)n * 256u + (unsigned)g * 16u;
#pragma unroll
    for (int t = 0; t < 2; ++t)
#pragma unroll
        for (int k = 0; k < 4; ++k) { Q[t][k] = ld16_sc1(qr, ro + k * 64, (unsigned)t * 4096u); K[t][k] = ld16_sc1(qr, ro + k * 64, (unsigned)t * 4096u + 32u * XH); }
    wait_mem();
    const unsigned qwin = XCH_KM + (unsigned)(s * 32 + h) * 6144u;
    u32x4 VM[2];
#pragma unroll
    for (int k = 0; k < 4; ++k) XM[k] = ld16_sc1(xr, (unsigned)lane * 16u + (k & 1) * 1536, qwin + (k >> 1) * 3072);
#pragma unroll
    for (int ts = 0; ts < 2; ++ts) VM[ts] = ld16_sc1(xr, (unsigned)(g >> 1) * 1536u + 1024u + (unsigned)(lane & 31) * 16u, qwin + ts * 3072);
#pragma unroll
    for (int ts = 0; ts < 2; ++ts) {
        const u32x4 v = VM[ts];
        const int m = n & 7, hf = n >> 3, row = 32 + m;
#pragma unroll
        for (int i = 0; i < 4; ++i) { const int e = 64 * ts + 16 * g + 4 * i + 2 * hf;
            *(LAS unsigned*)(my + row * 256 + (((e >> 3) ^ ((row & 7) << 1)) * 16) + (e & 7) * 2) = v[i]; }
    }
    const float csc = 0.08838834764831845f * 1.4426950408889634f, ninf = -__builtin_inff();
    u32x4 PX[2]; u32x2 PM[2];
#pragma unroll
    for (int qt = 0; qt < 2; ++qt) {
        f32x4 sm = {0.f, 0.f, 0.f, 0.f}, s0 = sm, s1 = sm;
#pragma unroll
        for (int k = 0; k < 4; ++k) { sm = mma32(asbf(XM[k]), asbf(Q[qt][k]), sm); s0 = mma32(asbf(K[0][k]), asbf(Q[qt][k]), s0); if (qt == 1) s1 = mma32(asbf(K[1][k]), asbf(Q[qt][k]), s1); }
#pragma unroll
        for (int i = 0; i < 4; ++i) {
            if (g < 2) sm[i] = ninf;
            if (qt == 0) { if (4 * g + i > n) s0[i] = ninf; s1[i] = ninf; }
            else if (4 * g + i > n) s1[i] = ninf;
        }
        float m = fmaxf(fmaxf(fmaxf(s0[0], s0[1]), fmaxf(s0[2], s0[3])), fmaxf(fmaxf(s1[0], s1[1]), fmaxf(s1[2], s1[3])));
        m = fmaxf(m, fmaxf(fmaxf(sm[0], sm[1]), fmaxf(sm[2], sm[3])));
        m = rows_max(m);
        const float mc = -m * csc; float sum = 0.f;
#pragma unroll
        for (int i = 0; i < 4; ++i) { s0[i] = __builtin_amdgcn_exp2f(fmaf(s0[i], csc, mc)); s1[i] = __builtin_amdgcn_exp2f(fmaf(s1[i], csc, mc)); sm[i] = __builtin_amdgcn_exp2f(fmaf(sm[i], csc, mc));
            sum += (s0[i] + s1[i]) + sm[i]; }
        sum = rows_sum(sum);
        const float inv = __builtin_amdgcn_rcpf(sum);
        PX[qt] = (u32x4){pk_bf16(s0[0] * inv, s0[1] * inv), pk_bf16(s0[2] * inv, s0[3] * inv), pk_bf16(s1[0] * inv, s1[1] * inv), pk_bf16(s1[2] * inv, s1[3] * inv)};
        PM[qt] = (u32x2){pk_bf16(sm[0] * inv, sm[1] * inv), pk_bf16(sm[2] * inv, sm[3] * inv)};
    }
    asm volatile("s_waitcnt vmcnt(0) lgkmcnt(0)" ::: "memory");
    const int q4 = n >> 2, p4 = n & 3;
    const int r0 = 4 * g + q4, rm = 32 + 4 * (g & 1) + q4;
    const rsrc_t orr = mkrsrc(p.ws + WS_OA + (size_t)((s >> 2) * 64 + 2 * h) * 16384 + (size_t)(s & 3) * 4096, 32768u);
    const unsigned oo = (unsigned)(n * 128 + (g & 1) * 32 + (g >> 1) * 16);
#pragma unroll
    for (int ep = 0; ep < 4; ++ep) {
        f32x4 d[2][2];
#pragma unroll
        for (int e2 = 0; e2 < 2; ++e2) {
            const int ch = (2 * ep + e2) * 2 + (p4 >> 1);
            const bf16x8 va = cat8(trd(my + r0 * 256 + ((ch ^ ((r0 & 7) << 1)) * 16) + (p4 & 1) * 8), trd(my + (r0 + 16) * 256 + ((ch ^ ((r0 & 7) << 1)) * 16) + (p4 & 1) * 8));
            const s16x4 vm = trd(my + rm * 256 + ((ch ^ ((rm & 7) << 1)) * 16) + (p4 & 1) * 8);
#pragma unroll
            for (int qt = 0; qt < 2; ++qt) { f32x4 c = {0.f, 0.f, 0.f, 0.f}; c = mma32(va, asbf(PX[qt]), c); d[qt][e2] = mma16(__builtin_bit_cast(u32x2, vm), PM[qt], c); }
        }
#pragma unroll
        for (int qt = 0; qt < 2; ++qt) store_pair(orr, oo + (ep & 1) * 64 + qt * 2048, (unsigned)(ep >> 1) * 16384u, d[qt][0], d[qt][1], true);
    }
    asm volatile("s_waitcnt lgkmcnt(0)" ::: "memory");
}

DI void attn_b_item(const Params& p, int hq, int qb, ldsp lds) {
    const int tid = fresh_tid(), w = __builtin_amdgcn_readfirstlane(tid >> 6), lane = tid & 63, g = lane >> 4, n = lane & 15;
    const int q0 = qb * 128;
    const rsrc_t br = mkrsrc(p.ws + WS_QKVB, 96u * BH);
    const rsrc_t orr = mkrsrc(p.ws + WS_OB + (size_t)qb * 16 * 16384, 16u * 16384u);
    constexpr int BUF = 57344;
    unsigned ko[3], vo[3];
#pragma unroll
    for (int i = 0; i < 3; ++i) { const int r = (w * 3 + i) * 16 + (lane >> 2), c = lane & 3; const unsigned rp = (unsigned)max(q0 - 256 + r, 0) * 64u;
        ko[i] = rp + 32u * BH + (unsigned)((c ^ (((r >> 3) & 1) * 3)) * 16); vo[i] = rp + 64u * BH + (unsigned)((c ^ (((r >> 2) & 1) << 1)) * 16); }
    const unsigned qo = (unsigned)(q0 + 16 * w + (lane >> 2)) * 64u + (unsigned)(((lane & 3) ^ (((lane >> 5) & 1) * 3)) * 16);
    auto stage = [&](int i) {
        const unsigned so = (unsigned)(hq * 4 + i) * BH; ldsp d = lds + (i & 1) * BUF;
#pragma unroll
        for (int k = 0; k < 3; ++k) {
            __builtin_amdgcn_raw_ptr_buffer_load_lds(br, (LAS void*)(d + (w * 3 + k) * 1024), 16, ko[k], so, 0, 16);
            __builtin_amdgcn_raw_ptr_buffer_load_lds(br, (LAS void*)(d + 24576 + (w * 3 + k) * 1024), 16, vo[k], so, 0, 16);
        }
        __builtin_amdgcn_raw_ptr_buffer_load_lds(br, (LAS void*)(d + 49152 + w * 1024), 16, qo, so, 0, 16);
    };
    const float ninf = -__builtin_inff(), csc = 0.17677669529663687f * 1.4426950408889634f;
    const int tmin = (256 - q0 - 16 * w) >> 4;
    const int q4 = n >> 2, p4 = n & 3, sw = (g & 1) << 1;
    const int kq = (16 * w + n) * 64 + ((g ^ (((n >> 3) & 1) * 3)) * 16);
    const unsigned vb = (unsigned)(size_t)lds + 24576u + (unsigned)((16 * w + 4 * g + q4) * 64 + (p4 & 1) * 8);
    stage(0);
#pragma unroll 1
    for (int i = 0; i < 4; ++i) {
        if (i < 3) { stage(i + 1); WAIT_V(7); } else WAIT_V(0);
        BAR(); asm volatile("" ::: "memory");
        const ldsp bp = lds + (i & 1) * BUF;
        const bf16x8 Q = *(const LAS bf16x8*)(bp + 49152 + kq);
        f32x4 sc[17];
#pragma unroll
        for (int t = 0; t < 17; ++t) sc[t] = mma32(*(const LAS bf16x8*)(bp + kq + t * 1024), Q, (f32x4){0.f, 0.f, 0.f, 0.f});
#pragma unroll
        for (int k = 0; k < 4; ++k) { if (4 * g + k <= n) sc[0][k] = ninf; if (4 * g + k > n) sc[16][k] = ninf; }
        if (tmin > 0) {
#pragma unroll
            for (int t = 0; t < 16; ++t) if (t < tmin) sc[t] = (f32x4){ninf, ninf, ninf, ninf};
        }
        float m = max3f(sc[16][0], sc[16][1], max3f(sc[16][2], sc[16][3], ninf));
#pragma unroll
        for (int t = 0; t < 16; ++t) m = max3f(max3f(m, sc[t][0], sc[t][1]), sc[t][2], sc[t][3]);
        m = rows_max(m);
        const float mc = -m * csc;
        f32x4 sum4 = {0.f, 0.f, 0.f, 0.f};
#pragma unroll
        for (int t = 0; t < 17; ++t) {
#pragma unroll
            for (int k = 0; k < 4; ++k) sc[t][k] = __builtin_amdgcn_exp2f(fmaf(sc[t][k], csc, mc));
            sum4 += sc[t];
        }
        const float inv = __builtin_amdgcn_rcpf(rows_sum((sum4[0] + sum4[1]) + (sum4[2] + sum4[3])));
        const unsigned v0 = vb + (unsigned)((i & 1) * BUF) + (unsigned)(((p4 >> 1) ^ sw) * 16), v1 = vb + (unsigned)((i & 1) * BUF) + (unsigned)(((2 + (p4 >> 1)) ^ sw) * 16);
        s16x4 V0[17], V1[17];
#define VRD(t) TRD(V0[t], v0, (t) * 1024); TRD(V1[t], v1, (t) * 1024)
        VRD(0); VRD(1); VRD(2); VRD(3); VRD(4); VRD(5); VRD(6); VRD(7); VRD(8); VRD(9); VRD(10); VRD(11); VRD(12); VRD(13); VRD(14); VRD(15); VRD(16);
#undef VRD
        u32x2 P[17];
#pragma unroll
        for (int t = 0; t < 17; ++t) P[t] = (u32x2){pk_bf16(sc[t][0], sc[t][1]), pk_bf16(sc[t][2], sc[t][3])};
        WAIT_L(0); SCHED();
        f32x4 d0 = {0.f, 0.f, 0.f, 0.f}, d1 = d0;
#pragma unroll
        for (int tp = 0; tp < 8; ++tp) {
            const bf16x8 PP = asbf((u32x4){P[2 * tp].x, P[2 * tp].y, P[2 * tp + 1].x, P[2 * tp + 1].y});
            d0 = mma32(cat8(V0[2 * tp], V0[2 * tp + 1]), PP, d0); d1 = mma32(cat8(V1[2 * tp], V1[2 * tp + 1]), PP, d1);
        }
        d0 = mma16(__builtin_bit_cast(u32x2, V0[16]), P[16], d0); d1 = mma16(__builtin_bit_cast(u32x2, V1[16]), P[16], d1);
        BAR();
        store_pair(orr, (unsigned)((16 * w + n) * 128 + (g & 1) * 32 + (g >> 1) * 16), (unsigned)(hq * 2 + (i >> 1)) * 16384u + (unsigned)(i & 1) * 64u, d0 * inv, d1 * inv, true);
    }
    WAIT_V(0);
    signal((unsigned*)(p.ws + WS_CTL), C_OB + qb * 32, 1);
}

DI void out_tail(const Params& p, int c, int q, ldsp lds) {
    const int tid = fresh_tid(), w = __builtin_amdgcn_readfirstlane(tid >> 6), lane = tid & 63, g = lane >> 4, n = lane & 15;
    unsigned* ctl = (unsigned*)(p.ws + WS_CTL);
    bf16x8 WT[16][2];
    {
        ldsp my = lds + w * 16384;
        const int toff = (8 * g + (n >> 2)) * 64 + (n & 3) * 8;
#pragma unroll
        for (int r = 0; r < 2; ++r) {
#pragma unroll
            for (int i = 0; i < 16; ++i)
                __builtin_amdgcn_global_load_lds((const unsigned*)(p.wo + (size_t)(512 * w + 256 * r + 16 * i + (lane >> 2)) * 1024 + 32 * c + (lane & 3) * 8), (LAS unsigned*)(my + i * 1024), 16, 0, 0);
            WAIT_V(0);
#pragma unroll
            for (int k = 0; k < 8; ++k)
#pragma unroll
                for (int ct = 0; ct < 2; ++ct) { WT[8 * r + k][ct] = cat8(trd(my + k * 2048 + ct * 32 + toff), trd(my + k * 2048 + ct * 32 + toff + 256)); }
#pragma unroll
            for (int k = 0; k < 8; ++k) asm volatile("" : "+v"(WT[8 * r + k][0]), "+v"(WT[8 * r + k][1]));
            WAIT_L(0);
        }
    }
    BAR();
#pragma unroll 1
    for (int hs = q; hs < 24; hs += 3) {
        const int s = 52 + (hs >> 1), rt = hs & 1;
        if (w == 0) wait_count(ctl, C_OS + (s - 52) * 32, 32u);
        asm volatile("" ::: "memory"); BAR(); asm volatile("" ::: "memory");
        const rsrc_t ar = mkrsrc(p.ws + WS_OA + (size_t)((s >> 2) * 64 + 8 * w) * 16384 + (size_t)(s & 3) * 4096 + (size_t)rt * 2048, 8u * 16384u);
        const unsigned ao = (unsigned)n * 128u + (unsigned)g * 16u;
        u32x4 A[16];
#pragma unroll
        for (int k = 0; k < 16; ++k) A[k] = ld16_sc1(ar, ao + (k & 1) * 64, (unsigned)(k >> 1) * 16384u);
#pragma unroll
        for (int ct = 0; ct < 2; ++ct) {
            f32x4 acc = {0.f, 0.f, 0.f, 0.f};
#pragma unroll
            for (int k = 0; k < 16; ++k) acc = mma32(WT[k][ct], asbf(A[k]), acc);
            *(LAS f32x4*)(lds + w * 2048 + ct * 1024 + lane * 16) = acc;
        }
        LDS_BAR();
        if (w < 2) {
            f32x4 t = {0.f, 0.f, 0.f, 0.f};
#pragma unroll
            for (int ww = 0; ww < 8; ++ww) t += *(const LAS f32x4*)(lds + ww * 2048 + w * 1024 + lane * 16);
            *(u32x2*)(p.out + (size_t)(32 * s + 16 * rt + n) * 2048 + 32 * c + 16 * w + 4 * g) = (u32x2){pk_bf16(t[0], t[1]), pk_bf16(t[2], t[3])};
        }
        WAIT_L(0);
    }
    BAR();
}

DI void big_unit(const Params& p, int id, Unit& u) {
    u.lda = DM * 2; u.nt = 16; u.ak = 128; u.ah = 128 * DM * 2;
    if (id < 384) {
        const int pm = id & 7, pn = id >> 3;
        const u16 *w0 = p.wq, *w1 = p.wk, *w2 = p.wv; const u16* W = pn < 16 ? w0 : (pn < 32 ? w1 : w2);
        u.A = (const char*)(p.ax + (size_t)pm * 256 * DM); u.B = (const char*)(W + (pn & 15) * 256); u.ldb = 4096 * 2;
        u.C = p.ws + WS_QKVX + (size_t)(2 * pn) * XH + (size_t)pm * 65536; u.ldc = 256; u.cw = 64; u.ch = XH;
        if (pn < 16) { u.sig = C_Q + pm * 32; u.nsig = 1; } else { u.sig = C_KV + 2 * pm * 512 + (pn & 15) * 32; u.nsig = 4; }
    } else if (id < 480) {
        const int pm = (id - 384) & 7, pn = (id - 384) >> 3;
        u.A = (const char*)(p.bx + (size_t)pm * 256 * DM); u.B = (const char*)(p.bwqkv + pn * 256); u.ldb = LDB3 * 2;
        u.C = p.ws + WS_QKVB + (size_t)(8 * pn) * BH + (size_t)pm * 16384; u.ldc = 64; u.cw = BH; u.ch = 4 * BH; u.sig = C_B + pm * 32; u.nsig = 1;
    } else {
        const bool isb = id < 512; const int q = isb ? id - 480 : id - 512, pm = q >> 2, pn = q & 3;
        const u16 *w0 = p.wo, *w1 = p.bwo;
        u.nt = isb ? 16 : 64; u.lda = 128; u.ak = 16384; u.ah = (unsigned)u.nt * 16384u;
        u.A = p.ws + (isb ? WS_OB : WS_OA) + (size_t)pm * 2 * u.ah; u.B = (const char*)((isb ? w1 : w0) + pn * 256); u.ldb = DM * 2;
        u.C = (char*)(p.out + (size_t)pm * 256 * 2048 + (isb ? 1024 : 0) + pn * 256); u.ldc = 4096; u.cw = 64; u.ch = 256; u.sig = 0; u.nsig = 0;
    }
}
struct BigSched { const Params& p; int id0, id1; DI bool next(int i, Unit& u) const { const int id = i == 0 ? id0 : (i == 1 ? id1 : -1); if (id < 0) return false; big_unit(p, id, u); return true; } };

__global__ __launch_bounds__(512, 2) void k_fused(Params p) {
    extern __shared__ __attribute__((aligned(16))) unsigned char shm[];
    ldsp lds = (ldsp)shm;
    unsigned* ctl = (unsigned*)(p.ws + WS_CTL);
    const int b = blockIdx.x, id = (b & 3) * 32 + (b >> 3);
#define WSTAMP(code)
    WSTAMP(1);
    if ((b & 7) < 4) {
        chain_role(p, id, lds, (unsigned long long*)(p.ws + WS_END));
        BAR();
        if (threadIdx.x < 64) signal(ctl, C_DONE + (id >> 5) * 32, 1);
        WSTAMP(2);
    } else {
        {
            const int rb = id >> 6, ct = id & 63;
            const u16 *w1 = p.wk, *w2 = p.wv;
            Unit u; u.A = (const char*)(p.ax + (size_t)rb * 128 * DM); u.lda = DM * 2; u.B = (const char*)((ct < 32 ? w1 : w2) + (ct & 31) * 128); u.ldb = 4096 * 2; u.nt = 16;
            u.C = p.ws + WS_QKVX + (size_t)(32 + ct) * XH + (size_t)rb * 32768; u.ldc = 256; u.cw = 64; u.ch = 0; u.ak = 128; u.ah = 0;
            u.sig = C_KV + rb * 512 + (ct & 31) * 16; u.nsig = 1;
            gemm_small(lds, u, ctl);
            WSTAMP(3);
        }
    }
    volatile LAS unsigned* qw = (volatile LAS unsigned*)(lds + 131072);
    const int w = __builtin_amdgcn_readfirstlane(threadIdx.x >> 6);
    bool fixed = (b & 7) >= 4;
#pragma unroll 1
    for (;;) {
        int kind, x, it = -1, x2 = -1;
        if (fixed) { kind = 6; x = id < 96 ? (16 + (id & 31)) * 8 + 1 + (id >> 5) : (id & 15) * 8 + ((id >> 4) & 1); x2 = (16 + (id & 31)) * 8 + 4 + (id >> 5); fixed = false; }
        else {
        if (threadIdx.x == 0) qw[0] = __hip_atomic_fetch_add(ctl + C_HEAD, 1u, RLX_AGENT) + 1u;
        LDS_BAR();
        it = (int)__builtin_amdgcn_readfirstlane(qw[0]);
        LDS_BAR();
        if (it < 16) { kind = 0; x = it; }
        else if (it < 112) { kind = 6; x = 384 + it - 16; }
        else if (it < 144) { kind = 6; x = ((it - 112) & 15) * 8 + 2 + ((it - 112) >> 4); }
        else if (it < 152) { kind = 6; x = 512 + it - 144; }
        else if (it < 184) { kind = 6; x = ((it - 152) & 15) * 8 + 4 + ((it - 152) >> 4); }
        else if (it < 200) { kind = 0; x = 16 + it - 184; }
        else if (it < 328) { kind = 1; x = it - 200; }
        else if (it < 360) { kind = 6; x = ((it - 328) & 15) * 8 + 6 + ((it - 328) >> 4); }
        else if (it < 368) { kind = 0; x = 32 + it - 360; }
        else if (it < 400) { kind = 2; x = 32 + it - 368; }
        else if (it < 432) { kind = 6; x = 480 + it - 400; }
        else if (it < 448) { kind = 4; x = it - 432; }
        else if (it < 480) { kind = 2; x = 64 + it - 448; }
        else if (it < 576) { kind = 5; x = it - 480; }
        else if (it < 584) { kind = 2; x = 96 + it - 576; }
        else break;
        }
        if (kind == 6) {
            if (x >= 480) { const int w0 = x < 512 ? C_OB + ((x - 480) >> 2) * 64 : C_OA + ((x - 512) >> 2) * 64; const unsigned need = x < 512 ? 64u : 32u; wg_wait(ctl, w0, need, w0 + 32, need); }
            BigSched S{p, x, x2};
            gemm_stream(lds, S, ctl);
        } else
        if (kind == 0) {
            const int s0 = (x >> 2) * 4;
            wait_count(ctl, C_Q + (s0 >> 3) * 32, 128u); wait_count(ctl, C_PROG, (unsigned)s0 + 5u);
            asm volatile("" ::: "memory");
#pragma unroll 1
            for (int s = s0; s < s0 + 4; ++s) attn_a_item(p, s, (x & 3) * 8 + w, lds + w * 10240, fresh_tid() & 63, []() {});
            WAIT_V(0); signal(ctl, C_OA + (x >> 2) * 32, 1);
            BAR();
        } else if (kind == 4) {
#pragma unroll 1
            for (int s = 40 + (x >> 2); s < NSEG; s += 4) {
                wait_count(ctl, C_Q + (s >> 3) * 32, 128u); wait_count(ctl, C_KV + (s >> 2) * 512 + ((x & 3) * 8 + w) * 16, 16u);
                asm volatile("" ::: "memory");
                attn_a_item(p, s, (x & 3) * 8 + w, lds + w * 10240, fresh_tid() & 63, [&]() {
                    if (s < NSEG - 1) wait_count(ctl, C_PROG, (unsigned)s + 2u); else wait_count(ctl, C_DONE + (x & 3) * 32, 32u);
                    asm volatile("" ::: "memory"); });
                WAIT_V(0); signal(ctl, s < 52 ? C_OA + (s >> 2) * 32 : C_OS + (s - 52) * 32, 1);
            }
            BAR();
        } else if (kind == 1) {
            const int qb = x >> 3;
            wait_count(ctl, C_B + (qb >> 1) * 32, 96u); wait_count(ctl, C_B + max((qb >> 1) - 1, 0) * 32, 96u);
            asm volatile("" ::: "memory");
            attn_b_item(p, x & 7, qb, lds);
        } else if (kind == 5) {
            out_tail(p, x & 31, x >> 5, lds);
        } else {
            const int rb = x >> 3, ct = x & 7;
            Unit u; u.ldb = DM * 2; u.ldc = 2048 * 2; u.sig = 0; u.nsig = 0; u.lda = 128; u.ak = 16384; u.ah = 0; u.cw = 64; u.ch = 0;
            u.A = p.ws + WS_OA + (size_t)rb * 64 * 16384; u.B = (const char*)(p.wo + ct * 128); u.nt = 64; u.C = (char*)(p.out + (size_t)rb * 128 * 2048 + ct * 128);
            wg_wait(ctl, C_OA + rb * 32, rb < 10 ? 32u : 128u, C_OA + rb * 32, 32u);
            gemm_small(lds, u, ctl);
        }
        WSTAMP(16 + (kind == 6 && x >= 480 ? (x < 512 ? 7 : 8) : kind) + ((it & 0xffff) << 8));
    }
}

extern "C" void kernel_launch(void* const* d_in, const int* in_sizes, int n_in, void* d_out, int out_size, void* d_ws, size_t ws_size, hipStream_t stream) {
    Params p;
    p.ax = (const u16*)d_in[0]; p.mem0 = (const u16*)d_in[1]; p.wq = (const u16*)d_in[2]; p.wk = (const u16*)d_in[3]; p.wv = (const u16*)d_in[4]; p.wo = (const u16*)d_in[5];
    p.bx = (const u16*)d_in[7]; p.bwqkv = (const u16*)d_in[8]; p.bwo = (const u16*)d_in[9];
    p.out = (u16*)d_out; p.ws = (char*)d_ws + ((0x200000u - ((uintptr_t)d_ws & 0x1FFFFFu)) & 0x1FFFFFu);
    p.d0 = 8; p.d1 = 7; p.d2 = 5; p.d3 = 0;
    (void)hipFuncSetAttribute((const void*)k_fused, hipFuncAttributeMaxDynamicSharedMemorySize, LDS_BYTES);
    (void)hipMemsetAsync(p.ws + WS_XCH, 0xFF, (size_t)NWIN_INIT * 16384, stream);
    k_fused<<<256, 512, LDS_BYTES, stream>>>(p);
}
```

```cpp
#include <hip/hip_runtime.h>
#include <stdint.h>
#include <stdlib.h>

#define LAS __attribute__((address_space(3)))
#define DI __device__ __forceinline__
typedef unsigned short u16;
typedef __attribute__((ext_vector_type(8))) __bf16 bf16x8;
typedef __attribute__((ext_vector_type(16))) __bf16 bf16x16;
typedef __attribute__((ext_vector_type(2))) __bf16 bf16x2;
typedef __attribute__((ext_vector_type(4))) short s16x4;
typedef __attribute__((ext_vector_type(8))) short s16x8;
typedef __attribute__((ext_vector_type(16))) short s16x16;
typedef __attribute__((ext_vector_type(2))) float f32x2;
typedef __attribute__((ext_vector_type(4))) float f32x4;
typedef __attribute__((ext_vector_type(2))) unsigned u32x2;
typedef __attribute__((ext_vector_type(4))) unsigned u32x4;
typedef LAS unsigned char* ldsp;
typedef __amdgpu_buffer_rsrc_t rsrc_t;

constexpr int S_LEN = 2048, DM = 1024, NH = 32, DH = 128, NSEG = 64;
constexpr int LDB3 = 3072;
constexpr unsigned XH = S_LEN * 256, BH = S_LEN * 64;
constexpr unsigned SENT = 0xFFFFFFFFu;
constexpr size_t WS_XCH = 2u << 20;
constexpr int NWIN = 640, NWIN_INIT = 6;
constexpr unsigned XCH_KM = NWIN * 16384u;
constexpr size_t XCH_BYTES = (size_t)XCH_KM + 64u * 128u * 1536u;
constexpr size_t WS_CTL = WS_XCH + 2 * 16384;
constexpr int C_TMO = 0, C_PAR = 64, C_HEAD = 2560, C_PROG = 192, C_Q = 256, C_B = C_Q + 8 * 32, C_OA = C_B + 8 * 32, C_OB = C_OA + 16 * 32, C_OS = C_OB + 16 * 32, C_DONE = C_OS + 12 * 32, C_KV = 4096, C_PROBE = 12288;
constexpr size_t WS_QKVX = 26u << 20;
constexpr size_t WS_QKVB = WS_QKVX + (size_t)96 * XH;
constexpr size_t WS_OA = WS_QKVB + (size_t)96 * BH;
constexpr size_t WS_OB = WS_OA + (size_t)S_LEN * 4096 * 2;
constexpr size_t WS_END = WS_OB + (size_t)S_LEN * 1024 * 2;
#define WS_SHIFT(sz) ((sz) - WS_END - (4u << 20))
constexpr int LDS_BYTES = 131072 + 1024;

DI unsigned pk_bf16(float a, float b) { f32x2 v = {a, b}; return __builtin_bit_cast(unsigned, __builtin_convertvector(v, bf16x2)); }
DI s16x4 trd(ldsp p) { return __builtin_amdgcn_ds_read_tr16_b64_v4i16((LAS s16x4*)p); }
DI bf16x8 cat8(s16x4 a, s16x4 b) { return __builtin_bit_cast(bf16x8, __builtin_shufflevector(a, b, 0, 1, 2, 3, 4, 5, 6, 7)); }
DI bf16x16 cat16(bf16x8 a, bf16x8 b) { return __builtin_shufflevector(a, b, 0, 1, 2, 3, 4, 5, 6, 7, 8, 9, 10, 11, 12, 13, 14, 15); }
DI bf16x8 lo8(bf16x16 v) { return __builtin_shufflevector(v, v, 0, 1, 2, 3, 4, 5, 6, 7); }
DI bf16x8 hi8(bf16x16 v) { return __builtin_shufflevector(v, v, 8, 9, 10, 11, 12, 13, 14, 15); }
DI bf16x8 asbf(u32x4 v) { return __builtin_bit_cast(bf16x8, v); }
DI f32x4 mma32(bf16x8 a, bf16x8 b, f32x4 c) { return __builtin_amdgcn_mfma_f32_16x16x32_bf16(a, b, c, 0, 0, 0); }
DI f32x4 mma16(u32x2 a, u32x2 b, f32x4 c) { return mma32(asbf((u32x4){a.x, a.y, 0u, 0u}), asbf((u32x4){b.x, b.y, 0u, 0u}), c); }
DI f32x4 smma(u32x4 a, bf16x16 b, f32x4 c, int idx) { return __builtin_amdgcn_smfmac_f32_16x16x64_bf16(asbf(a), b, c, idx, 0, 0); }
DI rsrc_t mkrsrc(const void* p, unsigned bytes) { return __builtin_amdgcn_make_buffer_rsrc((void*)p, 0, bytes, 0x00020000); }
DI u32x4 ld16_sc1(rsrc_t r, unsigned voff, unsigned soff) { return __builtin_amdgcn_raw_buffer_load_b128(r, voff, soff, 16); }
DI void swap16(unsigned& a, unsigned& b) { auto r = __builtin_amdgcn_permlane16_swap(a, b, false, false); a = r[0]; b = r[1]; }
DI void swap32(unsigned& a, unsigned& b) { auto r = __builtin_amdgcn_permlane32_swap(a, b, false, false); a = r[0]; b = r[1]; }
DI void swap16f(float& a, float& b) { unsigned x = __float_as_uint(a), y = __float_as_uint(b); swap16(x, y); a = __uint_as_float(x); b = __uint_as_float(y); }
DI void swap32f(float& a, float& b) { unsigned x = __float_as_uint(a), y = __float_as_uint(b); swap32(x, y); a = __uint_as_float(x); b = __uint_as_float(y); }
DI float rows_max(float v) { float a = v, b = v; swap16f(a, b); v = fmaxf(a, b); a = v; b = v; swap32f(a, b); return fmaxf(a, b); }
DI float rows_sum(float v) { float a = v, b = v; swap16f(a, b); v = a + b; a = v; b = v; swap32f(a, b); return a + b; }
DI unsigned umax4(u32x4 v) { return max(max(v.x, v.y), max(v.z, v.w)); }
#define WAIT_V(n) asm volatile("s_waitcnt vmcnt(" #n ")" ::: "memory")
#define WAIT_L(n) asm volatile("s_waitcnt lgkmcnt(" #n ")" ::: "memory")
#define BAR() __builtin_amdgcn_s_barrier()
#define SCHED() __builtin_amdgcn_sched_barrier(0)
#define LDS_BAR() do { asm volatile("s_waitcnt lgkmcnt(0)" ::: "memory"); __builtin_amdgcn_s_barrier(); asm volatile("" ::: "memory"); } while (0)

constexpr int HTB = 128 * 64 * 2;
DI int lds_byte(int r, int c) { const int st = (r >> 4) * 2 + (c >> 5), rr = r & 15, cc = c & 31, ob = rr * 64 + cc * 2; return st * 1024 + (ob ^ (((ob >> 9) & 1) << 5)); }
DI void stage_rc(int b, int& R, int& C) { const int st = b / 1024, sb = b % 1024, swz = sb ^ (((sb >> 9) & 1) << 5); R = (st >> 1) * 16 + swz / 64; C = (st & 1) * 32 + (swz % 64) / 2; }

struct Params {
    const u16 *ax, *mem0, *wq, *wk, *wv, *wo, *bx, *bwqkv, *bwo;
    u16* out; char* ws; unsigned d0, d1, d2, d3;
};
#define RLX_AGENT __ATOMIC_RELAXED, __HIP_MEMORY_SCOPE_AGENT
DI bool spin_out(unsigned& spins, unsigned* ctl) {
    spins = __builtin_amdgcn_readfirstlane(spins) + 1u;
    if ((spins & 63u) == 0u) {
        if (spins > (1u << 18)) { __hip_atomic_store(ctl + C_TMO, 1u, RLX_AGENT); return true; }
        if (__builtin_amdgcn_readfirstlane(__hip_atomic_load(ctl + C_TMO, RLX_AGENT)) != SENT) return true;
    }
    return false;
}
DI void wait_count(unsigned* ctl, int word, unsigned need) {
    unsigned spins = 0;
    while (__builtin_amdgcn_readfirstlane(__hip_atomic_load(ctl + word, RLX_AGENT)) + 1u < need) { __builtin_amdgcn_s_sleep(8); if (spin_out(spins, ctl)) break; }
}
DI void wg_wait(unsigned* ctl, int word0, unsigned need0, int word1, unsigned need1) {
    if (threadIdx.x < 64) {
        wait_count(ctl, word0, need0); wait_count(ctl, word1, need1);
        __builtin_amdgcn_fence(__ATOMIC_ACQUIRE, "agent");
        asm volatile("s_waitcnt vmcnt(0)" ::: "memory");
    }
    asm volatile("" ::: "memory"); BAR(); asm volatile("" ::: "memory");
}
struct Unit { const char* A; const char* B; char* C; unsigned lda, ldb, ldc, cw, ch, ak, ah; int nt, sig, nsig; };
DI int fresh_tid() { int t = threadIdx.x; asm volatile("" : "+v"(t)); return t; }
DI void signal(unsigned* ctl, int sig, int nsig) { const int lane = fresh_tid() & 63; if (lane < nsig) __hip_atomic_fetch_add(ctl + sig + (lane & 1) * 16 + (lane >> 1) * 512, 1u, RLX_AGENT); }
DI void store_pair(rsrc_t cr, unsigned voff, unsigned soff, f32x4 v0, f32x4 v1, bool wt) {
    unsigned ax = pk_bf16(v0[0], v0[1]), ay = pk_bf16(v0[2], v0[3]), bx = pk_bf16(v1[0], v1[1]), by = pk_bf16(v1[2], v1[3]);
    swap16(ax, bx); swap16(ay, by);
    if (wt) __builtin_amdgcn_raw_buffer_store_b128((u32x4){ax, ay, bx, by}, cr, voff, soff, 16);
    else __builtin_amdgcn_raw_buffer_store_b128((u32x4){ax, ay, bx, by}, cr, voff, soff, 0);
}
#define STAGE(bufoff, gbase, voff, step2) do { \
        __builtin_amdgcn_global_load_lds((const unsigned*)((gbase) + (voff)), (LAS unsigned*)(lds + (bufoff) + ldsw), 16, 0, 0); \
        __builtin_amdgcn_global_load_lds((const unsigned*)((gbase) + (step2) + (voff)), (LAS unsigned*)(lds + (bufoff) + ldsw + 8192), 16, 0, 0); } while (0)
#define LDA(dst, off) do { _Pragma("unroll") for (int m = 0; m < 4; ++m) _Pragma("unroll") for (int k = 0; k < 2; ++k) dst[m][k] = *(const LAS bf16x8*)(lds + (off) + aoff + m * 2048 + k * 1024); } while (0)
#define TRD(dst, base, off) asm volatile("ds_read_b64_tr_b16 %0, %1 offset:%2" : "=v"(dst) : "v"(base), "i"(off) : "memory")
#define LDB(dst, v0, v1, off) do { _Pragma("unroll") for (int n = 0; n < 2; ++n) _Pragma("unroll") for (int k = 0; k < 2; ++k) { s16x4 lo_, hi_; \
        if (n == 0 && k == 0) { TRD(lo_, v0, (off)); TRD(hi_, v1, (off)); } else if (n == 0) { TRD(lo_, v0, (off) + 8192); TRD(hi_, v1, (off) + 8192); } \
        else if (k == 0) { TRD(lo_, v0, (off) + 256); TRD(hi_, v1, (off) + 256); } else { TRD(lo_, v0, (off) + 8448); TRD(hi_, v1, (off) + 8448); } \
        dst[n][k] = cat8(lo_, hi_); } } while (0)
#define MMA(accv, At, Bt) do { SCHED(); __builtin_amdgcn_s_setprio(1); _Pragma("unroll") for (int m = 0; m < 4; ++m) _Pragma("unroll") for (int n = 0; n < 2; ++n) _Pragma("unroll") for (int k = 0; k < 2; ++k) \
        accv[m][n] = mma32(Bt[n][k], At[m][k], accv[m][n]); __builtin_amdgcn_s_setprio(0); } while (0)
#define GEMM_LANE_SETUP() \
    const int tid = fresh_tid(); \
    const int wid = __builtin_amdgcn_readfirstlane(tid >> 6), lane = tid & 63, wr = wid >> 2, wc = wid & 3, fr = lane & 15, fq = lane >> 4; \
    int RA, CA; stage_rc(tid * 16, RA, CA); \
    const int kB = 8 * (wid >> 1) + (((lane & 15) >> 1) ^ (((wid >> 1) & 1) << 2)); \
    const int nB2 = (((wid & 1) * 4 + (lane >> 4)) * 16 + (lane & 1) * 8) * 2; \
    const unsigned ldsw = (unsigned)wid * 1024u; \
    const int aoff = lds_byte(wr * 64 + fr, fq * 8); \
    const int boff0 = fq * 2048 + wc * 512 + (((fr >> 2) ^ ((fq & 1) << 2)) * 32) + (fr & 3) * 8, boff1 = boff0 ^ 128;

template <class Sched>
DI void gemm_stream(ldsp lds, const Sched& S, unsigned* ctl) {
    GEMM_LANE_SETUP();
#define SA(b, h) (((b) * 2 + (h)) * HTB)
#define SB(b, h) ((4 + (b) * 2 + (h)) * HTB)
    const unsigned vb0 = (unsigned)(size_t)lds + 65536u + (unsigned)boff0, vb1 = (unsigned)(size_t)lds + 65536u + (unsigned)boff1;
    Unit cur, nxt; int ui = 0;
    if (!S.next(0, cur)) return;
    f32x4 acc[2][2][4][2];
#pragma unroll
    for (int a = 0; a < 2; ++a)
#pragma unroll
        for (int b = 0; b < 2; ++b)
#pragma unroll
            for (int m = 0; m < 4; ++m)
#pragma unroll
                for (int n = 0; n < 2; ++n) acc[a][b][m][n] = (f32x4){0.f, 0.f, 0.f, 0.f};
    bf16x8 At[4][2], B0[2][2], B1[2][2];
    unsigned vA = (unsigned)RA * cur.lda + (unsigned)CA * 2u, vB = (unsigned)kB * cur.ldb + (unsigned)nB2;
    {
        const size_t a2s = (size_t)64 * cur.lda, ah = cur.ah, b2s = (size_t)32 * cur.ldb, bk = (size_t)64 * cur.ldb;
        STAGE(SB(0, 0), cur.B, vB, b2s); STAGE(SB(0, 1), cur.B + 256, vB, b2s); STAGE(SA(0, 0), cur.A, vA, a2s); STAGE(SA(0, 1), cur.A + ah, vA, a2s);
        if (wr == 1) BAR();
        WAIT_V(2); BAR();
        STAGE(SB(1, 0), cur.B + bk, vB, b2s); STAGE(SA(1, 0), cur.A + cur.ak, vA, a2s); STAGE(SB(1, 1), cur.B + bk + 256, vB, b2s);
        WAIT_V(6); BAR();
    }
    int psig = 0, pn = 0;
    for (;;) {
        const bool has_next = S.next(ui + 1, nxt);
        if (!has_next) nxt = cur;
        int nt = cur.nt; asm volatile("" : "+s"(nt));
        for (int t = 0; t < nt; t += 2) {
            const bool last = (t == nt - 2);
            const char* a1 = cur.A + (size_t)(t + 1) * cur.ak + cur.ah;
            const size_t a1s = (size_t)64 * cur.lda; const unsigned vA1 = vA;
            if (last) { const int tz = fresh_tid(); int R2, C2; stage_rc(tz * 16, R2, C2);
                vA = (unsigned)R2 * nxt.lda + (unsigned)C2 * 2u;
                vB = (unsigned)(8 * (wid >> 1) + (((tz & 15) >> 1) ^ (((wid >> 1) & 1) << 2))) * nxt.ldb + (unsigned)((((wid & 1) * 4 + ((tz >> 4) & 3)) * 16 + (tz & 1) * 8) * 2); }
            const unsigned lda = last ? nxt.lda : cur.lda, ldb = last ? nxt.ldb : cur.ldb;
            const char* a2 = last ? nxt.A : cur.A + (size_t)(t + 2) * cur.ak;
            const char* b2 = last ? nxt.B : cur.B + (size_t)(t + 2) * 64 * cur.ldb;
            const size_t a2s = (size_t)64 * lda, ah = last ? nxt.ah : cur.ah, b2s = (size_t)32 * ldb;
            const char* a3 = a2 + (last ? nxt.ak : cur.ak); const char* b3 = b2 + (size_t)64 * ldb;
            LDB(B0, vb0, vb1, 0); LDB(B1, vb0, vb1, HTB); SCHED(); LDA(At, SA(0, 0)); STAGE(SA(1, 1), a1, vA1, a1s);
            WAIT_V(8); WAIT_L(0); BAR(); MMA(acc[0][0], At, B0); MMA(acc[0][1], At, B1); BAR(); SCHED();
            LDA(At, SA(0, 1)); STAGE(SB(0, 0), b2, vB, b2s); STAGE(SB(0, 1), b2 + 256, vB, b2s); STAGE(SA(0, 0), a2, vA, a2s);
            WAIT_V(8);
            asm volatile("" : "+s"(pn));
            if (pn) { signal(ctl, psig, pn); pn = 0; }
            WAIT_L(0); BAR(); MMA(acc[1][0], At, B0); MMA(acc[1][1], At, B1); BAR(); SCHED();
            LDB(B0, vb0, vb1, 2 * HTB); LDB(B1, vb0, vb1, 3 * HTB); SCHED(); LDA(At, SA(1, 0)); STAGE(SA(0, 1), a2 + ah, vA, a2s);
            WAIT_V(8); WAIT_L(0); BAR(); MMA(acc[0][0], At, B0); MMA(acc[0][1], At, B1); BAR(); SCHED();
            LDA(At, SA(1, 1)); STAGE(SB(1, 0), b3, vB, b2s); STAGE(SB(1, 1), b3 + 256, vB, b2s); STAGE(SA(1, 0), a3, vA, a2s);
            WAIT_V(8); WAIT_L(0); BAR(); MMA(acc[1][0], At, B0); MMA(acc[1][1], At, B1); BAR(); SCHED();
        }
        if (wr == 0) BAR();
        {
            const rsrc_t cr = mkrsrc(cur.C, 0x1000000u);
            const int tz = fresh_tid();
            const unsigned co = (unsigned)(wr * 64 + (tz & 15)) * cur.ldc + (unsigned)wc * cur.cw + (unsigned)(((tz >> 4) & 1) * 32 + ((tz >> 5) & 1) * 16);
#pragma unroll
            for (int ai = 0; ai < 2; ++ai)
#pragma unroll
                for (int m = 0; m < 4; ++m)
#pragma unroll
                    for (int bj = 0; bj < 2; ++bj) store_pair(cr, co, (unsigned)(ai * 128 + m * 16) * cur.ldc + (unsigned)bj * cur.ch, acc[ai][bj][m][0], acc[ai][bj][m][1], true);
        }
        psig = cur.sig; pn = cur.nsig;
        if (!has_next) break;
#pragma unroll
        for (int a = 0; a < 2; ++a)
#pragma unroll
            for (int b = 0; b < 2; ++b)
#pragma unroll
                for (int m = 0; m < 4; ++m)
#pragma unroll
                    for (int n = 0; n < 2; ++n) acc[a][b][m][n] = (f32x4){0.f, 0.f, 0.f, 0.f};
        cur = nxt; ++ui;
        if (wr == 1) BAR();
    }
    WAIT_V(0);
    signal(ctl, psig, pn);
    BAR();
#undef SA
#undef SB
}

DI void gemm_small(ldsp lds, const Unit u, unsigned* ctl) {
    GEMM_LANE_SETUP();
    const unsigned vA = (unsigned)RA * u.lda + (unsigned)CA * 2u, vB = (unsigned)kB * u.ldb + (unsigned)nB2;
    const size_t a2s = (size_t)64 * u.lda, b2s = (size_t)32 * u.ldb, bk = (size_t)64 * u.ldb;
    f32x4 acc[4][2];
#pragma unroll
    for (int m = 0; m < 4; ++m)
#pragma unroll
        for (int n = 0; n < 2; ++n) acc[m][n] = (f32x4){0.f, 0.f, 0.f, 0.f};
    int nt = u.nt; asm volatile("" : "+s"(nt));
    const unsigned vb0 = (unsigned)(size_t)lds + (unsigned)boff0, vb1 = (unsigned)(size_t)lds + (unsigned)boff1, vb2 = vb0 + 65536u, vb3 = vb1 + 65536u;
#pragma unroll
    for (int t = 0; t < 3; ++t) { STAGE(t * 32768, u.A + (size_t)t * u.ak, vA, a2s); STAGE(t * 32768 + 16384, u.B + t * bk, vB, b2s); }
    for (int t0 = 0; t0 < nt; t0 += 4) {
#pragma unroll
        for (int i = 0; i < 4; ++i) {
            const int tn = min(t0 + i + 3, nt - 1);
            WAIT_V(8); BAR();
            bf16x8 At[4][2], Bt[2][2];
            if (i == 0) LDB(Bt, vb0, vb1, 16384); else if (i == 1) LDB(Bt, vb0, vb1, 49152); else if (i == 2) LDB(Bt, vb2, vb3, 16384); else LDB(Bt, vb2, vb3, 49152);
            LDA(At, i * 32768);
            STAGE(((i + 3) & 3) * 32768, u.A + (size_t)tn * u.ak, vA, a2s); STAGE(((i + 3) & 3) * 32768 + 16384, u.B + (size_t)tn * bk, vB, b2s);
            WAIT_L(0); SCHED();
            MMA(acc, At, Bt);
            SCHED();
        }
    }
    const rsrc_t cr = mkrsrc(u.C, 128u * u.ldc);
    const unsigned co = (unsigned)(wr * 64 + fr) * u.ldc + (unsigned)(wc * 32 + (fq & 1) * 16 + (fq >> 1) * 8) * 2u;
#pragma unroll
    for (int m = 0; m < 4; ++m) store_pair(cr, co, (unsigned)(m * 16) * u.ldc, acc[m][0], acc[m][1], true);
    WAIT_V(0);
    signal(ctl, u.sig, u.nsig);
    BAR();
}

#define win_off(idx) ((unsigned)(idx) * 16384u + ((((unsigned)(idx) >> 7) ^ par) & 1u) * 8192u)
#define W_MEM(s) ((s) == 0 ? 0 : 6 + 2 * (s))
#define W_PART(slot) (192 + (slot) * 64)
#define W_X(slot) (384 + (slot) * 128)
DI void dly(unsigned n) { for (unsigned i = 0; i < n; ++i) __builtin_amdgcn_s_sleep(1); }
DI bool all_set(unsigned mx) { return __builtin_amdgcn_ballot_w64(mx == SENT) == 0ull; }
DI u32x4 rows_gather(unsigned v) { unsigned a = v, b = v; swap16(a, b); unsigned c = a, d = b; swap32(a, c); swap32(b, d); return (u32x4){a, b, c, d}; }
DI float max3f(float a, float b, float c) { return fmaxf(fmaxf(a, b), c); }
constexpr int RED_M = 144;
constexpr int L_RED = 0, L_PX = 32768, L_PM = L_PX + 1024, L_ML = L_PM + 512, L_FLAG = L_ML + 256, L_VM = 34816, L_OWN = L_VM + 2048, L_X = L_OWN + 1536  ;

#define RETRY(i)
#define PH0()
#define PH1(e)
#define SEG(i)

template <int ROLE>
DI void chain_loop(const Params& p, const unsigned par, const int rank, const int w, ldsp lds, const bf16x16 (&W)[6][2], const bf16x16 (&WO)[2][2], unsigned long long* stamps) {
    const int lane = fresh_tid() & 63, g = lane >> 4, n = lane & 15;
    const int h = rank >> 2, j = rank & 3;
    unsigned* ctl = (unsigned*)(p.ws + WS_CTL);
    const rsrc_t xr = mkrsrc(p.ws + WS_XCH, (unsigned)XCH_BYTES);
    const rsrc_t qr = mkrsrc(p.ws + WS_QKVX + (size_t)(32 + h) * XH, 33u * XH);
    const int sidx = (n >> 3) ? 0xEEEE : 0x4444;
    const unsigned l16 = (unsigned)lane * 16u;
    const int redw = (w * 1024 + (n & 1) * 512 + ((g >> 1) * 8 + (n >> 1)) * 8 + 4 * (g & 1)) * 4;
    const int redr = (w < 4 ? (n >> 3) * RED_M + (4 * w + g) * 8 + (n & 7) : 2 * RED_M + (8 * (w & 1) + 2 * g + (n >> 3)) * 8 + (n & 7)) * 4;
    const bool vown = (g >> 1) == (j & 1);
    const unsigned vg1 = (unsigned)(((j & 2) ^ 2) + (g >> 1)) * 1536u + 1024u + (unsigned)(lane & 31) * 16u;
    const unsigned vg0 = vown ? 0x80000000u : (vg1 + (unsigned)(j & 2) * 3072u - 3072u);
    const unsigned pub_off = (g == j ? XCH_KM + (unsigned)rank * 1536u : (unsigned)(4 * h + g) * 16384u + (unsigned)j * 1536u) + (unsigned)w * 256u + (unsigned)n * 16u;
    const int red = ((j + 1) & 3) * 32 + h;
    const int rho = 4 * (8 * j + w) + g;
    const unsigned pst_off = (unsigned)(rho >> 1) * 16384u + (unsigned)(rho & 1) * 4096u + (unsigned)((h ^ rho) & 31) * 128u + (unsigned)(n & 7) * 16u;
    const int vxr = L_X + n * 256;
    const float csc = 0.08838834764831845f * 1.4426950408889634f;
    float ninf = -__builtin_inff(); asm volatile("" : "+v"(ninf));
    u32x4 KX[8], VWX[2];
    const unsigned x_src = (unsigned)(8 * (w & 3) + g) * 256u;
    auto dma_x = [&](int s) {
        if (ROLE == 0 && w < 4) {
            if ((s & 3) == 0) { wait_count(ctl, C_KV + (s >> 2) * 512 + h * 16, 16u); asm volatile("" ::: "memory"); }
#pragma unroll
            for (int i = 0; i < 2; ++i) {
                const unsigned vo = x_src + (unsigned)((n ^ ((8 * w + 4 * i + g) & 15)) * 16), so = (unsigned)(32 * s + 4 * i) * 256u;
                __builtin_amdgcn_raw_ptr_buffer_load_lds(qr, (LAS void*)(lds + L_X + (s & 1) * 16384 + (w * 2 + i) * 1024), 16, vo, so + 32u * XH, 0, 16);
                __builtin_amdgcn_raw_ptr_buffer_load_lds(qr, (LAS void*)(lds + L_X + (s & 1) * 16384 + 8192 + (w * 2 + i) * 1024), 16, vo, so, 0, 16);
            }
        }
    };
    auto load_kx = [&](int s) {
#pragma unroll
        for (int kt = 0; kt < 2; ++kt)
#pragma unroll
            for (int kk = 0; kk < 4; ++kk) KX[kt * 4 + kk] = *(const LAS u32x4*)(lds + vxr + (s & 1) * 16384 + 8192 + kt * 4096 + (((4 * ((j + kk) & 3) + g) ^ n) * 16));
    };
    auto vwx_kt = [&](int s, int kt) {
        bf16x8 VA[4];
#pragma unroll
        for (int kk = 0; kk < 4; ++kk) VA[kk] = *(const LAS bf16x8*)(lds + vxr + (s & 1) * 16384 + kt * 4096 + (((4 * (kk ^ (j & 2)) + g) ^ n) * 16));
#pragma unroll
        for (int ct = 0; ct < 2; ++ct) {
            f32x4 d = {0.f, 0.f, 0.f, 0.f};
#pragma unroll
            for (int kk = 0; kk < 4; ++kk) d = mma32(VA[kk], (kk & 1) ? hi8(WO[ct][kk >> 1]) : lo8(WO[ct][kk >> 1]), d);
            VWX[ct][2 * kt] = pk_bf16(d[0], d[1]); VWX[ct][2 * kt + 1] = pk_bf16(d[2], d[3]);
        }
    };
    auto rmax = [&](float v) { float a = v, b = v; swap16f(a, b); v = max3f(a, b, ninf); a = v; b = v; swap32f(a, b); return max3f(a, b, ninf); };
    dma_x(0); dma_x(1);
    if (ROLE == 1 && lane == 0) *(volatile LAS unsigned*)(lds + L_FLAG) = 0u;
    asm volatile("s_waitcnt vmcnt(0) lgkmcnt(0)" ::: "memory"); BAR(); asm volatile("" ::: "memory");
    vwx_kt(0, 0); vwx_kt(0, 1);
    if (ROLE == 2) load_kx(0);
    if (ROLE >= 1) __builtin_amdgcn_s_setprio(2);

    unsigned spins = 0;
    for (int s = 0; s < NSEG; ++s) {
        f32x4 c[6];
#pragma unroll
        for (int i = 0; i < 6; ++i) { c[i] = (f32x4){0.f, 0.f, 0.f, 0.f}; asm volatile("" : "+v"(c[i])); }
        const unsigned xw = win_off(W_X(s & 1));
        unsigned pub = pub_off + (g == j ? (unsigned)s * (128u * 1536u) : xw); asm volatile("" : "+v"(pub));
        u32x4 a0, a1;
        {
            PH0();
            const unsigned so = win_off(W_MEM(s) + (w >> 2)) + (unsigned)(w & 3) * 2048u;
            for (;;) {
                asm volatile("" ::: "memory");
                a0 = ld16_sc1(xr, l16, so); a1 = ld16_sc1(xr, l16 + 1024, so);
                if (__builtin_expect(all_set(max(umax4(a0), umax4(a1))), 1)) break;
                RETRY(0);
                if (spin_out(spins, ctl)) break;
            }
            PH1(0);
        }
        SEG(0);
        if (s > 0) {
            const u32x4 sv = {SENT, SENT, SENT, SENT};
            if (ROLE <= 1) __builtin_amdgcn_raw_buffer_store_b128(sv, xr, l16, win_off(W_X((s - 1) & 1)) + (unsigned)rank * 16384u + (unsigned)w * 1024u, 16);
            else {
                const unsigned so = win_off(W_PART((s - 1) & 1)) + (unsigned)(red >> 1) * 16384u + (unsigned)(red & 1) * 4096u + (unsigned)(w & 1) * 2048u;
                __builtin_amdgcn_raw_buffer_store_b128(sv, xr, l16, so, 16); __builtin_amdgcn_raw_buffer_store_b128(sv, xr, l16 + 1024, so, 16);
            }
        }
        auto fold = [&](int mt) {
            f32x4 r;
#pragma unroll
            for (int i = 0; i < 4; ++i) { float x = c[2 * mt][i], y = c[2 * mt + 1][i]; swap32f(x, y); r[i] = x + y; }
            *(LAS f32x4*)(lds + redw + mt * (RED_M * 4)) = r;
        };
        c[0] = smma(a0, W[0][0], c[0], sidx); c[1] = smma(a0, W[1][0], c[1], sidx); c[0] = smma(a1, W[0][1], c[0], sidx); c[1] = smma(a1, W[1][1], c[1], sidx);
        c[2] = smma(a0, W[2][0], c[2], sidx); c[3] = smma(a0, W[3][0], c[3], sidx);
        SCHED();
        c[2] = smma(a1, W[2][1], c[2], sidx); c[3] = smma(a1, W[3][1], c[3], sidx); c[4] = smma(a0, W[4][0], c[4], sidx); c[5] = smma(a0, W[5][0], c[5], sidx);
        fold(0);
        SCHED();
        c[4] = smma(a1, W[4][1], c[4], sidx); c[5] = smma(a1, W[5][1], c[5], sidx);
        fold(1);
        SCHED();
        fold(2);
        SEG(1);
        LDS_BAR();
        SEG(2);
        if (ROLE <= 1) {
            f32x2 v[8];
#pragma unroll
            for (int ww = 0; ww < 8; ++ww) { const LAS float* q = (const LAS float*)(lds + redr + ww * 4096); v[ww] = (f32x2){q[0], q[512]}; }
            const f32x2 t = ((v[0] + v[1]) + (v[2] + v[3])) + ((v[4] + v[5]) + (v[6] + v[7]));
            const u32x4 o = rows_gather(min(pk_bf16(t.x, t.y), SENT - 1));
            __builtin_amdgcn_raw_buffer_store_b128(o, xr, pub, 0, 16);
            if (lane < 16) *(LAS u32x4*)(lds + L_OWN + w * 256 + lane * 16) = o;
        } else if (ROLE == 3) {
            if (rank == 0 && lane == 0) __hip_atomic_store(ctl + C_PROG, (unsigned)s, RLX_AGENT);
        }
        SEG(3);
        if (s == NSEG - 1) break;
        f32x4 dv[2];
#pragma unroll
        for (int i = 0; i < 2; ++i) { dv[i] = (f32x4){0.f, 0.f, 0.f, 0.f}; asm volatile("" : "+v"(dv[i])); }
        LDS_BAR();
        if (ROLE >= 1) dly(p.d1);
        SEG(11);
        u32x4 X[4], v0, v1;
        if (ROLE >= 1) {
            PH0();
            const unsigned cw = xw + (unsigned)rank * 16384u;
            const u32x4 vo = *(const LAS u32x4*)(lds + L_OWN + 1024 + (lane & 31) * 16);
            if (ROLE >= 2) X[0] = *(const LAS u32x4*)(lds + L_OWN + (ROLE == 2 ? (l16 & ~128u) : l16));
            for (;;) {
                asm volatile("" ::: "memory");
                unsigned mx = 0;
                if (ROLE >= 2) {
#pragma unroll
                    for (int k = 1; k < 4; ++k) X[k] = ld16_sc1(xr, ROLE == 2 ? (l16 & ~128u) : l16, cw + (unsigned)((j + k) & 3) * 1536u);
                }
                v0 = ld16_sc1(xr, vg0, cw); v1 = ld16_sc1(xr, vg1, cw);
                if (ROLE >= 2) mx = max(umax4(X[1]), max(umax4(X[2]), umax4(X[3])));
                if (__builtin_expect(all_set(max(mx, max(umax4(v0), umax4(v1)))), 1)) break;
                RETRY(1);
                if (spin_out(spins, ctl)) break;
            }
            PH1(1);
            if (vown) v0 = vo;
        }
        SEG(4);
        if (ROLE == 1) {
            *(LAS u32x4*)(lds + L_VM + lane * 16) = v0; *(LAS u32x4*)(lds + L_VM + 1024 + lane * 16) = v1;
            asm volatile("" ::: "memory");
            *(volatile LAS unsigned*)(lds + L_FLAG) = (unsigned)s + 1u;
        } else if (ROLE == 0) {
            while (__builtin_amdgcn_readfirstlane(*(volatile LAS unsigned*)(lds + L_FLAG)) != (unsigned)s + 1u) __builtin_amdgcn_s_sleep(1);
            asm volatile("" ::: "memory");
            v0 = *(const LAS u32x4*)(lds + L_VM + lane * 16); v1 = *(const LAS u32x4*)(lds + L_VM + 1024 + lane * 16);
        }
        if (ROLE == 2) {
            f32x4 s0 = {0.f, 0.f, 0.f, 0.f}, s1 = s0;
#pragma unroll
            for (int k = 0; k < 4; ++k) { s0 = mma32(asbf(KX[k]), asbf(X[k]), s0); s1 = mma32(asbf(KX[4 + k]), asbf(X[k]), s1); }
            dv[0] = smma(v0, WO[0][0], dv[0], sidx);
            SCHED();
            const float m = rmax(max3f(max3f(s0[0], s0[1], s0[2]), max3f(s0[3], s1[0], s1[1]), max3f(s1[2], s1[3], ninf)));
            dv[1] = smma(v0, WO[1][0], dv[1], sidx);
            dv[0] = smma(v1, WO[0][1], dv[0], sidx);
            SCHED();
            const float mc = -m * csc;
#pragma unroll
            for (int i = 0; i < 4; ++i) { s0[i] = __builtin_amdgcn_exp2f(fmaf(s0[i], csc, mc)); s1[i] = __builtin_amdgcn_exp2f(fmaf(s1[i], csc, mc)); }
            dv[1] = smma(v1, WO[1][1], dv[1], sidx);
            *(LAS u32x4*)(lds + L_PX + lane * 16) = (u32x4){pk_bf16(s0[0], s0[1]), pk_bf16(s0[2], s0[3]), pk_bf16(s1[0], s1[1]), pk_bf16(s1[2], s1[3])};
            const f32x4 t = s0 + s1;
            const float l = rows_sum((t[0] + t[1]) + (t[2] + t[3]));
            *(LAS f32x2*)(lds + L_ML + n * 16) = (f32x2){mc, l};
        } else if (ROLE == 3) {
            f32x4 sm = {0.f, 0.f, 0.f, 0.f};
#pragma unroll
            for (int k = 0; k < 4; ++k) sm = mma32(asbf(X[k]), asbf(X[k]), sm);
#pragma unroll
            for (int ct = 0; ct < 2; ++ct) { dv[ct] = smma(v0, WO[ct][0], dv[ct], sidx); dv[ct] = smma(v1, WO[ct][1], dv[ct], sidx); }
            if (g < 2) sm = (f32x4){ninf, ninf, ninf, ninf};
            const float m = rmax(max3f(max3f(sm[0], sm[1], sm[2]), sm[3], ninf));
            const float mc = -m * csc;
#pragma unroll
            for (int i = 0; i < 4; ++i) sm[i] = __builtin_amdgcn_exp2f(fmaf(sm[i], csc, mc));
            *(LAS u32x2*)(lds + L_PM + lane * 8) = (u32x2){pk_bf16(sm[0], sm[1]), pk_bf16(sm[2], sm[3])};
            const float l = rows_sum((sm[0] + sm[1]) + (sm[2] + sm[3]));
            *(LAS f32x2*)(lds + L_ML + n * 16 + 8) = (f32x2){mc, l};
        } else {
#pragma unroll
            for (int ct = 0; ct < 2; ++ct) { dv[ct] = smma(v0, WO[ct][0], dv[ct], sidx); dv[ct] = smma(v1, WO[ct][1], dv[ct], sidx); }
        }
        u32x4 VWM[2];
#pragma unroll
        for (int ct = 0; ct < 2; ++ct) { VWM[ct] = (u32x4){pk_bf16(dv[ct][0], dv[ct][1]), pk_bf16(dv[ct][2], dv[ct][3]), 0u, 0u}; asm volatile("" : "+v"(VWM[ct])); }
        unsigned zz0 = 0u, zz1 = 0u; asm volatile("" : "+v"(zz0), "+v"(zz1));
        const unsigned pso = win_off(W_PART(s & 1));
        if (ROLE == 0) WAIT_V(0);
        SEG(5);
        LDS_BAR();
        SEG(6);
        {
            const f32x4 ml = *(const LAS f32x4*)(lds + L_ML + n * 16);
            const u32x4 px = *(const LAS u32x4*)(lds + L_PX + lane * 16);
            const u32x2 pm2 = *(const LAS u32x2*)(lds + L_PM + ((2 + (g & 1)) * 16 + n) * 8);
            const u32x4 pm = {pm2.x, pm2.y, zz0, zz1};
            f32x4 dx[2], dm[2];
#pragma unroll
            for (int ct = 0; ct < 2; ++ct) dx[ct] = mma32(asbf(VWX[ct]), asbf(px), (f32x4){0.f, 0.f, 0.f, 0.f});
#pragma unroll
            for (int ct = 0; ct < 2; ++ct) dm[ct] = mma32(asbf(VWM[ct]), asbf(pm), (f32x4){0.f, 0.f, 0.f, 0.f});
            const float dl = ml[0] - ml[2];
            const float e = __builtin_amdgcn_exp2f(-__builtin_fabsf(dl));
            const float ex = dl > 0.f ? e : 1.f, em = dl > 0.f ? 1.f : e;
            const float inv = __builtin_amdgcn_rcpf(fmaf(ex, ml[1], em * ml[3]));
            float ax = ex * inv, am = em * inv;
            asm volatile("" : "+v"(ax), "+v"(am));
            const f32x4 d0 = dx[0] * ax + dm[0] * am, d1 = dx[1] * ax + dm[1] * am;
            u32x4 o = {min(pk_bf16(d0[0], d0[1]), SENT - 1), min(pk_bf16(d0[2], d0[3]), SENT - 1), min(pk_bf16(d1[0], d1[1]), SENT - 1), min(pk_bf16(d1[2], d1[3]), SENT - 1)};
            asm volatile("" : "+v"(o));
            if (n < 8) __builtin_amdgcn_raw_buffer_store_b128(o, xr, pst_off, pso, 16);
        }
        SEG(7);
        SCHED();
        if (s + 2 < NSEG - 1) dma_x(s + 2);
        vwx_kt(s + 1, 0);
        SCHED();
        SEG(8);
        if (ROLE == 3) {
            u32x4 SEL;
            { const unsigned one = (n & 1) ? 0x3f800000u : 0x00003f80u; const int dw = (n & 7) >> 1; SEL = (u32x4){dw == 0 ? one : 0u, dw == 1 ? one : 0u, dw == 2 ? one : 0u, dw == 3 ? one : 0u}; }
            vwx_kt(s + 1, 1);
            SCHED();
            dly(p.d2);
            SEG(12);
            const unsigned so = pso + (unsigned)(red >> 1) * 16384u + (unsigned)(red & 1) * 4096u;
            const unsigned mo = win_off(W_MEM(s + 1) + (red >> 6)) + (unsigned)(red & 63) * 128u;
            u32x4 q[4];
            PH0();
            for (;;) {
                asm volatile("" ::: "memory");
#pragma unroll
                for (int i = 0; i < 4; ++i) q[i] = ld16_sc1(xr, l16 + i * 1024, so);
                if (__builtin_expect(all_set(max(max(umax4(q[0]), umax4(q[1])), max(umax4(q[2]), umax4(q[3])))), 1)) break;
                RETRY(2);
                if (spin_out(spins, ctl)) break;
            }
            PH1(2);
            f32x4 t = {0.f, 0.f, 0.f, 0.f};
#pragma unroll
            for (int i = 0; i < 4; ++i) t = mma32(asbf(SEL), asbf(q[i]), t);
#pragma unroll
            for (int i = 0; i < 4; ++i) t[i] += __uint_as_float(__builtin_amdgcn_update_dpp(0, __float_as_uint(t[i]), 0x128, 0xf, 0xf, false));
            unsigned a0 = min(pk_bf16(t[0], t[1]), SENT - 1), a1 = min(pk_bf16(t[2], t[3]), SENT - 1);
            unsigned b0 = a0, b1 = a1; swap16(a0, b0); swap16(a1, b1);
            if (lane < 8) __builtin_amdgcn_raw_buffer_store_b128((u32x4){a0, a1, b0, b1}, xr, l16, mo, 16);
        } else {
            vwx_kt(s + 1, 1);
            if (ROLE == 2) load_kx(s + 1);
        }
        SEG(9);
        LDS_BAR();
        dly(p.d0);
        SEG(10);
    }
    WAIT_V(0);
    __builtin_amdgcn_s_setprio(0);
}

DI void chain_role(const Params& p, int rank, ldsp lds, unsigned long long* stamps) {
    const int tid = fresh_tid(), w = __builtin_amdgcn_readfirstlane(tid >> 6), lane = tid & 63, g = lane >> 4, n = lane & 15;
    const int h = rank >> 2, j = rank & 3;
    const rsrc_t xr = mkrsrc(p.ws + WS_XCH, (unsigned)XCH_BYTES);
    unsigned* ctl = (unsigned*)(p.ws + WS_CTL);
    if (rank == 0 && w == 7) {
        unsigned best0 = ~0u, best1 = ~0u;
        for (int i = 0; i < 12; ++i) {
            const unsigned long long t0 = __builtin_amdgcn_s_memtime();
            __builtin_amdgcn_raw_buffer_store_b32(0u, xr, (unsigned)(2 * 16384 + C_PROBE * 4) + (unsigned)(i & 1) * 8192u + (unsigned)(i >> 1) * 256u, 0, 16);
            asm volatile("s_waitcnt vmcnt(0)" ::: "memory");
            const unsigned dt = (unsigned)(__builtin_amdgcn_s_memtime() - t0);
            if (i & 1) best1 = min(best1, dt); else best0 = min(best0, dt);
        }
        if (lane == 0) __hip_atomic_store(ctl + C_PAR, best1 < best0 ? 1u : 0u, RLX_AGENT);
    }
    bf16x16 W[6][2], WO[2][2];
    {
        ldsp my = lds + w * 16384;
        const int rrow = lane >> 2, rc = (lane & 3) * 8, q4 = n >> 2;
        const int toffo = (8 * g + q4) * 64 + (n & 3) * 8;
        const int toffq = (16 * (g & 1) + 8 * (g >> 1) + 4 * (q4 >> 1) + (q4 & 1)) * 64 + (n & 3) * 8;
#define FRAG(base, st) cat16(cat8(trd((base)), trd((base) + (st))), cat8(trd((base) + 2048), trd((base) + 2048 + (st))))
#pragma unroll
        for (int i = 0; i < 8; ++i) {
            __builtin_amdgcn_global_load_lds((const unsigned*)(p.wq + (size_t)(128 * w + 16 * i + rrow) * 4096 + h * 128 + 32 * j + rc), (LAS unsigned*)(my + i * 1024), 16, 0, 0);
            __builtin_amdgcn_global_load_lds((const unsigned*)(p.wk + (size_t)(128 * w + 16 * i + rrow) * 4096 + h * 128 + 32 * j + rc), (LAS unsigned*)(my + 8192 + i * 1024), 16, 0, 0);
        }
        WAIT_V(0);
#pragma unroll
        for (int T = 0; T < 4; ++T)
#pragma unroll
            for (int t = 0; t < 2; ++t) W[T][t] = FRAG(my + (T >> 1) * 8192 + t * 4096 + (T & 1) * 32 + toffq, 128);
#pragma unroll
        for (int T = 0; T < 4; ++T) asm volatile("" : "+v"(W[T][0]), "+v"(W[T][1]));
        WAIT_L(0);
#pragma unroll
        for (int i = 0; i < 8; ++i) {
            __builtin_amdgcn_global_load_lds((const unsigned*)(p.wv + (size_t)(128 * w + 16 * i + rrow) * 4096 + h * 128 + 32 * j + rc), (LAS unsigned*)(my + i * 1024), 16, 0, 0);
            __builtin_amdgcn_global_load_lds((const unsigned*)(p.wo + (size_t)(h * 128 + 16 * i + rrow) * 1024 + 256 * j + 32 * w + rc), (LAS unsigned*)(my + 8192 + i * 1024), 16, 0, 0);
        }
        WAIT_V(0);
#pragma unroll
        for (int T = 0; T < 2; ++T)
#pragma unroll
            for (int t = 0; t < 2; ++t) { W[4 + T][t] = FRAG(my + t * 4096 + T * 32 + toffq, 128); WO[T][t] = FRAG(my + 8192 + (t ^ (j >> 1)) * 4096 + T * 32 + toffo, 256); }
#pragma unroll
        for (int T = 0; T < 2; ++T) asm volatile("" : "+v"(W[4 + T][0]), "+v"(W[4 + T][1]), "+v"(WO[T][0]), "+v"(WO[T][1]));
        WAIT_L(0);
#undef FRAG
    }
    wait_count(ctl, C_PAR, 1u);
    const unsigned par = __builtin_amdgcn_readfirstlane(__hip_atomic_load(ctl + C_PAR, RLX_AGENT)) & 1u;
    const int red = ((j + 1) & 3) * 32 + h;
    {
        const u32x4 sv = {SENT, SENT, SENT, SENT};
        if (w < 6) {
            __builtin_amdgcn_raw_buffer_store_b128(sv, xr, (unsigned)lane * 16u, win_off(W_X(0)) + (unsigned)rank * 16384u + (unsigned)w * 1024u, 16);
            __builtin_amdgcn_raw_buffer_store_b128(sv, xr, (unsigned)lane * 16u, win_off(W_X(1)) + (unsigned)rank * 16384u + (unsigned)w * 1024u, 16);
        }
        __builtin_amdgcn_raw_buffer_store_b128(sv, xr, (unsigned)lane * 16u, win_off(W_PART(w >> 2)) + (unsigned)(red >> 1) * 16384u + (unsigned)(red & 1) * 4096u + (unsigned)(w & 3) * 1024u, 16);
        const int s = 8 * w + (lane >> 3);
        if (s > 0) __builtin_amdgcn_raw_buffer_store_b128(sv, xr, win_off(W_MEM(s) + (red >> 6)) + (unsigned)(red & 63) * 128u + (unsigned)(lane & 7) * 16u, 0, 16);
    }
    WAIT_V(0);
    BAR();
    if (w == 7 && lane < 8) {
        const unsigned* src = (const unsigned*)(p.mem0 + lane * DM + 32 * (red >> 2) + 4 * (red & 3));
        const u32x4 v = {min(src[0], SENT - 1), min(src[1], SENT - 1), min(src[8], SENT - 1), min(src[9], SENT - 1)};
        __builtin_amdgcn_raw_buffer_store_b128(v, xr, (unsigned)lane * 16u + (unsigned)(red & 63) * 128u, win_off(W_MEM(0) + (red >> 6)), 16);
    }
    if (w < 5) chain_loop<0>(p, par, rank, w, lds, W, WO, stamps);
    else if (w == 5) chain_loop<1>(p, par, rank, 5, lds, W, WO, stamps);
    else if (w == 6) chain_loop<2>(p, par, rank, 6, lds, W, WO, stamps);
    else chain_loop<3>(p, par, rank, 7, lds, W, WO, stamps);
}

template <class Wait>
DI void attn_a_item(const Params& p, int s, int h, ldsp my, int lane, const Wait& wait_mem) {
    const int g = lane >> 4, n = lane & 15;
    const rsrc_t xr = mkrsrc(p.ws + WS_XCH, (unsigned)XCH_BYTES);
    const rsrc_t qr = mkrsrc(p.ws + WS_QKVX + (size_t)h * XH + (size_t)s * 8192, 65u * XH);
#pragma unroll
    for (int i = 0; i < 8; ++i)
        __builtin_amdgcn_raw_ptr_buffer_load_lds(qr, (LAS void*)(my + i * 1024), 16, (unsigned)g * 256u + (unsigned)((n ^ (((4 * (i & 1) + g) & 7) << 1)) * 16), 64u * XH + (unsigned)i * 1024u, 0, 16);
    u32x4 Q[2][4], K[2][4], XM[4];
    const unsigned ro = (unsigned)n * 256u + (unsigned)g * 16u;
#pragma unroll
    for (int t = 0; t < 2; ++t)
#pragma unroll
        for (int k = 0; k < 4; ++k) { Q[t][k] = ld16_sc1(qr, ro + k * 64, (unsigned)t * 4096u); K[t][k] = ld16_sc1(qr, ro + k * 64, (unsigned)t * 4096u + 32u * XH); }
    wait_mem();
    const unsigned qwin = XCH_KM + (unsigned)(s * 32 + h) * 6144u;
    u32x4 VM[2];
#pragma unroll
    for (int k = 0; k < 4; ++k) XM[k] = ld16_sc1(xr, (unsigned)lane * 16u + (k & 1) * 1536, qwin + (k >> 1) * 3072);
#pragma unroll
    for (int ts = 0; ts < 2; ++ts) VM[ts] = ld16_sc1(xr, (unsigned)(g >> 1) * 1536u + 1024u + (unsigned)(lane & 31) * 16u, qwin + ts * 3072);
#pragma unroll
    for (int ts = 0; ts < 2; ++ts) {
        const u32x4 v = VM[ts];
        const int m = n & 7, hf = n >> 3, row = 32 + m;
#pragma unroll
        for (int i = 0; i < 4; ++i) { const int e = 64 * ts + 16 * g + 4 * i + 2 * hf;
            *(LAS unsigned*)(my + row * 256 + (((e >> 3) ^ ((row & 7) << 1)) * 16) + (e & 7) * 2) = v[i]; }
    }
    const float csc = 0.08838834764831845f * 1.4426950408889634f, ninf = -__builtin_inff();
    u32x4 PX[2]; u32x2 PM[2];
#pragma unroll
    for (int qt = 0; qt < 2; ++qt) {
        f32x4 sm = {0.f, 0.f, 0.f, 0.f}, s0 = sm, s1 = sm;
#pragma unroll
        for (int k = 0; k < 4; ++k) { sm = mma32(asbf(XM[k]), asbf(Q[qt][k]), sm); s0 = mma32(asbf(K[0][k]), asbf(Q[qt][k]), s0); if (qt == 1) s1 = mma32(asbf(K[1][k]), asbf(Q[qt][k]), s1); }
#pragma unroll
        for (int i = 0; i < 4; ++i) {
            if (g < 2) sm[i] = ninf;
            if (qt == 0) { if (4 * g + i > n) s0[i] = ninf; s1[i] = ninf; }
            else if (4 * g + i > n) s1[i] = ninf;
        }
        float m = fmaxf(fmaxf(fmaxf(s0[0], s0[1]), fmaxf(s0[2], s0[3])), fmaxf(fmaxf(s1[0], s1[1]), fmaxf(s1[2], s1[3])));
        m = fmaxf(m, fmaxf(fmaxf(sm[0], sm[1]), fmaxf(sm[2], sm[3])));
        m = rows_max(m);
        const float mc = -m * csc; float sum = 0.f;
#pragma unroll
        for (int i = 0; i < 4; ++i) { s0[i] = __builtin_amdgcn_exp2f(fmaf(s0[i], csc, mc)); s1[i] = __builtin_amdgcn_exp2f(fmaf(s1[i], csc, mc)); sm[i] = __builtin_amdgcn_exp2f(fmaf(sm[i], csc, mc));
            sum += (s0[i] + s1[i]) + sm[i]; }
        sum = rows_sum(sum);
        const float inv = __builtin_amdgcn_rcpf(sum);
        PX[qt] = (u32x4){pk_bf16(s0[0] * inv, s0[1] * inv), pk_bf16(s0[2] * inv, s0[3] * inv), pk_bf16(s1[0] * inv, s1[1] * inv), pk_bf16(s1[2] * inv, s1[3] * inv)};
        PM[qt] = (u32x2){pk_bf16(sm[0] * inv, sm[1] * inv), pk_bf16(sm[2] * inv, sm[3] * inv)};
    }
    asm volatile("s_waitcnt vmcnt(0) lgkmcnt(0)" ::: "memory");
    const int q4 = n >> 2, p4 = n & 3;
    const int r0 = 4 * g + q4, rm = 32 + 4 * (g & 1) + q4;
    const rsrc_t orr = mkrsrc(p.ws + WS_OA + (size_t)((s >> 2) * 64 + 2 * h) * 16384 + (size_t)(s & 3) * 4096, 32768u);
    const unsigned oo = (unsigned)(n * 128 + (g & 1) * 32 + (g >> 1) * 16);
#pragma unroll
    for (int ep = 0; ep < 4; ++ep) {
        f32x4 d[2][2];
#pragma unroll
        for (int e2 = 0; e2 < 2; ++e2) {
            const int ch = (2 * ep + e2) * 2 + (p4 >> 1);
            const bf16x8 va = cat8(trd(my + r0 * 256 + ((ch ^ ((r0 & 7) << 1)) * 16) + (p4 & 1) * 8), trd(my + (r0 + 16) * 256 + ((ch ^ ((r0 & 7) << 1)) * 16) + (p4 & 1) * 8));
            const s16x4 vm = trd(my + rm * 256 + ((ch ^ ((rm & 7) << 1)) * 16) + (p4 & 1) * 8);
#pragma unroll
            for (int qt = 0; qt < 2; ++qt) { f32x4 c = {0.f, 0.f, 0.f, 0.f}; c = mma32(va, asbf(PX[qt]), c); d[qt][e2] = mma16(__builtin_bit_cast(u32x2, vm), PM[qt], c); }
        }
#pragma unroll
        for (int qt = 0; qt < 2; ++qt) store_pair(orr, oo + (ep & 1) * 64 + qt * 2048, (unsigned)(ep >> 1) * 16384u, d[qt][0], d[qt][1], true);
    }
    asm volatile("s_waitcnt lgkmcnt(0)" ::: "memory");
}

DI void attn_b_item(const Params& p, int hq, int qb, ldsp lds) {
    const int tid = fresh_tid(), w = __builtin_amdgcn_readfirstlane(tid >> 6), lane = tid & 63, g = lane >> 4, n = lane & 15;
    const int q0 = qb * 128;
    const rsrc_t br = mkrsrc(p.ws + WS_QKVB, 96u * BH);
    const rsrc_t orr = mkrsrc(p.ws + WS_OB + (size_t)qb * 16 * 16384, 16u * 16384u);
    constexpr int BUF = 57344;
    unsigned ko[3], vo[3];
#pragma unroll
    for (int i = 0; i < 3; ++i) { const int r = (w * 3 + i) * 16 + (lane >> 2), c = lane & 3; const unsigned rp = (unsigned)max(q0 - 256 + r, 0) * 64u;
        ko[i] = rp + 32u * BH + (unsigned)((c ^ (((r >> 3) & 1) * 3)) * 16); vo[i] = rp + 64u * BH + (unsigned)((c ^ (((r >> 2) & 1) << 1)) * 16); }
    const unsigned qo = (unsigned)(q0 + 16 * w + (lane >> 2)) * 64u + (unsigned)(((lane & 3) ^ (((lane >> 5) & 1) * 3)) * 16);
    auto stage = [&](int i) {
        const unsigned so = (unsigned)(hq * 4 + i) * BH; ldsp d = lds + (i & 1) * BUF;
#pragma unroll
        for (int k = 0; k < 3; ++k) {
            __builtin_amdgcn_raw_ptr_buffer_load_lds(br, (LAS void*)(d + (w * 3 + k) * 1024), 16, ko[k], so, 0, 16);
            __builtin_amdgcn_raw_ptr_buffer_load_lds(br, (LAS void*)(d + 24576 + (w * 3 + k) * 1024), 16, vo[k], so, 0, 16);
        }
        __builtin_amdgcn_raw_ptr_buffer_load_lds(br, (LAS void*)(d + 49152 + w * 1024), 16, qo, so, 0, 16);
    };
    const float ninf = -__builtin_inff(), csc = 0.17677669529663687f * 1.4426950408889634f;
    const int tmin = (256 - q0 - 16 * w) >> 4;
    const int q4 = n >> 2, p4 = n & 3, sw = (g & 1) << 1;
    const int kq = (16 * w + n) * 64 + ((g ^ (((n >> 3) & 1) * 3)) * 16);
    const unsigned vb = (unsigned)(size_t)lds + 24576u + (unsigned)((16 * w + 4 * g + q4) * 64 + (p4 & 1) * 8);
    stage(0);
#pragma unroll 1
    for (int i = 0; i < 4; ++i) {
        if (i < 3) { stage(i + 1); WAIT_V(7); } else WAIT_V(0);
        BAR(); asm volatile("" ::: "memory");
        const ldsp bp = lds + (i & 1) * BUF;
        const bf16x8 Q = *(const LAS bf16x8*)(bp + 49152 + kq);
        f32x4 sc[17];
#pragma unroll
        for (int t = 0; t < 17; ++t) sc[t] = mma32(*(const LAS bf16x8*)(bp + kq + t * 1024), Q, (f32x4){0.f, 0.f, 0.f, 0.f});
#pragma unroll
        for (int k = 0; k < 4; ++k) { if (4 * g + k <= n) sc[0][k] = ninf; if (4 * g + k > n) sc[16][k] = ninf; }
        if (tmin > 0) {
#pragma unroll
            for (int t = 0; t < 16; ++t) if (t < tmin) sc[t] = (f32x4){ninf, ninf, ninf, ninf};
        }
        float m = max3f(sc[16][0], sc[16][1], max3f(sc[16][2], sc[16][3], ninf));
#pragma unroll
        for (int t = 0; t < 16; ++t) m = max3f(max3f(m, sc[t][0], sc[t][1]), sc[t][2], sc[t][3]);
        m = rows_max(m);
        const float mc = -m * csc;
        f32x4 sum4 = {0.f, 0.f, 0.f, 0.f};
#pragma unroll
        for (int t = 0; t < 17; ++t) {
#pragma unroll
            for (int k = 0; k < 4; ++k) sc[t][k] = __builtin_amdgcn_exp2f(fmaf(sc[t][k], csc, mc));
            sum4 += sc[t];
        }
        const float inv = __builtin_amdgcn_rcpf(rows_sum((sum4[0] + sum4[1]) + (sum4[2] + sum4[3])));
        const unsigned v0 = vb + (unsigned)((i & 1) * BUF) + (unsigned)(((p4 >> 1) ^ sw) * 16), v1 = vb + (unsigned)((i & 1) * BUF) + (unsigned)(((2 + (p4 >> 1)) ^ sw) * 16);
        s16x4 V0[17], V1[17];
#define VRD(t) TRD(V0[t], v0, (t) * 1024); TRD(V1[t], v1, (t) * 1024)
        VRD(0); VRD(1); VRD(2); VRD(3); VRD(4); VRD(5); VRD(6); VRD(7); VRD(8); VRD(9); VRD(10); VRD(11); VRD(12); VRD(13); VRD(14); VRD(15); VRD(16);
#undef VRD
        u32x2 P[17];
#pragma unroll
        for (int t = 0; t < 17; ++t) P[t] = (u32x2){pk_bf16(sc[t][0], sc[t][1]), pk_bf16(sc[t][2], sc[t][3])};
        WAIT_L(0); SCHED();
        f32x4 d0 = {0.f, 0.f, 0.f, 0.f}, d1 = d0;
#pragma unroll
        for (int tp = 0; tp < 8; ++tp) {
            const bf16x8 PP = asbf((u32x4){P[2 * tp].x, P[2 * tp].y, P[2 * tp + 1].x, P[2 * tp + 1].y});
            d0 = mma32(cat8(V0[2 * tp], V0[2 * tp + 1]), PP, d0); d1 = mma32(cat8(V1[2 * tp], V1[2 * tp + 1]), PP, d1);
        }
        d0 = mma16(__builtin_bit_cast(u32x2, V0[16]), P[16], d0); d1 = mma16(__builtin_bit_cast(u32x2, V1[16]), P[16], d1);
        BAR();
        store_pair(orr, (unsigned)((16 * w + n) * 128 + (g & 1) * 32 + (g >> 1) * 16), (unsigned)(hq * 2 + (i >> 1)) * 16384u + (unsigned)(i & 1) * 64u, d0 * inv, d1 * inv, true);
    }
    WAIT_V(0);
    signal((unsigned*)(p.ws + WS_CTL), C_OB + qb * 32, 1);
}

DI void out_tail(const Params& p, int c, int q, ldsp lds) {
    const int tid = fresh_tid(), w = __builtin_amdgcn_readfirstlane(tid >> 6), lane = tid & 63, g = lane >> 4, n = lane & 15;
    unsigned* ctl = (unsigned*)(p.ws + WS_CTL);
    bf16x8 WT[16][2];
    {
        ldsp my = lds + w * 16384;
        const int toff = (8 * g + (n >> 2)) * 64 + (n & 3) * 8;
#pragma unroll
        for (int r = 0; r < 2; ++r) {
#pragma unroll
            for (int i = 0; i < 16; ++i)
                __builtin_amdgcn_global_load_lds((const unsigned*)(p.wo + (size_t)(512 * w + 256 * r + 16 * i + (lane >> 2)) * 1024 + 32 * c + (lane & 3) * 8), (LAS unsigned*)(my + i * 1024), 16, 0, 0);
            WAIT_V(0);
#pragma unroll
            for (int k = 0; k < 8; ++k)
#pragma unroll
                for (int ct = 0; ct < 2; ++ct) { WT[8 * r + k][ct] = cat8(trd(my + k * 2048 + ct * 32 + toff), trd(my + k * 2048 + ct * 32 + toff + 256)); }
#pragma unroll
            for (int k = 0; k < 8; ++k) asm volatile("" : "+v"(WT[8 * r + k][0]), "+v"(WT[8 * r + k][1]));
            WAIT_L(0);
        }
    }
    BAR();
#pragma unroll 1
    for (int hs = q; hs < 24; hs += 3) {
        const int s = 52 + (hs >> 1), rt = hs & 1;
        if (w == 0) wait_count(ctl, C_OS + (s - 52) * 32, 32u);
        asm volatile("" ::: "memory"); BAR(); asm volatile("" ::: "memory");
        const rsrc_t ar = mkrsrc(p.ws + WS_OA + (size_t)((s >> 2) * 64 + 8 * w) * 16384 + (size_t)(s & 3) * 4096 + (size_t)rt * 2048, 8u * 16384u);
        const unsigned ao = (unsigned)n * 128u + (unsigned)g * 16u;
        u32x4 A[16];
#pragma unroll
        for (int k = 0; k < 16; ++k) A[k] = ld16_sc1(ar, ao + (k & 1) * 64, (unsigned)(k >> 1) * 16384u);
#pragma unroll
        for (int ct = 0; ct < 2; ++ct) {
            f32x4 acc = {0.f, 0.f, 0.f, 0.f};
#pragma unroll
            for (int k = 0; k < 16; ++k) acc = mma32(WT[k][ct], asbf(A[k]), acc);
            *(LAS f32x4*)(lds + w * 2048 + ct * 1024 + lane * 16) = acc;
        }
        LDS_BAR();
        if (w < 2) {
            f32x4 t = {0.f, 0.f, 0.f, 0.f};
#pragma unroll
            for (int ww = 0; ww < 8; ++ww) t += *(const LAS f32x4*)(lds + ww * 2048 + w * 1024 + lane * 16);
            *(u32x2*)(p.out + (size_t)(32 * s + 16 * rt + n) * 2048 + 32 * c + 16 * w + 4 * g) = (u32x2){pk_bf16(t[0], t[1]), pk_bf16(t[2], t[3])};
        }
        WAIT_L(0);
    }
    BAR();
}

DI void big_unit(const Params& p, int id, Unit& u) {
    u.lda = DM * 2; u.nt = 16; u.ak = 128; u.ah = 128 * DM * 2;
    if (id < 384) {
        const int pm = id & 7, pn = id >> 3;
        const u16 *w0 = p.wq, *w1 = p.wk, *w2 = p.wv; const u16* W = pn < 16 ? w0 : (pn < 32 ? w1 : w2);
        u.A = (const char*)(p.ax + (size_t)pm * 256 * DM); u.B = (const char*)(W + (pn & 15) * 256); u.ldb = 4096 * 2;
        u.C = p.ws + WS_QKVX + (size_t)(2 * pn) * XH + (size_t)pm * 65536; u.ldc = 256; u.cw = 64; u.ch = XH;
        if (pn < 16) { u.sig = C_Q + pm * 32; u.nsig = 1; } else { u.sig = C_KV + 2 * pm * 512 + (pn & 15) * 32; u.nsig = 4; }
    } else if (id < 480) {
        const int pm = (id - 384) & 7, pn = (id - 384) >> 3;
        u.A = (const char*)(p.bx + (size_t)pm * 256 * DM); u.B = (const char*)(p.bwqkv + pn * 256); u.ldb = LDB3 * 2;
        u.C = p.ws + WS_QKVB + (size_t)(8 * pn) * BH + (size_t)pm * 16384; u.ldc = 64; u.cw = BH; u.ch = 4 * BH; u.sig = C_B + pm * 32; u.nsig = 1;
    } else {
        const bool isb = id < 512; const int q = isb ? id - 480 : id - 512, pm = q >> 2, pn = q & 3;
        const u16 *w0 = p.wo, *w1 = p.bwo;
        u.nt = isb ? 16 : 64; u.lda = 128; u.ak = 16384; u.ah = (unsigned)u.nt * 16384u;
        u.A = p.ws + (isb ? WS_OB : WS_OA) + (size_t)pm * 2 * u.ah; u.B = (const char*)((isb ? w1 : w0) + pn * 256); u.ldb = DM * 2;
        u.C = (char*)(p.out + (size_t)pm * 256 * 2048 + (isb ? 1024 : 0) + pn * 256); u.ldc = 4096; u.cw = 64; u.ch = 256; u.sig = 0; u.nsig = 0;
    }
}
struct BigSched { const Params& p; int id0, id1; DI bool next(int i, Unit& u) const { const int id = i == 0 ? id0 : (i == 1 ? id1 : -1); if (id < 0) return false; big_unit(p, id, u); return true; } };

__global__ __launch_bounds__(512, 2) void k_fused(Params p) {
    extern __shared__ __attribute__((aligned(16))) unsigned char shm[];
    ldsp lds = (ldsp)shm;
    unsigned* ctl = (unsigned*)(p.ws + WS_CTL);
    const int b = blockIdx.x, id = (b & 3) * 32 + (b >> 3);
#define WSTAMP(code)
    WSTAMP(1);
    if ((b & 7) < 4) {
        chain_role(p, id, lds, (unsigned long long*)(p.ws + WS_END));
        BAR();
        if (threadIdx.x < 64) signal(ctl, C_DONE + (id >> 5) * 32, 1);
        WSTAMP(2);
    } else {
#pragma unroll 1
        for (int r = 0; r < 2; ++r) {
            const int rb = 2 * r + (id & 1), ct = id >> 1;
            const u16 *w1 = p.wk, *w2 = p.wv;
            Unit u; u.A = (const char*)(p.ax + (size_t)rb * 128 * DM); u.lda = DM * 2; u.B = (const char*)((ct < 32 ? w1 : w2) + (ct & 31) * 128); u.ldb = 4096 * 2; u.nt = 16;
            u.C = p.ws + WS_QKVX + (size_t)(32 + ct) * XH + (size_t)rb * 32768; u.ldc = 256; u.cw = 64; u.ch = 0; u.ak = 128; u.ah = 0;
            u.sig = C_KV + rb * 512 + (ct & 31) * 16; u.nsig = 1;
            gemm_small(lds, u, ctl);
            WSTAMP(3);
        }
    }
    volatile LAS unsigned* qw = (volatile LAS unsigned*)(lds + 131072);
    const int w = __builtin_amdgcn_readfirstlane(threadIdx.x >> 6);
    const int k = b & 3;
    bool fixed = (b & 7) >= 4;
#pragma unroll 1
    for (;;) {
        int kind, x, it = -1, x2 = -1;
        if (fixed) { const int i = id & 31; kind = 6; x = i < 24 ? (16 + 8 * k + (i & 7)) * 8 + 2 + (i >> 3) : (4 * k + (i & 3)) * 8 + ((i >> 2) & 1); x2 = x + (i < 24 ? 3 : 2); fixed = false; }
        else {
        if (threadIdx.x == 0) qw[0] = __hip_atomic_fetch_add(ctl + C_HEAD + k * 32, 1u, RLX_AGENT) + 1u;
        LDS_BAR();
        it = (int)__builtin_amdgcn_readfirstlane(qw[0]);
        LDS_BAR();
        if (it < 4) { kind = 0; x = it * 4 + k; }
        else if (it < 28) { kind = 6; x = 384 + 24 * k + it - 4; }
        else if (it < 30) { kind = 6; x = 512 + (it - 28) * 4 + k; }
        else if (it < 38) { kind = 6; x = (4 * k + ((it - 30) & 3)) * 8 + 4 + ((it - 30) >> 2); }
        else if (it < 42) { kind = 0; x = (it - 38 + 4) * 4 + k; }
        else if (it < 74) { kind = 1; x = ((it - 42) >> 1) * 8 + 2 * k + ((it - 42) & 1); }
        else if (it < 82) { kind = 6; x = (4 * k + ((it - 74) & 3)) * 8 + 6 + ((it - 74) >> 2); }
        else if (it < 84) { kind = 0; x = (it - 82 + 8) * 4 + k; }
        else if (it < 92) { kind = 2; x = (4 + ((it - 84) >> 1)) * 8 + 2 * k + ((it - 84) & 1); }
        else if (it < 100) { kind = 6; x = 480 + (it - 92) * 4 + k; }
        else if (it < 104) { kind = 4; x = (it - 100) * 4 + k; }
        else if (it < 112) { kind = 2; x = (8 + ((it - 104) >> 1)) * 8 + 2 * k + ((it - 104) & 1); }
        else if (it < 136) { kind = 5; x = ((it - 112) >> 3) * 32 + 8 * k + ((it - 112) & 7); }
        else if (it < 138) { kind = 2; x = 96 + 2 * k + it - 136; }
        else break;
        }
        if (kind == 6) {
            if (x >= 480) { const int w0 = x < 512 ? C_OB + ((x - 480) >> 2) * 64 : C_OA + ((x - 512) >> 2) * 64; const unsigned need = x < 512 ? 64u : 32u; wg_wait(ctl, w0, need, w0 + 32, need); }
            BigSched S{p, x, x2};
            gemm_stream(lds, S, ctl);
        } else
        if (kind == 0) {
            const int s0 = (x >> 2) * 4;
            wait_count(ctl, C_Q + (s0 >> 3) * 32, 128u); wait_count(ctl, C_PROG, (unsigned)s0 + 5u);
            asm volatile("" ::: "memory");
#pragma unroll 1
            for (int s = s0; s < s0 + 4; ++s) attn_a_item(p, s, (x & 3) * 8 + w, lds + w * 10240, fresh_tid() & 63, []() {});
            WAIT_V(0); signal(ctl, C_OA + (x >> 2) * 32, 1);
            BAR();
        } else if (kind == 4) {
#pragma unroll 1
            for (int s = 40 + (x >> 2); s < NSEG; s += 4) {
                wait_count(ctl, C_Q + (s >> 3) * 32, 128u); wait_count(ctl, C_KV + (s >> 2) * 512 + ((x & 3) * 8 + w) * 16, 16u);
                asm volatile("" ::: "memory");
                attn_a_item(p, s, (x & 3) * 8 + w, lds + w * 10240, fresh_tid() & 63, [&]() {
                    if (s < NSEG - 1) wait_count(ctl, C_PROG, (unsigned)s + 2u); else wait_count(ctl, C_DONE + (x & 3) * 32, 32u);
                    asm volatile("" ::: "memory"); });
                WAIT_V(0); signal(ctl, s < 52 ? C_OA + (s >> 2) * 32 : C_OS + (s - 52) * 32, 1);
            }
            BAR();
        } else if (kind == 1) {
            const int qb = x >> 3;
            wait_count(ctl, C_B + (qb >> 1) * 32, 96u); wait_count(ctl, C_B + max((qb >> 1) - 1, 0) * 32, 96u);
            asm volatile("" ::: "memory");
            attn_b_item(p, x & 7, qb, lds);
        } else if (kind == 5) {
            out_tail(p, x & 31, x >> 5, lds);
        } else {
            const int rb = x >> 3, ct = x & 7;
            Unit u; u.ldb = DM * 2; u.ldc = 2048 * 2; u.sig = 0; u.nsig = 0; u.lda = 128; u.ak = 16384; u.ah = 0; u.cw = 64; u.ch = 0;
            u.A = p.ws + WS_OA + (size_t)rb * 64 * 16384; u.B = (const char*)(p.wo + ct * 128); u.nt = 64; u.C = (char*)(p.out + (size_t)rb * 128 * 2048 + ct * 128);
            wg_wait(ctl, C_OA + rb * 32, rb < 10 ? 32u : 128u, C_OA + rb * 32, 32u);
            gemm_small(lds, u, ctl);
        }
        WSTAMP(16 + (kind == 6 && x >= 480 ? (x < 512 ? 7 : 8) : kind) + ((it & 0xffff) << 8));
    }
}

extern "C" void kernel_launch(void* const* d_in, const int* in_sizes, int n_in, void* d_out, int out_size, void* d_ws, size_t ws_size, hipStream_t stream) {
    Params p;
    p.ax = (const u16*)d_in[0]; p.mem0 = (const u16*)d_in[1]; p.wq = (const u16*)d_in[2]; p.wk = (const u16*)d_in[3]; p.wv = (const u16*)d_in[4]; p.wo = (const u16*)d_in[5];
    p.bx = (const u16*)d_in[7]; p.bwqkv = (const u16*)d_in[8]; p.bwo = (const u16*)d_in[9];
    p.out = (u16*)d_out; p.ws = (char*)(((uintptr_t)d_ws + WS_SHIFT(ws_size) + 0x1FFFFFu) & ~(uintptr_t)0x1FFFFFu);
    p.d0 = 8; p.d1 = 7; p.d2 = 5; p.d3 = 0;
    (void)hipFuncSetAttribute((const void*)k_fused, hipFuncAttributeMaxDynamicSharedMemorySize, LDS_BYTES);
    (void)hipMemsetAsync(p.ws + WS_XCH, 0xFF, (size_t)NWIN_INIT * 16384, stream);
    k_fused<<<256, 512, LDS_BYTES, stream>>>(p);
}
```

```cpp
#include <hip/hip_runtime.h>
#include <stdint.h>
#include <stdlib.h>

#define LAS __attribute__((address_space(3)))
#define DI __device__ __forceinline__
typedef unsigned short u16;
typedef __attribute__((ext_vector_type(8))) __bf16 bf16x8;
typedef __attribute__((ext_vector_type(16))) __bf16 bf16x16;
typedef __attribute__((ext_vector_type(2))) __bf16 bf16x2;
typedef __attribute__((ext_vector_type(4))) short s16x4;
typedef __attribute__((ext_vector_type(8))) short s16x8;
typedef __attribute__((ext_vector_type(16))) short s16x16;
typedef __attribute__((ext_vector_type(2))) float f32x2;
typedef __attribute__((ext_vector_type(4))) float f32x4;
typedef __attribute__((ext_vector_type(2))) unsigned u32x2;
typedef __attribute__((ext_vector_type(4))) unsigned u32x4;
typedef LAS unsigned char* ldsp;
typedef __amdgpu_buffer_rsrc_t rsrc_t;

constexpr int S_LEN = 2048, DM = 1024, NH = 32, DH = 128, NSEG = 64;
constexpr int LDB3 = 3072;
constexpr unsigned XH = S_LEN * 256, BH = S_LEN * 64;
constexpr unsigned SENT = 0xFFFFFFFFu;
constexpr size_t WS_XCH = 2u << 20;
constexpr int NWIN = 640, NWIN_INIT = 6;
constexpr unsigned XCH_KM = NWIN * 16384u;
constexpr size_t XCH_BYTES = (size_t)XCH_KM + 64u * 128u * 1536u;
constexpr size_t WS_CTL = WS_XCH + 2 * 16384;
constexpr int C_TMO = 0, C_PAR = 64, C_HEAD = 3072, C_PROG = 192, C_PACE = 128, C_Q = 256, C_B = C_Q + 8 * 32, C_OA = C_B + 8 * 32, C_OB = C_OA + 16 * 32, C_OS = C_OB + 16 * 32, C_DONE = C_OS + 8 * 128, C_KV = 4096, C_PROBE = 12288;
constexpr size_t WS_QKVX = 26u << 20;
constexpr size_t WS_QKVB = WS_QKVX + (size_t)96 * XH;
constexpr size_t WS_OA = WS_QKVB + (size_t)96 * BH;
constexpr size_t WS_OB = WS_OA + (size_t)S_LEN * 4096 * 2;
constexpr size_t WS_END = WS_OB + (size_t)S_LEN * 1024 * 2;
#define WS_SHIFT(sz) 0
constexpr int LDS_BYTES = 131072 + 1024 + 8192;

DI unsigned pk_bf16(float a, float b) { f32x2 v = {a, b}; return __builtin_bit_cast(unsigned, __builtin_convertvector(v, bf16x2)); }
DI s16x4 trd(ldsp p) { return __builtin_amdgcn_ds_read_tr16_b64_v4i16((LAS s16x4*)p); }
DI bf16x8 cat8(s16x4 a, s16x4 b) { return __builtin_bit_cast(bf16x8, __builtin_shufflevector(a, b, 0, 1, 2, 3, 4, 5, 6, 7)); }
DI bf16x16 cat16(bf16x8 a, bf16x8 b) { return __builtin_shufflevector(a, b, 0, 1, 2, 3, 4, 5, 6, 7, 8, 9, 10, 11, 12, 13, 14, 15); }
DI bf16x8 lo8(bf16x16 v) { return __builtin_shufflevector(v, v, 0, 1, 2, 3, 4, 5, 6, 7); }
DI bf16x8 hi8(bf16x16 v) { return __builtin_shufflevector(v, v, 8, 9, 10, 11, 12, 13, 14, 15); }
DI bf16x8 asbf(u32x4 v) { return __builtin_bit_cast(bf16x8, v); }
DI f32x4 mma32(bf16x8 a, bf16x8 b, f32x4 c) { return __builtin_amdgcn_mfma_f32_16x16x32_bf16(a, b, c, 0, 0, 0); }
DI f32x4 mma16(u32x2 a, u32x2 b, f32x4 c) { return mma32(asbf((u32x4){a.x, a.y, 0u, 0u}), asbf((u32x4){b.x, b.y, 0u, 0u}), c); }
DI f32x4 smma(u32x4 a, bf16x16 b, f32x4 c, int idx) { return __builtin_amdgcn_smfmac_f32_16x16x64_bf16(asbf(a), b, c, idx, 0, 0); }
DI rsrc_t mkrsrc(const void* p, unsigned bytes) { return __builtin_amdgcn_make_buffer_rsrc((void*)p, 0, bytes, 0x00020000); }
DI u32x4 ld16_sc1(rsrc_t r, unsigned voff, unsigned soff) { return __builtin_amdgcn_raw_buffer_load_b128(r, voff, soff, 16); }
DI void swap16(unsigned& a, unsigned& b) { auto r = __builtin_amdgcn_permlane16_swap(a, b, false, false); a = r[0]; b = r[1]; }
DI void swap32(unsigned& a, unsigned& b) { auto r = __builtin_amdgcn_permlane32_swap(a, b, false, false); a = r[0]; b = r[1]; }
DI void swap16f(float& a, float& b) { unsigned x = __float_as_uint(a), y = __float_as_uint(b); swap16(x, y); a = __uint_as_float(x); b = __uint_as_float(y); }
DI void swap32f(float& a, float& b) { unsigned x = __float_as_uint(a), y = __float_as_uint(b); swap32(x, y); a = __uint_as_float(x); b = __uint_as_float(y); }
DI float rows_max(float v) { float a = v, b = v; swap16f(a, b); v = fmaxf(a, b); a = v; b = v; swap32f(a, b); return fmaxf(a, b); }
DI float rows_sum(float v) { float a = v, b = v; swap16f(a, b); v = a + b; a = v; b = v; swap32f(a, b); return a + b; }
DI unsigned umax4(u32x4 v) { return max(max(v.x, v.y), max(v.z, v.w)); }
#define WAIT_V(n) asm volatile("s_waitcnt vmcnt(" #n ")" ::: "memory")
#define WAIT_L(n) asm volatile("s_waitcnt lgkmcnt(" #n ")" ::: "memory")
#define BAR() __builtin_amdgcn_s_barrier()
#define SCHED() __builtin_amdgcn_sched_barrier(0)
#define LDS_BAR() do { asm volatile("s_waitcnt lgkmcnt(0)" ::: "memory"); __builtin_amdgcn_s_barrier(); asm volatile("" ::: "memory"); } while (0)

constexpr int HTB = 128 * 64 * 2;
DI int lds_byte(int r, int c) { const int st = (r >> 4) * 2 + (c >> 5), rr = r & 15, cc = c & 31, ob = rr * 64 + cc * 2; return st * 1024 + (ob ^ (((ob >> 9) & 1) << 5)); }
DI void stage_rc(int b, int& R, int& C) { const int st = b / 1024, sb = b % 1024, swz = sb ^ (((sb >> 9) & 1) << 5); R = (st >> 1) * 16 + swz / 64; C = (st & 1) * 32 + (swz % 64) / 2; }

struct Params {
    const u16 *ax, *mem0, *wq, *wk, *wv, *wo, *bx, *bwqkv, *bwo;
    u16* out; char* ws;
};
#define RLX_AGENT __ATOMIC_RELAXED, __HIP_MEMORY_SCOPE_AGENT
DI bool spin_out(unsigned& spins, unsigned* ctl) {
    spins = __builtin_amdgcn_readfirstlane(spins) + 1u;
    if ((spins & 63u) == 0u) {
        if (spins > (1u << 18)) { __hip_atomic_store(ctl + C_TMO, 1u, RLX_AGENT); return true; }
        if (__builtin_amdgcn_readfirstlane(__hip_atomic_load(ctl + C_TMO, RLX_AGENT)) != SENT) return true;
    }
    return false;
}
template <int NAP = 8>
DI void wait_count(unsigned* ctl, int word, unsigned need) {
    unsigned spins = 0;
    while (__builtin_amdgcn_readfirstlane(__hip_atomic_load(ctl + word, RLX_AGENT)) + 1u < need) { __builtin_amdgcn_s_sleep(NAP); if (spin_out(spins, ctl)) break; }
}
DI void wg_wait(unsigned* ctl, int word0, unsigned need0, int word1, unsigned need1) {
    if (threadIdx.x < 64) {
        wait_count(ctl, word0, need0); wait_count(ctl, word1, need1);
        __builtin_amdgcn_fence(__ATOMIC_ACQUIRE, "agent");
        asm volatile("s_waitcnt vmcnt(0)" ::: "memory");
    }
    asm volatile("" ::: "memory"); BAR(); asm volatile("" ::: "memory");
}
struct Unit { const char* A; const char* B; char* C; unsigned lda, ldb, ldc, cw, ch, ak, ah; int nt, sig, nsig; };
DI int fresh_tid() { int t = threadIdx.x; asm volatile("" : "+v"(t)); return t; }
DI void signal(unsigned* ctl, int sig, int nsig) { const int lane = fresh_tid() & 63; if (lane < nsig) __hip_atomic_fetch_add(ctl + sig + (lane & 1) * 16 + (lane >> 1) * 512, 1u, RLX_AGENT); }
DI void store_pair(rsrc_t cr, unsigned voff, unsigned soff, f32x4 v0, f32x4 v1, bool wt) {
    unsigned ax = pk_bf16(v0[0], v0[1]), ay = pk_bf16(v0[2], v0[3]), bx = pk_bf16(v1[0], v1[1]), by = pk_bf16(v1[2], v1[3]);
    swap16(ax, bx); swap16(ay, by);
    if (wt) __builtin_amdgcn_raw_buffer_store_b128((u32x4){ax, ay, bx, by}, cr, voff, soff, 16);
    else __builtin_amdgcn_raw_buffer_store_b128((u32x4){ax, ay, bx, by}, cr, voff, soff, 0);
}
#define STAGE(bufoff, gbase, voff, step2) do { \
        __builtin_amdgcn_global_load_lds((const unsigned*)((gbase) + (voff)), (LAS unsigned*)(lds + (bufoff) + ldsw), 16, 0, 0); \
        __builtin_amdgcn_global_load_lds((const unsigned*)((gbase) + (step2) + (voff)), (LAS unsigned*)(lds + (bufoff) + ldsw + 8192), 16, 0, 0); } while (0)
#define LDA(dst, off) do { _Pragma("unroll") for (int m = 0; m < 4; ++m) _Pragma("unroll") for (int k = 0; k < 2; ++k) dst[m][k] = *(const LAS bf16x8*)(lds + (off) + aoff + m * 2048 + k * 1024); } while (0)
#define TRD(dst, base, off) asm volatile("ds_read_b64_tr_b16 %0, %1 offset:%2" : "=v"(dst) : "v"(base), "i"(off) : "memory")
#define LDB(dst, v0, v1, off) do { _Pragma("unroll") for (int n = 0; n < 2; ++n) _Pragma("unroll") for (int k = 0; k < 2; ++k) { s16x4 lo_, hi_; \
        if (n == 0 && k == 0) { TRD(lo_, v0, (off)); TRD(hi_, v1, (off)); } else if (n == 0) { TRD(lo_, v0, (off) + 8192); TRD(hi_, v1, (off) + 8192); } \
        else if (k == 0) { TRD(lo_, v0, (off) + 256); TRD(hi_, v1, (off) + 256); } else { TRD(lo_, v0, (off) + 8448); TRD(hi_, v1, (off) + 8448); } \
        dst[n][k] = cat8(lo_, hi_); } } while (0)
#define MMA(accv, At, Bt) do { SCHED(); __builtin_amdgcn_s_setprio(1); _Pragma("unroll") for (int m = 0; m < 4; ++m) _Pragma("unroll") for (int n = 0; n < 2; ++n) _Pragma("unroll") for (int k = 0; k < 2; ++k) \
        accv[m][n] = mma32(Bt[n][k], At[m][k], accv[m][n]); __builtin_amdgcn_s_setprio(0); } while (0)
#define GEMM_LANE_SETUP() \
    const int tid = fresh_tid(); \
    const int wid = __builtin_amdgcn_readfirstlane(tid >> 6), lane = tid & 63, wr = wid >> 2, wc = wid & 3, fr = lane & 15, fq = lane >> 4; \
    int RA, CA; stage_rc(tid * 16, RA, CA); \
    const int kB = 8 * (wid >> 1) + (((lane & 15) >> 1) ^ (((wid >> 1) & 1) << 2)); \
    const int nB2 = (((wid & 1) * 4 + (lane >> 4)) * 16 + (lane & 1) * 8) * 2; \
    const unsigned ldsw = (unsigned)wid * 1024u; \
    const int aoff = lds_byte(wr * 64 + fr, fq * 8); \
    const int boff0 = fq * 2048 + wc * 512 + (((fr >> 2) ^ ((fq & 1) << 2)) * 32) + (fr & 3) * 8, boff1 = boff0 ^ 128;

template <class Sched>
DI void gemm_stream(ldsp lds, const Sched& S, unsigned* ctl) {
    GEMM_LANE_SETUP();
#define SA(b, h) (((b) * 2 + (h)) * HTB)
#define SB(b, h) ((4 + (b) * 2 + (h)) * HTB)
    const unsigned vb0 = (unsigned)(size_t)lds + 65536u + (unsigned)boff0, vb1 = (unsigned)(size_t)lds + 65536u + (unsigned)boff1;
    Unit cur, nxt; int ui = 0;
    if (!S.next(0, cur)) return;
    f32x4 acc[2][2][4][2];
#pragma unroll
    for (int a = 0; a < 2; ++a)
#pragma unroll
        for (int b = 0; b < 2; ++b)
#pragma unroll
            for (int m = 0; m < 4; ++m)
#pragma unroll
                for (int n = 0; n < 2; ++n) acc[a][b][m][n] = (f32x4){0.f, 0.f, 0.f, 0.f};
    bf16x8 At[4][2], B0[2][2], B1[2][2];
    unsigned vA = (unsigned)RA * cur.lda + (unsigned)CA * 2u, vB = (unsigned)kB * cur.ldb + (unsigned)nB2;
    {
        const size_t a2s = (size_t)64 * cur.lda, ah = cur.ah, b2s = (size_t)32 * cur.ldb, bk = (size_t)64 * cur.ldb;
        STAGE(SB(0, 0), cur.B, vB, b2s); STAGE(SB(0, 1), cur.B + 256, vB, b2s); STAGE(SA(0, 0), cur.A, vA, a2s); STAGE(SA(0, 1), cur.A + ah, vA, a2s);
        if (wr == 1) BAR();
        WAIT_V(2); BAR();
        STAGE(SB(1, 0), cur.B + bk, vB, b2s); STAGE(SA(1, 0), cur.A + cur.ak, vA, a2s); STAGE(SB(1, 1), cur.B + bk + 256, vB, b2s);
        WAIT_V(6); BAR();
    }
    int psig = 0, pn = 0;
    for (;;) {
        const bool has_next = S.next(ui + 1, nxt);
        if (!has_next) nxt = cur;
        int nt = cur.nt; asm volatile("" : "+s"(nt));
        for (int t = 0; t < nt; t += 2) {
            const bool last = (t == nt - 2);
            const char* a1 = cur.A + (size_t)(t + 1) * cur.ak + cur.ah;
            const size_t a1s = (size_t)64 * cur.lda; const unsigned vA1 = vA;
            if (last) { const int tz = fresh_tid(); int R2, C2; stage_rc(tz * 16, R2, C2);
                vA = (unsigned)R2 * nxt.lda + (unsigned)C2 * 2u;
                vB = (unsigned)(8 * (wid >> 1) + (((tz & 15) >> 1) ^ (((wid >> 1) & 1) << 2))) * nxt.ldb + (unsigned)((((wid & 1) * 4 + ((tz >> 4) & 3)) * 16 + (tz & 1) * 8) * 2); }
            const unsigned lda = last ? nxt.lda : cur.lda, ldb = last ? nxt.ldb : cur.ldb;
            const char* a2 = last ? nxt.A : cur.A + (size_t)(t + 2) * cur.ak;
            const char* b2 = last ? nxt.B : cur.B + (size_t)(t + 2) * 64 * cur.ldb;
            const size_t a2s = (size_t)64 * lda, ah = last ? nxt.ah : cur.ah, b2s = (size_t)32 * ldb;
            const char* a3 = a2 + (last ? nxt.ak : cur.ak); const char* b3 = b2 + (size_t)64 * ldb;
            LDB(B0, vb0, vb1, 0); LDB(B1, vb0, vb1, HTB); SCHED(); LDA(At, SA(0, 0)); STAGE(SA(1, 1), a1, vA1, a1s);
            WAIT_V(8); WAIT_L(0); BAR(); MMA(acc[0][0], At, B0); MMA(acc[0][1], At, B1); BAR(); SCHED();
            LDA(At, SA(0, 1)); STAGE(SB(0, 0), b2, vB, b2s); STAGE(SB(0, 1), b2 + 256, vB, b2s); STAGE(SA(0, 0), a2, vA, a2s);
            WAIT_V(8);
            asm volatile("" : "+s"(pn));
            if (pn) { signal(ctl, psig, pn); pn = 0; }
            WAIT_L(0); BAR(); MMA(acc[1][0], At, B0); MMA(acc[1][1], At, B1); BAR(); SCHED();
            LDB(B0, vb0, vb1, 2 * HTB); LDB(B1, vb0, vb1, 3 * HTB); SCHED(); LDA(At, SA(1, 0)); STAGE(SA(0, 1), a2 + ah, vA, a2s);
            WAIT_V(8); WAIT_L(0); BAR(); MMA(acc[0][0], At, B0); MMA(acc[0][1], At, B1); BAR(); SCHED();
            LDA(At, SA(1, 1)); STAGE(SB(1, 0), b3, vB, b2s); STAGE(SB(1, 1), b3 + 256, vB, b2s); STAGE(SA(1, 0), a3, vA, a2s);
            WAIT_V(8); WAIT_L(0); BAR(); MMA(acc[1][0], At, B0); MMA(acc[1][1], At, B1); BAR(); SCHED();
        }
        if (wr == 0) BAR();
        {
            const rsrc_t cr = mkrsrc(cur.C, 0x1000000u);
            const int tz = fresh_tid();
            const unsigned co = (unsigned)(wr * 64 + (tz & 15)) * cur.ldc + (unsigned)wc * cur.cw + (unsigned)(((tz >> 4) & 1) * 32 + ((tz >> 5) & 1) * 16);
#pragma unroll
            for (int ai = 0; ai < 2; ++ai)
#pragma unroll
                for (int m = 0; m < 4; ++m)
#pragma unroll
                    for (int bj = 0; bj < 2; ++bj) store_pair(cr, co, (unsigned)(ai * 128 + m * 16) * cur.ldc + (unsigned)bj * cur.ch, acc[ai][bj][m][0], acc[ai][bj][m][1], true);
        }
        psig = cur.sig; pn = cur.nsig;
        if (!has_next) break;
#pragma unroll
        for (int a = 0; a < 2; ++a)
#pragma unroll
            for (int b = 0; b < 2; ++b)
#pragma unroll
                for (int m = 0; m < 4; ++m)
#pragma unroll
                    for (int n = 0; n < 2; ++n) acc[a][b][m][n] = (f32x4){0.f, 0.f, 0.f, 0.f};
        cur = nxt; ++ui;
        if (wr == 1) BAR();
    }
    WAIT_V(0);
    signal(ctl, psig, pn);
    BAR();
#undef SA
#undef SB
}

DI void gemm_small(ldsp lds, const Unit u, unsigned* ctl) {
    GEMM_LANE_SETUP();
    const unsigned vA = (unsigned)RA * u.lda + (unsigned)CA * 2u, vB = (unsigned)kB * u.ldb + (unsigned)nB2;
    const size_t a2s = (size_t)64 * u.lda, b2s = (size_t)32 * u.ldb, bk = (size_t)64 * u.ldb;
    f32x4 acc[4][2];
#pragma unroll
    for (int m = 0; m < 4; ++m)
#pragma unroll
        for (int n = 0; n < 2; ++n) acc[m][n] = (f32x4){0.f, 0.f, 0.f, 0.f};
    int nt = u.nt; asm volatile("" : "+s"(nt));
    const unsigned vb0 = (unsigned)(size_t)lds + (unsigned)boff0, vb1 = (unsigned)(size_t)lds + (unsigned)boff1, vb2 = vb0 + 65536u, vb3 = vb1 + 65536u;
#pragma unroll
    for (int t = 0; t < 3; ++t) { STAGE(t * 32768, u.A + (size_t)t * u.ak, vA, a2s); STAGE(t * 32768 + 16384, u.B + t * bk, vB, b2s); }
    for (int t0 = 0; t0 < nt; t0 += 4) {
#pragma unroll
        for (int i = 0; i < 4; ++i) {
            const int tn = min(t0 + i + 3, nt - 1);
            WAIT_V(8); BAR();
            bf16x8 At[4][2], Bt[2][2];
            if (i == 0) LDB(Bt, vb0, vb1, 16384); else if (i == 1) LDB(Bt, vb0, vb1, 49152); else if (i == 2) LDB(Bt, vb2, vb3, 16384); else LDB(Bt, vb2, vb3, 49152);
            LDA(At, i * 32768);
            STAGE(((i + 3) & 3) * 32768, u.A + (size_t)tn * u.ak, vA, a2s); STAGE(((i + 3) & 3) * 32768 + 16384, u.B + (size_t)tn * bk, vB, b2s);
            WAIT_L(0); SCHED();
            MMA(acc, At, Bt);
            SCHED();
        }
    }
    const rsrc_t cr = mkrsrc(u.C, 128u * u.ldc);
    const unsigned co = (unsigned)(wr * 64 + fr) * u.ldc + (unsigned)(wc * 32 + (fq & 1) * 16 + (fq >> 1) * 8) * 2u;
#pragma unroll
    for (int m = 0; m < 4; ++m) store_pair(cr, co, (unsigned)(m * 16) * u.ldc, acc[m][0], acc[m][1], true);
    WAIT_V(0);
    signal(ctl, u.sig, u.nsig);
    BAR();
}

#define win_off(idx) ((unsigned)(idx) * 16384u + ((((unsigned)(idx) >> 7) ^ par) & 1u) * 8192u)
#define W_MEM(s) ((s) == 0 ? 0 : 6 + 2 * (s))
#define W_PART(slot) (192 + (slot) * 64)
#define W_X(slot) (384 + (slot) * 128)
DI bool all_set(unsigned mx) { return __builtin_amdgcn_ballot_w64(mx == SENT) == 0ull; }
DI u32x4 rows_gather(unsigned v) { unsigned a = v, b = v; swap16(a, b); unsigned c = a, d = b; swap32(a, c); swap32(b, d); return (u32x4){a, b, c, d}; }
DI float max3f(float a, float b, float c) { return fmaxf(fmaxf(a, b), c); }
#define KD0 7
#define KD1 7
#define KD2 7
#define KRA 3
#define KDM 0
#define KPM 8
constexpr int RED_M = 144;
constexpr int L_PX = 32768, L_PM = L_PX + 512, L_ML = L_PM + 512, L_FLAG = L_ML + 256, L_PACE = L_FLAG + 16, L_VM = 34816, L_OWN = L_VM + 2048, L_X = L_OWN + 1536  ;
constexpr unsigned OOB = 0x80000000u;
#define RETRY(i)
#define SEG(i)
#define PIN_S(x) asm volatile("" : "+s"(x))
#define PIN_V(x) asm volatile("" : "+v"(x))
DI float ror8(float v) { return __uint_as_float(__builtin_amdgcn_update_dpp(0, __float_as_uint(v), 0x128, 0xf, 0xf, true)); }

template <int ROLE>
DI void chain_loop(const Params& p, const unsigned par, const int rank, const int w, ldsp lds, const bf16x16 (&W)[6][2], const bf16x16 (&WO)[2][2], unsigned long long* stamps) {
    const int lane = fresh_tid() & 63, g = lane >> 4, n = lane & 15;
    const int h = rank >> 2, j = rank & 3;
    unsigned* ctl = (unsigned*)(p.ws + WS_CTL);
    const rsrc_t xr = mkrsrc(p.ws + WS_XCH, (unsigned)XCH_BYTES);
    const rsrc_t qr = mkrsrc(p.ws + WS_QKVX + (size_t)(32 + h) * XH, 33u * XH);
    const int sidx = (n & 2) ? 0xEEEE : 0x4444;
    const unsigned l16 = (unsigned)lane * 16u;
    const int redw = (w * 1024 + (n & 1) * 512 + (n >> 1) * 8 + 2 * g) * 4;
    const int redr = (w < 4 ? (n >> 3) * RED_M + (4 * w + g) * 8 + (n & 7) : 2 * RED_M + (8 * (w & 1) + 2 * g + ((n >> 1) & 1)) * 8 + 2 * (n >> 2) + (n & 1)) * 4;
    const bool vown = (g >> 1) == (j & 1);
    const unsigned vg1 = (unsigned)(((j & 2) ^ 2) + (g >> 1)) * 1536u + 1024u + (unsigned)(lane & 31) * 16u;
    const unsigned vg0 = vown ? OOB : (vg1 + (unsigned)(j & 2) * 3072u - 3072u);
    const unsigned pub_off = (g == j ? XCH_KM + (unsigned)rank * 1536u : (unsigned)(4 * h + g) * 16384u + (unsigned)j * 1536u) + (unsigned)w * 256u + (unsigned)n * 16u;
    const int red = ((j + 1) & 3) * 32 + h;
    const int rho = 4 * (8 * j + w) + 2 * (g >> 1) + ((n >> 2) & 1);
    const unsigned pst_off = n < 8 ? (unsigned)(rho >> 1) * 16384u + (unsigned)(rho & 1) * 4096u + (unsigned)((h ^ rho) & 31) * 128u + (unsigned)((g & 1) * 64 + (n & 3) * 16) : OOB;
    const unsigned mem_off = lane < 8 ? (unsigned)(4 * ((n >> 1) & 1) + 2 * (n >> 2) + (n & 1)) * 16u : OOB;
    const unsigned pw0 = win_off(W_PART(0)), pw1 = win_off(W_PART(1)), xw0 = win_off(W_X(0)), xw1 = win_off(W_X(1));
    const unsigned redo = (unsigned)(red >> 1) * 16384u + (unsigned)(red & 1) * 4096u;
    const int vxr = L_X + n * 256;
    const float csc = 0.08838834764831845f * 1.4426950408889634f;
    float ninf = -__builtin_inff(); PIN_V(ninf);
    u32x4 KX[8], VWX[2];
    const unsigned x_src = (unsigned)(8 * (w & 3) + g) * 256u;
    auto dma_x = [&](int s) {
        if (ROLE == 0 && w < 4) {
            if ((s & 3) == 0) { wait_count(ctl, C_KV + (s >> 2) * 512 + h * 16, 16u); asm volatile("" ::: "memory"); }
#pragma unroll
            for (int i = 0; i < 2; ++i) {
                const unsigned vo = x_src + (unsigned)((n ^ ((8 * w + 4 * i + g) & 15)) * 16), so = (unsigned)(32 * s + 4 * i) * 256u;
                __builtin_amdgcn_raw_ptr_buffer_load_lds(qr, (LAS void*)(lds + L_X + (s & 1) * 16384 + (w * 2 + i) * 1024), 16, vo, so + 32u * XH, 0, 16);
                __builtin_amdgcn_raw_ptr_buffer_load_lds(qr, (LAS void*)(lds + L_X + (s & 1) * 16384 + 8192 + (w * 2 + i) * 1024), 16, vo, so, 0, 16);
            }
        }
    };
    auto load_kx = [&](int s) {
#pragma unroll
        for (int kt = 0; kt < 2; ++kt)
#pragma unroll
            for (int kk = 0; kk < 4; ++kk) KX[kt * 4 + kk] = *(const LAS u32x4*)(lds + vxr + (s & 1) * 16384 + 8192 + kt * 4096 + (((4 * ((j + kk) & 3) + g) ^ n) * 16));
    };
    auto vwx_kt = [&](int s, int kt) {
        bf16x8 VA[4];
#pragma unroll
        for (int kk = 0; kk < 4; ++kk) VA[kk] = *(const LAS bf16x8*)(lds + vxr + (s & 1) * 16384 + kt * 4096 + (((4 * (kk ^ (j & 2)) + g) ^ n) * 16));
#pragma unroll
        for (int ct = 0; ct < 2; ++ct) {
            f32x4 d = {0.f, 0.f, 0.f, 0.f};
#pragma unroll
            for (int kk = 0; kk < 4; ++kk) d = mma32(VA[kk], (kk & 1) ? hi8(WO[ct][kk >> 1]) : lo8(WO[ct][kk >> 1]), d);
            VWX[ct][2 * kt] = pk_bf16(d[0], d[1]); VWX[ct][2 * kt + 1] = pk_bf16(d[2], d[3]);
        }
    };
    auto rmax = [&](float v) { float a = v, b = v; swap16f(a, b); v = max3f(a, b, ninf); a = v; b = v; swap32f(a, b); return max3f(a, b, ninf); };
    auto lo2 = [](f32x4 v) { return (f32x2){v[0], v[1]}; };
    auto hi2 = [](f32x4 v) { return (f32x2){v[2], v[3]}; };
    dma_x(0); dma_x(1);
    asm volatile("s_waitcnt vmcnt(0) lgkmcnt(0)" ::: "memory"); BAR(); asm volatile("" ::: "memory");
    vwx_kt(0, 0); vwx_kt(0, 1);
    if (ROLE == 2) load_kx(0);
    __builtin_amdgcn_s_setprio(2);

    unsigned iv_t0 = 0, iv_acc = 0;
    unsigned spins = 0, pace_c = 0x7fffffffu, pace_t = 0;
    const u32x4 sv = {SENT, SENT, SENT, SENT};
    for (int s = 0;; ++s) {
        unsigned xw = (s & 1) ? xw1 : xw0, pso = (s & 1) ? pw1 : pw0;
        unsigned mso = win_off(W_MEM(s) + (w >> 2)) + (unsigned)(w & 3) * 2048u;
        unsigned rso = ROLE <= 1 ? (xw0 ^ xw1 ^ xw) + (unsigned)rank * 16384u + (unsigned)w * 1024u : (pw0 ^ pw1 ^ pso) + redo + (unsigned)(w & 1) * 2048u;
        unsigned pub = pub_off + (g == j ? (unsigned)s * (128u * 1536u) : xw);
        PIN_S(xw); PIN_S(pso); PIN_S(mso); PIN_S(rso); PIN_V(pub);
        {
            if (KD0) __builtin_amdgcn_s_sleep(KD0);
            const u32x2 pv = *(const LAS u32x2*)(lds + L_PACE);
            unsigned target = __builtin_amdgcn_readfirstlane(pv.x); const bool valid = __builtin_amdgcn_readfirstlane(pv.y) + 1u == (unsigned)s && s > 3;
            unsigned now = (unsigned)__builtin_amdgcn_s_memrealtime();
            if (ROLE == 3 && s > 2) pace_c = min(pace_c, now - pace_t);
            if (valid && (int)(target - now) < 100) { while ((int)(target - now) > 0) now = (unsigned)__builtin_amdgcn_s_memrealtime(); }
        }
        SEG(10);
        f32x4 c[6]; u32x4 a1;
        auto rearm = [&]() {
            __builtin_amdgcn_raw_buffer_store_b128(sv, xr, l16, rso, 16);
            if (ROLE >= 2) __builtin_amdgcn_raw_buffer_store_b128(sv, xr, l16 + 1024, rso, 16);
        };
        u32x4 a0;
        auto ld_a = [&]() { asm volatile("" ::: "memory"); a0 = ld16_sc1(xr, l16, mso); a1 = ld16_sc1(xr, l16 + 1024, mso); };
        auto ok_a = [&]() { return all_set(max(umax4(a0), umax4(a1))); };
        auto mm_a = [&]() {
#pragma unroll
            for (int i = 0; i < 6; ++i) { c[i] = (f32x4){0.f, 0.f, 0.f, 0.f}; PIN_V(c[i]); }
            SCHED();
            c[0] = smma(a0, W[0][0], c[0], sidx); c[1] = smma(a0, W[1][0], c[1], sidx); c[0] = smma(a1, W[0][1], c[0], sidx); c[1] = smma(a1, W[1][1], c[1], sidx);
            c[2] = smma(a0, W[2][0], c[2], sidx); c[3] = smma(a0, W[3][0], c[3], sidx);
        };
        auto put = [&](int i) { *(LAS f32x2*)(lds + redw + ((i >> 1) * RED_M + (i & 1) * 64) * 4) = lo2(c[i]) + hi2(c[i]); };
        ld_a(); SCHED();
        if (KRA == 0) rearm();
        mm_a();
        if (__builtin_expect(!ok_a(), 0)) { bool out; do { RETRY(0); out = spin_out(spins, ctl); ld_a(); } while (!(ok_a() | out)); mm_a(); }
        SEG(0);
        if (KRA == 1) rearm();
        c[2] = smma(a1, W[2][1], c[2], sidx); c[3] = smma(a1, W[3][1], c[3], sidx);
        put(0); put(1);
        SCHED();
        c[4] = smma(a0, W[4][0], c[4], sidx); c[5] = smma(a0, W[5][0], c[5], sidx); c[4] = smma(a1, W[4][1], c[4], sidx);
        put(2); put(3);
        c[5] = smma(a1, W[5][1], c[5], sidx);
        SCHED();
        if (KRA == 3) rearm();
        put(4); put(5);
        SEG(1);
        if (KRA == 2) { SCHED(); rearm(); }
        LDS_BAR();
        SEG(2);
        if (ROLE <= 1) {
            f32x2 v[8];
#pragma unroll
            for (int ww = 0; ww < 8; ++ww) { const LAS float* q = (const LAS float*)(lds + redr + ww * 4096); v[ww] = (f32x2){q[0], q[512]}; }
            const f32x2 t = ((v[0] + v[1]) + (v[2] + v[3])) + ((v[4] + v[5]) + (v[6] + v[7]));
            const u32x4 o = rows_gather(min(pk_bf16(t.x, t.y), SENT - 1));
            __builtin_amdgcn_raw_buffer_store_b128(o, xr, pub, 0, 16);
            *(LAS u32x4*)(lds + L_OWN + w * 256 + n * 16) = o;
        } else {
            if (ROLE == 3 && rank == 0) {
                pace_t = (unsigned)__builtin_amdgcn_s_memrealtime();
                if (lane == 0) { __hip_atomic_store(ctl + C_PROG, (unsigned)s, RLX_AGENT);
                    __hip_atomic_store((unsigned long long*)(ctl + C_PACE), (unsigned long long)(pace_t + pace_c + KPM) | ((unsigned long long)s << 32), RLX_AGENT); }
            }
        }
        SEG(3);
        if (s == NSEG - 1) break;
        LDS_BAR();
        SEG(13);
        unsigned VWM[2];
        auto vwm_pack = [&](const f32x4 (&dv)[2]) {
#pragma unroll
            for (int ct = 0; ct < 2; ++ct) { const f32x2 t = lo2(dv[ct]) + hi2(dv[ct]); VWM[ct] = pk_bf16(t.x, t.y); PIN_V(VWM[ct]); }
        };
        f32x4 dv[2] = {{0.f, 0.f, 0.f, 0.f}, {0.f, 0.f, 0.f, 0.f}}, s0, s1;
        PIN_V(dv[0]); PIN_V(dv[1]);
        u32x4 v0, v1;
        if (ROLE == 0) {
            if (KDM == 1) { WAIT_V(0); dma_x(min(s + 2, NSEG - 2)); }
            else if (KDM == 2) { dma_x(min(s + 2, NSEG - 2)); if (w < 4) WAIT_V(4); else WAIT_V(0); }
            else WAIT_V(0);
        } else {
            unsigned cw = xw + (unsigned)rank * 16384u, c1 = cw + (unsigned)((j + 1) & 3) * 1536u, c2 = cw + (unsigned)((j + 2) & 3) * 1536u, c3 = cw + (unsigned)((j + 3) & 3) * 1536u;
            PIN_S(cw); PIN_S(c1); PIN_S(c2); PIN_S(c3);
            const unsigned xl = ROLE == 2 ? (l16 & ~128u) : l16;
            u32x4 vo;
            f32x4 e0 = {0.f, 0.f, 0.f, 0.f}, e1 = e0;
            if (ROLE == 1) { vo = *(const LAS u32x4*)(lds + L_OWN + 1024 + (lane & 31) * 16); if (!vown) vo = (u32x4){0u, 0u, 0u, 0u}; PIN_V(vo); }
            else {
                const u32x4 X0 = *(const LAS u32x4*)(lds + L_OWN + xl);
                if (ROLE == 2) { e0 = mma32(asbf(KX[0]), asbf(X0), e0); e1 = mma32(asbf(KX[4]), asbf(X0), e1); } else e0 = mma32(asbf(X0), asbf(X0), e0);
                PIN_V(e0); PIN_V(e1);
            }
            if (KD1) __builtin_amdgcn_s_sleep(KD1);
            u32x4 X1, X2, X3;
            auto ld_b = [&]() {
                asm volatile("" ::: "memory");
                if (ROLE >= 2) { X1 = ld16_sc1(xr, xl, c1); X2 = ld16_sc1(xr, xl, c2); X3 = ld16_sc1(xr, xl, c3); }
                else { v0 = ld16_sc1(xr, vg0, cw); v1 = ld16_sc1(xr, vg1, cw); }
            };
            auto ok_b = [&]() { return all_set(ROLE >= 2 ? max(umax4(X1), max(umax4(X2), umax4(X3))) : max(umax4(v0), umax4(v1))); };
            auto mm_b = [&]() {
                if (ROLE == 2) {
                    s0 = mma32(asbf(KX[1]), asbf(X1), e0); s1 = mma32(asbf(KX[5]), asbf(X1), e1);
                    s0 = mma32(asbf(KX[2]), asbf(X2), s0); s1 = mma32(asbf(KX[6]), asbf(X2), s1);
                    s0 = mma32(asbf(KX[3]), asbf(X3), s0); s1 = mma32(asbf(KX[7]), asbf(X3), s1);
                } else if (ROLE == 3) {
                    s0 = mma32(asbf(X1), asbf(X1), e0); s1 = mma32(asbf(X2), asbf(X2), e1); s0 = mma32(asbf(X3), asbf(X3), s0);
                } else {
                    v0 |= vo;
                    *(LAS u32x4*)(lds + L_VM + lane * 16) = v0; *(LAS u32x4*)(lds + L_VM + 1024 + lane * 16) = v1;
                }
            };
            SEG(11);
            ld_b(); SCHED(); mm_b();
            if (__builtin_expect(!ok_b(), 0)) { bool out; do { RETRY(1); out = spin_out(spins, ctl); ld_b(); } while (!(ok_b() | out)); mm_b(); }
            SEG(4);
        }
        float m;
        f32x4 sc;
        if (ROLE >= 2) {
            sc = ROLE == 2 ? (n < 8 ? s0 : s1) : s0 + s1;
            if (ROLE == 3 && g < 2) sc = (f32x4){ninf, ninf, ninf, ninf};
            m = max3f(max3f(sc[0], sc[1], sc[2]), sc[3], ninf);
            if (ROLE == 2) m = max3f(m, ror8(m), ninf);
            m = rmax(m);
            PIN_V(m);
        }
        LDS_BAR();
        if (ROLE != 1) { v0 = *(const LAS u32x4*)(lds + L_VM + lane * 16); v1 = *(const LAS u32x4*)(lds + L_VM + 1024 + lane * 16); }
        if (ROLE >= 2) {
            const float mc = -m * csc;
#pragma unroll
            for (int i = 0; i < 4; ++i) sc[i] = __builtin_amdgcn_exp2f(fmaf(sc[i], csc, mc));
            *(LAS u32x2*)(lds + (ROLE == 2 ? L_PX + (g * 8 + (n & 7)) * 16 + (n >> 3) * 8 : L_PM + (g < 2 ? 256 : 0) + n * 16 + (g & 1) * 8)) = (u32x2){pk_bf16(sc[0], sc[1]), pk_bf16(sc[2], sc[3])};
            SCHED();
            dv[0] = smma(v0, WO[0][0], dv[0], sidx); dv[1] = smma(v0, WO[1][0], dv[1], sidx);
            float l = (sc[0] + sc[1]) + (sc[2] + sc[3]);
            if (ROLE == 2) l += ror8(l);
            dv[0] = smma(v1, WO[0][1], dv[0], sidx); dv[1] = smma(v1, WO[1][1], dv[1], sidx);
            l = rows_sum(l);
            *(LAS f32x2*)(lds + L_ML + n * 16 + (ROLE == 2 ? 0 : 8)) = (f32x2){mc, l};
        } else {
#pragma unroll
            for (int ct = 0; ct < 2; ++ct) { dv[ct] = smma(v0, WO[ct][0], dv[ct], sidx); dv[ct] = smma(v1, WO[ct][1], dv[ct], sidx); }
        }
        vwm_pack(dv);
        SEG(5);
        u32x4 am0 = {VWM[0], 0u, 0u, 0u}, am1 = {VWM[1], 0u, 0u, 0u}, pm = {0u, 0u, 0u, 0u};
        PIN_V(am0); PIN_V(am1); PIN_V(pm);
        LDS_BAR();
        SEG(6);
        {
            const f32x4 ml = *(const LAS f32x4*)(lds + L_ML + n * 16);
            const u32x4 px = *(const LAS u32x4*)(lds + L_PX + (g * 8 + (n & 7)) * 16);
            pm.x = *(const LAS unsigned*)(lds + L_PM + n * 16 + g * 4);
            const f32x4 z = {0.f, 0.f, 0.f, 0.f};
            const f32x4 dx0 = mma32(asbf(VWX[0]), asbf(px), z), dx1 = mma32(asbf(VWX[1]), asbf(px), z), dm0 = mma32(asbf(am0), asbf(pm), z), dm1 = mma32(asbf(am1), asbf(pm), z);
            const float dl = ml[0] - ml[2];
            const float e = __builtin_amdgcn_exp2f(-__builtin_fabsf(dl));
            const float ex = dl > 0.f ? e : 1.f, em = dl > 0.f ? 1.f : e;
            const float inv = __builtin_amdgcn_rcpf(fmaf(ex, ml[1], em * ml[3]));
            const f32x4 d0 = (dx0 * ex + dm0 * em) * inv, d1 = (dx1 * ex + dm1 * em) * inv;
            const u32x4 o = {min(pk_bf16(d0[0], d0[1]), SENT - 1), min(pk_bf16(d0[2], d0[3]), SENT - 1), min(pk_bf16(d1[0], d1[1]), SENT - 1), min(pk_bf16(d1[2], d1[3]), SENT - 1)};
            __builtin_amdgcn_raw_buffer_store_b128(o, xr, pst_off, pso, 16);
        }
        SCHED();
        SEG(7);
        __builtin_amdgcn_s_setprio(ROLE == 3 ? 1 : 0);
        if (KDM == 3 && s + 2 < NSEG - 1) dma_x(s + 2);
        vwx_kt(s + 1, 0); vwx_kt(s + 1, 1);
        SCHED();
        if (ROLE == 3) __builtin_amdgcn_s_setprio(2);
        if (ROLE == 3) {
            u32x4 SEL;
            { const unsigned one = (n & 1) ? 0x3f800000u : 0x00003f80u; const int dw = (n & 7) >> 1; SEL = (u32x4){dw == 0 ? one : 0u, dw == 1 ? one : 0u, dw == 2 ? one : 0u, dw == 3 ? one : 0u}; }
            unsigned so = pso + redo, mo = win_off(W_MEM(s + 1) + (red >> 6)) + (unsigned)(red & 63) * 128u;
            PIN_S(so); PIN_S(mo); PIN_V(SEL);
            if (KD2) __builtin_amdgcn_s_sleep(KD2);
            f32x4 ta, tb;
            u32x4 q[4];
            auto ld_c = [&]() {
                asm volatile("" ::: "memory");
#pragma unroll
                for (int i = 0; i < 4; ++i) q[i] = ld16_sc1(xr, l16 + i * 1024, so);
            };
            auto ok_c = [&]() { return all_set(max(max(umax4(q[0]), umax4(q[1])), max(umax4(q[2]), umax4(q[3])))); };
            auto mm_c = [&]() {
                ta = mma32(asbf(SEL), asbf(q[0]), (f32x4){0.f, 0.f, 0.f, 0.f}); tb = mma32(asbf(SEL), asbf(q[1]), (f32x4){0.f, 0.f, 0.f, 0.f});
                ta = mma32(asbf(SEL), asbf(q[2]), ta); tb = mma32(asbf(SEL), asbf(q[3]), tb);
            };
            SEG(12);
            ld_c(); mm_c();
            if (__builtin_expect(!ok_c(), 0)) { bool out; do { RETRY(2); out = spin_out(spins, ctl); ld_c(); } while (!(ok_c() | out)); mm_c(); }
            SEG(8);
            f32x4 t = ta + tb;
#pragma unroll
            for (int i = 0; i < 4; ++i) t[i] += ror8(t[i]);
            unsigned a0 = min(pk_bf16(t[0], t[1]), SENT - 1), a1 = min(pk_bf16(t[2], t[3]), SENT - 1);
            unsigned b0 = a0, b1 = a1; swap16(a0, b0); swap16(a1, b1);
            const u32x4 o = {a0, a1, b0, b1};
            __builtin_amdgcn_raw_buffer_store_b128(o, xr, mem_off, mo, 16);
        } else {
            if (KDM == 0 && s + 2 < NSEG - 1) dma_x(s + 2);
            if (ROLE == 2) load_kx(s + 1);
            if (ROLE == 1) { const unsigned long long pv = __hip_atomic_load((unsigned long long*)(ctl + C_PACE), RLX_AGENT); *(LAS u32x2*)(lds + L_PACE) = (u32x2){(unsigned)pv, (unsigned)(pv >> 32)}; }
        }
        SEG(9);
        if (ROLE != 3) __builtin_amdgcn_s_setprio(2);
        LDS_BAR();
        SEG(14);
    }
    WAIT_V(0);
    __builtin_amdgcn_s_setprio(0);
}

DI void chain_role(const Params& p, int rank, ldsp lds, unsigned long long* stamps) {
    const int tid = fresh_tid(), w = __builtin_amdgcn_readfirstlane(tid >> 6), lane = tid & 63, g = lane >> 4, n = lane & 15;
    const int h = rank >> 2, j = rank & 3;
    const rsrc_t xr = mkrsrc(p.ws + WS_XCH, (unsigned)XCH_BYTES);
    unsigned* ctl = (unsigned*)(p.ws + WS_CTL);
    if (rank == 0 && w == 7) {
        unsigned best0 = ~0u, best1 = ~0u;
        for (int i = 0; i < 12; ++i) {
            const unsigned long long t0 = __builtin_amdgcn_s_memtime();
            __builtin_amdgcn_raw_buffer_store_b32(0u, xr, (unsigned)(2 * 16384 + C_PROBE * 4) + (unsigned)(i & 1) * 8192u + (unsigned)(i >> 1) * 256u, 0, 16);
            asm volatile("s_waitcnt vmcnt(0)" ::: "memory");
            const unsigned dt = (unsigned)(__builtin_amdgcn_s_memtime() - t0);
            if (i & 1) best1 = min(best1, dt); else best0 = min(best0, dt);
        }
        if (lane == 0) __hip_atomic_store(ctl + C_PAR, best1 < best0 ? 1u : 0u, RLX_AGENT);
    }
    bf16x16 W[6][2], WO[2][2];
    {
        ldsp my = lds + w * 16384;
        const int rrow = lane >> 2, rc = (lane & 3) * 8, q4 = n >> 2;
        const int toffo = (8 * g + q4) * 64 + (n & 3) * 8;
        const int toffq = (16 * (g & 1) + 8 * (g >> 1) + 4 * (q4 >> 1) + (q4 & 1)) * 64 + (n & 3) * 8;
#define FRAG(base, st) cat16(cat8(trd((base)), trd((base) + (st))), cat8(trd((base) + 2048), trd((base) + 2048 + (st))))
#pragma unroll
        for (int i = 0; i < 8; ++i) {
            __builtin_amdgcn_global_load_lds((const unsigned*)(p.wq + (size_t)(128 * w + 16 * i + rrow) * 4096 + h * 128 + 32 * j + rc), (LAS unsigned*)(my + i * 1024), 16, 0, 0);
            __builtin_amdgcn_global_load_lds((const unsigned*)(p.wk + (size_t)(128 * w + 16 * i + rrow) * 4096 + h * 128 + 32 * j + rc), (LAS unsigned*)(my + 8192 + i * 1024), 16, 0, 0);
        }
        WAIT_V(0);
#pragma unroll
        for (int T = 0; T < 4; ++T)
#pragma unroll
            for (int t = 0; t < 2; ++t) W[T][t] = FRAG(my + (T >> 1) * 8192 + t * 4096 + (T & 1) * 32 + toffq, 128);
#pragma unroll
        for (int T = 0; T < 4; ++T) asm volatile("" : "+v"(W[T][0]), "+v"(W[T][1]));
        WAIT_L(0);
#pragma unroll
        for (int i = 0; i < 8; ++i) {
            __builtin_amdgcn_global_load_lds((const unsigned*)(p.wv + (size_t)(128 * w + 16 * i + rrow) * 4096 + h * 128 + 32 * j + rc), (LAS unsigned*)(my + i * 1024), 16, 0, 0);
            __builtin_amdgcn_global_load_lds((const unsigned*)(p.wo + (size_t)(h * 128 + 16 * i + rrow) * 1024 + 256 * j + 32 * w + rc), (LAS unsigned*)(my + 8192 + i * 1024), 16, 0, 0);
        }
        WAIT_V(0);
#pragma unroll
        for (int T = 0; T < 2; ++T)
#pragma unroll
            for (int t = 0; t < 2; ++t) { W[4 + T][t] = FRAG(my + t * 4096 + T * 32 + toffq, 128); WO[T][t] = FRAG(my + 8192 + (t ^ (j >> 1)) * 4096 + T * 32 + toffo, 256); }
#pragma unroll
        for (int T = 0; T < 2; ++T) asm volatile("" : "+v"(W[4 + T][0]), "+v"(W[4 + T][1]), "+v"(WO[T][0]), "+v"(WO[T][1]));
        WAIT_L(0);
#undef FRAG
    }
    wait_count(ctl, C_PAR, 1u);
    const unsigned par = __builtin_amdgcn_readfirstlane(__hip_atomic_load(ctl + C_PAR, RLX_AGENT)) & 1u;
    const int red = ((j + 1) & 3) * 32 + h;
    {
        const u32x4 sv = {SENT, SENT, SENT, SENT};
        if (w < 6) {
            __builtin_amdgcn_raw_buffer_store_b128(sv, xr, (unsigned)lane * 16u, win_off(W_X(0)) + (unsigned)rank * 16384u + (unsigned)w * 1024u, 16);
            __builtin_amdgcn_raw_buffer_store_b128(sv, xr, (unsigned)lane * 16u, win_off(W_X(1)) + (unsigned)rank * 16384u + (unsigned)w * 1024u, 16);
        }
        __builtin_amdgcn_raw_buffer_store_b128(sv, xr, (unsigned)lane * 16u, win_off(W_PART(w >> 2)) + (unsigned)(red >> 1) * 16384u + (unsigned)(red & 1) * 4096u + (unsigned)(w & 3) * 1024u, 16);
        const int s = 8 * w + (lane >> 3);
        if (s > 0) __builtin_amdgcn_raw_buffer_store_b128(sv, xr, win_off(W_MEM(s) + (red >> 6)) + (unsigned)(red & 63) * 128u + (unsigned)(lane & 7) * 16u, 0, 16);
    }
    WAIT_V(0);
    BAR();
    if (w == 7 && lane < 8) {
        const unsigned* src = (const unsigned*)(p.mem0 + (4 * (red & 1) + 2 * (lane >> 2) + (lane & 1)) * DM + 32 * (red >> 2) + 8 * ((red >> 1) & 1) + 4 * ((lane >> 1) & 1));
        const u32x4 v = {min(src[0], SENT - 1), min(src[1], SENT - 1), min(src[8], SENT - 1), min(src[9], SENT - 1)};
        __builtin_amdgcn_raw_buffer_store_b128(v, xr, (unsigned)lane * 16u + (unsigned)(red & 63) * 128u, win_off(W_MEM(0) + (red >> 6)), 16);
    }
    if (w < 5) chain_loop<0>(p, par, rank, w, lds, W, WO, stamps);
    else if (w == 5) chain_loop<1>(p, par, rank, 5, lds, W, WO, stamps);
    else if (w == 6) chain_loop<2>(p, par, rank, 6, lds, W, WO, stamps);
    else chain_loop<3>(p, par, rank, 7, lds, W, WO, stamps);
}

template <class Wait>
DI void attn_a_item(const Params& p, int s, int h, ldsp my, int lane, const Wait& wait_mem) {
    const int g = lane >> 4, n = lane & 15;
    const rsrc_t xr = mkrsrc(p.ws + WS_XCH, (unsigned)XCH_BYTES);
    const rsrc_t qr = mkrsrc(p.ws + WS_QKVX + (size_t)h * XH + (size_t)s * 8192, 65u * XH);
#pragma unroll
    for (int i = 0; i < 8; ++i)
        __builtin_amdgcn_raw_ptr_buffer_load_lds(qr, (LAS void*)(my + i * 1024), 16, (unsigned)g * 256u + (unsigned)((n ^ (((4 * (i & 1) + g) & 7) << 1)) * 16), 64u * XH + (unsigned)i * 1024u, 0, 16);
    u32x4 Q[2][4], K[2][4], XM[4];
    const unsigned ro = (unsigned)n * 256u + (unsigned)g * 16u;
#pragma unroll
    for (int t = 0; t < 2; ++t)
#pragma unroll
        for (int k = 0; k < 4; ++k) { Q[t][k] = ld16_sc1(qr, ro + k * 64, (unsigned)t * 4096u); K[t][k] = ld16_sc1(qr, ro + k * 64, (unsigned)t * 4096u + 32u * XH); }
    wait_mem();
    const unsigned qwin = XCH_KM + (unsigned)(s * 32 + h) * 6144u;
    u32x4 VM[2];
#pragma unroll
    for (int k = 0; k < 4; ++k) XM[k] = ld16_sc1(xr, (unsigned)lane * 16u + (k & 1) * 1536, qwin + (k >> 1) * 3072);
#pragma unroll
    for (int ts = 0; ts < 2; ++ts) VM[ts] = ld16_sc1(xr, (unsigned)(g >> 1) * 1536u + 1024u + (unsigned)(lane & 31) * 16u, qwin + ts * 3072);
#pragma unroll
    for (int ts = 0; ts < 2; ++ts) {
        const u32x4 v = VM[ts];
        const int m = 2 * (n >> 2) + (n & 1), hf = (n >> 1) & 1, row = 32 + m;
#pragma unroll
        for (int i = 0; i < 4; ++i) { const int e = 64 * ts + 16 * g + 4 * i + 2 * hf;
            *(LAS unsigned*)(my + row * 256 + (((e >> 3) ^ ((row & 7) << 1)) * 16) + (e & 7) * 2) = v[i]; }
    }
    const float csc = 0.08838834764831845f * 1.4426950408889634f, ninf = -__builtin_inff();
    u32x4 PX[2]; u32x2 PM[2];
#pragma unroll
    for (int qt = 0; qt < 2; ++qt) {
        f32x4 sm = {0.f, 0.f, 0.f, 0.f}, s0 = sm, s1 = sm;
#pragma unroll
        for (int k = 0; k < 4; ++k) { sm = mma32(asbf(XM[k]), asbf(Q[qt][k]), sm); s0 = mma32(asbf(K[0][k]), asbf(Q[qt][k]), s0); if (qt == 1) s1 = mma32(asbf(K[1][k]), asbf(Q[qt][k]), s1); }
#pragma unroll
        for (int i = 0; i < 4; ++i) {
            if (g < 2) sm[i] = ninf;
            if (qt == 0) { if (4 * g + i > n) s0[i] = ninf; s1[i] = ninf; }
            else if (4 * g + i > n) s1[i] = ninf;
        }
        float m = fmaxf(fmaxf(fmaxf(s0[0], s0[1]), fmaxf(s0[2], s0[3])), fmaxf(fmaxf(s1[0], s1[1]), fmaxf(s1[2], s1[3])));
        m = fmaxf(m, fmaxf(fmaxf(sm[0], sm[1]), fmaxf(sm[2], sm[3])));
        m = rows_max(m);
        const float mc = -m * csc; float sum = 0.f;
#pragma unroll
        for (int i = 0; i < 4; ++i) { s0[i] = __builtin_amdgcn_exp2f(fmaf(s0[i], csc, mc)); s1[i] = __builtin_amdgcn_exp2f(fmaf(s1[i], csc, mc)); sm[i] = __builtin_amdgcn_exp2f(fmaf(sm[i], csc, mc));
            sum += (s0[i] + s1[i]) + sm[i]; }
        sum = rows_sum(sum);
        const float inv = __builtin_amdgcn_rcpf(sum);
        PX[qt] = (u32x4){pk_bf16(s0[0] * inv, s0[1] * inv), pk_bf16(s0[2] * inv, s0[3] * inv), pk_bf16(s1[0] * inv, s1[1] * inv), pk_bf16(s1[2] * inv, s1[3] * inv)};
        PM[qt] = (u32x2){pk_bf16(sm[0] * inv, sm[1] * inv), pk_bf16(sm[2] * inv, sm[3] * inv)};
    }
    asm volatile("s_waitcnt vmcnt(0) lgkmcnt(0)" ::: "memory");
    const int q4 = n >> 2, p4 = n & 3;
    const int r0 = 4 * g + q4, rm = 32 + 4 * (g & 1) + q4;
    const rsrc_t orr = mkrsrc(p.ws + WS_OA + (size_t)((s >> 2) * 64 + 2 * h) * 16384 + (size_t)(s & 3) * 4096, 32768u);
    const unsigned oo = (unsigned)(n * 128 + (g & 1) * 32 + (g >> 1) * 16);
#pragma unroll
    for (int ep = 0; ep < 4; ++ep) {
        f32x4 d[2][2];
#pragma unroll
        for (int e2 = 0; e2 < 2; ++e2) {
            const int ch = (2 * ep + e2) * 2 + (p4 >> 1);
            const bf16x8 va = cat8(trd(my + r0 * 256 + ((ch ^ ((r0 & 7) << 1)) * 16) + (p4 & 1) * 8), trd(my + (r0 + 16) * 256 + ((ch ^ ((r0 & 7) << 1)) * 16) + (p4 & 1) * 8));
            const s16x4 vm = trd(my + rm * 256 + ((ch ^ ((rm & 7) << 1)) * 16) + (p4 & 1) * 8);
#pragma unroll
            for (int qt = 0; qt < 2; ++qt) { f32x4 c = {0.f, 0.f, 0.f, 0.f}; c = mma32(va, asbf(PX[qt]), c); d[qt][e2] = mma16(__builtin_bit_cast(u32x2, vm), PM[qt], c); }
        }
#pragma unroll
        for (int qt = 0; qt < 2; ++qt) store_pair(orr, oo + (ep & 1) * 64 + qt * 2048, (unsigned)(ep >> 1) * 16384u, d[qt][0], d[qt][1], true);
    }
    asm volatile("s_waitcnt lgkmcnt(0)" ::: "memory");
}

DI void attn_b_item(const Params& p, int hq, int qb, ldsp lds) {
    const int tid = fresh_tid(), w = __builtin_amdgcn_readfirstlane(tid >> 6), lane = tid & 63, g = lane >> 4, n = lane & 15;
    const int q0 = qb * 128;
    const rsrc_t br = mkrsrc(p.ws + WS_QKVB, 96u * BH);
    const rsrc_t orr = mkrsrc(p.ws + WS_OB + (size_t)qb * 16 * 16384, 16u * 16384u);
    constexpr int BUF = 57344;
    unsigned ko[3], vo[3];
#pragma unroll
    for (int i = 0; i < 3; ++i) { const int r = (w * 3 + i) * 16 + (lane >> 2), c = lane & 3; const unsigned rp = (unsigned)max(q0 - 256 + r, 0) * 64u;
        ko[i] = rp + 32u * BH + (unsigned)((c ^ (((r >> 3) & 1) * 3)) * 16); vo[i] = rp + 64u * BH + (unsigned)((c ^ (((r >> 2) & 1) << 1)) * 16); }
    const unsigned qo = (unsigned)(q0 + 16 * w + (lane >> 2)) * 64u + (unsigned)(((lane & 3) ^ (((lane >> 5) & 1) * 3)) * 16);
    auto stage = [&](int i) {
        const unsigned so = (unsigned)(hq * 4 + i) * BH; ldsp d = lds + (i & 1) * BUF;
#pragma unroll
        for (int k = 0; k < 3; ++k) {
            __builtin_amdgcn_raw_ptr_buffer_load_lds(br, (LAS void*)(d + (w * 3 + k) * 1024), 16, ko[k], so, 0, 16);
            __builtin_amdgcn_raw_ptr_buffer_load_lds(br, (LAS void*)(d + 24576 + (w * 3 + k) * 1024), 16, vo[k], so, 0, 16);
        }
        __builtin_amdgcn_raw_ptr_buffer_load_lds(br, (LAS void*)(d + 49152 + w * 1024), 16, qo, so, 0, 16);
    };
    const float ninf = -__builtin_inff(), csc = 0.17677669529663687f * 1.4426950408889634f;
    const int tmin = (256 - q0 - 16 * w) >> 4;
    const int q4 = n >> 2, p4 = n & 3, sw = (g & 1) << 1;
    const int kq = (16 * w + n) * 64 + ((g ^ (((n >> 3) & 1) * 3)) * 16);
    const unsigned vb = (unsigned)(size_t)lds + 24576u + (unsigned)((16 * w + 4 * g + q4) * 64 + (p4 & 1) * 8);
    stage(0);
#pragma unroll 1
    for (int i = 0; i < 4; ++i) {
        if (i < 3) { stage(i + 1); WAIT_V(7); } else WAIT_V(0);
        BAR(); asm volatile("" ::: "memory");
        const ldsp bp = lds + (i & 1) * BUF;
        const bf16x8 Q = *(const LAS bf16x8*)(bp + 49152 + kq);
        f32x4 sc[17];
#pragma unroll
        for (int t = 0; t < 17; ++t) sc[t] = mma32(*(const LAS bf16x8*)(bp + kq + t * 1024), Q, (f32x4){0.f, 0.f, 0.f, 0.f});
#pragma unroll
        for (int k = 0; k < 4; ++k) { if (4 * g + k <= n) sc[0][k] = ninf; if (4 * g + k > n) sc[16][k] = ninf; }
        if (tmin > 0) {
#pragma unroll
            for (int t = 0; t < 16; ++t) if (t < tmin) sc[t] = (f32x4){ninf, ninf, ninf, ninf};
        }
        float m = max3f(sc[16][0], sc[16][1], max3f(sc[16][2], sc[16][3], ninf));
#pragma unroll
        for (int t = 0; t < 16; ++t) m = max3f(max3f(m, sc[t][0], sc[t][1]), sc[t][2], sc[t][3]);
        m = rows_max(m);
        const float mc = -m * csc;
        f32x4 sum4 = {0.f, 0.f, 0.f, 0.f};
#pragma unroll
        for (int t = 0; t < 17; ++t) {
#pragma unroll
            for (int k = 0; k < 4; ++k) sc[t][k] = __builtin_amdgcn_exp2f(fmaf(sc[t][k], csc, mc));
            sum4 += sc[t];
        }
        const float inv = __builtin_amdgcn_rcpf(rows_sum((sum4[0] + sum4[1]) + (sum4[2] + sum4[3])));
        const unsigned v0 = vb + (unsigned)((i & 1) * BUF) + (unsigned)(((p4 >> 1) ^ sw) * 16), v1 = vb + (unsigned)((i & 1) * BUF) + (unsigned)(((2 + (p4 >> 1)) ^ sw) * 16);
        s16x4 V0[17], V1[17];
#define VRD(t) TRD(V0[t], v0, (t) * 1024); TRD(V1[t], v1, (t) * 1024)
        VRD(0); VRD(1); VRD(2); VRD(3); VRD(4); VRD(5); VRD(6); VRD(7); VRD(8); VRD(9); VRD(10); VRD(11); VRD(12); VRD(13); VRD(14); VRD(15); VRD(16);
#undef VRD
        u32x2 P[17];
#pragma unroll
        for (int t = 0; t < 17; ++t) P[t] = (u32x2){pk_bf16(sc[t][0], sc[t][1]), pk_bf16(sc[t][2], sc[t][3])};
        WAIT_L(0); SCHED();
        f32x4 d0 = {0.f, 0.f, 0.f, 0.f}, d1 = d0;
#pragma unroll
        for (int tp = 0; tp < 8; ++tp) {
            const bf16x8 PP = asbf((u32x4){P[2 * tp].x, P[2 * tp].y, P[2 * tp + 1].x, P[2 * tp + 1].y});
            d0 = mma32(cat8(V0[2 * tp], V0[2 * tp + 1]), PP, d0); d1 = mma32(cat8(V1[2 * tp], V1[2 * tp + 1]), PP, d1);
        }
        d0 = mma16(__builtin_bit_cast(u32x2, V0[16]), P[16], d0); d1 = mma16(__builtin_bit_cast(u32x2, V1[16]), P[16], d1);
        BAR();
        store_pair(orr, (unsigned)((16 * w + n) * 128 + (g & 1) * 32 + (g >> 1) * 16), (unsigned)(hq * 2 + (i >> 1)) * 16384u + (unsigned)(i & 1) * 64u, d0 * inv, d1 * inv, true);
    }
    WAIT_V(0);
    signal((unsigned*)(p.ws + WS_CTL), C_OB + qb * 32, 1);
}

DI void out_tail(const Params& p, int c, int q, ldsp lds) {
    const int tid = fresh_tid(), w = __builtin_amdgcn_readfirstlane(tid >> 6), lane = tid & 63, g = lane >> 4, n = lane & 15;
    unsigned* ctl = (unsigned*)(p.ws + WS_CTL);
    bf16x8 WT[16][2];
    {
        ldsp my = lds + w * 16384;
        const int toff = (8 * g + (n >> 2)) * 64 + (n & 3) * 8;
#pragma unroll
        for (int r = 0; r < 2; ++r) {
#pragma unroll
            for (int i = 0; i < 16; ++i)
                __builtin_amdgcn_global_load_lds((const unsigned*)(p.wo + (size_t)(512 * w + 256 * r + 16 * i + (lane >> 2)) * 1024 + 32 * c + (lane & 3) * 8), (LAS unsigned*)(my + i * 1024), 16, 0, 0);
            WAIT_V(0);
#pragma unroll
            for (int k = 0; k < 8; ++k)
#pragma unroll
                for (int ct = 0; ct < 2; ++ct) { WT[8 * r + k][ct] = cat8(trd(my + k * 2048 + ct * 32 + toff), trd(my + k * 2048 + ct * 32 + toff + 256)); }
#pragma unroll
            for (int k = 0; k < 8; ++k) asm volatile("" : "+v"(WT[8 * r + k][0]), "+v"(WT[8 * r + k][1]));
            WAIT_L(0);
        }
    }
    BAR();
#pragma unroll 1
    for (int hs = q; hs < 16; hs += 3) {
        const int s = 56 + (hs >> 1), rt = hs & 1;
        if (w == 0) wait_count<2>(ctl, C_OS + (s - 56) * 128 + (c >> 3) * 32, 32u);
        asm volatile("" ::: "memory"); BAR(); asm volatile("" ::: "memory");
        const rsrc_t ar = mkrsrc(p.ws + WS_OA + (size_t)((s >> 2) * 64 + 8 * w) * 16384 + (size_t)(s & 3) * 4096 + (size_t)rt * 2048, 8u * 16384u);
        const unsigned ao = (unsigned)n * 128u + (unsigned)g * 16u;
        u32x4 A[16];
#pragma unroll
        for (int k = 0; k < 16; ++k) A[k] = ld16_sc1(ar, ao + (k & 1) * 64, (unsigned)(k >> 1) * 16384u);
#pragma unroll
        for (int ct = 0; ct < 2; ++ct) {
            f32x4 acc = {0.f, 0.f, 0.f, 0.f};
#pragma unroll
            for (int k = 0; k < 16; ++k) acc = mma32(WT[k][ct], asbf(A[k]), acc);
            *(LAS f32x4*)(lds + w * 2048 + ct * 1024 + lane * 16) = acc;
        }
        LDS_BAR();
        if (w < 2) {
            f32x4 t = {0.f, 0.f, 0.f, 0.f};
#pragma unroll
            for (int ww = 0; ww < 8; ++ww) t += *(const LAS f32x4*)(lds + ww * 2048 + w * 1024 + lane * 16);
            *(u32x2*)(p.out + (size_t)(32 * s + 16 * rt + n) * 2048 + 32 * c + 16 * w + 4 * g) = (u32x2){pk_bf16(t[0], t[1]), pk_bf16(t[2], t[3])};
        }
        WAIT_L(0);
    }
    BAR();
}

DI void big_unit(const Params& p, int id, Unit& u) {
    u.lda = DM * 2; u.nt = 16; u.ak = 128; u.ah = 128 * DM * 2;
    if (id < 384) {
        const int pm = id & 7, pn = id >> 3;
        const u16 *w0 = p.wq, *w1 = p.wk, *w2 = p.wv; const u16* W = pn < 16 ? w0 : (pn < 32 ? w1 : w2);
        u.A = (const char*)(p.ax + (size_t)pm * 256 * DM); u.B = (const char*)(W + (pn & 15) * 256); u.ldb = 4096 * 2;
        u.C = p.ws + WS_QKVX + (size_t)(2 * pn) * XH + (size_t)pm * 65536; u.ldc = 256; u.cw = 64; u.ch = XH;
        if (pn < 16) { u.sig = C_Q + pm * 32; u.nsig = 1; } else { u.sig = C_KV + 2 * pm * 512 + (pn & 15) * 32; u.nsig = 4; }
    } else if (id < 480) {
        const int pm = (id - 384) & 7, pn = (id - 384) >> 3;
        u.A = (const char*)(p.bx + (size_t)pm * 256 * DM); u.B = (const char*)(p.bwqkv + pn * 256); u.ldb = LDB3 * 2;
        u.C = p.ws + WS_QKVB + (size_t)(8 * pn) * BH + (size_t)pm * 16384; u.ldc = 64; u.cw = BH; u.ch = 4 * BH; u.sig = C_B + pm * 32; u.nsig = 1;
    } else {
        const bool isb = id < 512; const int q = isb ? id - 480 : id - 512, pm = q >> 2, pn = q & 3;
        const u16 *w0 = p.wo, *w1 = p.bwo;
        u.nt = isb ? 16 : 64; u.lda = 128; u.ak = 16384; u.ah = (unsigned)u.nt * 16384u;
        u.A = p.ws + (isb ? WS_OB : WS_OA) + (size_t)pm * 2 * u.ah; u.B = (const char*)((isb ? w1 : w0) + pn * 256); u.ldb = DM * 2;
        u.C = (char*)(p.out + (size_t)pm * 256 * 2048 + (isb ? 1024 : 0) + pn * 256); u.ldc = 4096; u.cw = 64; u.ch = 256; u.sig = 0; u.nsig = 0;
    }
}
struct BigSched { const Params& p; int id0, id1; DI bool next(int i, Unit& u) const { const int id = i == 0 ? id0 : (i == 1 ? id1 : -1); if (id < 0) return false; big_unit(p, id, u); return true; } };

__global__ __launch_bounds__(512, 2) void k_fused(Params p) {
    extern __shared__ __attribute__((aligned(16))) unsigned char shm[];
    ldsp lds = (ldsp)shm;
    unsigned* ctl = (unsigned*)(p.ws + WS_CTL);
    const int b = blockIdx.x, id = (b & 3) * 32 + (b >> 3);
#define WSTAMP(code)
    WSTAMP(1);
    if ((b & 7) < 4) {
        chain_role(p, id, lds, (unsigned long long*)(p.ws + WS_END));
        BAR();
        if (threadIdx.x < 64) signal(ctl, C_DONE + (id >> 5) * 32, 1);
        WSTAMP(2);
    } else {
#pragma unroll 1
        for (int r = 0; r < 2; ++r) {
            const int rb = 2 * r + (id & 1), ct = id >> 1;
            const u16 *w1 = p.wk, *w2 = p.wv;
            Unit u; u.A = (const char*)(p.ax + (size_t)rb * 128 * DM); u.lda = DM * 2; u.B = (const char*)((ct < 32 ? w1 : w2) + (ct & 31) * 128); u.ldb = 4096 * 2; u.nt = 16;
            u.C = p.ws + WS_QKVX + (size_t)(32 + ct) * XH + (size_t)rb * 32768; u.ldc = 256; u.cw = 64; u.ch = 0; u.ak = 128; u.ah = 0;
            u.sig = C_KV + rb * 512 + (ct & 31) * 16; u.nsig = 1;
            gemm_small(lds, u, ctl);
            WSTAMP(3);
        }
        {
            const int t = fresh_tid(), wv = __builtin_amdgcn_readfirstlane(t >> 6);
            const char* src; const int c = id & 7, q = id >> 3;
            if (c < 2) src = (const char*)p.ax + (size_t)(q * 2 + c) * 131072; else if (c < 4) src = (const char*)p.bx + (size_t)(q * 2 + c - 2) * 131072;
            else if (c < 7) src = (const char*)p.bwqkv + (size_t)(q * 3 + c - 4) * 131072; else src = (const char*)p.bwo + (size_t)q * 131072;
#pragma unroll
            for (int i = 0; i < 16; ++i) __builtin_amdgcn_global_load_lds((const unsigned*)(src + i * 8192 + t * 16), (LAS unsigned*)(lds + 132096 + wv * 1024), 16, 0, 0);
        }
    }
    volatile LAS unsigned* qw = (volatile LAS unsigned*)(lds + 131072);
    const int w = __builtin_amdgcn_readfirstlane(threadIdx.x >> 6);
    const int k = b & 3;
    bool fixed = (b & 7) >= 4;
#pragma unroll 1
    for (;;) {
        int kind, x, it = -1, x2 = -1;
        if (fixed) { const int i = id & 31; kind = 6; x = i < 24 ? (16 + 8 * k + (i & 7)) * 8 + 2 + (i >> 3) : (4 * k + (i & 3)) * 8 + ((i >> 2) & 1); x2 = x + (i < 24 ? 3 : 2); fixed = false; }
        else {
        if (threadIdx.x == 0) qw[0] = __hip_atomic_fetch_add(ctl + C_HEAD + k * 32, 1u, RLX_AGENT) + 1u;
        LDS_BAR();
        it = (int)__builtin_amdgcn_readfirstlane(qw[0]);
        LDS_BAR();
        if (it < 4) { kind = 0; x = it * 4 + k; }
        else if (it < 28) { kind = 6; x = 384 + 24 * k + it - 4; }
        else if (it < 30) { kind = 6; x = 512 + (it - 28) * 4 + k; }
        else if (it < 38) { kind = 6; x = (4 * k + ((it - 30) & 3)) * 8 + 4 + ((it - 30) >> 2); }
        else if (it < 42) { kind = 0; x = (it - 38 + 4) * 4 + k; }
        else if (it < 74) { kind = 1; x = ((it - 42) >> 1) * 8 + 2 * k + ((it - 42) & 1); }
        else if (it < 82) { kind = 6; x = (4 * k + ((it - 74) & 3)) * 8 + 6 + ((it - 74) >> 2); }
        else if (it < 84) { kind = 0; x = (it - 82 + 8) * 4 + k; }
        else if (it < 92) { kind = 2; x = (4 + ((it - 84) >> 1)) * 8 + 2 * k + ((it - 84) & 1); }
        else if (it < 100) { kind = 6; x = 480 + (it - 92) * 4 + k; }
        else if (it < 104) { kind = 4; x = (it - 100) * 4 + k; }
        else if (it < 116) { kind = 2; x = (8 + ((it - 104) >> 1)) * 8 + 2 * k + ((it - 104) & 1); }
        else if (it < 140) { kind = 5; x = ((it - 116) >> 3) * 32 + 8 * k + ((it - 116) & 7); }
        else break;
        }
        if (kind == 6) {
            if (x >= 480) { const int w0 = x < 512 ? C_OB + ((x - 480) >> 2) * 64 : C_OA + ((x - 512) >> 2) * 64; const unsigned need = x < 512 ? 64u : 32u; wg_wait(ctl, w0, need, w0 + 32, need); }
            BigSched S{p, x, x2};
            gemm_stream(lds, S, ctl);
        } else
        if (kind == 0) {
            const int s0 = (x >> 2) * 4;
            wait_count(ctl, C_Q + (s0 >> 3) * 32, 128u); wait_count(ctl, C_PROG, (unsigned)s0 + 5u);
            asm volatile("" ::: "memory");
#pragma unroll 1
            for (int s = s0; s < s0 + 4; ++s) attn_a_item(p, s, (x & 3) * 8 + w, lds + w * 10240, fresh_tid() & 63, []() {});
            WAIT_V(0); signal(ctl, C_OA + (x >> 2) * 32, 1);
            BAR();
        } else if (kind == 4) {
#pragma unroll 1
            for (int s = 40 + (x >> 2); s < NSEG; s += 4) {
                wait_count(ctl, C_Q + (s >> 3) * 32, 128u); wait_count(ctl, C_KV + (s >> 2) * 512 + ((x & 3) * 8 + w) * 16, 16u);
                asm volatile("" ::: "memory");
                attn_a_item(p, s, (x & 3) * 8 + w, lds + w * 10240, fresh_tid() & 63, [&]() {
                    if (s < NSEG - 1) wait_count<4>(ctl, C_PROG, (unsigned)s + 2u); else wait_count<2>(ctl, C_DONE + (x & 3) * 32, 32u);
                    asm volatile("" ::: "memory"); });
                WAIT_V(0);
                if (s < 56) signal(ctl, C_OA + (s >> 2) * 32, 1);
                else { const int l = fresh_tid() & 63; if (l < 4) __hip_atomic_fetch_add(ctl + C_OS + (s - 56) * 128 + l * 32, 1u, RLX_AGENT); }
            }
            BAR();
        } else if (kind == 1) {
            const int qb = x >> 3;
            wait_count(ctl, C_B + (qb >> 1) * 32, 96u); wait_count(ctl, C_B + max((qb >> 1) - 1, 0) * 32, 96u);
            asm volatile("" ::: "memory");
            attn_b_item(p, x & 7, qb, lds);
        } else if (kind == 5) {
            out_tail(p, x & 31, x >> 5, lds);
        } else {
            const int rb = x >> 3, ct = x & 7;
            Unit u; u.ldb = DM * 2; u.ldc = 2048 * 2; u.sig = 0; u.nsig = 0; u.lda = 128; u.ak = 16384; u.ah = 0; u.cw = 64; u.ch = 0;
            u.A = p.ws + WS_OA + (size_t)rb * 64 * 16384; u.B = (const char*)(p.wo + ct * 128); u.nt = 64; u.C = (char*)(p.out + (size_t)rb * 128 * 2048 + ct * 128);
            wg_wait(ctl, C_OA + rb * 32, rb < 10 ? 32u : 128u, C_OA + rb * 32, 32u);
            gemm_small(lds, u, ctl);
        }
        WSTAMP(16 + (kind == 6 && x >= 480 ? (x < 512 ? 7 : 8) : kind) + ((it & 0xffff) << 8));
    }
}

extern "C" void kernel_launch(void* const* d_in, const int* in_sizes, int n_in, void* d_out, int out_size, void* d_ws, size_t ws_size, hipStream_t stream) {
    Params p;
    p.ax = (const u16*)d_in[0]; p.mem0 = (const u16*)d_in[1]; p.wq = (const u16*)d_in[2]; p.wk = (const u16*)d_in[3]; p.wv = (const u16*)d_in[4]; p.wo = (const u16*)d_in[5];
    p.bx = (const u16*)d_in[7]; p.bwqkv = (const u16*)d_in[8]; p.bwo = (const u16*)d_in[9];
    p.out = (u16*)d_out; p.ws = (char*)(((uintptr_t)d_ws + WS_SHIFT(ws_size) + 0x1FFFFFu) & ~(uintptr_t)0x1FFFFFu);
    (void)hipFuncSetAttribute((const void*)k_fused, hipFuncAttributeMaxDynamicSharedMemorySize, LDS_BYTES);
    (void)hipMemsetAsync(p.ws + WS_XCH, 0xFF, (size_t)NWIN_INIT * 16384, stream);
    k_fused<<<256, 512, LDS_BYTES, stream>>>(p);
}
```

```cpp
#include <hip/hip_runtime.h>
#include <stdint.h>
#include <stdlib.h>

#define LAS __attribute__((address_space(3)))
#define DI __device__ __forceinline__
typedef unsigned short u16;
typedef __attribute__((ext_vector_type(8))) __bf16 bf16x8;
typedef __attribute__((ext_vector_type(16))) __bf16 bf16x16;
typedef __attribute__((ext_vector_type(2))) __bf16 bf16x2;
typedef __attribute__((ext_vector_type(4))) short s16x4;
typedef __attribute__((ext_vector_type(8))) short s16x8;
typedef __attribute__((ext_vector_type(16))) short s16x16;
typedef __attribute__((ext_vector_type(2))) float f32x2;
typedef __attribute__((ext_vector_type(4))) float f32x4;
typedef __attribute__((ext_vector_type(2))) unsigned u32x2;
typedef __attribute__((ext_vector_type(4))) unsigned u32x4;
typedef LAS unsigned char* ldsp;
typedef __amdgpu_buffer_rsrc_t rsrc_t;

constexpr int S_LEN = 2048, DM = 1024, NH = 32, DH = 128, NSEG = 64;
constexpr int LDB3 = 3072;
constexpr unsigned XH = S_LEN * 256, BH = S_LEN * 64;
constexpr unsigned SENT = 0xFFFFFFFFu;
constexpr size_t WS_XCH = 2u << 20;
constexpr int NWIN = 640, NWIN_INIT = 7;
constexpr unsigned XCH_KM = NWIN * 16384u;
constexpr size_t XCH_BYTES = (size_t)XCH_KM + 64u * 128u * 1536u;
constexpr size_t WS_CTL = WS_XCH + 2 * 16384;
constexpr int C_TMO = 0, C_PAR = 64, C_HEAD = 3072, C_PROG = 192, C_PACE = 128, C_Q = 256, C_B = C_Q + 8 * 32, C_OA = C_B + 8 * 32, C_OB = C_OA + 16 * 32, C_OS = 16384, C_KV = 4096, C_PROBE = 12288;
constexpr size_t WS_QKVX = 26u << 20;
constexpr size_t WS_QKVB = WS_QKVX + (size_t)96 * XH;
constexpr size_t WS_OA = WS_QKVB + (size_t)96 * BH;
constexpr size_t WS_OB = WS_OA + (size_t)S_LEN * 4096 * 2;
constexpr size_t WS_END = WS_OB + (size_t)S_LEN * 1024 * 2;
#define WS_SHIFT(sz) 0
constexpr int LDS_BYTES = 131072 + 1024 + 8192;

DI unsigned pk_bf16(float a, float b) { f32x2 v = {a, b}; return __builtin_bit_cast(unsigned, __builtin_convertvector(v, bf16x2)); }
DI s16x4 trd(ldsp p) { return __builtin_amdgcn_ds_read_tr16_b64_v4i16((LAS s16x4*)p); }
DI bf16x8 cat8(s16x4 a, s16x4 b) { return __builtin_bit_cast(bf16x8, __builtin_shufflevector(a, b, 0, 1, 2, 3, 4, 5, 6, 7)); }
DI bf16x16 cat16(bf16x8 a, bf16x8 b) { return __builtin_shufflevector(a, b, 0, 1, 2, 3, 4, 5, 6, 7, 8, 9, 10, 11, 12, 13, 14, 15); }
DI bf16x8 lo8(bf16x16 v) { return __builtin_shufflevector(v, v, 0, 1, 2, 3, 4, 5, 6, 7); }
DI bf16x8 hi8(bf16x16 v) { return __builtin_shufflevector(v, v, 8, 9, 10, 11, 12, 13, 14, 15); }
DI bf16x8 asbf(u32x4 v) { return __builtin_bit_cast(bf16x8, v); }
DI f32x4 mma32(bf16x8 a, bf16x8 b, f32x4 c) { return __builtin_amdgcn_mfma_f32_16x16x32_bf16(a, b, c, 0, 0, 0); }
DI f32x4 mma16(u32x2 a, u32x2 b, f32x4 c) { return mma32(asbf((u32x4){a.x, a.y, 0u, 0u}), asbf((u32x4){b.x, b.y, 0u, 0u}), c); }
DI f32x4 smma(u32x4 a, bf16x16 b, f32x4 c, int idx) { return __builtin_amdgcn_smfmac_f32_16x16x64_bf16(asbf(a), b, c, idx, 0, 0); }
DI rsrc_t mkrsrc(const void* p, unsigned bytes) { return __builtin_amdgcn_make_buffer_rsrc((void*)p, 0, bytes, 0x00020000); }
DI u32x4 ld16_sc1(rsrc_t r, unsigned voff, unsigned soff) { return __builtin_amdgcn_raw_buffer_load_b128(r, voff, soff, 16); }
#define ST16(v, r, voff, soff, aux) do { const u32x4 st_v = (v); __builtin_amdgcn_raw_buffer_store_b128(st_v, r, voff, soff, aux); asm volatile("s_nop 1" :: "v"(st_v)); } while (0)
DI void swap16(unsigned& a, unsigned& b) { auto r = __builtin_amdgcn_permlane16_swap(a, b, false, false); a = r[0]; b = r[1]; }
DI void swap32(unsigned& a, unsigned& b) { auto r = __builtin_amdgcn_permlane32_swap(a, b, false, false); a = r[0]; b = r[1]; }
DI void swap16f(float& a, float& b) { unsigned x = __float_as_uint(a), y = __float_as_uint(b); swap16(x, y); a = __uint_as_float(x); b = __uint_as_float(y); }
DI void swap32f(float& a, float& b) { unsigned x = __float_as_uint(a), y = __float_as_uint(b); swap32(x, y); a = __uint_as_float(x); b = __uint_as_float(y); }
DI float rows_max(float v) { float a = v, b = v; swap16f(a, b); v = fmaxf(a, b); a = v; b = v; swap32f(a, b); return fmaxf(a, b); }
DI float rows_sum(float v) { float a = v, b = v; swap16f(a, b); v = a + b; a = v; b = v; swap32f(a, b); return a + b; }
DI unsigned umax4(u32x4 v) { return max(max(v.x, v.y), max(v.z, v.w)); }
#define WAIT_V(n) asm volatile("s_waitcnt vmcnt(" #n ")" ::: "memory")
#define WAIT_L(n) asm volatile("s_waitcnt lgkmcnt(" #n ")" ::: "memory")
#define BAR() __builtin_amdgcn_s_barrier()
#define SCHED() __builtin_amdgcn_sched_barrier(0)
#define LDS_BAR() do { asm volatile("s_waitcnt lgkmcnt(0)" ::: "memory"); __builtin_amdgcn_s_barrier(); asm volatile("" ::: "memory"); } while (0)

constexpr int HTB = 128 * 64 * 2;
DI int lds_byte(int r, int c) { const int st = (r >> 4) * 2 + (c >> 5), rr = r & 15, cc = c & 31, ob = rr * 64 + cc * 2; return st * 1024 + (ob ^ (((ob >> 9) & 1) << 5)); }
DI void stage_rc(int b, int& R, int& C) { const int st = b / 1024, sb = b % 1024, swz = sb ^ (((sb >> 9) & 1) << 5); R = (st >> 1) * 16 + swz / 64; C = (st & 1) * 32 + (swz % 64) / 2; }

struct Params {
    const u16 *ax, *mem0, *wq, *wk, *wv, *wo, *bx, *bwqkv, *bwo;
    u16* out; char* ws;
};
#define RLX_AGENT __ATOMIC_RELAXED, __HIP_MEMORY_SCOPE_AGENT
DI bool spin_out(unsigned& spins, unsigned* ctl) {
    spins = __builtin_amdgcn_readfirstlane(spins) + 1u;
    if ((spins & 63u) == 0u) {
        if (spins > (1u << 18)) { __hip_atomic_store(ctl + C_TMO, 1u, RLX_AGENT); return true; }
        if (__builtin_amdgcn_readfirstlane(__hip_atomic_load(ctl + C_TMO, RLX_AGENT)) != SENT) return true;
    }
    return false;
}
template <int NAP = 8>
DI void wait_count(unsigned* ctl, int word, unsigned need) {
    unsigned spins = 0;
    while (__builtin_amdgcn_readfirstlane(__hip_atomic_load(ctl + word, RLX_AGENT)) + 1u < need) { __builtin_amdgcn_s_sleep(NAP); if (spin_out(spins, ctl)) break; }
}
DI void wg_wait(unsigned* ctl, int word0, unsigned need0, int word1, unsigned need1) {
    if (threadIdx.x < 64) {
        wait_count(ctl, word0, need0); wait_count(ctl, word1, need1);
        __builtin_amdgcn_fence(__ATOMIC_ACQUIRE, "agent");
        asm volatile("s_waitcnt vmcnt(0)" ::: "memory");
    }
    asm volatile("" ::: "memory"); BAR(); asm volatile("" ::: "memory");
}
struct Unit { const char* A; const char* B; char* C; unsigned lda, ldb, ldc, cw, ch, ak, ah; int nt, sig, nsig; };
DI int fresh_tid() { int t = threadIdx.x; asm volatile("" : "+v"(t)); return t; }
DI void signal(unsigned* ctl, int sig, int nsig) { const int lane = fresh_tid() & 63; if (lane < nsig) __hip_atomic_fetch_add(ctl + sig + (lane & 1) * 16 + (lane >> 1) * 512, 1u, RLX_AGENT); }
DI u32x4 pack_pair(f32x4 v0, f32x4 v1) {
    unsigned ax = pk_bf16(v0[0], v0[1]), ay = pk_bf16(v0[2], v0[3]), bx = pk_bf16(v1[0], v1[1]), by = pk_bf16(v1[2], v1[3]);
    swap16(ax, bx); swap16(ay, by);
    return (u32x4){ax, ay, bx, by};
}
DI unsigned frag_off(int lane) { return (unsigned)((((lane >> 4) & 1) * 2 + (lane >> 5)) * 256 + (lane & 15) * 16); }
DI void store_frag(rsrc_t cr, unsigned voff, unsigned soff, f32x4 v0, f32x4 v1) { ST16(pack_pair(v0, v1), cr, voff, soff, 16); }
DI u32x4 to_rows(ldsp sc, int lane, f32x4 v0, f32x4 v1) {
    *(volatile LAS u32x4*)(sc + ((lane & 15) * 4 + ((lane >> 4) & 1) * 2 + (lane >> 5)) * 16) = pack_pair(v0, v1);
    return *(volatile LAS u32x4*)(sc + lane * 16);
}
constexpr int L_SCR = 132096;
#define STAGE(bufoff, gbase, voff, step2) do { \
        __builtin_amdgcn_global_load_lds((const unsigned*)((gbase) + (voff)), (LAS unsigned*)(lds + (bufoff) + ldsw), 16, 0, 0); \
        __builtin_amdgcn_global_load_lds((const unsigned*)((gbase) + (step2) + (voff)), (LAS unsigned*)(lds + (bufoff) + ldsw + 8192), 16, 0, 0); } while (0)
#define LDA(dst, off) do { _Pragma("unroll") for (int m = 0; m < 4; ++m) _Pragma("unroll") for (int k = 0; k < 2; ++k) dst[m][k] = *(const LAS bf16x8*)(lds + (off) + aoff + m * 2048 + k * 1024); } while (0)
#define TRD(dst, base, off) asm volatile("ds_read_b64_tr_b16 %0, %1 offset:%2" : "=v"(dst) : "v"(base), "i"(off) : "memory")
#define LDB(dst, v0, v1, off) do { s16x4 l0, h0, l1, h1, l2, h2, l3, h3; \
        TRD(l0, v0, (off)); TRD(h0, v0, (off) + 256); TRD(l1, v0, (off) + 8192); TRD(h1, v0, (off) + 8448); \
        TRD(l2, v1, (off)); TRD(h2, v1, (off) + 256); TRD(l3, v1, (off) + 8192); TRD(h3, v1, (off) + 8448); \
        dst[0][0] = cat8(l0, h0); dst[0][1] = cat8(l1, h1); dst[1][0] = cat8(l2, h2); dst[1][1] = cat8(l3, h3); } while (0)
#define MMA(accv, At, Bt) do { SCHED(); __builtin_amdgcn_s_setprio(1); _Pragma("unroll") for (int m = 0; m < 4; ++m) _Pragma("unroll") for (int n = 0; n < 2; ++n) _Pragma("unroll") for (int k = 0; k < 2; ++k) \
        accv[m][n] = mma32(Bt[n][k], At[m][k], accv[m][n]); __builtin_amdgcn_s_setprio(0); } while (0)
DI unsigned va_of(int tid, unsigned lda, int nt) { int R, C; stage_rc(tid * 16, R, C); return lda ? (unsigned)R * lda + (unsigned)C * 2u : (unsigned)((tid >> 7) * nt * 2048 + (tid & 127) * 16); }
DI unsigned vb_of(int tid, int wid, unsigned ldb) { return (unsigned)(8 * (wid >> 1) + ((tid >> 2) & 7)) * ldb + (unsigned)((2 * (wid & 1) + ((tid >> 5) & 1)) * 64 + (((tid & 3) ^ (((wid >> 1) & 1) << 1)) * 16)); }
#define GEMM_LANE_SETUP(lda0) \
    const int tid = fresh_tid(); \
    const int wid = __builtin_amdgcn_readfirstlane(tid >> 6), lane = tid & 63, wr = wid >> 2, wc = wid & 3, fr = lane & 15, fq = lane >> 4; \
    const unsigned ldsw = (unsigned)wid * 1024u; \
    const int aoff = (lda0) ? lds_byte(wr * 64 + fr, fq * 8) : wr * 8192 + lane * 16; \
    const int boff0 = fq * 2048 + wc * 512 + (fr >> 2) * 64 + (fq & 1) * 32 + (fr & 3) * 8, boff1 = boff0 ^ 32;

template <class Sched>
DI void gemm_stream(ldsp lds, const Sched& S, unsigned* ctl) {
    Unit cur, nxt; int ui = 0;
    if (!S.next(0, cur)) return;
    GEMM_LANE_SETUP(cur.lda);
#define SA(b, h) (((b) * 2 + (h)) * HTB)
#define SB(b, h) ((4 + (b) * 2 + (h)) * HTB)
    const unsigned vb0 = (unsigned)(size_t)lds + 65536u + (unsigned)boff0, vb1 = (unsigned)(size_t)lds + 65536u + (unsigned)boff1;
    f32x4 acc[2][2][4][2];
#pragma unroll
    for (int a = 0; a < 2; ++a)
#pragma unroll
        for (int b = 0; b < 2; ++b)
#pragma unroll
            for (int m = 0; m < 4; ++m)
#pragma unroll
                for (int n = 0; n < 2; ++n) acc[a][b][m][n] = (f32x4){0.f, 0.f, 0.f, 0.f};
    bf16x8 At[4][2], B0[2][2], B1[2][2];
    const unsigned vA = va_of(tid, cur.lda, cur.nt); unsigned vB = vb_of(tid, wid, cur.ldb);
    const size_t a2s = cur.lda ? (size_t)64 * cur.lda : (size_t)cur.nt * 8192;
    {
        const size_t ah = cur.ah, b2s = (size_t)32 * cur.ldb, bk = (size_t)64 * cur.ldb;
        STAGE(SB(0, 0), cur.B, vB, b2s); STAGE(SB(0, 1), cur.B + 256, vB, b2s); STAGE(SA(0, 0), cur.A, vA, a2s); STAGE(SA(0, 1), cur.A + ah, vA, a2s);
        if (wr == 1) BAR();
        WAIT_V(2); BAR();
        STAGE(SB(1, 0), cur.B + bk, vB, b2s); STAGE(SA(1, 0), cur.A + cur.ak, vA, a2s); STAGE(SB(1, 1), cur.B + bk + 256, vB, b2s);
        WAIT_V(6); BAR();
    }
    int psig = 0, pn = 0;
    for (;;) {
        const bool has_next = S.next(ui + 1, nxt);
        if (!has_next) nxt = cur;
        int nt = cur.nt; asm volatile("" : "+s"(nt));
        for (int t = 0; t < nt; t += 2) {
            const bool last = (t == nt - 2);
            const char* a1 = cur.A + (size_t)(t + 1) * cur.ak + cur.ah;
            if (last) vB = vb_of(fresh_tid(), wid, nxt.ldb);
            const unsigned ldb = last ? nxt.ldb : cur.ldb;
            const char* a2 = last ? nxt.A : cur.A + (size_t)(t + 2) * cur.ak;
            const char* b2 = last ? nxt.B : cur.B + (size_t)(t + 2) * 64 * cur.ldb;
            const size_t ah = cur.ah, b2s = (size_t)32 * ldb;
            const char* a3 = a2 + cur.ak; const char* b3 = b2 + (size_t)64 * ldb;
            LDB(B0, vb0, vb1, 0); LDB(B1, vb0, vb1, HTB); SCHED(); LDA(At, SA(0, 0)); STAGE(SA(1, 1), a1, vA, a2s);
            WAIT_V(8); WAIT_L(0); BAR(); MMA(acc[0][0], At, B0); MMA(acc[0][1], At, B1); BAR(); SCHED();
            LDA(At, SA(0, 1)); STAGE(SB(0, 0), b2, vB, b2s); STAGE(SB(0, 1), b2 + 256, vB, b2s); STAGE(SA(0, 0), a2, vA, a2s);
            WAIT_V(8);
            asm volatile("" : "+s"(pn));
            if (pn) { signal(ctl, psig, pn); pn = 0; }
            WAIT_L(0); BAR(); MMA(acc[1][0], At, B0); MMA(acc[1][1], At, B1); BAR(); SCHED();
            LDB(B0, vb0, vb1, 2 * HTB); LDB(B1, vb0, vb1, 3 * HTB); SCHED(); LDA(At, SA(1, 0)); STAGE(SA(0, 1), a2 + ah, vA, a2s);
            WAIT_V(8); WAIT_L(0); BAR(); MMA(acc[0][0], At, B0); MMA(acc[0][1], At, B1); BAR(); SCHED();
            LDA(At, SA(1, 1)); STAGE(SB(1, 0), b3, vB, b2s); STAGE(SB(1, 1), b3 + 256, vB, b2s); STAGE(SA(1, 0), a3, vA, a2s);
            WAIT_V(8); WAIT_L(0); BAR(); MMA(acc[1][0], At, B0); MMA(acc[1][1], At, B1); BAR(); SCHED();
        }
        if (wr == 0) BAR();
        {
            const rsrc_t cr = mkrsrc(cur.C, 0x1000000u);
            const int tz = fresh_tid() & 63;
            if (cur.nsig) {
                const unsigned co = (unsigned)(wr * 64) * cur.ldc + (unsigned)wc * cur.cw + frag_off(tz);
#pragma unroll
                for (int ai = 0; ai < 2; ++ai)
#pragma unroll
                    for (int m = 0; m < 4; ++m)
#pragma unroll
                        for (int bj = 0; bj < 2; ++bj) store_frag(cr, co, (unsigned)(ai * 128 + m * 16) * cur.ldc + (unsigned)bj * cur.ch, acc[ai][bj][m][0], acc[ai][bj][m][1]);
            } else {
                const unsigned co = (unsigned)(wr * 64 + (tz >> 2)) * cur.ldc + (unsigned)(wc * 64 + (tz & 3) * 16);
#pragma unroll
                for (int ai = 0; ai < 2; ++ai)
#pragma unroll
                    for (int m = 0; m < 4; ++m)
#pragma unroll
                        for (int bj = 0; bj < 2; ++bj)
                            ST16(to_rows(lds + L_SCR + wid * 1024, tz, acc[ai][bj][m][0], acc[ai][bj][m][1]), cr, co, (unsigned)(ai * 128 + m * 16) * cur.ldc + (unsigned)bj * 256u, 0);
            }
        }
        psig = cur.sig; pn = cur.nsig;
        if (!has_next) break;
#pragma unroll
        for (int a = 0; a < 2; ++a)
#pragma unroll
            for (int b = 0; b < 2; ++b)
#pragma unroll
                for (int m = 0; m < 4; ++m)
#pragma unroll
                    for (int n = 0; n < 2; ++n) acc[a][b][m][n] = (f32x4){0.f, 0.f, 0.f, 0.f};
        cur = nxt; ++ui;
        if (wr == 1) BAR();
    }
    WAIT_V(0);
    signal(ctl, psig, pn);
    BAR();
#undef SA
#undef SB
}

DI void gemm_small(ldsp lds, const Unit u, unsigned* ctl) {
    GEMM_LANE_SETUP(u.lda);
    const unsigned vA = va_of(tid, u.lda, u.nt), vB = vb_of(tid, wid, u.ldb);
    const size_t a2s = u.lda ? (size_t)64 * u.lda : (size_t)u.nt * 8192, b2s = (size_t)32 * u.ldb, bk = (size_t)64 * u.ldb;
    f32x4 acc[4][2];
#pragma unroll
    for (int m = 0; m < 4; ++m)
#pragma unroll
        for (int n = 0; n < 2; ++n) acc[m][n] = (f32x4){0.f, 0.f, 0.f, 0.f};
    int nt = u.nt; asm volatile("" : "+s"(nt));
    const unsigned vb0 = (unsigned)(size_t)lds + (unsigned)boff0, vb1 = (unsigned)(size_t)lds + (unsigned)boff1, vb2 = vb0 + 65536u, vb3 = vb1 + 65536u;
#pragma unroll
    for (int t = 0; t < 4; ++t) { STAGE(t * 32768, u.A + (size_t)t * u.ak, vA, a2s); STAGE(t * 32768 + 16384, u.B + t * bk, vB, b2s); }
    bf16x8 A0[4][2], B0[2][2], A1[4][2], B1[2][2];
    WAIT_V(12); BAR();
    LDB(B0, vb0, vb1, 16384); LDA(A0, 0);
#define RDA(dst, m, off) do { dst[m][0] = *(const LAS bf16x8*)(lds + (off) + aoff + (m) * 2048); dst[m][1] = *(const LAS bf16x8*)(lds + (off) + aoff + (m) * 2048 + 1024); } while (0)
#define RDB(dst, nn, v, off) do { s16x4 l0, h0, l1, h1; TRD(l0, v, (off)); TRD(h0, v, (off) + 256); TRD(l1, v, (off) + 8192); TRD(h1, v, (off) + 8448); dst[nn][0] = cat8(l0, h0); dst[nn][1] = cat8(l1, h1); } while (0)
#define MM4(Ac, Bc, m) do { SCHED(); _Pragma("unroll") for (int n = 0; n < 2; ++n) _Pragma("unroll") for (int k = 0; k < 2; ++k) acc[m][n] = mma32(Bc[n][k], Ac[m][k], acc[m][n]); SCHED(); } while (0)
#define KSTEP(i, Ac, Bc, An, Bn, v0, v1, boffn) do { \
        const int tn = min(t0 + (i) + 4, nt - 1); \
        WAIT_L(0); WAIT_V(8); BAR(); \
        MM4(Ac, Bc, 0); RDB(Bn, 0, v0, boffn); RDA(An, 0, (((i) + 1) & 3) * 32768); \
        MM4(Ac, Bc, 1); RDB(Bn, 1, v1, boffn); RDA(An, 1, (((i) + 1) & 3) * 32768); \
        MM4(Ac, Bc, 2); RDA(An, 2, (((i) + 1) & 3) * 32768); STAGE((i) * 32768, u.A + (size_t)tn * u.ak, vA, a2s); \
        MM4(Ac, Bc, 3); RDA(An, 3, (((i) + 1) & 3) * 32768); STAGE((i) * 32768 + 16384, u.B + (size_t)tn * bk, vB, b2s); } while (0)
    for (int t0 = 0; t0 < nt; t0 += 4) {
        KSTEP(0, A0, B0, A1, B1, vb0, vb1, 49152);
        KSTEP(1, A1, B1, A0, B0, vb2, vb3, 16384);
        KSTEP(2, A0, B0, A1, B1, vb2, vb3, 49152);
        KSTEP(3, A1, B1, A0, B0, vb0, vb1, 16384);
    }
    WAIT_L(0); SCHED();
#undef RDA
#undef RDB
#undef MM4
#undef KSTEP
    const rsrc_t cr = mkrsrc(u.C, 128u * u.ldc);
    if (u.nsig) {
#pragma unroll
        for (int m = 0; m < 4; ++m) store_frag(cr, (unsigned)(wr * 64) * u.ldc + (unsigned)wc * u.cw + frag_off(lane), (unsigned)(m * 16) * u.ldc, acc[m][0], acc[m][1]);
    } else {
#pragma unroll
        for (int m = 0; m < 4; ++m)
            ST16(to_rows(lds + L_SCR + wid * 1024, lane, acc[m][0], acc[m][1]), cr, (unsigned)(wr * 64 + (lane >> 2)) * u.ldc + (unsigned)(wc * 64 + (lane & 3) * 16), (unsigned)(m * 16) * u.ldc, 0);
    }
    WAIT_V(0);
    signal(ctl, u.sig, u.nsig);
    BAR();
}

#define win_off(idx) ((unsigned)(idx) * 16384u + ((((unsigned)(idx) >> 7) ^ par) & 1u) * 8192u)
#define W_MEM(s) ((s) == 0 ? 0 : 6 + 2 * (s))
#define W_PART(slot) (192 + (slot) * 64)
#define W_X(slot) (384 + (slot) * 128)
DI bool all_set(unsigned mx) { return __builtin_amdgcn_ballot_w64(mx == SENT) == 0ull; }
DI u32x4 rows_gather(unsigned v) { unsigned a = v, b = v; swap16(a, b); unsigned c = a, d = b; swap32(a, c); swap32(b, d); return (u32x4){a, b, c, d}; }
DI float max3f(float a, float b, float c) { return fmaxf(fmaxf(a, b), c); }
#define KD0 7
#define KD1 7
#define KD2 6
#define KRA 3
#define KDM 0
#define KPM 8
constexpr int RED_M = 144;
constexpr int L_PX = 32768, L_PM = L_PX + 512, L_ML = L_PM + 512, L_FLAG = L_ML + 256, L_PACE = L_FLAG + 16, L_VM = 34816, L_OWN = L_VM + 2048, L_X = L_OWN + 1536  ;
constexpr unsigned OOB = 0x80000000u;
#define RETRY(i)
#define SEG(i)
#define PIN_S(x) asm volatile("" : "+s"(x))
#define PIN_V(x) asm volatile("" : "+v"(x))
DI float ror8(float v) { return __uint_as_float(__builtin_amdgcn_update_dpp(0, __float_as_uint(v), 0x128, 0xf, 0xf, true)); }

template <int ROLE>
DI void chain_loop(const Params& p, const unsigned par, const int rank, const int w, ldsp lds, const bf16x16 (&W)[6][2], const bf16x16 (&WO)[2][2], unsigned long long* stamps) {
    const int lane = fresh_tid() & 63, g = lane >> 4, n = lane & 15;
    const int h = rank >> 2, j = rank & 3;
    unsigned* ctl = (unsigned*)(p.ws + WS_CTL);
    const rsrc_t xr = mkrsrc(p.ws + WS_XCH, (unsigned)XCH_BYTES);
    const rsrc_t qr = mkrsrc(p.ws + WS_QKVX + (size_t)(32 + h) * XH, 33u * XH);
    const int sidx = (n & 2) ? 0xEEEE : 0x4444;
    const unsigned l16 = (unsigned)lane * 16u;
    const int redw = (w * 1024 + (n & 1) * 512 + (n >> 1) * 8 + 2 * g) * 4;
    const int redr = (w < 4 ? (n >> 3) * RED_M + (4 * w + g) * 8 + (n & 7) : 2 * RED_M + (8 * (w & 1) + 2 * g + ((n >> 1) & 1)) * 8 + 2 * (n >> 2) + (n & 1)) * 4;
    const bool vown = (g >> 1) == (j & 1);
    const unsigned vg1 = (unsigned)(((j & 2) ^ 2) + (g >> 1)) * 1536u + 1024u + (unsigned)(lane & 31) * 16u;
    const unsigned vg0 = vown ? OOB : (vg1 + (unsigned)(j & 2) * 3072u - 3072u);
    const unsigned pub_off = (g == j ? XCH_KM + (unsigned)rank * 1536u : (unsigned)(4 * h + g) * 16384u + (unsigned)j * 1536u) + (unsigned)w * 256u + (unsigned)n * 16u;
    const int red = ((j + 1) & 3) * 32 + h;
    const int rho = 4 * (8 * j + w) + 2 * (g >> 1) + ((n >> 2) & 1);
    const unsigned pst_off = n < 8 ? (unsigned)(rho >> 1) * 16384u + (unsigned)(rho & 1) * 4096u + (unsigned)((h ^ rho) & 31) * 128u + (unsigned)((g & 1) * 64 + (n & 3) * 16) : OOB;
    const unsigned mem_off = lane < 8 ? (unsigned)(4 * ((n >> 1) & 1) + 2 * (n >> 2) + (n & 1)) * 16u : OOB;
    const unsigned pw0 = win_off(W_PART(0)), pw1 = win_off(W_PART(1)), xw0 = win_off(W_X(0)), xw1 = win_off(W_X(1));
    const unsigned redo = (unsigned)(red >> 1) * 16384u + (unsigned)(red & 1) * 4096u;
    const int vxr = L_X + lane * 16;
    const float csc = 0.08838834764831845f * 1.4426950408889634f;
    float ninf = -__builtin_inff(); PIN_V(ninf);
    u32x4 KX[8], VWX[2];
    auto dma_x = [&](int s) {
        if (ROLE == 0 && w < 4) {
            if ((s & 3) == 0) { wait_count(ctl, C_KV + (s >> 2) * 512 + h * 16, 16u); asm volatile("" ::: "memory"); }
#pragma unroll
            for (int i = 0; i < 2; ++i) {
                const unsigned vo = l16, so = (unsigned)s * 8192u + (unsigned)(w * 2 + i) * 1024u;
                __builtin_amdgcn_raw_ptr_buffer_load_lds(qr, (LAS void*)(lds + L_X + (s & 1) * 16384 + (w * 2 + i) * 1024), 16, vo, so + 32u * XH, 0, 16);
                __builtin_amdgcn_raw_ptr_buffer_load_lds(qr, (LAS void*)(lds + L_X + (s & 1) * 16384 + 8192 + (w * 2 + i) * 1024), 16, vo, so, 0, 16);
            }
        }
    };
    auto load_kx = [&](int s) {
#pragma unroll
        for (int kt = 0; kt < 2; ++kt)
#pragma unroll
            for (int kk = 0; kk < 4; ++kk) KX[kt * 4 + kk] = *(const LAS u32x4*)(lds + vxr + (s & 1) * 16384 + 8192 + kt * 4096 + ((j + kk) & 3) * 1024);
    };
    auto vwx_kt = [&](int s, int kt) {
        bf16x8 VA[4];
#pragma unroll
        for (int kk = 0; kk < 4; ++kk) VA[kk] = *(const LAS bf16x8*)(lds + vxr + (s & 1) * 16384 + kt * 4096 + (kk ^ (j & 2)) * 1024);
#pragma unroll
        for (int ct = 0; ct < 2; ++ct) {
            f32x4 d = {0.f, 0.f, 0.f, 0.f};
#pragma unroll
            for (int kk = 0; kk < 4; ++kk) d = mma32(VA[kk], (kk & 1) ? hi8(WO[ct][kk >> 1]) : lo8(WO[ct][kk >> 1]), d);
            VWX[ct][2 * kt] = pk_bf16(d[0], d[1]); VWX[ct][2 * kt + 1] = pk_bf16(d[2], d[3]);
        }
    };
    auto rmax = [&](float v) { float a = v, b = v; swap16f(a, b); v = max3f(a, b, ninf); a = v; b = v; swap32f(a, b); return max3f(a, b, ninf); };
    auto lo2 = [](f32x4 v) { return (f32x2){v[0], v[1]}; };
    auto hi2 = [](f32x4 v) { return (f32x2){v[2], v[3]}; };
    dma_x(0); dma_x(1);
    asm volatile("s_waitcnt vmcnt(0) lgkmcnt(0)" ::: "memory"); BAR(); asm volatile("" ::: "memory");
    vwx_kt(0, 0); vwx_kt(0, 1);
    if (ROLE == 2) load_kx(0);
    __builtin_amdgcn_s_setprio(2);

    unsigned iv_t0 = 0, iv_acc = 0;
    unsigned spins = 0, pace_c = 0x7fffffffu, pace_t = 0;
    const u32x4 sv = {SENT, SENT, SENT, SENT};
    for (int s = 0;; ++s) {
        unsigned xw = (s & 1) ? xw1 : xw0, pso = (s & 1) ? pw1 : pw0;
        unsigned mso = win_off(W_MEM(s) + (w >> 2)) + (unsigned)(w & 3) * 2048u;
        unsigned rso = ROLE <= 1 ? (xw0 ^ xw1 ^ xw) + (unsigned)rank * 16384u + (unsigned)w * 1024u : (pw0 ^ pw1 ^ pso) + redo + (unsigned)(w & 1) * 2048u;
        unsigned pub = pub_off + (g == j ? (unsigned)s * (128u * 1536u) : xw);
        PIN_S(xw); PIN_S(pso); PIN_S(mso); PIN_S(rso); PIN_V(pub);
        {
            if (KD0) __builtin_amdgcn_s_sleep(KD0);
            const u32x2 pv = *(const LAS u32x2*)(lds + L_PACE);
            unsigned target = __builtin_amdgcn_readfirstlane(pv.x); const bool valid = __builtin_amdgcn_readfirstlane(pv.y) + 1u == (unsigned)s && s > 3;
            unsigned now = (unsigned)__builtin_amdgcn_s_memrealtime();
            if (ROLE == 3 && s > 2) pace_c = min(pace_c, now - pace_t);
            if (valid && (int)(target - now) < 100) { while ((int)(target - now) > 0) now = (unsigned)__builtin_amdgcn_s_memrealtime(); }
        }
        SEG(10);
        f32x4 c[6]; u32x4 a1;
        auto rearm = [&]() {
            ST16(sv, xr, l16, rso, 16);
            if (ROLE >= 2) ST16(sv, xr, l16 + 1024, rso, 16);
        };
        u32x4 a0;
        auto ld_a = [&]() { asm volatile("" ::: "memory"); a0 = ld16_sc1(xr, l16, mso); a1 = ld16_sc1(xr, l16 + 1024, mso); };
        auto ok_a = [&]() { return all_set(max(umax4(a0), umax4(a1))); };
        auto mm_a = [&]() {
#pragma unroll
            for (int i = 0; i < 6; ++i) { c[i] = (f32x4){0.f, 0.f, 0.f, 0.f}; PIN_V(c[i]); }
            SCHED();
            c[0] = smma(a0, W[0][0], c[0], sidx); c[1] = smma(a0, W[1][0], c[1], sidx); c[0] = smma(a1, W[0][1], c[0], sidx); c[1] = smma(a1, W[1][1], c[1], sidx);
            c[2] = smma(a0, W[2][0], c[2], sidx); c[3] = smma(a0, W[3][0], c[3], sidx);
        };
        auto put = [&](int i) { *(LAS f32x2*)(lds + redw + ((i >> 1) * RED_M + (i & 1) * 64) * 4) = lo2(c[i]) + hi2(c[i]); };
        ld_a(); SCHED();
        if (KRA == 0) rearm();
        mm_a();
        if (__builtin_expect(!ok_a(), 0)) { bool out; do { RETRY(0); out = spin_out(spins, ctl); ld_a(); } while (!(ok_a() | out)); mm_a(); }
        SEG(0);
        if (KRA == 1) rearm();
        c[2] = smma(a1, W[2][1], c[2], sidx); c[3] = smma(a1, W[3][1], c[3], sidx);
        put(0); put(1);
        SCHED();
        c[4] = smma(a0, W[4][0], c[4], sidx); c[5] = smma(a0, W[5][0], c[5], sidx); c[4] = smma(a1, W[4][1], c[4], sidx);
        put(2); put(3);
        c[5] = smma(a1, W[5][1], c[5], sidx);
        SCHED();
        if (KRA == 3) rearm();
        put(4); put(5);
        SEG(1);
        if (KRA == 2) { SCHED(); rearm(); }
        LDS_BAR();
        SEG(2);
        if (ROLE <= 1) {
            f32x2 v[8];
#pragma unroll
            for (int ww = 0; ww < 8; ++ww) { const LAS float* q = (const LAS float*)(lds + redr + ww * 4096); v[ww] = (f32x2){q[0], q[512]}; }
            const f32x2 t = ((v[0] + v[1]) + (v[2] + v[3])) + ((v[4] + v[5]) + (v[6] + v[7]));
            const u32x4 o = rows_gather(min(pk_bf16(t.x, t.y), SENT - 1));
            ST16(o, xr, pub, 0, 16);
            *(LAS u32x4*)(lds + L_OWN + w * 256 + n * 16) = o;
        } else {
            if (ROLE == 3 && rank == 0) {
                pace_t = (unsigned)__builtin_amdgcn_s_memrealtime();
                if (lane == 0) { __hip_atomic_store(ctl + C_PROG, (unsigned)s, RLX_AGENT);
                    __hip_atomic_store((unsigned long long*)(ctl + C_PACE), (unsigned long long)(pace_t + pace_c + KPM) | ((unsigned long long)s << 32), RLX_AGENT); }
            }
        }
        SEG(3);
        if (s == NSEG - 1) break;
        LDS_BAR();
        SEG(13);
        unsigned VWM[2];
        auto vwm_pack = [&](const f32x4 (&dv)[2]) {
#pragma unroll
            for (int ct = 0; ct < 2; ++ct) { const f32x2 t = lo2(dv[ct]) + hi2(dv[ct]); VWM[ct] = pk_bf16(t.x, t.y); PIN_V(VWM[ct]); }
        };
        f32x4 dv[2] = {{0.f, 0.f, 0.f, 0.f}, {0.f, 0.f, 0.f, 0.f}}, s0, s1;
        PIN_V(dv[0]); PIN_V(dv[1]);
        u32x4 v0, v1;
        if (ROLE == 0) {
            if (KDM == 1) { WAIT_V(0); dma_x(min(s + 2, NSEG - 2)); }
            else if (KDM == 2) { dma_x(min(s + 2, NSEG - 2)); if (w < 4) WAIT_V(4); else WAIT_V(0); }
            else WAIT_V(0);
        } else {
            unsigned cw = xw + (unsigned)rank * 16384u, c1 = cw + (unsigned)((j + 1) & 3) * 1536u, c2 = cw + (unsigned)((j + 2) & 3) * 1536u, c3 = cw + (unsigned)((j + 3) & 3) * 1536u;
            PIN_S(cw); PIN_S(c1); PIN_S(c2); PIN_S(c3);
            const unsigned xl = ROLE == 2 ? (l16 & ~128u) : l16;
            u32x4 vo;
            f32x4 e0 = {0.f, 0.f, 0.f, 0.f}, e1 = e0;
            if (ROLE == 1) { vo = *(const LAS u32x4*)(lds + L_OWN + 1024 + (lane & 31) * 16); if (!vown) vo = (u32x4){0u, 0u, 0u, 0u}; PIN_V(vo); }
            else {
                const u32x4 X0 = *(const LAS u32x4*)(lds + L_OWN + xl);
                if (ROLE == 2) { e0 = mma32(asbf(KX[0]), asbf(X0), e0); e1 = mma32(asbf(KX[4]), asbf(X0), e1); } else e0 = mma32(asbf(X0), asbf(X0), e0);
                PIN_V(e0); PIN_V(e1);
            }
            if (KD1) __builtin_amdgcn_s_sleep(KD1);
            u32x4 X1, X2, X3;
            auto ld_b = [&]() {
                asm volatile("" ::: "memory");
                if (ROLE >= 2) { X1 = ld16_sc1(xr, xl, c1); X2 = ld16_sc1(xr, xl, c2); X3 = ld16_sc1(xr, xl, c3); }
                else { v0 = ld16_sc1(xr, vg0, cw); v1 = ld16_sc1(xr, vg1, cw); }
            };
            auto ok_b = [&]() { return all_set(ROLE >= 2 ? max(umax4(X1), max(umax4(X2), umax4(X3))) : max(umax4(v0), umax4(v1))); };
            auto mm_b = [&]() {
                if (ROLE == 2) {
                    s0 = mma32(asbf(KX[1]), asbf(X1), e0); s1 = mma32(asbf(KX[5]), asbf(X1), e1);
                    s0 = mma32(asbf(KX[2]), asbf(X2), s0); s1 = mma32(asbf(KX[6]), asbf(X2), s1);
                    s0 = mma32(asbf(KX[3]), asbf(X3), s0); s1 = mma32(asbf(KX[7]), asbf(X3), s1);
                } else if (ROLE == 3) {
                    s0 = mma32(asbf(X1), asbf(X1), e0); s1 = mma32(asbf(X2), asbf(X2), e1); s0 = mma32(asbf(X3), asbf(X3), s0);
                } else {
                    v0 |= vo;
                    *(LAS u32x4*)(lds + L_VM + lane * 16) = v0; *(LAS u32x4*)(lds + L_VM + 1024 + lane * 16) = v1;
                }
            };
            SEG(11);
            ld_b(); SCHED(); mm_b();
            if (__builtin_expect(!ok_b(), 0)) { bool out; do { RETRY(1); out = spin_out(spins, ctl); ld_b(); } while (!(ok_b() | out)); mm_b(); }
            SEG(4);
        }
        float m;
        f32x4 sc;
        if (ROLE >= 2) {
            sc = ROLE == 2 ? (n < 8 ? s0 : s1) : s0 + s1;
            if (ROLE == 3 && g < 2) sc = (f32x4){ninf, ninf, ninf, ninf};
            m = max3f(max3f(sc[0], sc[1], sc[2]), sc[3], ninf);
            if (ROLE == 2) m = max3f(m, ror8(m), ninf);
            m = rmax(m);
            PIN_V(m);
        }
        LDS_BAR();
        if (ROLE != 1) { v0 = *(const LAS u32x4*)(lds + L_VM + lane * 16); v1 = *(const LAS u32x4*)(lds + L_VM + 1024 + lane * 16); }
        if (ROLE >= 2) {
            const float mc = -m * csc;
#pragma unroll
            for (int i = 0; i < 4; ++i) sc[i] = __builtin_amdgcn_exp2f(fmaf(sc[i], csc, mc));
            *(LAS u32x2*)(lds + (ROLE == 2 ? L_PX + (g * 8 + (n & 7)) * 16 + (n >> 3) * 8 : L_PM + (g < 2 ? 256 : 0) + n * 16 + (g & 1) * 8)) = (u32x2){pk_bf16(sc[0], sc[1]), pk_bf16(sc[2], sc[3])};
            SCHED();
            dv[0] = smma(v0, WO[0][0], dv[0], sidx); dv[1] = smma(v0, WO[1][0], dv[1], sidx);
            float l = (sc[0] + sc[1]) + (sc[2] + sc[3]);
            if (ROLE == 2) l += ror8(l);
            dv[0] = smma(v1, WO[0][1], dv[0], sidx); dv[1] = smma(v1, WO[1][1], dv[1], sidx);
            l = rows_sum(l);
            *(LAS f32x2*)(lds + L_ML + n * 16 + (ROLE == 2 ? 0 : 8)) = (f32x2){mc, l};
        } else {
#pragma unroll
            for (int ct = 0; ct < 2; ++ct) { dv[ct] = smma(v0, WO[ct][0], dv[ct], sidx); dv[ct] = smma(v1, WO[ct][1], dv[ct], sidx); }
        }
        vwm_pack(dv);
        SEG(5);
        u32x4 am0 = {VWM[0], 0u, 0u, 0u}, am1 = {VWM[1], 0u, 0u, 0u}, pm = {0u, 0u, 0u, 0u};
        PIN_V(am0); PIN_V(am1); PIN_V(pm);
        LDS_BAR();
        SEG(6);
        {
            const f32x4 ml = *(const LAS f32x4*)(lds + L_ML + n * 16);
            const u32x4 px = *(const LAS u32x4*)(lds + L_PX + (g * 8 + (n & 7)) * 16);
            pm.x = *(const LAS unsigned*)(lds + L_PM + n * 16 + g * 4);
            const f32x4 z = {0.f, 0.f, 0.f, 0.f};
            const f32x4 dx0 = mma32(asbf(VWX[0]), asbf(px), z), dx1 = mma32(asbf(VWX[1]), asbf(px), z), dm0 = mma32(asbf(am0), asbf(pm), z), dm1 = mma32(asbf(am1), asbf(pm), z);
            const float dl = ml[0] - ml[2];
            const float e = __builtin_amdgcn_exp2f(-__builtin_fabsf(dl));
            const float ex = dl > 0.f ? e : 1.f, em = dl > 0.f ? 1.f : e;
            const float inv = __builtin_amdgcn_rcpf(fmaf(ex, ml[1], em * ml[3]));
            const f32x4 d0 = (dx0 * ex + dm0 * em) * inv, d1 = (dx1 * ex + dm1 * em) * inv;
            const u32x4 o = {min(pk_bf16(d0[0], d0[1]), SENT - 1), min(pk_bf16(d0[2], d0[3]), SENT - 1), min(pk_bf16(d1[0], d1[1]), SENT - 1), min(pk_bf16(d1[2], d1[3]), SENT - 1)};
            ST16(o, xr, pst_off, pso, 16);
        }
        SCHED();
        SEG(7);
        __builtin_amdgcn_s_setprio(ROLE == 3 ? 1 : 0);
        if (KDM == 3 && s + 2 < NSEG - 1) dma_x(s + 2);
        vwx_kt(s + 1, 0); vwx_kt(s + 1, 1);
        SCHED();
        if (ROLE == 3) __builtin_amdgcn_s_setprio(2);
        if (ROLE == 3) {
            u32x4 SEL;
            { const unsigned one = (n & 1) ? 0x3f800000u : 0x00003f80u; const int dw = (n & 7) >> 1; SEL = (u32x4){dw == 0 ? one : 0u, dw == 1 ? one : 0u, dw == 2 ? one : 0u, dw == 3 ? one : 0u}; }
            unsigned so = pso + redo, mo = win_off(W_MEM(s + 1) + (red >> 6)) + (unsigned)(red & 63) * 128u;
            PIN_S(so); PIN_S(mo); PIN_V(SEL);
            if (KD2) __builtin_amdgcn_s_sleep(KD2);
            f32x4 ta, tb;
            u32x4 q[4];
            auto ld_c = [&]() {
                asm volatile("" ::: "memory");
#pragma unroll
                for (int i = 0; i < 4; ++i) q[i] = ld16_sc1(xr, l16 + i * 1024, so);
            };
            auto ok_c = [&]() { return all_set(max(max(umax4(q[0]), umax4(q[1])), max(umax4(q[2]), umax4(q[3])))); };
            auto mm_c = [&]() {
                ta = mma32(asbf(SEL), asbf(q[0]), (f32x4){0.f, 0.f, 0.f, 0.f}); tb = mma32(asbf(SEL), asbf(q[1]), (f32x4){0.f, 0.f, 0.f, 0.f});
                ta = mma32(asbf(SEL), asbf(q[2]), ta); tb = mma32(asbf(SEL), asbf(q[3]), tb);
            };
            SEG(12);
            ld_c(); mm_c();
            if (__builtin_expect(!ok_c(), 0)) { bool out; do { RETRY(2); out = spin_out(spins, ctl); ld_c(); } while (!(ok_c() | out)); mm_c(); }
            SEG(8);
            f32x4 t = ta + tb;
#pragma unroll
            for (int i = 0; i < 4; ++i) t[i] += ror8(t[i]);
            unsigned a0 = min(pk_bf16(t[0], t[1]), SENT - 1), a1 = min(pk_bf16(t[2], t[3]), SENT - 1);
            unsigned b0 = a0, b1 = a1; swap16(a0, b0); swap16(a1, b1);
            const u32x4 o = {a0, a1, b0, b1};
            ST16(o, xr, mem_off, mo, 16);
        } else {
            if (KDM == 0 && s + 2 < NSEG - 1) dma_x(s + 2);
            if (ROLE == 2) load_kx(s + 1);
            if (ROLE == 1) { const unsigned long long pv = __hip_atomic_load((unsigned long long*)(ctl + C_PACE), RLX_AGENT); *(LAS u32x2*)(lds + L_PACE) = (u32x2){(unsigned)pv, (unsigned)(pv >> 32)}; }
        }
        SEG(9);
        if (ROLE != 3) __builtin_amdgcn_s_setprio(2);
        LDS_BAR();
        SEG(14);
    }
    WAIT_V(0);
    __builtin_amdgcn_s_setprio(0);
}

DI void chain_role(const Params& p, int rank, ldsp lds, unsigned long long* stamps) {
    const int tid = fresh_tid(), w = __builtin_amdgcn_readfirstlane(tid >> 6), lane = tid & 63, g = lane >> 4, n = lane & 15;
    const int h = rank >> 2, j = rank & 3;
    const rsrc_t xr = mkrsrc(p.ws + WS_XCH, (unsigned)XCH_BYTES);
    unsigned* ctl = (unsigned*)(p.ws + WS_CTL);
    if (rank == 0 && w == 7) {
        unsigned best0 = ~0u, best1 = ~0u;
        for (int i = 0; i < 12; ++i) {
            const unsigned long long t0 = __builtin_amdgcn_s_memtime();
            __builtin_amdgcn_raw_buffer_store_b32(0u, xr, (unsigned)(2 * 16384 + C_PROBE * 4) + (unsigned)(i & 1) * 8192u + (unsigned)(i >> 1) * 256u, 0, 16);
            asm volatile("s_waitcnt vmcnt(0)" ::: "memory");
            const unsigned dt = (unsigned)(__builtin_amdgcn_s_memtime() - t0);
            if (i & 1) best1 = min(best1, dt); else best0 = min(best0, dt);
        }
        if (lane == 0) __hip_atomic_store(ctl + C_PAR, best1 < best0 ? 1u : 0u, RLX_AGENT);
    }
    bf16x16 W[6][2], WO[2][2];
    {
        ldsp my = lds + w * 16384;
        const int rrow = lane >> 2, rc = (lane & 3) * 8, q4 = n >> 2;
        const int toffo = (8 * g + q4) * 64 + (n & 3) * 8;
        const int toffq = (16 * (g & 1) + 8 * (g >> 1) + 4 * (q4 >> 1) + (q4 & 1)) * 64 + (n & 3) * 8;
#define FRAG(base, st) cat16(cat8(trd((base)), trd((base) + (st))), cat8(trd((base) + 2048), trd((base) + 2048 + (st))))
#pragma unroll
        for (int i = 0; i < 8; ++i) {
            __builtin_amdgcn_global_load_lds((const unsigned*)(p.wq + (size_t)(128 * w + 16 * i + rrow) * 4096 + h * 128 + 32 * j + rc), (LAS unsigned*)(my + i * 1024), 16, 0, 0);
            __builtin_amdgcn_global_load_lds((const unsigned*)(p.wk + (size_t)(128 * w + 16 * i + rrow) * 4096 + h * 128 + 32 * j + rc), (LAS unsigned*)(my + 8192 + i * 1024), 16, 0, 0);
        }
        WAIT_V(0);
#pragma unroll
        for (int T = 0; T < 4; ++T)
#pragma unroll
            for (int t = 0; t < 2; ++t) W[T][t] = FRAG(my + (T >> 1) * 8192 + t * 4096 + (T & 1) * 32 + toffq, 128);
#pragma unroll
        for (int T = 0; T < 4; ++T) asm volatile("" : "+v"(W[T][0]), "+v"(W[T][1]));
        WAIT_L(0);
#pragma unroll
        for (int i = 0; i < 8; ++i) {
            __builtin_amdgcn_global_load_lds((const unsigned*)(p.wv + (size_t)(128 * w + 16 * i + rrow) * 4096 + h * 128 + 32 * j + rc), (LAS unsigned*)(my + i * 1024), 16, 0, 0);
            __builtin_amdgcn_global_load_lds((const unsigned*)(p.wo + (size_t)(h * 128 + 16 * i + rrow) * 1024 + 256 * j + 32 * w + rc), (LAS unsigned*)(my + 8192 + i * 1024), 16, 0, 0);
        }
        WAIT_V(0);
#pragma unroll
        for (int T = 0; T < 2; ++T)
#pragma unroll
            for (int t = 0; t < 2; ++t) { W[4 + T][t] = FRAG(my + t * 4096 + T * 32 + toffq, 128); WO[T][t] = FRAG(my + 8192 + (t ^ (j >> 1)) * 4096 + T * 32 + toffo, 256); }
#pragma unroll
        for (int T = 0; T < 2; ++T) asm volatile("" : "+v"(W[4 + T][0]), "+v"(W[4 + T][1]), "+v"(WO[T][0]), "+v"(WO[T][1]));
        WAIT_L(0);
#undef FRAG
    }
    wait_count(ctl, C_PAR, 1u);
    const unsigned par = __builtin_amdgcn_readfirstlane(__hip_atomic_load(ctl + C_PAR, RLX_AGENT)) & 1u;
    const int red = ((j + 1) & 3) * 32 + h;
    {
        const u32x4 sv = {SENT, SENT, SENT, SENT};
        if (w < 6) {
            ST16(sv, xr, (unsigned)lane * 16u, win_off(W_X(0)) + (unsigned)rank * 16384u + (unsigned)w * 1024u, 16);
            ST16(sv, xr, (unsigned)lane * 16u, win_off(W_X(1)) + (unsigned)rank * 16384u + (unsigned)w * 1024u, 16);
        }
        ST16(sv, xr, (unsigned)lane * 16u, win_off(W_PART(w >> 2)) + (unsigned)(red >> 1) * 16384u + (unsigned)(red & 1) * 4096u + (unsigned)(w & 3) * 1024u, 16);
#pragma unroll
        for (int i = 0; i < 4; ++i) {
            ST16(sv, xr, (unsigned)lane * 16u, XCH_KM + (unsigned)((32 + 4 * w + i) * 128 + rank) * 1536u, 16);
            ST16(sv, xr, lane < 32 ? (unsigned)lane * 16u + 1024u : OOB, XCH_KM + (unsigned)((32 + 4 * w + i) * 128 + rank) * 1536u, 16);
        }
        const int s = 8 * w + (lane >> 3);
        if (s > 0) ST16(sv, xr, win_off(W_MEM(s) + (red >> 6)) + (unsigned)(red & 63) * 128u + (unsigned)(lane & 7) * 16u, 0, 16);
    }
    WAIT_V(0);
    BAR();
    if (w == 7 && lane < 8) {
        const unsigned* src = (const unsigned*)(p.mem0 + (4 * (red & 1) + 2 * (lane >> 2) + (lane & 1)) * DM + 32 * (red >> 2) + 8 * ((red >> 1) & 1) + 4 * ((lane >> 1) & 1));
        const u32x4 v = {min(src[0], SENT - 1), min(src[1], SENT - 1), min(src[8], SENT - 1), min(src[9], SENT - 1)};
        ST16(v, xr, (unsigned)lane * 16u + (unsigned)(red & 63) * 128u, win_off(W_MEM(0) + (red >> 6)), 16);
    }
    if (w < 5) chain_loop<0>(p, par, rank, w, lds, W, WO, stamps);
    else if (w == 5) chain_loop<1>(p, par, rank, 5, lds, W, WO, stamps);
    else if (w == 6) chain_loop<2>(p, par, rank, 6, lds, W, WO, stamps);
    else chain_loop<3>(p, par, rank, 7, lds, W, WO, stamps);
}

template <bool POLL>
DI void attn_a_item(const Params& p, int s, int h, ldsp my, int lane) {
    const int g = lane >> 4, n = lane & 15;
    const rsrc_t xr = mkrsrc(p.ws + WS_XCH, (unsigned)XCH_BYTES);
    const rsrc_t qr = mkrsrc(p.ws + WS_QKVX + (size_t)h * XH + (size_t)s * 8192, 65u * XH);
#pragma unroll
    for (int i = 0; i < 8; ++i)
        __builtin_amdgcn_raw_ptr_buffer_load_lds(qr, (LAS void*)(my + i * 1024), 16, (unsigned)(g * 256 + (n ^ ((g & 1) * 8)) * 16), 64u * XH + (unsigned)i * 1024u, 0, 16);
    u32x4 Q[2][4], K[2][4], XM[4];
    const unsigned ro = (unsigned)lane * 16u;
#pragma unroll
    for (int t = 0; t < 2; ++t)
#pragma unroll
        for (int k = 0; k < 4; ++k) { Q[t][k] = ld16_sc1(qr, ro + k * 1024, (unsigned)t * 4096u); K[t][k] = ld16_sc1(qr, ro + k * 1024, (unsigned)t * 4096u + 32u * XH); }
    const unsigned qwin = XCH_KM + (unsigned)(s * 32 + h) * 6144u;
    u32x4 VM[2];
    if (POLL) { wait_count<4>((unsigned*)(p.ws + WS_CTL), C_PROG, (unsigned)s + 1u); __builtin_amdgcn_s_sleep(16); }
    for (unsigned spins = 0;;) {
        asm volatile("" ::: "memory");
#pragma unroll
        for (int k = 0; k < 4; ++k) XM[k] = ld16_sc1(xr, (unsigned)lane * 16u + (k & 1) * 1536, qwin + (k >> 1) * 3072);
#pragma unroll
        for (int ts = 0; ts < 2; ++ts) VM[ts] = ld16_sc1(xr, (unsigned)(g >> 1) * 1536u + 1024u + (unsigned)(lane & 31) * 16u, qwin + ts * 3072);
        if (!POLL) break;
        if (all_set(max(max(max(umax4(XM[0]), umax4(XM[1])), max(umax4(XM[2]), umax4(XM[3]))), max(umax4(VM[0]), umax4(VM[1]))))) break;
        __builtin_amdgcn_s_sleep(4);
        if (spin_out(spins, (unsigned*)(p.ws + WS_CTL))) break;
    }
    SCHED();
#pragma unroll
    for (int ts = 0; ts < 2; ++ts) {
        const u32x4 v = VM[ts];
        const int m = 2 * (n >> 2) + (n & 1), hf = (n >> 1) & 1;
#pragma unroll
        for (int i = 0; i < 4; ++i) { const int e = 64 * ts + 16 * g + 4 * i + 2 * hf;
            *(LAS unsigned*)(my + 8192 + (e >> 3) * 256 + (m ^ (e & 8)) * 16 + (e & 7) * 2) = v[i]; }
    }
    const float csc = 0.08838834764831845f * 1.4426950408889634f, ninf = -__builtin_inff();
    u32x4 PX[2]; u32x2 PM[2];
#pragma unroll
    for (int qt = 0; qt < 2; ++qt) {
        f32x4 sm = {0.f, 0.f, 0.f, 0.f}, s0 = sm, s1 = sm;
#pragma unroll
        for (int k = 0; k < 4; ++k) { sm = mma32(asbf(XM[k]), asbf(Q[qt][k]), sm); s0 = mma32(asbf(K[0][k]), asbf(Q[qt][k]), s0); if (qt == 1) s1 = mma32(asbf(K[1][k]), asbf(Q[qt][k]), s1); }
#pragma unroll
        for (int i = 0; i < 4; ++i) {
            if (g < 2) sm[i] = ninf;
            if (qt == 0) { if (4 * g + i > n) s0[i] = ninf; s1[i] = ninf; }
            else if (4 * g + i > n) s1[i] = ninf;
        }
        float m = fmaxf(fmaxf(fmaxf(s0[0], s0[1]), fmaxf(s0[2], s0[3])), fmaxf(fmaxf(s1[0], s1[1]), fmaxf(s1[2], s1[3])));
        m = fmaxf(m, fmaxf(fmaxf(sm[0], sm[1]), fmaxf(sm[2], sm[3])));
        m = rows_max(m);
        const float mc = -m * csc; float sum = 0.f;
#pragma unroll
        for (int i = 0; i < 4; ++i) { s0[i] = __builtin_amdgcn_exp2f(fmaf(s0[i], csc, mc)); s1[i] = __builtin_amdgcn_exp2f(fmaf(s1[i], csc, mc)); sm[i] = __builtin_amdgcn_exp2f(fmaf(sm[i], csc, mc));
            sum += (s0[i] + s1[i]) + sm[i]; }
        sum = rows_sum(sum);
        const float inv = __builtin_amdgcn_rcpf(sum);
        PX[qt] = (u32x4){pk_bf16(s0[0] * inv, s0[1] * inv), pk_bf16(s0[2] * inv, s0[3] * inv), pk_bf16(s1[0] * inv, s1[1] * inv), pk_bf16(s1[2] * inv, s1[3] * inv)};
        PM[qt] = (u32x2){pk_bf16(sm[0] * inv, sm[1] * inv), pk_bf16(sm[2] * inv, sm[3] * inv)};
    }
    asm volatile("s_waitcnt vmcnt(0) lgkmcnt(0)" ::: "memory");
    const int q4 = n >> 2, p4 = n & 3;
    const ldsp vx = my + (p4 >> 1) * 256 + ((4 * g + q4) ^ ((p4 >> 1) * 8)) * 16 + (p4 & 1) * 8, vmm = my + 8192 + (p4 >> 1) * 256 + ((4 * (g & 1) + q4) ^ ((p4 >> 1) * 8)) * 16 + (p4 & 1) * 8;
    const rsrc_t orr = mkrsrc(p.ws + WS_OA + (size_t)s * 262144 + (size_t)h * 4096, 262144u);
    const unsigned oo = frag_off(lane);
    bf16x8 VA[8]; s16x4 VMf[8];
#pragma unroll
    for (int et = 0; et < 8; ++et) { VA[et] = cat8(trd(vx + et * 512), trd(vx + 4096 + et * 512)); VMf[et] = trd(vmm + et * 512); }
    SCHED();
#pragma unroll
    for (int ep = 0; ep < 4; ++ep) {
        f32x4 d[2][2];
#pragma unroll
        for (int e2 = 0; e2 < 2; ++e2)
#pragma unroll
            for (int qt = 0; qt < 2; ++qt) d[qt][e2] = mma16(__builtin_bit_cast(u32x2, VMf[2 * ep + e2]), PM[qt], mma32(VA[2 * ep + e2], asbf(PX[qt]), (f32x4){0.f, 0.f, 0.f, 0.f}));
#pragma unroll
        for (int qt = 0; qt < 2; ++qt) store_frag(orr, oo + ep * 1024, (unsigned)qt * 131072u, d[qt][0], d[qt][1]);
    }
    asm volatile("s_waitcnt lgkmcnt(0)" ::: "memory");
}

DI void attn_b_item(const Params& p, int hq, int qb, ldsp lds) {
    const int tid = fresh_tid(), w = __builtin_amdgcn_readfirstlane(tid >> 6), lane = tid & 63, g = lane >> 4, n = lane & 15;
    const int q0 = qb * 128;
    const rsrc_t br = mkrsrc(p.ws + WS_QKVB, 96u * BH);
    const rsrc_t orr = mkrsrc(p.ws + WS_OB + (size_t)qb * 16 * 16384, 16u * 16384u);
    constexpr int BUF = 57344;
    unsigned to[3];
#pragma unroll
    for (int i = 0; i < 3; ++i) to[i] = (unsigned)max(8 * qb - 16 + w * 3 + i, 0) * 1024u;
    const unsigned l16 = (unsigned)lane * 16u, vsw = (unsigned)(g * 256 + (n ^ ((g & 1) * 8)) * 16);
    auto stage = [&](int i) {
        const unsigned so = (unsigned)(hq * 4 + i) * BH; ldsp d = lds + (i & 1) * BUF;
#pragma unroll
        for (int k = 0; k < 3; ++k) {
            __builtin_amdgcn_raw_ptr_buffer_load_lds(br, (LAS void*)(d + (w * 3 + k) * 1024), 16, l16, so + 32u * BH + to[k], 0, 16);
            __builtin_amdgcn_raw_ptr_buffer_load_lds(br, (LAS void*)(d + 24576 + (w * 3 + k) * 1024), 16, vsw, so + 64u * BH + to[k], 0, 16);
        }
        __builtin_amdgcn_raw_ptr_buffer_load_lds(br, (LAS void*)(d + 49152 + w * 1024), 16, l16, so + (unsigned)(8 * qb + w) * 1024u, 0, 16);
    };
    const float ninf = -__builtin_inff(), csc = 0.17677669529663687f * 1.4426950408889634f;
    const int tmin = (256 - q0 - 16 * w) >> 4;
    const int q4 = n >> 2, p4 = n & 3;
    const int kq = w * 1024 + lane * 16;
    const unsigned vb = (unsigned)(size_t)lds + 24576u + (unsigned)(w * 1024 + (p4 >> 1) * 256 + ((4 * g + q4) ^ ((p4 >> 1) * 8)) * 16 + (p4 & 1) * 8);
    stage(0);
#pragma unroll 1
    for (int i = 0; i < 4; ++i) {
        if (i < 3) { stage(i + 1); WAIT_V(7); } else WAIT_V(0);
        BAR(); asm volatile("" ::: "memory");
        const ldsp bp = lds + (i & 1) * BUF;
        const bf16x8 Q = *(const LAS bf16x8*)(bp + 49152 + kq);
        f32x4 sc[17]; bf16x8 KF[17];
#pragma unroll
        for (int t = 0; t < 17; ++t) KF[t] = *(const LAS bf16x8*)(bp + kq + t * 1024);
        SCHED();
#pragma unroll
        for (int t = 0; t < 17; ++t) sc[t] = mma32(KF[t], Q, (f32x4){0.f, 0.f, 0.f, 0.f});
#pragma unroll
        for (int k = 0; k < 4; ++k) { if (4 * g + k <= n) sc[0][k] = ninf; if (4 * g + k > n) sc[16][k] = ninf; }
        if (tmin > 0) {
#pragma unroll
            for (int t = 0; t < 16; ++t) if (t < tmin) sc[t] = (f32x4){ninf, ninf, ninf, ninf};
        }
        float m = max3f(sc[16][0], sc[16][1], max3f(sc[16][2], sc[16][3], ninf));
#pragma unroll
        for (int t = 0; t < 16; ++t) m = max3f(max3f(m, sc[t][0], sc[t][1]), sc[t][2], sc[t][3]);
        m = rows_max(m);
        const float mc = -m * csc;
        f32x4 sum4 = {0.f, 0.f, 0.f, 0.f};
#pragma unroll
        for (int t = 0; t < 17; ++t) {
#pragma unroll
            for (int k = 0; k < 4; ++k) sc[t][k] = __builtin_amdgcn_exp2f(fmaf(sc[t][k], csc, mc));
            sum4 += sc[t];
        }
        const float inv = __builtin_amdgcn_rcpf(rows_sum((sum4[0] + sum4[1]) + (sum4[2] + sum4[3])));
        const unsigned v0 = vb + (unsigned)((i & 1) * BUF);
        s16x4 V0[17], V1[17];
#define VRD(t) TRD(V0[t], v0, (t) * 1024); TRD(V1[t], v0, (t) * 1024 + 512)
        VRD(0); VRD(1); VRD(2); VRD(3); VRD(4); VRD(5); VRD(6); VRD(7); VRD(8); VRD(9); VRD(10); VRD(11); VRD(12); VRD(13); VRD(14); VRD(15); VRD(16);
#undef VRD
        u32x2 P[17];
#pragma unroll
        for (int t = 0; t < 17; ++t) P[t] = (u32x2){pk_bf16(sc[t][0], sc[t][1]), pk_bf16(sc[t][2], sc[t][3])};
        WAIT_L(0); SCHED();
        f32x4 d0 = {0.f, 0.f, 0.f, 0.f}, d1 = d0;
#pragma unroll
        for (int tp = 0; tp < 8; ++tp) {
            const bf16x8 PP = asbf((u32x4){P[2 * tp].x, P[2 * tp].y, P[2 * tp + 1].x, P[2 * tp + 1].y});
            d0 = mma32(cat8(V0[2 * tp], V0[2 * tp + 1]), PP, d0); d1 = mma32(cat8(V1[2 * tp], V1[2 * tp + 1]), PP, d1);
        }
        d0 = mma16(__builtin_bit_cast(u32x2, V0[16]), P[16], d0); d1 = mma16(__builtin_bit_cast(u32x2, V1[16]), P[16], d1);
        BAR();
        store_frag(orr, frag_off(lane), (unsigned)(w * 32768 + (hq * 4 + i) * 1024), d0 * inv, d1 * inv);
    }
    WAIT_V(0);
    signal((unsigned*)(p.ws + WS_CTL), C_OB + qb * 32, 1);
}

DI void out_tail(const Params& p, int c, int q, ldsp lds) {
    const int tid = fresh_tid(), w = __builtin_amdgcn_readfirstlane(tid >> 6), lane = tid & 63, g = lane >> 4, n = lane & 15;
    unsigned* ctl = (unsigned*)(p.ws + WS_CTL);
    bf16x8 WT[16][2];
    {
        ldsp my = lds + w * 16384;
        const int toff = (8 * g + (n >> 2)) * 64 + (n & 3) * 8;
#pragma unroll
        for (int r = 0; r < 2; ++r) {
#pragma unroll
            for (int i = 0; i < 16; ++i)
                __builtin_amdgcn_global_load_lds((const unsigned*)(p.wo + (size_t)(512 * w + 256 * r + 16 * i + (lane >> 2)) * 1024 + 32 * c + (lane & 3) * 8), (LAS unsigned*)(my + i * 1024), 16, 0, 0);
            WAIT_V(0);
#pragma unroll
            for (int k = 0; k < 8; ++k)
#pragma unroll
                for (int ct = 0; ct < 2; ++ct) { WT[8 * r + k][ct] = cat8(trd(my + k * 2048 + ct * 32 + toff), trd(my + k * 2048 + ct * 32 + toff + 256)); }
#pragma unroll
            for (int k = 0; k < 8; ++k) asm volatile("" : "+v"(WT[8 * r + k][0]), "+v"(WT[8 * r + k][1]));
            WAIT_L(0);
        }
    }
    BAR();
#pragma unroll 1
    for (int hs = q; hs < 32; hs += 3) {
        const int s = 48 + (hs >> 1), rt = hs & 1;
        if (w == 0) wait_count<3>(ctl, C_OS + (s - 48) * 256 + (c >> 3) * 64, 4u);
        asm volatile("" ::: "memory"); BAR(); asm volatile("" ::: "memory");
        const rsrc_t ar = mkrsrc(p.ws + WS_OA + (size_t)(2 * s + rt) * 131072 + (size_t)w * 16384, 16384u);
        const unsigned ao = (unsigned)lane * 16u;
        u32x4 A[16];
#pragma unroll
        for (int k = 0; k < 16; ++k) A[k] = ld16_sc1(ar, ao + (k & 3) * 1024, (unsigned)(k >> 2) * 4096u);
        SCHED();
#pragma unroll
        for (int ct = 0; ct < 2; ++ct) {
            f32x4 acc = {0.f, 0.f, 0.f, 0.f};
#pragma unroll
            for (int k = 0; k < 16; ++k) acc = mma32(WT[k][ct], asbf(A[k]), acc);
            *(LAS f32x4*)(lds + w * 2048 + ct * 1024 + lane * 16) = acc;
        }
        LDS_BAR();
        if (w < 2) {
            f32x4 r[8];
#pragma unroll
            for (int ww = 0; ww < 8; ++ww) r[ww] = *(const LAS f32x4*)(lds + ww * 2048 + w * 1024 + lane * 16);
            SCHED();
            const f32x4 t = ((r[0] + r[1]) + (r[2] + r[3])) + ((r[4] + r[5]) + (r[6] + r[7]));
            *(u32x2*)(p.out + (size_t)(32 * s + 16 * rt + n) * 2048 + 32 * c + 16 * w + 4 * g) = (u32x2){pk_bf16(t[0], t[1]), pk_bf16(t[2], t[3])};
        }
        WAIT_L(0);
    }
    BAR();
}

DI void big_unit(const Params& p, int id, Unit& u) {
    u.lda = DM * 2; u.nt = 16; u.ak = 128; u.ah = 128 * DM * 2;
    if (id < 384) {
        const int pm = id & 7, pn = id >> 3;
        const u16 *w0 = p.wq, *w1 = p.wk, *w2 = p.wv; const u16* W = pn < 16 ? w0 : (pn < 32 ? w1 : w2);
        u.A = (const char*)(p.ax + (size_t)pm * 256 * DM); u.B = (const char*)(W + (pn & 15) * 256); u.ldb = 4096 * 2;
        u.C = p.ws + WS_QKVX + (size_t)(2 * pn) * XH + (size_t)pm * 65536; u.ldc = 256; u.cw = 1024; u.ch = XH;
        if (pn < 16) { u.sig = C_Q + pm * 32; u.nsig = 1; } else { u.sig = C_KV + 2 * pm * 512 + (pn & 15) * 32; u.nsig = 4; }
    } else if (id < 480) {
        const int pm = (id - 384) & 7, pn = (id - 384) >> 3;
        u.A = (const char*)(p.bx + (size_t)pm * 256 * DM); u.B = (const char*)(p.bwqkv + pn * 256); u.ldb = LDB3 * 2;
        u.C = p.ws + WS_QKVB + (size_t)(8 * pn) * BH + (size_t)pm * 16384; u.ldc = 64; u.cw = BH; u.ch = 4 * BH; u.sig = C_B + pm * 32; u.nsig = 1;
    } else {
        const bool isb = id < 512; const int q = isb ? id - 480 : id - 512, pm = q >> 2, pn = q & 3;
        const u16 *w0 = p.wo, *w1 = p.bwo;
        u.nt = isb ? 16 : 64; u.lda = 0; u.ak = 2048; u.ah = (unsigned)u.nt * 16384u;
        u.A = p.ws + (isb ? WS_OB : WS_OA) + (size_t)pm * 2 * u.ah; u.B = (const char*)((isb ? w1 : w0) + pn * 256); u.ldb = DM * 2;
        u.C = (char*)(p.out + (size_t)pm * 256 * 2048 + (isb ? 1024 : 0) + pn * 256); u.ldc = 4096; u.cw = 64; u.ch = 256; u.sig = 0; u.nsig = 0;
    }
}
struct BigSched { const Params& p; int id0, id1; DI bool next(int i, Unit& u) const { const int id = i == 0 ? id0 : (i == 1 ? id1 : -1); if (id < 0) return false; big_unit(p, id, u); return true; } };

__global__ __launch_bounds__(512, 2) void k_fused(Params p) {
    extern __shared__ __attribute__((aligned(16))) unsigned char shm[];
    ldsp lds = (ldsp)shm;
    unsigned* ctl = (unsigned*)(p.ws + WS_CTL);
    const int b = blockIdx.x, id = (b & 3) * 32 + (b >> 3);
#define WSTAMP(code)
    WSTAMP(1);
    if ((b & 7) < 4) {
        chain_role(p, id, lds, (unsigned long long*)(p.ws + WS_END));
        BAR();
        WSTAMP(2);
    } else {
#pragma unroll 1
        for (int r = 0; r < 2; ++r) {
            const int rb = 2 * r + (id & 1), ct = id >> 1;
            const u16 *w1 = p.wk, *w2 = p.wv;
            Unit u; u.A = (const char*)(p.ax + (size_t)rb * 128 * DM); u.lda = DM * 2; u.B = (const char*)((ct < 32 ? w1 : w2) + (ct & 31) * 128); u.ldb = 4096 * 2; u.nt = 16;
            u.C = p.ws + WS_QKVX + (size_t)(32 + ct) * XH + (size_t)rb * 32768; u.ldc = 256; u.cw = 1024; u.ch = 0; u.ak = 128; u.ah = 0;
            u.sig = C_KV + rb * 512 + (ct & 31) * 16; u.nsig = 1;
            gemm_small(lds, u, ctl);
            WSTAMP(3);
        }
        {
            const int t = fresh_tid(), wv = __builtin_amdgcn_readfirstlane(t >> 6);
            const char* src; const int c = id & 7, q = id >> 3;
            if (c < 2) src = (const char*)p.ax + (size_t)(q * 2 + c) * 131072; else if (c < 4) src = (const char*)p.bx + (size_t)(q * 2 + c - 2) * 131072;
            else if (c < 7) src = (const char*)p.bwqkv + (size_t)(q * 3 + c - 4) * 131072; else src = (const char*)p.bwo + (size_t)q * 131072;
#pragma unroll
            for (int i = 0; i < 16; ++i) __builtin_amdgcn_global_load_lds((const unsigned*)(src + i * 8192 + t * 16), (LAS unsigned*)(lds + L_SCR + wv * 1024), 16, 0, 0);
        }
    }
    volatile LAS unsigned* qw = (volatile LAS unsigned*)(lds + 131072);
    const int w = __builtin_amdgcn_readfirstlane(threadIdx.x >> 6);
    const int k = b & 3;
    bool fixed = (b & 7) >= 4;
#pragma unroll 1
    for (;;) {
        int kind, x, it = -1, x2 = -1;
        if (fixed) { const int i = id & 31; kind = 6; x = i < 24 ? (16 + 8 * k + (i & 7)) * 8 + 2 + (i >> 3) : (4 * k + (i & 3)) * 8 + ((i >> 2) & 1); x2 = x + (i < 24 ? 3 : 2); fixed = false; }
        else {
        if (threadIdx.x == 0) qw[0] = __hip_atomic_fetch_add(ctl + C_HEAD + k * 32, 1u, RLX_AGENT) + 1u;
        LDS_BAR();
        it = (int)__builtin_amdgcn_readfirstlane(qw[0]);
        LDS_BAR();
        if (it < 4) { kind = 0; x = it * 4 + k; }
        else if (it < 28) { kind = 6; x = 384 + 24 * k + it - 4; }
        else if (it < 36) { kind = 6; x = (4 * k + ((it - 28) & 3)) * 8 + 4 + ((it - 28) >> 2); }
        else if (it < 38) { kind = 6; x = 512 + (it - 36) * 4 + k; }
        else if (it < 42) { kind = 0; x = (it - 38 + 4) * 4 + k; }
        else if (it < 74) { kind = 1; x = ((it - 42) >> 1) * 8 + 2 * k + ((it - 42) & 1); }
        else if (it < 82) { kind = 6; x = (4 * k + ((it - 74) & 3)) * 8 + 6 + ((it - 74) >> 2); }
        else if (it < 86) { kind = 4; x = (it - 82) * 4 + k; }
        else if (it < 94) { kind = 6; x = 480 + (it - 86) * 4 + k; }
        else if (it < 110) { kind = 2; x = (4 + ((it - 94) >> 1)) * 8 + 2 * k + ((it - 94) & 1); }
        else if (it < 134) { kind = 5; x = ((it - 110) >> 3) * 32 + 8 * k + ((it - 110) & 7); }
        else break;
        }
        if (kind == 6) {
            if (x >= 480) { const int w0 = x < 512 ? C_OB + ((x - 480) >> 2) * 64 : C_OA + ((x - 512) >> 2) * 64; const unsigned need = x < 512 ? 64u : 32u; wg_wait(ctl, w0, need, w0 + 32, need); }
            BigSched S{p, x, x2};
            gemm_stream(lds, S, ctl);
        } else
        if (kind == 0) {
            const int s0 = (x >> 2) * 4;
            wait_count(ctl, C_Q + (s0 >> 3) * 32, 128u); wait_count(ctl, C_PROG, (unsigned)s0 + 5u);
            asm volatile("" ::: "memory");
#pragma unroll 1
            for (int s = s0; s < s0 + 4; ++s) attn_a_item<false>(p, s, (x & 3) * 8 + w, lds + w * 12288, fresh_tid() & 63);
            WAIT_V(0); signal(ctl, C_OA + (x >> 2) * 32, 1);
            BAR();
        } else if (kind == 4) {
#pragma unroll 1
            for (int s = 32 + (x >> 2); s < NSEG; s += 4) {
                wait_count(ctl, C_Q + (s >> 3) * 32, 128u); wait_count(ctl, C_KV + (s >> 2) * 512 + ((x & 3) * 8 + w) * 16, 16u);
                asm volatile("" ::: "memory");
                attn_a_item<true>(p, s, (x & 3) * 8 + w, lds + w * 12288, fresh_tid() & 63);
                WAIT_V(0);
                if (s < 48) signal(ctl, C_OA + (s >> 2) * 32, 1);
                else { BAR(); const int l = fresh_tid(); if (l < 4) __hip_atomic_fetch_add(ctl + C_OS + (s - 48) * 256 + l * 64, 1u, RLX_AGENT); }
            }
            BAR();
        } else if (kind == 1) {
            const int qb = x >> 3;
            wait_count(ctl, C_B + (qb >> 1) * 32, 96u); wait_count(ctl, C_B + max((qb >> 1) - 1, 0) * 32, 96u);
            asm volatile("" ::: "memory");
            attn_b_item(p, x & 7, qb, lds);
        } else if (kind == 5) {
            out_tail(p, x & 31, x >> 5, lds);
        } else {
            const int rb = x >> 3, ct = x & 7;
            Unit u; u.ldb = DM * 2; u.ldc = 2048 * 2; u.sig = 0; u.nsig = 0; u.lda = 0; u.ak = 2048; u.ah = 0; u.cw = 64; u.ch = 0;
            u.A = p.ws + WS_OA + (size_t)rb * 64 * 16384; u.B = (const char*)(p.wo + ct * 128); u.nt = 64; u.C = (char*)(p.out + (size_t)rb * 128 * 2048 + ct * 128);
            wg_wait(ctl, C_OA + rb * 32, rb < 8 ? 32u : 128u, C_OA + rb * 32, 32u);
            gemm_small(lds, u, ctl);
        }
        WSTAMP(16 + (kind == 6 && x >= 480 ? (x < 512 ? 7 : 8) : kind) + ((it & 0xffff) << 8));
    }
}

extern "C" void kernel_launch(void* const* d_in, const int* in_sizes, int n_in, void* d_out, int out_size, void* d_ws, size_t ws_size, hipStream_t stream) {
    Params p;
    p.ax = (const u16*)d_in[0]; p.mem0 = (const u16*)d_in[1]; p.wq = (const u16*)d_in[2]; p.wk = (const u16*)d_in[3]; p.wv = (const u16*)d_in[4]; p.wo = (const u16*)d_in[5];
    p.bx = (const u16*)d_in[7]; p.bwqkv = (const u16*)d_in[8]; p.bwo = (const u16*)d_in[9];
    p.out = (u16*)d_out; p.ws = (char*)(((uintptr_t)d_ws + WS_SHIFT(ws_size) + 0x1FFFFFu) & ~(uintptr_t)0x1FFFFFu);
    (void)hipFuncSetAttribute((const void*)k_fused, hipFuncAttributeMaxDynamicSharedMemorySize, LDS_BYTES);
    (void)hipMemsetAsync(p.ws + WS_XCH, 0xFF, (size_t)NWIN_INIT * 16384, stream);
    k_fused<<<256, 512, LDS_BYTES, stream>>>(p);
}
```

```cpp
#include <hip/hip_runtime.h>
#include <stdint.h>
#include <stdlib.h>

#define LAS __attribute__((address_space(3)))
#define DI __device__ __forceinline__
typedef unsigned short u16;
typedef __attribute__((ext_vector_type(8))) __bf16 bf16x8;
typedef __attribute__((ext_vector_type(16))) __bf16 bf16x16;
typedef __attribute__((ext_vector_type(2))) __bf16 bf16x2;
typedef __attribute__((ext_vector_type(4))) short s16x4;
typedef __attribute__((ext_vector_type(8))) short s16x8;
typedef __attribute__((ext_vector_type(16))) short s16x16;
typedef __attribute__((ext_vector_type(2))) float f32x2;
typedef __attribute__((ext_vector_type(4))) float f32x4;
typedef __attribute__((ext_vector_type(2))) unsigned u32x2;
typedef __attribute__((ext_vector_type(4))) unsigned u32x4;
typedef LAS unsigned char* ldsp;
typedef __amdgpu_buffer_rsrc_t rsrc_t;

constexpr int S_LEN = 2048, DM = 1024, NH = 32, DH = 128, NSEG = 64;
constexpr int LDB3 = 3072;
constexpr unsigned XH = S_LEN * 256, BH = S_LEN * 64;
constexpr unsigned SENT = 0xFFFFFFFFu;
constexpr size_t WS_XCH = 2u << 20;
constexpr int NWIN = 640, NWIN_INIT = 7;
constexpr unsigned XCH_KM = NWIN * 16384u;
constexpr size_t XCH_BYTES = (size_t)XCH_KM + 64u * 128u * 1536u;
constexpr size_t WS_CTL = WS_XCH + 2 * 16384;
constexpr int C_TMO = 0, C_PAR = 13312  , C_PHASE = 13314, C_HEAD = 3072, C_PROG = 192, C_PACE = 128, C_Q = 256, C_B = C_Q + 8 * 32, C_OA = C_B + 8 * 32, C_OB = C_OA + 16 * 32, C_OS = 16384, C_KV = 4096, C_PROBE = 12288;
constexpr size_t WS_QKVX = 26u << 20;
constexpr size_t WS_QKVB = WS_QKVX + (size_t)96 * XH;
constexpr size_t WS_OA = WS_QKVB + (size_t)96 * BH;
constexpr size_t WS_OB = WS_OA + (size_t)S_LEN * 4096 * 2;
constexpr size_t WS_END = WS_OB + (size_t)S_LEN * 1024 * 2;
#define WS_SHIFT(sz) 0
constexpr int LDS_BYTES = 131072 + 1024 + 8192;

DI unsigned pk_bf16(float a, float b) { f32x2 v = {a, b}; return __builtin_bit_cast(unsigned, __builtin_convertvector(v, bf16x2)); }
DI s16x4 trd(ldsp p) { return __builtin_amdgcn_ds_read_tr16_b64_v4i16((LAS s16x4*)p); }
DI bf16x8 cat8(s16x4 a, s16x4 b) { return __builtin_bit_cast(bf16x8, __builtin_shufflevector(a, b, 0, 1, 2, 3, 4, 5, 6, 7)); }
DI bf16x16 cat16(bf16x8 a, bf16x8 b) { return __builtin_shufflevector(a, b, 0, 1, 2, 3, 4, 5, 6, 7, 8, 9, 10, 11, 12, 13, 14, 15); }
DI bf16x8 lo8(bf16x16 v) { return __builtin_shufflevector(v, v, 0, 1, 2, 3, 4, 5, 6, 7); }
DI bf16x8 hi8(bf16x16 v) { return __builtin_shufflevector(v, v, 8, 9, 10, 11, 12, 13, 14, 15); }
DI bf16x8 asbf(u32x4 v) { return __builtin_bit_cast(bf16x8, v); }
DI f32x4 mma32(bf16x8 a, bf16x8 b, f32x4 c) { return __builtin_amdgcn_mfma_f32_16x16x32_bf16(a, b, c, 0, 0, 0); }
DI f32x4 mma16(u32x2 a, u32x2 b, f32x4 c) { return mma32(asbf((u32x4){a.x, a.y, 0u, 0u}), asbf((u32x4){b.x, b.y, 0u, 0u}), c); }
DI f32x4 smma(u32x4 a, bf16x16 b, f32x4 c, int idx) { return __builtin_amdgcn_smfmac_f32_16x16x64_bf16(asbf(a), b, c, idx, 0, 0); }
DI rsrc_t mkrsrc(const void* p, unsigned bytes) { return __builtin_amdgcn_make_buffer_rsrc((void*)p, 0, bytes, 0x00020000); }
DI u32x4 ld16_sc1(rsrc_t r, unsigned voff, unsigned soff) { return __builtin_amdgcn_raw_buffer_load_b128(r, voff, soff, 16); }
#define ST16(v, r, voff, soff, aux) do { const u32x4 st_v = (v); __builtin_amdgcn_raw_buffer_store_b128(st_v, r, voff, soff, aux); asm volatile("s_nop 1" :: "v"(st_v)); } while (0)
DI void swap16(unsigned& a, unsigned& b) { auto r = __builtin_amdgcn_permlane16_swap(a, b, false, false); a = r[0]; b = r[1]; }
DI void swap32(unsigned& a, unsigned& b) { auto r = __builtin_amdgcn_permlane32_swap(a, b, false, false); a = r[0]; b = r[1]; }
DI void swap16f(float& a, float& b) { unsigned x = __float_as_uint(a), y = __float_as_uint(b); swap16(x, y); a = __uint_as_float(x); b = __uint_as_float(y); }
DI void swap32f(float& a, float& b) { unsigned x = __float_as_uint(a), y = __float_as_uint(b); swap32(x, y); a = __uint_as_float(x); b = __uint_as_float(y); }
DI float rows_max(float v) { float a = v, b = v; swap16f(a, b); v = fmaxf(a, b); a = v; b = v; swap32f(a, b); return fmaxf(a, b); }
DI float rows_sum(float v) { float a = v, b = v; swap16f(a, b); v = a + b; a = v; b = v; swap32f(a, b); return a + b; }
DI unsigned umax4(u32x4 v) { return max(max(v.x, v.y), max(v.z, v.w)); }
#define WAIT_V(n) asm volatile("s_waitcnt vmcnt(" #n ")" ::: "memory")
#define WAIT_L(n) asm volatile("s_waitcnt lgkmcnt(" #n ")" ::: "memory")
#define BAR() __builtin_amdgcn_s_barrier()
#define SCHED() __builtin_amdgcn_sched_barrier(0)
#define LDS_BAR() do { asm volatile("s_waitcnt lgkmcnt(0)" ::: "memory"); __builtin_amdgcn_s_barrier(); asm volatile("" ::: "memory"); } while (0)

constexpr int HTB = 128 * 64 * 2;
DI int lds_byte(int r, int c) { const int st = (r >> 4) * 2 + (c >> 5), rr = r & 15, cc = c & 31, ob = rr * 64 + cc * 2; return st * 1024 + (ob ^ (((ob >> 9) & 1) << 5)); }
DI void stage_rc(int b, int& R, int& C) { const int st = b / 1024, sb = b % 1024, swz = sb ^ (((sb >> 9) & 1) << 5); R = (st >> 1) * 16 + swz / 64; C = (st & 1) * 32 + (swz % 64) / 2; }

struct Params {
    const u16 *ax, *mem0, *wq, *wk, *wv, *wo, *bx, *bwqkv, *bwo;
    u16* out; char* ws;
};
#define RLX_AGENT __ATOMIC_RELAXED, __HIP_MEMORY_SCOPE_AGENT
DI bool spin_out(unsigned& spins, unsigned* ctl) {
    spins = __builtin_amdgcn_readfirstlane(spins) + 1u;
    if ((spins & 63u) == 0u) {
        if (spins > (1u << 18)) { __hip_atomic_store(ctl + C_TMO, 1u, RLX_AGENT); return true; }
        if (__builtin_amdgcn_readfirstlane(__hip_atomic_load(ctl + C_TMO, RLX_AGENT)) != SENT) return true;
    }
    return false;
}
template <int NAP = 8>
DI void wait_count(unsigned* ctl, int word, unsigned need) {
    unsigned spins = 0;
    while (__builtin_amdgcn_readfirstlane(__hip_atomic_load(ctl + word, RLX_AGENT)) + 1u < need) { __builtin_amdgcn_s_sleep(NAP); if (spin_out(spins, ctl)) break; }
}
DI void wg_wait(unsigned* ctl, int word0, unsigned need0, int word1, unsigned need1) {
    if (threadIdx.x < 64) {
        wait_count(ctl, word0, need0); wait_count(ctl, word1, need1);
        __builtin_amdgcn_fence(__ATOMIC_ACQUIRE, "agent");
        asm volatile("s_waitcnt vmcnt(0)" ::: "memory");
    }
    asm volatile("" ::: "memory"); BAR(); asm volatile("" ::: "memory");
}
struct Unit { const char* A; const char* B; char* C; unsigned lda, ldb, ldc, cw, ch, ak, ah; int nt, sig, nsig; };
DI int fresh_tid() { int t = threadIdx.x; asm volatile("" : "+v"(t)); return t; }
DI void signal(unsigned* ctl, int sig, int nsig) { const int lane = fresh_tid() & 63; if (lane < nsig) __hip_atomic_fetch_add(ctl + sig + (lane & 1) * 16 + (lane >> 1) * 512, 1u, RLX_AGENT); }
DI u32x4 pack_pair(f32x4 v0, f32x4 v1) {
    unsigned ax = pk_bf16(v0[0], v0[1]), ay = pk_bf16(v0[2], v0[3]), bx = pk_bf16(v1[0], v1[1]), by = pk_bf16(v1[2], v1[3]);
    swap16(ax, bx); swap16(ay, by);
    return (u32x4){ax, ay, bx, by};
}
DI unsigned frag_off(int lane) { return (unsigned)((((lane >> 4) & 1) * 2 + (lane >> 5)) * 256 + (lane & 15) * 16); }
DI void store_frag(rsrc_t cr, unsigned voff, unsigned soff, f32x4 v0, f32x4 v1) { ST16(pack_pair(v0, v1), cr, voff, soff, 16); }
DI u32x4 to_rows(ldsp sc, int lane, f32x4 v0, f32x4 v1) {
    *(volatile LAS u32x4*)(sc + ((lane & 15) * 4 + ((lane >> 4) & 1) * 2 + (lane >> 5)) * 16) = pack_pair(v0, v1);
    return *(volatile LAS u32x4*)(sc + lane * 16);
}
constexpr int L_SCR = 132096;
#define STAGE(bufoff, gbase, voff, step2) do { \
        __builtin_amdgcn_global_load_lds((const unsigned*)((gbase) + (voff)), (LAS unsigned*)(lds + (bufoff) + ldsw), 16, 0, 0); \
        __builtin_amdgcn_global_load_lds((const unsigned*)((gbase) + (step2) + (voff)), (LAS unsigned*)(lds + (bufoff) + ldsw + 8192), 16, 0, 0); } while (0)
#define LDA(dst, off) do { _Pragma("unroll") for (int m = 0; m < 4; ++m) _Pragma("unroll") for (int k = 0; k < 2; ++k) dst[m][k] = *(const LAS bf16x8*)(lds + (off) + aoff + m * 2048 + k * 1024); } while (0)
#define TRD(dst, base, off) asm volatile("ds_read_b64_tr_b16 %0, %1 offset:%2" : "=v"(dst) : "v"(base), "i"(off) : "memory")
#define LDB(dst, v0, v1, off) do { s16x4 l0, h0, l1, h1, l2, h2, l3, h3; \
        TRD(l0, v0, (off)); TRD(h0, v0, (off) + 256); TRD(l1, v0, (off) + 8192); TRD(h1, v0, (off) + 8448); \
        TRD(l2, v1, (off)); TRD(h2, v1, (off) + 256); TRD(l3, v1, (off) + 8192); TRD(h3, v1, (off) + 8448); \
        dst[0][0] = cat8(l0, h0); dst[0][1] = cat8(l1, h1); dst[1][0] = cat8(l2, h2); dst[1][1] = cat8(l3, h3); } while (0)
#define MMA(accv, At, Bt) do { SCHED(); __builtin_amdgcn_s_setprio(1); _Pragma("unroll") for (int m = 0; m < 4; ++m) _Pragma("unroll") for (int n = 0; n < 2; ++n) _Pragma("unroll") for (int k = 0; k < 2; ++k) \
        accv[m][n] = mma32(Bt[n][k], At[m][k], accv[m][n]); __builtin_amdgcn_s_setprio(0); } while (0)
DI unsigned va_of(int tid, unsigned lda, int nt) { int R, C; stage_rc(tid * 16, R, C); return lda ? (unsigned)R * lda + (unsigned)C * 2u : (unsigned)((tid >> 7) * nt * 2048 + (tid & 127) * 16); }
DI unsigned vb_of(int tid, int wid, unsigned ldb) { return (unsigned)(8 * (wid >> 1) + ((tid >> 2) & 7)) * ldb + (unsigned)((2 * (wid & 1) + ((tid >> 5) & 1)) * 64 + (((tid & 3) ^ (((wid >> 1) & 1) << 1)) * 16)); }
#define GEMM_LANE_SETUP(lda0) \
    const int tid = fresh_tid(); \
    const int wid = __builtin_amdgcn_readfirstlane(tid >> 6), lane = tid & 63, wr = wid >> 2, wc = wid & 3, fr = lane & 15, fq = lane >> 4; \
    const unsigned ldsw = (unsigned)wid * 1024u; \
    const int aoff = (lda0) ? lds_byte(wr * 64 + fr, fq * 8) : wr * 8192 + lane * 16; \
    const int boff0 = fq * 2048 + wc * 512 + (fr >> 2) * 64 + (fq & 1) * 32 + (fr & 3) * 8, boff1 = boff0 ^ 32;

template <class Sched>
DI void gemm_stream(ldsp lds, const Sched& S, unsigned* ctl) {
    Unit cur, nxt; int ui = 0;
    if (!S.next(0, cur)) return;
    GEMM_LANE_SETUP(cur.lda);
#define SA(b, h) (((b) * 2 + (h)) * HTB)
#define SB(b, h) ((4 + (b) * 2 + (h)) * HTB)
    const unsigned vb0 = (unsigned)(size_t)lds + 65536u + (unsigned)boff0, vb1 = (unsigned)(size_t)lds + 65536u + (unsigned)boff1;
    f32x4 acc[2][2][4][2];
#pragma unroll
    for (int a = 0; a < 2; ++a)
#pragma unroll
        for (int b = 0; b < 2; ++b)
#pragma unroll
            for (int m = 0; m < 4; ++m)
#pragma unroll
                for (int n = 0; n < 2; ++n) acc[a][b][m][n] = (f32x4){0.f, 0.f, 0.f, 0.f};
    bf16x8 At[4][2], B0[2][2], B1[2][2];
    const unsigned vA = va_of(tid, cur.lda, cur.nt); unsigned vB = vb_of(tid, wid, cur.ldb);
    const size_t a2s = cur.lda ? (size_t)64 * cur.lda : (size_t)cur.nt * 8192;
    {
        const size_t ah = cur.ah, b2s = (size_t)32 * cur.ldb, bk = (size_t)64 * cur.ldb;
        STAGE(SB(0, 0), cur.B, vB, b2s); STAGE(SB(0, 1), cur.B + 256, vB, b2s); STAGE(SA(0, 0), cur.A, vA, a2s); STAGE(SA(0, 1), cur.A + ah, vA, a2s);
        if (wr == 1) BAR();
        WAIT_V(2); BAR();
        STAGE(SB(1, 0), cur.B + bk, vB, b2s); STAGE(SA(1, 0), cur.A + cur.ak, vA, a2s); STAGE(SB(1, 1), cur.B + bk + 256, vB, b2s);
        WAIT_V(6); BAR();
    }
    int psig = 0, pn = 0;
    for (;;) {
        const bool has_next = S.next(ui + 1, nxt);
        if (!has_next) nxt = cur;
        int nt = cur.nt; asm volatile("" : "+s"(nt));
        for (int t = 0; t < nt; t += 2) {
            const bool last = (t == nt - 2);
            const char* a1 = cur.A + (size_t)(t + 1) * cur.ak + cur.ah;
            if (last) vB = vb_of(fresh_tid(), wid, nxt.ldb);
            const unsigned ldb = last ? nxt.ldb : cur.ldb;
            const char* a2 = last ? nxt.A : cur.A + (size_t)(t + 2) * cur.ak;
            const char* b2 = last ? nxt.B : cur.B + (size_t)(t + 2) * 64 * cur.ldb;
            const size_t ah = cur.ah, b2s = (size_t)32 * ldb;
            const char* a3 = a2 + cur.ak; const char* b3 = b2 + (size_t)64 * ldb;
            LDB(B0, vb0, vb1, 0); LDB(B1, vb0, vb1, HTB); SCHED(); LDA(At, SA(0, 0)); STAGE(SA(1, 1), a1, vA, a2s);
            WAIT_V(8); WAIT_L(0); BAR(); MMA(acc[0][0], At, B0); MMA(acc[0][1], At, B1); BAR(); SCHED();
            LDA(At, SA(0, 1)); STAGE(SB(0, 0), b2, vB, b2s); STAGE(SB(0, 1), b2 + 256, vB, b2s); STAGE(SA(0, 0), a2, vA, a2s);
            WAIT_V(8);
            asm volatile("" : "+s"(pn));
            if (pn) { signal(ctl, psig, pn); pn = 0; }
            WAIT_L(0); BAR(); MMA(acc[1][0], At, B0); MMA(acc[1][1], At, B1); BAR(); SCHED();
            LDB(B0, vb0, vb1, 2 * HTB); LDB(B1, vb0, vb1, 3 * HTB); SCHED(); LDA(At, SA(1, 0)); STAGE(SA(0, 1), a2 + ah, vA, a2s);
            WAIT_V(8); WAIT_L(0); BAR(); MMA(acc[0][0], At, B0); MMA(acc[0][1], At, B1); BAR(); SCHED();
            LDA(At, SA(1, 1)); STAGE(SB(1, 0), b3, vB, b2s); STAGE(SB(1, 1), b3 + 256, vB, b2s); STAGE(SA(1, 0), a3, vA, a2s);
            WAIT_V(8); WAIT_L(0); BAR(); MMA(acc[1][0], At, B0); MMA(acc[1][1], At, B1); BAR(); SCHED();
        }
        if (wr == 0) BAR();
        {
            const rsrc_t cr = mkrsrc(cur.C, 0x1000000u);
            const int tz = fresh_tid() & 63;
            if (cur.nsig) {
                const unsigned co = (unsigned)(wr * 64) * cur.ldc + (unsigned)wc * cur.cw + frag_off(tz);
#pragma unroll
                for (int ai = 0; ai < 2; ++ai)
#pragma unroll
                    for (int m = 0; m < 4; ++m)
#pragma unroll
                        for (int bj = 0; bj < 2; ++bj) store_frag(cr, co, (unsigned)(ai * 128 + m * 16) * cur.ldc + (unsigned)bj * cur.ch, acc[ai][bj][m][0], acc[ai][bj][m][1]);
            } else {
                const unsigned co = (unsigned)(wr * 64 + (tz >> 2)) * cur.ldc + (unsigned)(wc * 64 + (tz & 3) * 16);
#pragma unroll
                for (int ai = 0; ai < 2; ++ai)
#pragma unroll
                    for (int m = 0; m < 4; ++m)
#pragma unroll
                        for (int bj = 0; bj < 2; ++bj)
                            ST16(to_rows(lds + L_SCR + wid * 1024, tz, acc[ai][bj][m][0], acc[ai][bj][m][1]), cr, co, (unsigned)(ai * 128 + m * 16) * cur.ldc + (unsigned)bj * 256u, 0);
            }
        }
        psig = cur.sig; pn = cur.nsig;
        if (!has_next) break;
#pragma unroll
        for (int a = 0; a < 2; ++a)
#pragma unroll
            for (int b = 0; b < 2; ++b)
#pragma unroll
                for (int m = 0; m < 4; ++m)
#pragma unroll
                    for (int n = 0; n < 2; ++n) acc[a][b][m][n] = (f32x4){0.f, 0.f, 0.f, 0.f};
        cur = nxt; ++ui;
        if (wr == 1) BAR();
    }
    WAIT_V(0);
    signal(ctl, psig, pn);
    BAR();
#undef SA
#undef SB
}

DI void gemm_small(ldsp lds, const Unit u, unsigned* ctl) {
    GEMM_LANE_SETUP(u.lda);
    const unsigned vA = va_of(tid, u.lda, u.nt), vB = vb_of(tid, wid, u.ldb);
    const size_t a2s = u.lda ? (size_t)64 * u.lda : (size_t)u.nt * 8192, b2s = (size_t)32 * u.ldb, bk = (size_t)64 * u.ldb;
    f32x4 acc[4][2];
#pragma unroll
    for (int m = 0; m < 4; ++m)
#pragma unroll
        for (int n = 0; n < 2; ++n) acc[m][n] = (f32x4){0.f, 0.f, 0.f, 0.f};
    int nt = u.nt; asm volatile("" : "+s"(nt));
    const unsigned vb0 = (unsigned)(size_t)lds + (unsigned)boff0, vb1 = (unsigned)(size_t)lds + (unsigned)boff1, vb2 = vb0 + 65536u, vb3 = vb1 + 65536u;
#pragma unroll
    for (int t = 0; t < 4; ++t) { STAGE(t * 32768, u.A + (size_t)t * u.ak, vA, a2s); STAGE(t * 32768 + 16384, u.B + t * bk, vB, b2s); }
    bf16x8 A0[4][2], B0[2][2], A1[4][2], B1[2][2];
    WAIT_V(12); BAR();
    LDB(B0, vb0, vb1, 16384); LDA(A0, 0);
#define RDA(dst, m, off) do { dst[m][0] = *(const LAS bf16x8*)(lds + (off) + aoff + (m) * 2048); dst[m][1] = *(const LAS bf16x8*)(lds + (off) + aoff + (m) * 2048 + 1024); } while (0)
#define RDB(dst, nn, v, off) do { s16x4 l0, h0, l1, h1; TRD(l0, v, (off)); TRD(h0, v, (off) + 256); TRD(l1, v, (off) + 8192); TRD(h1, v, (off) + 8448); dst[nn][0] = cat8(l0, h0); dst[nn][1] = cat8(l1, h1); } while (0)
#define MM4(Ac, Bc, m) do { SCHED(); _Pragma("unroll") for (int n = 0; n < 2; ++n) _Pragma("unroll") for (int k = 0; k < 2; ++k) acc[m][n] = mma32(Bc[n][k], Ac[m][k], acc[m][n]); SCHED(); } while (0)
#define KSTEP(i, Ac, Bc, An, Bn, v0, v1, boffn) do { \
        const int tn = min(t0 + (i) + 4, nt - 1); \
        WAIT_L(0); WAIT_V(8); BAR(); \
        MM4(Ac, Bc, 0); RDB(Bn, 0, v0, boffn); RDA(An, 0, (((i) + 1) & 3) * 32768); \
        MM4(Ac, Bc, 1); RDB(Bn, 1, v1, boffn); RDA(An, 1, (((i) + 1) & 3) * 32768); \
        MM4(Ac, Bc, 2); RDA(An, 2, (((i) + 1) & 3) * 32768); STAGE((i) * 32768, u.A + (size_t)tn * u.ak, vA, a2s); \
        MM4(Ac, Bc, 3); RDA(An, 3, (((i) + 1) & 3) * 32768); STAGE((i) * 32768 + 16384, u.B + (size_t)tn * bk, vB, b2s); } while (0)
    for (int t0 = 0; t0 < nt; t0 += 4) {
        KSTEP(0, A0, B0, A1, B1, vb0, vb1, 49152);
        KSTEP(1, A1, B1, A0, B0, vb2, vb3, 16384);
        KSTEP(2, A0, B0, A1, B1, vb2, vb3, 49152);
        KSTEP(3, A1, B1, A0, B0, vb0, vb1, 16384);
    }
    WAIT_L(0); SCHED();
#undef RDA
#undef RDB
#undef MM4
#undef KSTEP
    const rsrc_t cr = mkrsrc(u.C, 128u * u.ldc);
    if (u.nsig) {
#pragma unroll
        for (int m = 0; m < 4; ++m) store_frag(cr, (unsigned)(wr * 64) * u.ldc + (unsigned)wc * u.cw + frag_off(lane), (unsigned)(m * 16) * u.ldc, acc[m][0], acc[m][1]);
    } else {
#pragma unroll
        for (int m = 0; m < 4; ++m)
            ST16(to_rows(lds + L_SCR + wid * 1024, lane, acc[m][0], acc[m][1]), cr, (unsigned)(wr * 64 + (lane >> 2)) * u.ldc + (unsigned)(wc * 64 + (lane & 3) * 16), (unsigned)(m * 16) * u.ldc, 0);
    }
    WAIT_V(0);
    signal(ctl, u.sig, u.nsig);
    BAR();
}

#define win_off(idx) ((unsigned)(idx) * 16384u + ((((unsigned)(idx) >> 7) ^ par) & 1u) * 8192u)
#define W_MEM(s) ((s) == 0 ? 0 : 6 + 2 * (s))
#define W_PART(slot) (192 + (slot) * 64)
#define W_X(slot) (384 + (slot) * 128)
DI bool all_set(unsigned mx) { return __builtin_amdgcn_ballot_w64(mx == SENT) == 0ull; }
DI u32x4 rows_gather(unsigned v) { unsigned a = v, b = v; swap16(a, b); unsigned c = a, d = b; swap32(a, c); swap32(b, d); return (u32x4){a, b, c, d}; }
DI float max3f(float a, float b, float c) { return fmaxf(fmaxf(a, b), c); }
#define KD0 7
#define KD1 7
#define KD2 6
#define KRA 3
#define KDM 3
#define KWP 4
#define KNT1 0
#define KNT2 0
#define KPM 8
constexpr int RED_M = 144;
constexpr int L_PX = 32768, L_PM = L_PX + 512, L_ML = L_PM + 512, L_FLAG = L_ML + 256, L_PACE = L_FLAG + 16, L_VM = 34816, L_OWN = L_VM + 2048, L_X = L_OWN + 1536  ;
constexpr unsigned OOB = 0x80000000u;
#define RETRY(i)
#define SEG(i)
#define PST(i)
#define PIN_S(x) asm volatile("" : "+s"(x))
#define PIN_V(x) asm volatile("" : "+v"(x))
DI float ror8(float v) { return __uint_as_float(__builtin_amdgcn_update_dpp(0, __float_as_uint(v), 0x128, 0xf, 0xf, true)); }

template <int ROLE>
DI void chain_loop(const Params& p, const unsigned par, const int rank, const int w, ldsp lds, const bf16x16 (&W)[6][2], const bf16x16 (&WO)[2][2], unsigned long long* stamps) {
    const int lane = fresh_tid() & 63, g = lane >> 4, n = lane & 15;
    const int h = rank >> 2, j = rank & 3;
    unsigned* ctl = (unsigned*)(p.ws + WS_CTL);
    const rsrc_t xr = mkrsrc(p.ws + WS_XCH, (unsigned)XCH_BYTES);
    const rsrc_t qr = mkrsrc(p.ws + WS_QKVX + (size_t)(32 + h) * XH, 33u * XH);
    const int sidx = (n & 2) ? 0xEEEE : 0x4444;
    const unsigned l16 = (unsigned)lane * 16u;
    const int redw = (w * 1024 + (n & 1) * 512 + (n >> 1) * 8 + 2 * g) * 4;
    const int redr = (w < 4 ? (n >> 3) * RED_M + (4 * w + g) * 8 + (n & 7) : 2 * RED_M + (8 * (w & 1) + 2 * g + ((n >> 1) & 1)) * 8 + 2 * (n >> 2) + (n & 1)) * 4;
    const bool vown = (g >> 1) == (j & 1);
    const unsigned vg1 = (unsigned)(((j & 2) ^ 2) + (g >> 1)) * 1536u + 1024u + (unsigned)(lane & 31) * 16u;
    const unsigned vg0 = vown ? OOB : (vg1 + (unsigned)(j & 2) * 3072u - 3072u);
    const unsigned pub_off = (g == j ? XCH_KM + (unsigned)rank * 1536u : (unsigned)(4 * h + g) * 16384u + (unsigned)j * 1536u) + (unsigned)w * 256u + (unsigned)n * 16u;
    const int red = ((j + 1) & 3) * 32 + h;
    const int rho = 4 * (8 * j + w) + 2 * (g >> 1) + ((n >> 2) & 1);
    const unsigned pst_off = n < 8 ? (unsigned)(rho >> 1) * 16384u + (unsigned)(rho & 1) * 4096u + (unsigned)((h ^ rho) & 31) * 128u + (unsigned)((g & 1) * 64 + (n & 3) * 16) : OOB;
    const unsigned mem_off = lane < 8 ? (unsigned)(4 * ((n >> 1) & 1) + 2 * (n >> 2) + (n & 1)) * 16u : OOB;
    const unsigned pw0 = win_off(W_PART(0)), pw1 = win_off(W_PART(1)), xw0 = win_off(W_X(0)), xw1 = win_off(W_X(1));
    const unsigned redo = (unsigned)(red >> 1) * 16384u + (unsigned)(red & 1) * 4096u;
    const int vxr = L_X + lane * 16;
    const float csc = 0.08838834764831845f * 1.4426950408889634f;
    float ninf = -__builtin_inff(); PIN_V(ninf);
    u32x4 KX[8], VWX[2];
    auto dma_x = [&](int s) {
        if (ROLE == 0 && w < 4) {
            if ((s & 3) == 0) { wait_count(ctl, C_KV + (s >> 2) * 512 + h * 16, 16u); asm volatile("" ::: "memory"); }
#pragma unroll
            for (int i = 0; i < 2; ++i) {
                const unsigned vo = l16, so = (unsigned)s * 8192u + (unsigned)(w * 2 + i) * 1024u;
                __builtin_amdgcn_raw_ptr_buffer_load_lds(qr, (LAS void*)(lds + L_X + (s & 1) * 16384 + (w * 2 + i) * 1024), 16, vo, so + 32u * XH, 0, 16);
                __builtin_amdgcn_raw_ptr_buffer_load_lds(qr, (LAS void*)(lds + L_X + (s & 1) * 16384 + 8192 + (w * 2 + i) * 1024), 16, vo, so, 0, 16);
            }
        }
    };
    auto load_kx = [&](int s) {
#pragma unroll
        for (int kt = 0; kt < 2; ++kt)
#pragma unroll
            for (int kk = 0; kk < 4; ++kk) KX[kt * 4 + kk] = *(const LAS u32x4*)(lds + vxr + (s & 1) * 16384 + 8192 + kt * 4096 + ((j + kk) & 3) * 1024);
    };
    auto vwx_kt = [&](int s, int kt) {
        bf16x8 VA[4];
#pragma unroll
        for (int kk = 0; kk < 4; ++kk) VA[kk] = *(const LAS bf16x8*)(lds + vxr + (s & 1) * 16384 + kt * 4096 + (kk ^ (j & 2)) * 1024);
#pragma unroll
        for (int ct = 0; ct < 2; ++ct) {
            f32x4 d = {0.f, 0.f, 0.f, 0.f};
#pragma unroll
            for (int kk = 0; kk < 4; ++kk) d = mma32(VA[kk], (kk & 1) ? hi8(WO[ct][kk >> 1]) : lo8(WO[ct][kk >> 1]), d);
            VWX[ct][2 * kt] = pk_bf16(d[0], d[1]); VWX[ct][2 * kt + 1] = pk_bf16(d[2], d[3]);
        }
    };
    auto rmax = [&](float v) { float a = v, b = v; swap16f(a, b); v = max3f(a, b, ninf); a = v; b = v; swap32f(a, b); return max3f(a, b, ninf); };
    auto lo2 = [](f32x4 v) { return (f32x2){v[0], v[1]}; };
    auto hi2 = [](f32x4 v) { return (f32x2){v[2], v[3]}; };
    dma_x(0); dma_x(1);
    asm volatile("s_waitcnt vmcnt(0) lgkmcnt(0)" ::: "memory"); BAR(); asm volatile("" ::: "memory");
    PST(5);
    vwx_kt(0, 0); vwx_kt(0, 1);
    if (ROLE == 2) load_kx(0);
    __builtin_amdgcn_s_setprio(2);

    unsigned iv_t0 = 0, iv_acc = 0;
    unsigned spins = 0, pace_c = 0x7fffffffu, pace_t = 0;
    const u32x4 sv = {SENT, SENT, SENT, SENT};
    for (int s = 0;; ++s) {
        unsigned xw = (s & 1) ? xw1 : xw0, pso = (s & 1) ? pw1 : pw0;
        unsigned mso = win_off(W_MEM(s) + (w >> 2)) + (unsigned)(w & 3) * 2048u;
        unsigned rso = ROLE <= 1 ? (xw0 ^ xw1 ^ xw) + (unsigned)rank * 16384u + (unsigned)w * 1024u : (pw0 ^ pw1 ^ pso) + redo + (unsigned)(w & 1) * 2048u;
        unsigned pub = pub_off + (g == j ? (unsigned)s * (128u * 1536u) : xw);
        PIN_S(xw); PIN_S(pso); PIN_S(mso); PIN_S(rso); PIN_V(pub);
        {
            if (KD0) __builtin_amdgcn_s_sleep(KD0);
            const u32x2 pv = *(const LAS u32x2*)(lds + L_PACE);
            unsigned target = __builtin_amdgcn_readfirstlane(pv.x); const bool valid = __builtin_amdgcn_readfirstlane(pv.y) + 1u == (unsigned)s && s > 3;
            unsigned now = (unsigned)__builtin_amdgcn_s_memrealtime();
            if (ROLE == 3 && s > 2) pace_c = min(pace_c, now - pace_t);
            if (valid && (int)(target - now) < 100) { while ((int)(target - now) > 0) now = (unsigned)__builtin_amdgcn_s_memrealtime(); }
        }
        SEG(10);
        f32x4 c[6]; u32x4 a1;
        auto rearm = [&]() {
            ST16(sv, xr, l16, rso, 16);
            if (ROLE >= 2) ST16(sv, xr, l16 + 1024, rso, 16);
        };
        u32x4 a0;
        auto ld_a = [&]() { asm volatile("" ::: "memory"); a0 = ld16_sc1(xr, l16, mso); a1 = ld16_sc1(xr, l16 + 1024, mso); };
        auto ok_a = [&]() { return all_set(max(umax4(a0), umax4(a1))); };
        auto mm_a = [&]() {
#pragma unroll
            for (int i = 0; i < 6; ++i) { c[i] = (f32x4){0.f, 0.f, 0.f, 0.f}; PIN_V(c[i]); }
            SCHED();
            c[0] = smma(a0, W[0][0], c[0], sidx); c[1] = smma(a0, W[1][0], c[1], sidx); c[0] = smma(a1, W[0][1], c[0], sidx); c[1] = smma(a1, W[1][1], c[1], sidx);
            c[2] = smma(a0, W[2][0], c[2], sidx); c[3] = smma(a0, W[3][0], c[3], sidx);
        };
        auto put = [&](int i) { *(LAS f32x2*)(lds + redw + ((i >> 1) * RED_M + (i & 1) * 64) * 4) = lo2(c[i]) + hi2(c[i]); };
        ld_a(); SCHED();
        if (KRA == 0) rearm();
        mm_a();
        if (__builtin_expect(!ok_a(), 0)) { bool out; do { RETRY(0); out = spin_out(spins, ctl); ld_a(); } while (!(ok_a() | out)); mm_a(); }
        SEG(0);
        if (KRA == 1) rearm();
        c[2] = smma(a1, W[2][1], c[2], sidx); c[3] = smma(a1, W[3][1], c[3], sidx);
        put(0); put(1);
        SCHED();
        c[4] = smma(a0, W[4][0], c[4], sidx); c[5] = smma(a0, W[5][0], c[5], sidx); c[4] = smma(a1, W[4][1], c[4], sidx);
        put(2); put(3);
        c[5] = smma(a1, W[5][1], c[5], sidx);
        SCHED();
        if (KRA == 3) rearm();
        put(4); put(5);
        SEG(1);
        if (KRA == 2) { SCHED(); rearm(); }
        LDS_BAR();
        SEG(2);
        if (ROLE <= 1) {
            f32x2 v[8];
#pragma unroll
            for (int ww = 0; ww < 8; ++ww) { const LAS float* q = (const LAS float*)(lds + redr + ww * 4096); v[ww] = (f32x2){q[0], q[512]}; }
            const f32x2 t = ((v[0] + v[1]) + (v[2] + v[3])) + ((v[4] + v[5]) + (v[6] + v[7]));
            const u32x4 o = rows_gather(min(pk_bf16(t.x, t.y), SENT - 1));
            ST16(o, xr, pub, 0, 16);
            *(LAS u32x4*)(lds + L_OWN + w * 256 + n * 16) = o;
        } else {
            if (ROLE == 3 && rank == 0) {
                pace_t = (unsigned)__builtin_amdgcn_s_memrealtime();
                if (lane == 0) { __hip_atomic_store(ctl + C_PROG, (unsigned)s, RLX_AGENT);
                    __hip_atomic_store((unsigned long long*)(ctl + C_PACE + par * 2048), (unsigned long long)(pace_t + pace_c + KPM) | ((unsigned long long)s << 32), RLX_AGENT); }
            }
        }
        SEG(3);
        if (s == NSEG - 1) break;
        LDS_BAR();
        SEG(13);
        unsigned VWM[2];
        auto vwm_pack = [&](const f32x4 (&dv)[2]) {
#pragma unroll
            for (int ct = 0; ct < 2; ++ct) { const f32x2 t = lo2(dv[ct]) + hi2(dv[ct]); VWM[ct] = pk_bf16(t.x, t.y); PIN_V(VWM[ct]); }
        };
        f32x4 dv[2] = {{0.f, 0.f, 0.f, 0.f}, {0.f, 0.f, 0.f, 0.f}}, s0, s1;
        PIN_V(dv[0]); PIN_V(dv[1]);
        u32x4 v0, v1;
        if (ROLE == 0) {
            if (KDM == 1) { WAIT_V(0); dma_x(min(s + 2, NSEG - 2)); }
            else if (KDM == 2) { dma_x(min(s + 2, NSEG - 2)); if (w < 4) WAIT_V(4); else WAIT_V(0); }
            else WAIT_V(0);
        } else {
            unsigned cw = xw + (unsigned)rank * 16384u, c1 = cw + (unsigned)((j + 1) & 3) * 1536u, c2 = cw + (unsigned)((j + 2) & 3) * 1536u, c3 = cw + (unsigned)((j + 3) & 3) * 1536u;
            PIN_S(cw); PIN_S(c1); PIN_S(c2); PIN_S(c3);
            const unsigned xl = ROLE == 2 ? (l16 & ~128u) : l16;
            u32x4 vo;
            f32x4 e0 = {0.f, 0.f, 0.f, 0.f}, e1 = e0;
            if (ROLE == 1) { vo = *(const LAS u32x4*)(lds + L_OWN + 1024 + (lane & 31) * 16); if (!vown) vo = (u32x4){0u, 0u, 0u, 0u}; PIN_V(vo); }
            else {
                const u32x4 X0 = *(const LAS u32x4*)(lds + L_OWN + xl);
                if (ROLE == 2) { e0 = mma32(asbf(KX[0]), asbf(X0), e0); e1 = mma32(asbf(KX[4]), asbf(X0), e1); } else e0 = mma32(asbf(X0), asbf(X0), e0);
                PIN_V(e0); PIN_V(e1);
            }
            if (KD1) __builtin_amdgcn_s_sleep(KD1);
            u32x4 X1, X2, X3;
            auto ld_b = [&]() {
                asm volatile("" ::: "memory");
                if (ROLE >= 2) { X1 = ld16_sc1(xr, xl, c1); X2 = ld16_sc1(xr, xl, c2); X3 = ld16_sc1(xr, xl, c3); }
                else { v0 = ld16_sc1(xr, vg0, cw); v1 = ld16_sc1(xr, vg1, cw); }
            };
            auto ok_b = [&]() { return all_set(ROLE >= 2 ? max(umax4(X1), max(umax4(X2), umax4(X3))) : max(umax4(v0), umax4(v1))); };
            auto mm_b = [&]() {
                if (ROLE == 2) {
                    s0 = mma32(asbf(KX[1]), asbf(X1), e0); s1 = mma32(asbf(KX[5]), asbf(X1), e1);
                    s0 = mma32(asbf(KX[2]), asbf(X2), s0); s1 = mma32(asbf(KX[6]), asbf(X2), s1);
                    s0 = mma32(asbf(KX[3]), asbf(X3), s0); s1 = mma32(asbf(KX[7]), asbf(X3), s1);
                } else if (ROLE == 3) {
                    s0 = mma32(asbf(X1), asbf(X1), e0); s1 = mma32(asbf(X2), asbf(X2), e1); s0 = mma32(asbf(X3), asbf(X3), s0);
                } else {
                    v0 |= vo;
                    *(LAS u32x4*)(lds + L_VM + lane * 16) = v0; *(LAS u32x4*)(lds + L_VM + 1024 + lane * 16) = v1;
                }
            };
            SEG(11);
            ld_b(); SCHED(); mm_b();
            if (__builtin_expect(!ok_b(), 0)) { bool out; do { RETRY(1); out = spin_out(spins, ctl); ld_b(); } while (!(ok_b() | out)); mm_b(); }
            SEG(4);
        }
        float m;
        f32x4 sc;
        if (ROLE >= 2) {
            sc = ROLE == 2 ? (n < 8 ? s0 : s1) : s0 + s1;
            if (ROLE == 3 && g < 2) sc = (f32x4){ninf, ninf, ninf, ninf};
            m = max3f(max3f(sc[0], sc[1], sc[2]), sc[3], ninf);
            if (ROLE == 2) m = max3f(m, ror8(m), ninf);
            m = rmax(m);
            PIN_V(m);
        }
        LDS_BAR();
        if (ROLE != 1) { v0 = *(const LAS u32x4*)(lds + L_VM + lane * 16); v1 = *(const LAS u32x4*)(lds + L_VM + 1024 + lane * 16); }
        if (ROLE >= 2) {
            const float mc = -m * csc;
#pragma unroll
            for (int i = 0; i < 4; ++i) sc[i] = __builtin_amdgcn_exp2f(fmaf(sc[i], csc, mc));
            *(LAS u32x2*)(lds + (ROLE == 2 ? L_PX + (g * 8 + (n & 7)) * 16 + (n >> 3) * 8 : L_PM + (g < 2 ? 256 : 0) + n * 16 + (g & 1) * 8)) = (u32x2){pk_bf16(sc[0], sc[1]), pk_bf16(sc[2], sc[3])};
            SCHED();
            dv[0] = smma(v0, WO[0][0], dv[0], sidx); dv[1] = smma(v0, WO[1][0], dv[1], sidx);
            float l = (sc[0] + sc[1]) + (sc[2] + sc[3]);
            if (ROLE == 2) l += ror8(l);
            dv[0] = smma(v1, WO[0][1], dv[0], sidx); dv[1] = smma(v1, WO[1][1], dv[1], sidx);
            l = rows_sum(l);
            *(LAS f32x2*)(lds + L_ML + n * 16 + (ROLE == 2 ? 0 : 8)) = (f32x2){mc, l};
        } else {
#pragma unroll
            for (int ct = 0; ct < 2; ++ct) { dv[ct] = smma(v0, WO[ct][0], dv[ct], sidx); dv[ct] = smma(v1, WO[ct][1], dv[ct], sidx); }
        }
        vwm_pack(dv);
        SEG(5);
        u32x4 am0 = {VWM[0], 0u, 0u, 0u}, am1 = {VWM[1], 0u, 0u, 0u}, pm = {0u, 0u, 0u, 0u};
        PIN_V(am0); PIN_V(am1); PIN_V(pm);
        LDS_BAR();
        SEG(6);
        {
            const f32x4 ml = *(const LAS f32x4*)(lds + L_ML + n * 16);
            const u32x4 px = *(const LAS u32x4*)(lds + L_PX + (g * 8 + (n & 7)) * 16);
            pm.x = *(const LAS unsigned*)(lds + L_PM + n * 16 + g * 4);
            const f32x4 z = {0.f, 0.f, 0.f, 0.f};
            const f32x4 dx0 = mma32(asbf(VWX[0]), asbf(px), z), dx1 = mma32(asbf(VWX[1]), asbf(px), z), dm0 = mma32(asbf(am0), asbf(pm), z), dm1 = mma32(asbf(am1), asbf(pm), z);
            const float dl = ml[0] - ml[2];
            const float e = __builtin_amdgcn_exp2f(-__builtin_fabsf(dl));
            const float ex = dl > 0.f ? e : 1.f, em = dl > 0.f ? 1.f : e;
            const float inv = __builtin_amdgcn_rcpf(fmaf(ex, ml[1], em * ml[3]));
            const f32x4 d0 = (dx0 * ex + dm0 * em) * inv, d1 = (dx1 * ex + dm1 * em) * inv;
            const u32x4 o = {min(pk_bf16(d0[0], d0[1]), SENT - 1), min(pk_bf16(d0[2], d0[3]), SENT - 1), min(pk_bf16(d1[0], d1[1]), SENT - 1), min(pk_bf16(d1[2], d1[3]), SENT - 1)};
            ST16(o, xr, pst_off, pso, 16);
        }
        SCHED();
        SEG(7);
        __builtin_amdgcn_s_setprio(ROLE == 3 ? 1 : 0);
        if (KDM == 3 && s + 2 < NSEG - 1) dma_x(s + 2);
        unsigned long long pv = 0;
        if (ROLE == 1) { pv = __hip_atomic_load((unsigned long long*)(ctl + C_PACE + par * 2048), RLX_AGENT); SCHED(); }
        vwx_kt(s + 1, 0); vwx_kt(s + 1, 1);
        SCHED();
        if (ROLE == 3) __builtin_amdgcn_s_setprio(2);
        if (ROLE == 3) {
            u32x4 SEL;
            { const unsigned one = (n & 1) ? 0x3f800000u : 0x00003f80u; const int dw = (n & 7) >> 1; SEL = (u32x4){dw == 0 ? one : 0u, dw == 1 ? one : 0u, dw == 2 ? one : 0u, dw == 3 ? one : 0u}; }
            unsigned so = pso + redo, mo = win_off(W_MEM(s + 1) + (red >> 6)) + (unsigned)(red & 63) * 128u;
            PIN_S(so); PIN_S(mo); PIN_V(SEL);
            if (KD2) __builtin_amdgcn_s_sleep(KD2);
            f32x4 ta, tb;
            u32x4 q[4];
            auto ld_c = [&]() {
                asm volatile("" ::: "memory");
#pragma unroll
                for (int i = 0; i < 4; ++i) q[i] = ld16_sc1(xr, l16 + i * 1024, so);
            };
            auto ok_c = [&]() { return all_set(max(max(umax4(q[0]), umax4(q[1])), max(umax4(q[2]), umax4(q[3])))); };
            auto mm_c = [&]() {
                ta = mma32(asbf(SEL), asbf(q[0]), (f32x4){0.f, 0.f, 0.f, 0.f}); tb = mma32(asbf(SEL), asbf(q[1]), (f32x4){0.f, 0.f, 0.f, 0.f});
                ta = mma32(asbf(SEL), asbf(q[2]), ta); tb = mma32(asbf(SEL), asbf(q[3]), tb);
            };
            SEG(12);
            ld_c(); mm_c();
            if (__builtin_expect(!ok_c(), 0)) { bool out; do { RETRY(2); out = spin_out(spins, ctl); ld_c(); } while (!(ok_c() | out)); mm_c(); }
            SEG(8);
            f32x4 t = ta + tb;
#pragma unroll
            for (int i = 0; i < 4; ++i) t[i] += ror8(t[i]);
            unsigned a0 = min(pk_bf16(t[0], t[1]), SENT - 1), a1 = min(pk_bf16(t[2], t[3]), SENT - 1);
            unsigned b0 = a0, b1 = a1; swap16(a0, b0); swap16(a1, b1);
            const u32x4 o = {a0, a1, b0, b1};
            ST16(o, xr, mem_off, mo, 16);
        } else {
            if (KDM == 0 && s + 2 < NSEG - 1) dma_x(s + 2);
            if (ROLE == 2) load_kx(s + 1);
            if (ROLE == 1) *(LAS u32x2*)(lds + L_PACE) = (u32x2){(unsigned)pv, (unsigned)(pv >> 32)};
        }
        SEG(9);
        if (ROLE != 3) __builtin_amdgcn_s_setprio(2);
        LDS_BAR();
        SEG(14);
    }
    WAIT_V(0);
    __builtin_amdgcn_s_setprio(0);
}

DI void chain_role(const Params& p, int rank, ldsp lds, unsigned long long* stamps) {
    const int tid = fresh_tid(), w = __builtin_amdgcn_readfirstlane(tid >> 6), lane = tid & 63, g = lane >> 4, n = lane & 15;
    const int h = rank >> 2, j = rank & 3;
    const rsrc_t xr = mkrsrc(p.ws + WS_XCH, (unsigned)XCH_BYTES);
    unsigned* ctl = (unsigned*)(p.ws + WS_CTL);
    PST(0);
    const unsigned par = (unsigned)__builtin_amdgcn_readfirstlane(ctl[C_PAR + 1]) < (unsigned)__builtin_amdgcn_readfirstlane(ctl[C_PAR]) ? 1u : 0u;
    const int red = ((j + 1) & 3) * 32 + h;
    bf16x16 W[6][2], WO[2][2];
    {
        ldsp my = lds + w * 16384;
        const int rrow = lane >> 2, rc = (lane & 3) * 8, q4 = n >> 2;
        const int toffo = (8 * g + q4) * 64 + (n & 3) * 8;
        const int toffq = (16 * (g & 1) + 8 * (g >> 1) + 4 * (q4 >> 1) + (q4 & 1)) * 64 + (n & 3) * 8;
#define FRAG(base, st) cat16(cat8(trd((base)), trd((base) + (st))), cat8(trd((base) + 2048), trd((base) + 2048 + (st))))
#pragma unroll
        for (int i = 0; i < 8; ++i) {
            __builtin_amdgcn_global_load_lds((const unsigned*)(p.wq + (size_t)(128 * w + 16 * i + rrow) * 4096 + h * 128 + 32 * j + rc), (LAS unsigned*)(my + i * 1024), 16, 0, KNT1);
            __builtin_amdgcn_global_load_lds((const unsigned*)(p.wo + (size_t)(h * 128 + 16 * i + rrow) * 1024 + 256 * j + 32 * w + rc), (LAS unsigned*)(my + 8192 + i * 1024), 16, 0, KNT1);
        }
        {
            const u32x4 sv = {SENT, SENT, SENT, SENT};
            if (w < 6) {
                ST16(sv, xr, (unsigned)lane * 16u, win_off(W_X(0)) + (unsigned)rank * 16384u + (unsigned)w * 1024u, 16);
                ST16(sv, xr, (unsigned)lane * 16u, win_off(W_X(1)) + (unsigned)rank * 16384u + (unsigned)w * 1024u, 16);
            }
            ST16(sv, xr, (unsigned)lane * 16u, win_off(W_PART(w >> 2)) + (unsigned)(red >> 1) * 16384u + (unsigned)(red & 1) * 4096u + (unsigned)(w & 3) * 1024u, 16);
            ST16(sv, xr, (unsigned)lane * 16u, XCH_KM + (unsigned)((56 + w) * 128 + rank) * 1536u, 16);
            ST16(sv, xr, lane < 32 ? (unsigned)lane * 16u + 1024u : OOB, XCH_KM + (unsigned)((56 + w) * 128 + rank) * 1536u, 16);
            const int s = 8 * w + (lane >> 3);
            if (s > 0) ST16(sv, xr, win_off(W_MEM(s) + (red >> 6)) + (unsigned)(red & 63) * 128u + (unsigned)(lane & 7) * 16u, 0, 16);
        }
        PST(3);
        WAIT_V(0);
#pragma unroll
        for (int T = 0; T < 2; ++T)
#pragma unroll
            for (int t = 0; t < 2; ++t) { W[T][t] = FRAG(my + t * 4096 + T * 32 + toffq, 128); WO[T][t] = FRAG(my + 8192 + (t ^ (j >> 1)) * 4096 + T * 32 + toffo, 256); }
#pragma unroll
        for (int T = 0; T < 2; ++T) asm volatile("" : "+v"(W[T][0]), "+v"(W[T][1]), "+v"(WO[T][0]), "+v"(WO[T][1]));
        WAIT_L(0);
        PST(1);
#pragma unroll
        for (int i = 0; i < 8; ++i) {
            __builtin_amdgcn_global_load_lds((const unsigned*)(p.wk + (size_t)(128 * w + 16 * i + rrow) * 4096 + h * 128 + 32 * j + rc), (LAS unsigned*)(my + i * 1024), 16, 0, KNT2);
            __builtin_amdgcn_global_load_lds((const unsigned*)(p.wv + (size_t)(128 * w + 16 * i + rrow) * 4096 + h * 128 + 32 * j + rc), (LAS unsigned*)(my + 8192 + i * 1024), 16, 0, KNT2);
        }
        WAIT_V(0);
#pragma unroll
        for (int T = 0; T < 4; ++T)
#pragma unroll
            for (int t = 0; t < 2; ++t) W[2 + T][t] = FRAG(my + (T >> 1) * 8192 + t * 4096 + (T & 1) * 32 + toffq, 128);
#pragma unroll
        for (int T = 0; T < 4; ++T) asm volatile("" : "+v"(W[2 + T][0]), "+v"(W[2 + T][1]));
        WAIT_L(0);
#undef FRAG
    }
    PST(2);
    BAR();
    PST(4);
    if (w == 7 && lane < 8) {
        const unsigned* src = (const unsigned*)(p.mem0 + (4 * (red & 1) + 2 * (lane >> 2) + (lane & 1)) * DM + 32 * (red >> 2) + 8 * ((red >> 1) & 1) + 4 * ((lane >> 1) & 1));
        const u32x4 v = {min(src[0], SENT - 1), min(src[1], SENT - 1), min(src[8], SENT - 1), min(src[9], SENT - 1)};
        ST16(v, xr, (unsigned)lane * 16u + (unsigned)(red & 63) * 128u, win_off(W_MEM(0) + (red >> 6)), 16);
    }
    if (w < 5) chain_loop<0>(p, par, rank, w, lds, W, WO, stamps);
    else if (w == 5) chain_loop<1>(p, par, rank, 5, lds, W, WO, stamps);
    else if (w == 6) chain_loop<2>(p, par, rank, 6, lds, W, WO, stamps);
    else chain_loop<3>(p, par, rank, 7, lds, W, WO, stamps);
}

template <bool POLL>
DI void attn_a_item(const Params& p, int s, int h, ldsp my, int lane) {
    const int g = lane >> 4, n = lane & 15;
    const rsrc_t xr = mkrsrc(p.ws + WS_XCH, (unsigned)XCH_BYTES);
    const rsrc_t qr = mkrsrc(p.ws + WS_QKVX + (size_t)h * XH + (size_t)s * 8192, 65u * XH);
#pragma unroll
    for (int i = 0; i < 8; ++i)
        __builtin_amdgcn_raw_ptr_buffer_load_lds(qr, (LAS void*)(my + i * 1024), 16, (unsigned)(g * 256 + (n ^ ((g & 1) * 8)) * 16), 64u * XH + (unsigned)i * 1024u, 0, 16);
    u32x4 Q[2][4], K[2][4], XM[4];
    const unsigned ro = (unsigned)lane * 16u;
#pragma unroll
    for (int t = 0; t < 2; ++t)
#pragma unroll
        for (int k = 0; k < 4; ++k) { Q[t][k] = ld16_sc1(qr, ro + k * 1024, (unsigned)t * 4096u); K[t][k] = ld16_sc1(qr, ro + k * 1024, (unsigned)t * 4096u + 32u * XH); }
    const unsigned qwin = XCH_KM + (unsigned)(s * 32 + h) * 6144u;
    u32x4 VM[2];
    if (POLL) {
        wait_count<4>((unsigned*)(p.ws + WS_CTL), C_PROG, (unsigned)s + (s < 56 ? 2u : 1u));
        if (s >= 56) __builtin_amdgcn_s_sleep(16);
    }
    for (unsigned spins = 0;;) {
        asm volatile("" ::: "memory");
#pragma unroll
        for (int k = 0; k < 4; ++k) XM[k] = ld16_sc1(xr, (unsigned)lane * 16u + (k & 1) * 1536, qwin + (k >> 1) * 3072);
#pragma unroll
        for (int ts = 0; ts < 2; ++ts) VM[ts] = ld16_sc1(xr, (unsigned)(g >> 1) * 1536u + 1024u + (unsigned)(lane & 31) * 16u, qwin + ts * 3072);
        if (!POLL || s < 56) break;
        if (all_set(max(max(max(umax4(XM[0]), umax4(XM[1])), max(umax4(XM[2]), umax4(XM[3]))), max(umax4(VM[0]), umax4(VM[1]))))) break;
        __builtin_amdgcn_s_sleep(4);
        if (spin_out(spins, (unsigned*)(p.ws + WS_CTL))) break;
    }
    SCHED();
#pragma unroll
    for (int ts = 0; ts < 2; ++ts) {
        const u32x4 v = VM[ts];
        const int m = 2 * (n >> 2) + (n & 1), hf = (n >> 1) & 1;
#pragma unroll
        for (int i = 0; i < 4; ++i) { const int e = 64 * ts + 16 * g + 4 * i + 2 * hf;
            *(LAS unsigned*)(my + 8192 + (e >> 3) * 256 + (m ^ (e & 8)) * 16 + (e & 7) * 2) = v[i]; }
    }
    const float csc = 0.08838834764831845f * 1.4426950408889634f, ninf = -__builtin_inff();
    u32x4 PX[2]; u32x2 PM[2];
#pragma unroll
    for (int qt = 0; qt < 2; ++qt) {
        f32x4 sm = {0.f, 0.f, 0.f, 0.f}, s0 = sm, s1 = sm;
#pragma unroll
        for (int k = 0; k < 4; ++k) { sm = mma32(asbf(XM[k]), asbf(Q[qt][k]), sm); s0 = mma32(asbf(K[0][k]), asbf(Q[qt][k]), s0); if (qt == 1) s1 = mma32(asbf(K[1][k]), asbf(Q[qt][k]), s1); }
#pragma unroll
        for (int i = 0; i < 4; ++i) {
            if (g < 2) sm[i] = ninf;
            if (qt == 0) { if (4 * g + i > n) s0[i] = ninf; s1[i] = ninf; }
            else if (4 * g + i > n) s1[i] = ninf;
        }
        float m = fmaxf(fmaxf(fmaxf(s0[0], s0[1]), fmaxf(s0[2], s0[3])), fmaxf(fmaxf(s1[0], s1[1]), fmaxf(s1[2], s1[3])));
        m = fmaxf(m, fmaxf(fmaxf(sm[0], sm[1]), fmaxf(sm[2], sm[3])));
        m = rows_max(m);
        const float mc = -m * csc; float sum = 0.f;
#pragma unroll
        for (int i = 0; i < 4; ++i) { s0[i] = __builtin_amdgcn_exp2f(fmaf(s0[i], csc, mc)); s1[i] = __builtin_amdgcn_exp2f(fmaf(s1[i], csc, mc)); sm[i] = __builtin_amdgcn_exp2f(fmaf(sm[i], csc, mc));
            sum += (s0[i] + s1[i]) + sm[i]; }
        sum = rows_sum(sum);
        const float inv = __builtin_amdgcn_rcpf(sum);
        PX[qt] = (u32x4){pk_bf16(s0[0] * inv, s0[1] * inv), pk_bf16(s0[2] * inv, s0[3] * inv), pk_bf16(s1[0] * inv, s1[1] * inv), pk_bf16(s1[2] * inv, s1[3] * inv)};
        PM[qt] = (u32x2){pk_bf16(sm[0] * inv, sm[1] * inv), pk_bf16(sm[2] * inv, sm[3] * inv)};
    }
    asm volatile("s_waitcnt vmcnt(0) lgkmcnt(0)" ::: "memory");
    const int q4 = n >> 2, p4 = n & 3;
    const ldsp vx = my + (p4 >> 1) * 256 + ((4 * g + q4) ^ ((p4 >> 1) * 8)) * 16 + (p4 & 1) * 8, vmm = my + 8192 + (p4 >> 1) * 256 + ((4 * (g & 1) + q4) ^ ((p4 >> 1) * 8)) * 16 + (p4 & 1) * 8;
    const rsrc_t orr = mkrsrc(p.ws + WS_OA + (size_t)s * 262144 + (size_t)h * 4096, 262144u);
    const unsigned oo = frag_off(lane);
    bf16x8 VA[8]; s16x4 VMf[8];
#pragma unroll
    for (int et = 0; et < 8; ++et) { VA[et] = cat8(trd(vx + et * 512), trd(vx + 4096 + et * 512)); VMf[et] = trd(vmm + et * 512); }
    SCHED();
#pragma unroll
    for (int ep = 0; ep < 4; ++ep) {
        f32x4 d[2][2];
#pragma unroll
        for (int e2 = 0; e2 < 2; ++e2)
#pragma unroll
            for (int qt = 0; qt < 2; ++qt) d[qt][e2] = mma16(__builtin_bit_cast(u32x2, VMf[2 * ep + e2]), PM[qt], mma32(VA[2 * ep + e2], asbf(PX[qt]), (f32x4){0.f, 0.f, 0.f, 0.f}));
#pragma unroll
        for (int qt = 0; qt < 2; ++qt) store_frag(orr, oo + ep * 1024, (unsigned)qt * 131072u, d[qt][0], d[qt][1]);
    }
    asm volatile("s_waitcnt lgkmcnt(0)" ::: "memory");
}

DI void attn_b_item(const Params& p, int hq, int qb, ldsp lds) {
    const int tid = fresh_tid(), w = __builtin_amdgcn_readfirstlane(tid >> 6), lane = tid & 63, g = lane >> 4, n = lane & 15;
    const int q0 = qb * 128;
    const rsrc_t br = mkrsrc(p.ws + WS_QKVB, 96u * BH);
    const rsrc_t orr = mkrsrc(p.ws + WS_OB + (size_t)qb * 16 * 16384, 16u * 16384u);
    constexpr int BUF = 57344;
    unsigned to[3];
#pragma unroll
    for (int i = 0; i < 3; ++i) to[i] = (unsigned)max(8 * qb - 16 + w * 3 + i, 0) * 1024u;
    const unsigned l16 = (unsigned)lane * 16u, vsw = (unsigned)(g * 256 + (n ^ ((g & 1) * 8)) * 16);
    auto stage = [&](int i) {
        const unsigned so = (unsigned)(hq * 4 + i) * BH; ldsp d = lds + (i & 1) * BUF;
#pragma unroll
        for (int k = 0; k < 3; ++k) {
            __builtin_amdgcn_raw_ptr_buffer_load_lds(br, (LAS void*)(d + (w * 3 + k) * 1024), 16, l16, so + 32u * BH + to[k], 0, 16);
            __builtin_amdgcn_raw_ptr_buffer_load_lds(br, (LAS void*)(d + 24576 + (w * 3 + k) * 1024), 16, vsw, so + 64u * BH + to[k], 0, 16);
        }
        __builtin_amdgcn_raw_ptr_buffer_load_lds(br, (LAS void*)(d + 49152 + w * 1024), 16, l16, so + (unsigned)(8 * qb + w) * 1024u, 0, 16);
    };
    const float ninf = -__builtin_inff(), csc = 0.17677669529663687f * 1.4426950408889634f;
    const int tmin = (256 - q0 - 16 * w) >> 4;
    const int q4 = n >> 2, p4 = n & 3;
    const int kq = w * 1024 + lane * 16;
    const unsigned vb = (unsigned)(size_t)lds + 24576u + (unsigned)(w * 1024 + (p4 >> 1) * 256 + ((4 * g + q4) ^ ((p4 >> 1) * 8)) * 16 + (p4 & 1) * 8);
    stage(0);
#pragma unroll 1
    for (int i = 0; i < 4; ++i) {
        if (i < 3) { stage(i + 1); WAIT_V(7); } else WAIT_V(0);
        BAR(); asm volatile("" ::: "memory");
        const ldsp bp = lds + (i & 1) * BUF;
        const bf16x8 Q = *(const LAS bf16x8*)(bp + 49152 + kq);
        f32x4 sc[17]; bf16x8 KF[17];
#pragma unroll
        for (int t = 0; t < 17; ++t) KF[t] = *(const LAS bf16x8*)(bp + kq + t * 1024);
        SCHED();
#pragma unroll
        for (int t = 0; t < 17; ++t) sc[t] = mma32(KF[t], Q, (f32x4){0.f, 0.f, 0.f, 0.f});
#pragma unroll
        for (int k = 0; k < 4; ++k) { if (4 * g + k <= n) sc[0][k] = ninf; if (4 * g + k > n) sc[16][k] = ninf; }
        if (tmin > 0) {
#pragma unroll
            for (int t = 0; t < 16; ++t) if (t < tmin) sc[t] = (f32x4){ninf, ninf, ninf, ninf};
        }
        float m = max3f(sc[16][0], sc[16][1], max3f(sc[16][2], sc[16][3], ninf));
#pragma unroll
        for (int t = 0; t < 16; ++t) m = max3f(max3f(m, sc[t][0], sc[t][1]), sc[t][2], sc[t][3]);
        m = rows_max(m);
        const float mc = -m * csc;
        f32x4 sum4 = {0.f, 0.f, 0.f, 0.f};
#pragma unroll
        for (int t = 0; t < 17; ++t) {
#pragma unroll
            for (int k = 0; k < 4; ++k) sc[t][k] = __builtin_amdgcn_exp2f(fmaf(sc[t][k], csc, mc));
            sum4 += sc[t];
        }
        const float inv = __builtin_amdgcn_rcpf(rows_sum((sum4[0] + sum4[1]) + (sum4[2] + sum4[3])));
        const unsigned v0 = vb + (unsigned)((i & 1) * BUF);
        s16x4 V0[17], V1[17];
#define VRD(t) TRD(V0[t], v0, (t) * 1024); TRD(V1[t], v0, (t) * 1024 + 512)
        VRD(0); VRD(1); VRD(2); VRD(3); VRD(4); VRD(5); VRD(6); VRD(7); VRD(8); VRD(9); VRD(10); VRD(11); VRD(12); VRD(13); VRD(14); VRD(15); VRD(16);
#undef VRD
        u32x2 P[17];
#pragma unroll
        for (int t = 0; t < 17; ++t) P[t] = (u32x2){pk_bf16(sc[t][0], sc[t][1]), pk_bf16(sc[t][2], sc[t][3])};
        WAIT_L(0); SCHED();
        f32x4 d0 = {0.f, 0.f, 0.f, 0.f}, d1 = d0;
#pragma unroll
        for (int tp = 0; tp < 8; ++tp) {
            const bf16x8 PP = asbf((u32x4){P[2 * tp].x, P[2 * tp].y, P[2 * tp + 1].x, P[2 * tp + 1].y});
            d0 = mma32(cat8(V0[2 * tp], V0[2 * tp + 1]), PP, d0); d1 = mma32(cat8(V1[2 * tp], V1[2 * tp + 1]), PP, d1);
        }
        d0 = mma16(__builtin_bit_cast(u32x2, V0[16]), P[16], d0); d1 = mma16(__builtin_bit_cast(u32x2, V1[16]), P[16], d1);
        BAR();
        store_frag(orr, frag_off(lane), (unsigned)(w * 32768 + (hq * 4 + i) * 1024), d0 * inv, d1 * inv);
    }
    WAIT_V(0);
    signal((unsigned*)(p.ws + WS_CTL), C_OB + qb * 32, 1);
}

DI void out_tail(const Params& p, int c, int q, ldsp lds) {
    const int tid = fresh_tid(), w = __builtin_amdgcn_readfirstlane(tid >> 6), lane = tid & 63, g = lane >> 4, n = lane & 15;
    unsigned* ctl = (unsigned*)(p.ws + WS_CTL);
    bf16x8 WT[16][2];
    {
        ldsp my = lds + w * 16384;
        const int toff = (8 * g + (n >> 2)) * 64 + (n & 3) * 8;
#pragma unroll
        for (int r = 0; r < 2; ++r) {
#pragma unroll
            for (int i = 0; i < 16; ++i)
                __builtin_amdgcn_global_load_lds((const unsigned*)(p.wo + (size_t)(512 * w + 256 * r + 16 * i + (lane >> 2)) * 1024 + 32 * c + (lane & 3) * 8), (LAS unsigned*)(my + i * 1024), 16, 0, 0);
            WAIT_V(0);
#pragma unroll
            for (int k = 0; k < 8; ++k)
#pragma unroll
                for (int ct = 0; ct < 2; ++ct) { WT[8 * r + k][ct] = cat8(trd(my + k * 2048 + ct * 32 + toff), trd(my + k * 2048 + ct * 32 + toff + 256)); }
#pragma unroll
            for (int k = 0; k < 8; ++k) asm volatile("" : "+v"(WT[8 * r + k][0]), "+v"(WT[8 * r + k][1]));
            WAIT_L(0);
        }
    }
    BAR();
#pragma unroll 1
    for (int hs = q; hs < 32; hs += 3) {
        const int s = 48 + (hs >> 1), rt = hs & 1;
        if (w == 0) wait_count<3>(ctl, C_OS + (s - 48) * 256 + (c >> 3) * 64, 4u);
        asm volatile("" ::: "memory"); BAR(); asm volatile("" ::: "memory");
        const rsrc_t ar = mkrsrc(p.ws + WS_OA + (size_t)(2 * s + rt) * 131072 + (size_t)w * 16384, 16384u);
        const unsigned ao = (unsigned)lane * 16u;
        u32x4 A[16];
#pragma unroll
        for (int k = 0; k < 16; ++k) A[k] = ld16_sc1(ar, ao + (k & 3) * 1024, (unsigned)(k >> 2) * 4096u);
        SCHED();
#pragma unroll
        for (int ct = 0; ct < 2; ++ct) {
            f32x4 acc = {0.f, 0.f, 0.f, 0.f};
#pragma unroll
            for (int k = 0; k < 16; ++k) acc = mma32(WT[k][ct], asbf(A[k]), acc);
            *(LAS f32x4*)(lds + w * 2048 + ct * 1024 + lane * 16) = acc;
        }
        LDS_BAR();
        if (w < 2) {
            f32x4 r[8];
#pragma unroll
            for (int ww = 0; ww < 8; ++ww) r[ww] = *(const LAS f32x4*)(lds + ww * 2048 + w * 1024 + lane * 16);
            SCHED();
            const f32x4 t = ((r[0] + r[1]) + (r[2] + r[3])) + ((r[4] + r[5]) + (r[6] + r[7]));
            *(u32x2*)(p.out + (size_t)(32 * s + 16 * rt + n) * 2048 + 32 * c + 16 * w + 4 * g) = (u32x2){pk_bf16(t[0], t[1]), pk_bf16(t[2], t[3])};
        }
        WAIT_L(0);
    }
    BAR();
}

DI void big_unit(const Params& p, int id, Unit& u) {
    u.lda = DM * 2; u.nt = 16; u.ak = 128; u.ah = 128 * DM * 2;
    if (id < 384) {
        const int pm = id & 7, pn = id >> 3;
        const u16 *w0 = p.wq, *w1 = p.wk, *w2 = p.wv; const u16* W = pn < 16 ? w0 : (pn < 32 ? w1 : w2);
        u.A = (const char*)(p.ax + (size_t)pm * 256 * DM); u.B = (const char*)(W + (pn & 15) * 256); u.ldb = 4096 * 2;
        u.C = p.ws + WS_QKVX + (size_t)(2 * pn) * XH + (size_t)pm * 65536; u.ldc = 256; u.cw = 1024; u.ch = XH;
        if (pn < 16) { u.sig = C_Q + pm * 32; u.nsig = 1; } else { u.sig = C_KV + 2 * pm * 512 + (pn & 15) * 32; u.nsig = 4; }
    } else if (id < 480) {
        const int pm = (id - 384) & 7, pn = (id - 384) >> 3;
        u.A = (const char*)(p.bx + (size_t)pm * 256 * DM); u.B = (const char*)(p.bwqkv + pn * 256); u.ldb = LDB3 * 2;
        u.C = p.ws + WS_QKVB + (size_t)(8 * pn) * BH + (size_t)pm * 16384; u.ldc = 64; u.cw = BH; u.ch = 4 * BH; u.sig = C_B + pm * 32; u.nsig = 1;
    } else {
        const bool isb = id < 512; const int q = isb ? id - 480 : id - 512, pm = q >> 2, pn = q & 3;
        const u16 *w0 = p.wo, *w1 = p.bwo;
        u.nt = isb ? 16 : 64; u.lda = 0; u.ak = 2048; u.ah = (unsigned)u.nt * 16384u;
        u.A = p.ws + (isb ? WS_OB : WS_OA) + (size_t)pm * 2 * u.ah; u.B = (const char*)((isb ? w1 : w0) + pn * 256); u.ldb = DM * 2;
        u.C = (char*)(p.out + (size_t)pm * 256 * 2048 + (isb ? 1024 : 0) + pn * 256); u.ldc = 4096; u.cw = 64; u.ch = 256; u.sig = 0; u.nsig = 0;
    }
}
struct BigSched { const Params& p; int id0, id1; DI bool next(int i, Unit& u) const { const int id = i == 0 ? id0 : (i == 1 ? id1 : -1); if (id < 0) return false; big_unit(p, id, u); return true; } };

__global__ __launch_bounds__(512, 2) void k_fused(Params p) {
    extern __shared__ __attribute__((aligned(16))) unsigned char shm[];
    ldsp lds = (ldsp)shm;
    unsigned* ctl = (unsigned*)(p.ws + WS_CTL);
    const int b = (blockIdx.x & ~7) | ((blockIdx.x + __builtin_amdgcn_readfirstlane(ctl[C_PHASE])) & 7), id = (b & 3) * 32 + (b >> 3);
#define WSTAMP(code)
    WSTAMP(1);
    if ((b & 7) < 4) {
        chain_role(p, id, lds, (unsigned long long*)(p.ws + WS_END));
        BAR();
        WSTAMP(2);
    } else {
#pragma unroll 1
        for (int r = 0; r < 2; ++r) {
            const int rb = 2 * r + (id & 1), ct = id >> 1;
            const u16 *w1 = p.wk, *w2 = p.wv;
            Unit u; u.A = (const char*)(p.ax + (size_t)rb * 128 * DM); u.lda = DM * 2; u.B = (const char*)((ct < 32 ? w1 : w2) + (ct & 31) * 128); u.ldb = 4096 * 2; u.nt = 16;
            u.C = p.ws + WS_QKVX + (size_t)(32 + ct) * XH + (size_t)rb * 32768; u.ldc = 256; u.cw = 1024; u.ch = 0; u.ak = 128; u.ah = 0;
            u.sig = C_KV + rb * 512 + (ct & 31) * 16; u.nsig = 1;
            gemm_small(lds, u, ctl);
            WSTAMP(3);
        }
        {
            const int t = fresh_tid(), wv = __builtin_amdgcn_readfirstlane(t >> 6);
            const char* src; const int c = id & 7, q = id >> 3;
            if (c < 2) src = (const char*)p.ax + (size_t)(q * 2 + c) * 131072; else if (c < 4) src = (const char*)p.bx + (size_t)(q * 2 + c - 2) * 131072;
            else if (c < 7) src = (const char*)p.bwqkv + (size_t)(q * 3 + c - 4) * 131072; else src = (const char*)p.bwo + (size_t)q * 131072;
#pragma unroll
            for (int i = 0; i < 16; ++i) { __builtin_amdgcn_global_load_lds((const unsigned*)(src + i * 8192 + t * 16), (LAS unsigned*)(lds + L_SCR + wv * 1024), 16, 0, 0); if ((i & (KWP - 1)) == KWP - 1) WAIT_V(0); }
            if (KNT1) {
#pragma unroll
                for (int i = 0; i < 8; ++i) { __builtin_amdgcn_global_load_lds((const unsigned*)((const char*)p.wo + (size_t)id * 65536 + i * 8192 + t * 16), (LAS unsigned*)(lds + L_SCR + wv * 1024), 16, 0, 0); if ((i & (KWP - 1)) == KWP - 1) WAIT_V(0); }
            }
        }
    }
    volatile LAS unsigned* qw = (volatile LAS unsigned*)(lds + 131072);
    const int w = __builtin_amdgcn_readfirstlane(threadIdx.x >> 6);
    const int k = b & 3;
    bool fixed = (b & 7) >= 4;
#pragma unroll 1
    for (;;) {
        int kind, x, it = -1, x2 = -1;
        if (fixed) { const int i = id & 31; kind = 6; x = i < 24 ? (16 + 8 * k + (i & 7)) * 8 + 2 + (i >> 3) : (4 * k + (i & 3)) * 8 + ((i >> 2) & 1); x2 = x + (i < 24 ? 3 : 2); fixed = false; }
        else {
        if (threadIdx.x == 0) qw[0] = __hip_atomic_fetch_add(ctl + C_HEAD + k * 32, 1u, RLX_AGENT) + 1u;
        LDS_BAR();
        it = (int)__builtin_amdgcn_readfirstlane(qw[0]);
        LDS_BAR();
        if (it < 4) { kind = 0; x = it * 4 + k; }
        else if (it < 28) { kind = 6; x = 384 + 24 * k + it - 4; }
        else if (it < 36) { kind = 6; x = (4 * k + ((it - 28) & 3)) * 8 + 4 + ((it - 28) >> 2); }
        else if (it < 38) { kind = 6; x = 512 + (it - 36) * 4 + k; }
        else if (it < 42) { kind = 0; x = (it - 38 + 4) * 4 + k; }
        else if (it < 74) { kind = 1; x = ((it - 42) >> 1) * 8 + 2 * k + ((it - 42) & 1); }
        else if (it < 82) { kind = 6; x = (4 * k + ((it - 74) & 3)) * 8 + 6 + ((it - 74) >> 2); }
        else if (it < 86) { kind = 4; x = (it - 82) * 4 + k; }
        else if (it < 94) { kind = 6; x = 480 + (it - 86) * 4 + k; }
        else if (it < 110) { kind = 2; x = (4 + ((it - 94) >> 1)) * 8 + 2 * k + ((it - 94) & 1); }
        else if (it < 134) { kind = 5; x = ((it - 110) >> 3) * 32 + 8 * k + ((it - 110) & 7); }
        else break;
        }
        if (kind == 6) {
            if (x >= 480) { const int w0 = x < 512 ? C_OB + ((x - 480) >> 2) * 64 : C_OA + ((x - 512) >> 2) * 64; const unsigned need = x < 512 ? 64u : 32u; wg_wait(ctl, w0, need, w0 + 32, need); }
            BigSched S{p, x, x2};
            gemm_stream(lds, S, ctl);
        } else
        if (kind == 0) {
            const int s0 = (x >> 2) * 4;
            wait_count(ctl, C_Q + (s0 >> 3) * 32, 128u); wait_count(ctl, C_PROG, (unsigned)s0 + 5u);
            asm volatile("" ::: "memory");
#pragma unroll 1
            for (int s = s0; s < s0 + 4; ++s) attn_a_item<false>(p, s, (x & 3) * 8 + w, lds + w * 12288, fresh_tid() & 63);
            WAIT_V(0); signal(ctl, C_OA + (x >> 2) * 32, 1);
            BAR();
        } else if (kind == 4) {
#pragma unroll 1
            for (int s = 32 + (x >> 2); s < NSEG; s += 4) {
                wait_count(ctl, C_Q + (s >> 3) * 32, 128u); wait_count(ctl, C_KV + (s >> 2) * 512 + ((x & 3) * 8 + w) * 16, 16u);
                asm volatile("" ::: "memory");
                attn_a_item<true>(p, s, (x & 3) * 8 + w, lds + w * 12288, fresh_tid() & 63);
                WAIT_V(0);
                if (s < 48) signal(ctl, C_OA + (s >> 2) * 32, 1);
                else { BAR(); const int l = fresh_tid(); if (l < 4) __hip_atomic_fetch_add(ctl + C_OS + (s - 48) * 256 + l * 64, 1u, RLX_AGENT); }
            }
            BAR();
        } else if (kind == 1) {
            const int qb = x >> 3;
            wait_count(ctl, C_B + (qb >> 1) * 32, 96u); wait_count(ctl, C_B + max((qb >> 1) - 1, 0) * 32, 96u);
            asm volatile("" ::: "memory");
            attn_b_item(p, x & 7, qb, lds);
        } else if (kind == 5) {
            out_tail(p, x & 31, x >> 5, lds);
        } else {
            const int rb = x >> 3, ct = x & 7;
            Unit u; u.ldb = DM * 2; u.ldc = 2048 * 2; u.sig = 0; u.nsig = 0; u.lda = 0; u.ak = 2048; u.ah = 0; u.cw = 64; u.ch = 0;
            u.A = p.ws + WS_OA + (size_t)rb * 64 * 16384; u.B = (const char*)(p.wo + ct * 128); u.nt = 64; u.C = (char*)(p.out + (size_t)rb * 128 * 2048 + ct * 128);
            wg_wait(ctl, C_OA + rb * 32, rb < 8 ? 32u : 128u, C_OA + rb * 32, 32u);
            gemm_small(lds, u, ctl);
        }
        WSTAMP(16 + (kind == 6 && x >= 480 ? (x < 512 ? 7 : 8) : kind) + ((it & 0xffff) << 8));
    }
}

__global__ __launch_bounds__(512) void k_init(char* ws) {
    const rsrc_t xr = mkrsrc(ws + WS_XCH, NWIN_INIT * 16384u);
    unsigned* ctl = (unsigned*)(ws + WS_CTL);
    const u32x4 sv = {SENT, SENT, SENT, SENT};
    const unsigned xcc = (unsigned)__builtin_amdgcn_s_getreg(20 | (3 << 11)) & 7u, half = blockIdx.x >> 3, win = blockIdx.x & 7;
    if (threadIdx.x < 64 && xcc == 1) {
        unsigned b = ~0u;
        for (int i = 0; i < 5; ++i) {
            const unsigned long long t0 = __builtin_amdgcn_s_memtime();
            __builtin_amdgcn_raw_buffer_store_b32(0u, xr, (unsigned)(2 * 16384 + C_PROBE * 4) + half * 8192u + (unsigned)i * 256u, 0, 16);
            asm volatile("s_waitcnt vmcnt(0)" ::: "memory");
            b = min(b, (unsigned)(__builtin_amdgcn_s_memtime() - t0));
        }
        if (threadIdx.x == 0) ctl[C_PAR + half] = b;
    }
    __syncthreads();
    if (win < 7 && win != 5) ST16(sv, xr, win * 16384u + half * 8192u + threadIdx.x * 16u, 0, 16);
    if (blockIdx.x == 0 && threadIdx.x == 0) ctl[C_PHASE] = xcc;
}

extern "C" void kernel_launch(void* const* d_in, const int* in_sizes, int n_in, void* d_out, int out_size, void* d_ws, size_t ws_size, hipStream_t stream) {
    Params p;
    p.ax = (const u16*)d_in[0]; p.mem0 = (const u16*)d_in[1]; p.wq = (const u16*)d_in[2]; p.wk = (const u16*)d_in[3]; p.wv = (const u16*)d_in[4]; p.wo = (const u16*)d_in[5];
    p.bx = (const u16*)d_in[7]; p.bwqkv = (const u16*)d_in[8]; p.bwo = (const u16*)d_in[9];
    p.out = (u16*)d_out; p.ws = (char*)(((uintptr_t)d_ws + WS_SHIFT(ws_size) + 0x1FFFFFu) & ~(uintptr_t)0x1FFFFFu);
    (void)hipFuncSetAttribute((const void*)k_fused, hipFuncAttributeMaxDynamicSharedMemorySize, LDS_BYTES);
    k_init<<<16, 512, 0, stream>>>(p.ws);
    k_fused<<<256, 512, LDS_BYTES, stream>>>(p);
}
```

```cpp
#include <hip/hip_runtime.h>
#include <stdint.h>
#include <stdlib.h>

#define LAS __attribute__((address_space(3)))
#define DI __device__ __forceinline__
typedef unsigned short u16;
typedef __attribute__((ext_vector_type(8))) __bf16 bf16x8;
typedef __attribute__((ext_vector_type(16))) __bf16 bf16x16;
typedef __attribute__((ext_vector_type(2))) __bf16 bf16x2;
typedef __attribute__((ext_vector_type(4))) short s16x4;
typedef __attribute__((ext_vector_type(8))) short s16x8;
typedef __attribute__((ext_vector_type(16))) short s16x16;
typedef __attribute__((ext_vector_type(2))) float f32x2;
typedef __attribute__((ext_vector_type(4))) float f32x4;
typedef __attribute__((ext_vector_type(2))) unsigned u32x2;
typedef __attribute__((ext_vector_type(4))) unsigned u32x4;
typedef LAS unsigned char* ldsp;
typedef __amdgpu_buffer_rsrc_t rsrc_t;

constexpr int S_LEN = 2048, DM = 1024, NH = 32, DH = 128, NSEG = 64;
constexpr int LDB3 = 3072;
constexpr unsigned XH = S_LEN * 256, BH = S_LEN * 64;
constexpr unsigned SENT = 0xFFFFFFFFu;
constexpr size_t WS_XCH = 2u << 20;
constexpr int NWIN = 640, NWIN_INIT = 7;
constexpr unsigned XCH_KM = NWIN * 16384u;
constexpr size_t XCH_BYTES = (size_t)XCH_KM + 64u * 128u * 1536u;
constexpr size_t WS_CTL = WS_XCH + 2 * 16384;
constexpr int C_TMO = 0, C_PAR = 13312  , C_PHASE = 13314, C_HEAD = 3072, C_PROG = 192, C_PACE = 128, C_Q = 256, C_B = C_Q + 8 * 32, C_OA = C_B + 8 * 32, C_OB = C_OA + 16 * 32, C_OS = 16384, C_KV = 4096, C_PROBE = 12288;
constexpr size_t WS_QKVX = 26u << 20;
constexpr size_t WS_QKVB = WS_QKVX + (size_t)96 * XH;
constexpr size_t WS_OA = WS_QKVB + (size_t)96 * BH;
constexpr size_t WS_OB = WS_OA + (size_t)S_LEN * 4096 * 2;
constexpr size_t WS_END = WS_OB + (size_t)S_LEN * 1024 * 2;
#define WS_SHIFT(sz) 0
constexpr int LDS_BYTES = 131072 + 1024 + 8192;

DI unsigned pk_bf16(float a, float b) { f32x2 v = {a, b}; return __builtin_bit_cast(unsigned, __builtin_convertvector(v, bf16x2)); }
DI s16x4 trd(ldsp p) { return __builtin_amdgcn_ds_read_tr16_b64_v4i16((LAS s16x4*)p); }
DI bf16x8 cat8(s16x4 a, s16x4 b) { return __builtin_bit_cast(bf16x8, __builtin_shufflevector(a, b, 0, 1, 2, 3, 4, 5, 6, 7)); }
DI bf16x16 cat16(bf16x8 a, bf16x8 b) { return __builtin_shufflevector(a, b, 0, 1, 2, 3, 4, 5, 6, 7, 8, 9, 10, 11, 12, 13, 14, 15); }
DI bf16x8 lo8(bf16x16 v) { return __builtin_shufflevector(v, v, 0, 1, 2, 3, 4, 5, 6, 7); }
DI bf16x8 hi8(bf16x16 v) { return __builtin_shufflevector(v, v, 8, 9, 10, 11, 12, 13, 14, 15); }
DI bf16x8 asbf(u32x4 v) { return __builtin_bit_cast(bf16x8, v); }
DI f32x4 mma32(bf16x8 a, bf16x8 b, f32x4 c) { return __builtin_amdgcn_mfma_f32_16x16x32_bf16(a, b, c, 0, 0, 0); }
DI f32x4 mma16(u32x2 a, u32x2 b, f32x4 c) { return mma32(asbf((u32x4){a.x, a.y, 0u, 0u}), asbf((u32x4){b.x, b.y, 0u, 0u}), c); }
DI f32x4 smma(u32x4 a, bf16x16 b, f32x4 c, int idx) { return __builtin_amdgcn_smfmac_f32_16x16x64_bf16(asbf(a), b, c, idx, 0, 0); }
DI rsrc_t mkrsrc(const void* p, unsigned bytes) { return __builtin_amdgcn_make_buffer_rsrc((void*)p, 0, bytes, 0x00020000); }
DI u32x4 ld16_sc1(rsrc_t r, unsigned voff, unsigned soff) { return __builtin_amdgcn_raw_buffer_load_b128(r, voff, soff, 16); }
#define ST16(v, r, voff, soff, aux) do { const u32x4 st_v = (v); __builtin_amdgcn_raw_buffer_store_b128(st_v, r, voff, soff, aux); asm volatile("s_nop 1" :: "v"(st_v)); } while (0)
DI void swap16(unsigned& a, unsigned& b) { auto r = __builtin_amdgcn_permlane16_swap(a, b, false, false); a = r[0]; b = r[1]; }
DI void swap32(unsigned& a, unsigned& b) { auto r = __builtin_amdgcn_permlane32_swap(a, b, false, false); a = r[0]; b = r[1]; }
DI void swap16f(float& a, float& b) { unsigned x = __float_as_uint(a), y = __float_as_uint(b); swap16(x, y); a = __uint_as_float(x); b = __uint_as_float(y); }
DI void swap32f(float& a, float& b) { unsigned x = __float_as_uint(a), y = __float_as_uint(b); swap32(x, y); a = __uint_as_float(x); b = __uint_as_float(y); }
DI float rows_max(float v) { float a = v, b = v; swap16f(a, b); v = fmaxf(a, b); a = v; b = v; swap32f(a, b); return fmaxf(a, b); }
DI float rows_sum(float v) { float a = v, b = v; swap16f(a, b); v = a + b; a = v; b = v; swap32f(a, b); return a + b; }
DI unsigned umax4(u32x4 v) { return max(max(v.x, v.y), max(v.z, v.w)); }
#define WAIT_V(n) asm volatile("s_waitcnt vmcnt(" #n ")" ::: "memory")
#define WAIT_L(n) asm volatile("s_waitcnt lgkmcnt(" #n ")" ::: "memory")
#define BAR() __builtin_amdgcn_s_barrier()
#define SCHED() __builtin_amdgcn_sched_barrier(0)
#define LDS_BAR() do { asm volatile("s_waitcnt lgkmcnt(0)" ::: "memory"); __builtin_amdgcn_s_barrier(); asm volatile("" ::: "memory"); } while (0)

constexpr int HTB = 128 * 64 * 2;
DI int lds_byte(int r, int c) { const int st = (r >> 4) * 2 + (c >> 5), rr = r & 15, cc = c & 31, ob = rr * 64 + cc * 2; return st * 1024 + (ob ^ (((ob >> 9) & 1) << 5)); }
DI void stage_rc(int b, int& R, int& C) { const int st = b / 1024, sb = b % 1024, swz = sb ^ (((sb >> 9) & 1) << 5); R = (st >> 1) * 16 + swz / 64; C = (st & 1) * 32 + (swz % 64) / 2; }

struct Params {
    const u16 *ax, *mem0, *wq, *wk, *wv, *wo, *bx, *bwqkv, *bwo;
    u16* out; char* ws;
};
#define RLX_AGENT __ATOMIC_RELAXED, __HIP_MEMORY_SCOPE_AGENT
DI bool spin_out(unsigned& spins, unsigned* ctl) {
    spins = __builtin_amdgcn_readfirstlane(spins) + 1u;
    if ((spins & 63u) == 0u) {
        if (spins > (1u << 18)) { __hip_atomic_store(ctl + C_TMO, 1u, RLX_AGENT); return true; }
        if (__builtin_amdgcn_readfirstlane(__hip_atomic_load(ctl + C_TMO, RLX_AGENT)) != SENT) return true;
    }
    return false;
}
template <int NAP = 8>
DI void wait_count(unsigned* ctl, int word, unsigned need) {
    unsigned spins = 0;
    while (__builtin_amdgcn_readfirstlane(__hip_atomic_load(ctl + word, RLX_AGENT)) + 1u < need) { __builtin_amdgcn_s_sleep(NAP); if (spin_out(spins, ctl)) break; }
}
DI void wg_wait(unsigned* ctl, int word0, unsigned need0, int word1, unsigned need1) {
    if (threadIdx.x < 64) {
        wait_count(ctl, word0, need0); wait_count(ctl, word1, need1);
        __builtin_amdgcn_fence(__ATOMIC_ACQUIRE, "agent");
        asm volatile("s_waitcnt vmcnt(0)" ::: "memory");
    }
    asm volatile("" ::: "memory"); BAR(); asm volatile("" ::: "memory");
}
struct Unit { const char* A; const char* B; char* C; unsigned lda, ldb, ldc, cw, ch, ak, ah; int nt, sig, nsig; };
DI int fresh_tid() { int t = threadIdx.x; asm volatile("" : "+v"(t)); return t; }
DI void signal(unsigned* ctl, int sig, int nsig) { const int lane = fresh_tid() & 63; if (lane < nsig) __hip_atomic_fetch_add(ctl + sig + (lane & 1) * 16 + (lane >> 1) * 512, 1u, RLX_AGENT); }
DI u32x4 pack_pair(f32x4 v0, f32x4 v1) {
    unsigned ax = pk_bf16(v0[0], v0[1]), ay = pk_bf16(v0[2], v0[3]), bx = pk_bf16(v1[0], v1[1]), by = pk_bf16(v1[2], v1[3]);
    swap16(ax, bx); swap16(ay, by);
    return (u32x4){ax, ay, bx, by};
}
DI unsigned frag_off(int lane) { return (unsigned)((((lane >> 4) & 1) * 2 + (lane >> 5)) * 256 + (lane & 15) * 16); }
DI void store_frag(rsrc_t cr, unsigned voff, unsigned soff, f32x4 v0, f32x4 v1) { ST16(pack_pair(v0, v1), cr, voff, soff, 16); }
DI u32x4 to_rows(ldsp sc, int lane, f32x4 v0, f32x4 v1) {
    *(volatile LAS u32x4*)(sc + ((lane & 15) * 4 + ((lane >> 4) & 1) * 2 + (lane >> 5)) * 16) = pack_pair(v0, v1);
    return *(volatile LAS u32x4*)(sc + lane * 16);
}
constexpr int L_SCR = 132096;
#define STAGE(bufoff, gbase, voff, step2) do { \
        __builtin_amdgcn_global_load_lds((const unsigned*)((gbase) + (voff)), (LAS unsigned*)(lds + (bufoff) + ldsw), 16, 0, 0); \
        __builtin_amdgcn_global_load_lds((const unsigned*)((gbase) + (step2) + (voff)), (LAS unsigned*)(lds + (bufoff) + ldsw + 8192), 16, 0, 0); } while (0)
#define LDA(dst, off) do { _Pragma("unroll") for (int m = 0; m < 4; ++m) _Pragma("unroll") for (int k = 0; k < 2; ++k) dst[m][k] = *(const LAS bf16x8*)(lds + (off) + aoff + m * 2048 + k * 1024); } while (0)
#define TRD(dst, base, off) asm volatile("ds_read_b64_tr_b16 %0, %1 offset:%2" : "=v"(dst) : "v"(base), "i"(off) : "memory")
#define LDB(dst, v0, v1, off) do { s16x4 l0, h0, l1, h1, l2, h2, l3, h3; \
        TRD(l0, v0, (off)); TRD(h0, v0, (off) + 256); TRD(l1, v0, (off) + 8192); TRD(h1, v0, (off) + 8448); \
        TRD(l2, v1, (off)); TRD(h2, v1, (off) + 256); TRD(l3, v1, (off) + 8192); TRD(h3, v1, (off) + 8448); \
        dst[0][0] = cat8(l0, h0); dst[0][1] = cat8(l1, h1); dst[1][0] = cat8(l2, h2); dst[1][1] = cat8(l3, h3); } while (0)
#define MMA(accv, At, Bt) do { SCHED(); __builtin_amdgcn_s_setprio(1); _Pragma("unroll") for (int m = 0; m < 4; ++m) _Pragma("unroll") for (int n = 0; n < 2; ++n) _Pragma("unroll") for (int k = 0; k < 2; ++k) \
        accv[m][n] = mma32(Bt[n][k], At[m][k], accv[m][n]); __builtin_amdgcn_s_setprio(0); } while (0)
DI unsigned va_of(int tid, unsigned lda, int nt) { int R, C; stage_rc(tid * 16, R, C); return lda ? (unsigned)R * lda + (unsigned)C * 2u : (unsigned)((tid >> 7) * nt * 2048 + (tid & 127) * 16); }
DI unsigned vb_of(int tid, int wid, unsigned ldb) { return (unsigned)(8 * (wid >> 1) + ((tid >> 2) & 7)) * ldb + (unsigned)((2 * (wid & 1) + ((tid >> 5) & 1)) * 64 + (((tid & 3) ^ (((wid >> 1) & 1) << 1)) * 16)); }
#define GEMM_LANE_SETUP(lda0) \
    const int tid = fresh_tid(); \
    const int wid = __builtin_amdgcn_readfirstlane(tid >> 6), lane = tid & 63, wr = wid >> 2, wc = wid & 3, fr = lane & 15, fq = lane >> 4; \
    const unsigned ldsw = (unsigned)wid * 1024u; \
    const int aoff = (lda0) ? lds_byte(wr * 64 + fr, fq * 8) : wr * 8192 + lane * 16; \
    const int boff0 = fq * 2048 + wc * 512 + (fr >> 2) * 64 + (fq & 1) * 32 + (fr & 3) * 8, boff1 = boff0 ^ 32;

template <class Sched>
DI void gemm_stream(ldsp lds, const Sched& S, unsigned* ctl) {
    Unit cur, nxt; int ui = 0;
    if (!S.next(0, cur)) return;
    GEMM_LANE_SETUP(cur.lda);
#define SA(b, h) (((b) * 2 + (h)) * HTB)
#define SB(b, h) ((4 + (b) * 2 + (h)) * HTB)
    const unsigned vb0 = (unsigned)(size_t)lds + 65536u + (unsigned)boff0, vb1 = (unsigned)(size_t)lds + 65536u + (unsigned)boff1;
    f32x4 acc[2][2][4][2];
#pragma unroll
    for (int a = 0; a < 2; ++a)
#pragma unroll
        for (int b = 0; b < 2; ++b)
#pragma unroll
            for (int m = 0; m < 4; ++m)
#pragma unroll
                for (int n = 0; n < 2; ++n) acc[a][b][m][n] = (f32x4){0.f, 0.f, 0.f, 0.f};
    bf16x8 At[4][2], B0[2][2], B1[2][2];
    const unsigned vA = va_of(tid, cur.lda, cur.nt); unsigned vB = vb_of(tid, wid, cur.ldb);
    const size_t a2s = cur.lda ? (size_t)64 * cur.lda : (size_t)cur.nt * 8192;
    {
        const size_t ah = cur.ah, b2s = (size_t)32 * cur.ldb, bk = (size_t)64 * cur.ldb;
        STAGE(SB(0, 0), cur.B, vB, b2s); STAGE(SB(0, 1), cur.B + 256, vB, b2s); STAGE(SA(0, 0), cur.A, vA, a2s); STAGE(SA(0, 1), cur.A + ah, vA, a2s);
        if (wr == 1) BAR();
        WAIT_V(2); BAR();
        STAGE(SB(1, 0), cur.B + bk, vB, b2s); STAGE(SA(1, 0), cur.A + cur.ak, vA, a2s); STAGE(SB(1, 1), cur.B + bk + 256, vB, b2s);
        WAIT_V(6); BAR();
    }
    int psig = 0, pn = 0;
    for (;;) {
        const bool has_next = S.next(ui + 1, nxt);
        if (!has_next) nxt = cur;
        int nt = cur.nt; asm volatile("" : "+s"(nt));
        for (int t = 0; t < nt; t += 2) {
            const bool last = (t == nt - 2);
            const char* a1 = cur.A + (size_t)(t + 1) * cur.ak + cur.ah;
            if (last) vB = vb_of(fresh_tid(), wid, nxt.ldb);
            const unsigned ldb = last ? nxt.ldb : cur.ldb;
            const char* a2 = last ? nxt.A : cur.A + (size_t)(t + 2) * cur.ak;
            const char* b2 = last ? nxt.B : cur.B + (size_t)(t + 2) * 64 * cur.ldb;
            const size_t ah = cur.ah, b2s = (size_t)32 * ldb;
            const char* a3 = a2 + cur.ak; const char* b3 = b2 + (size_t)64 * ldb;
            LDB(B0, vb0, vb1, 0); LDB(B1, vb0, vb1, HTB); SCHED(); LDA(At, SA(0, 0)); STAGE(SA(1, 1), a1, vA, a2s);
            WAIT_V(8); WAIT_L(0); BAR(); MMA(acc[0][0], At, B0); MMA(acc[0][1], At, B1); BAR(); SCHED();
            LDA(At, SA(0, 1)); STAGE(SB(0, 0), b2, vB, b2s); STAGE(SB(0, 1), b2 + 256, vB, b2s); STAGE(SA(0, 0), a2, vA, a2s);
            WAIT_V(8);
            asm volatile("" : "+s"(pn));
            if (pn) { signal(ctl, psig, pn); pn = 0; }
            WAIT_L(0); BAR(); MMA(acc[1][0], At, B0); MMA(acc[1][1], At, B1); BAR(); SCHED();
            LDB(B0, vb0, vb1, 2 * HTB); LDB(B1, vb0, vb1, 3 * HTB); SCHED(); LDA(At, SA(1, 0)); STAGE(SA(0, 1), a2 + ah, vA, a2s);
            WAIT_V(8); WAIT_L(0); BAR(); MMA(acc[0][0], At, B0); MMA(acc[0][1], At, B1); BAR(); SCHED();
            LDA(At, SA(1, 1)); STAGE(SB(1, 0), b3, vB, b2s); STAGE(SB(1, 1), b3 + 256, vB, b2s); STAGE(SA(1, 0), a3, vA, a2s);
            WAIT_V(8); WAIT_L(0); BAR(); MMA(acc[1][0], At, B0); MMA(acc[1][1], At, B1); BAR(); SCHED();
        }
        if (wr == 0) BAR();
        {
            const rsrc_t cr = mkrsrc(cur.C, 0x1000000u);
            const int tz = fresh_tid() & 63;
            if (cur.nsig) {
                const unsigned co = (unsigned)(wr * 64) * cur.ldc + (unsigned)wc * cur.cw + frag_off(tz);
#pragma unroll
                for (int ai = 0; ai < 2; ++ai)
#pragma unroll
                    for (int m = 0; m < 4; ++m)
#pragma unroll
                        for (int bj = 0; bj < 2; ++bj) store_frag(cr, co, (unsigned)(ai * 128 + m * 16) * cur.ldc + (unsigned)bj * cur.ch, acc[ai][bj][m][0], acc[ai][bj][m][1]);
            } else {
                const unsigned co = (unsigned)(wr * 64 + (tz >> 2)) * cur.ldc + (unsigned)(wc * 64 + (tz & 3) * 16);
#pragma unroll
                for (int ai = 0; ai < 2; ++ai)
#pragma unroll
                    for (int m = 0; m < 4; ++m)
#pragma unroll
                        for (int bj = 0; bj < 2; ++bj)
                            ST16(to_rows(lds + L_SCR + wid * 1024, tz, acc[ai][bj][m][0], acc[ai][bj][m][1]), cr, co, (unsigned)(ai * 128 + m * 16) * cur.ldc + (unsigned)bj * 256u, 0);
            }
        }
        psig = cur.sig; pn = cur.nsig;
        if (!has_next) break;
#pragma unroll
        for (int a = 0; a < 2; ++a)
#pragma unroll
            for (int b = 0; b < 2; ++b)
#pragma unroll
                for (int m = 0; m < 4; ++m)
#pragma unroll
                    for (int n = 0; n < 2; ++n) acc[a][b][m][n] = (f32x4){0.f, 0.f, 0.f, 0.f};
        cur = nxt; ++ui;
        if (wr == 1) BAR();
    }
    WAIT_V(0);
    signal(ctl, psig, pn);
    BAR();
#undef SA
#undef SB
}

DI void gemm_small(ldsp lds, const Unit u, unsigned* ctl) {
    GEMM_LANE_SETUP(u.lda);
    const unsigned vA = va_of(tid, u.lda, u.nt), vB = vb_of(tid, wid, u.ldb);
    const size_t a2s = u.lda ? (size_t)64 * u.lda : (size_t)u.nt * 8192, b2s = (size_t)32 * u.ldb, bk = (size_t)64 * u.ldb;
    f32x4 acc[4][2];
#pragma unroll
    for (int m = 0; m < 4; ++m)
#pragma unroll
        for (int n = 0; n < 2; ++n) acc[m][n] = (f32x4){0.f, 0.f, 0.f, 0.f};
    int nt = u.nt; asm volatile("" : "+s"(nt));
    const unsigned vb0 = (unsigned)(size_t)lds + (unsigned)boff0, vb1 = (unsigned)(size_t)lds + (unsigned)boff1, vb2 = vb0 + 65536u, vb3 = vb1 + 65536u;
#pragma unroll
    for (int t = 0; t < 4; ++t) { STAGE(t * 32768, u.A + (size_t)t * u.ak, vA, a2s); STAGE(t * 32768 + 16384, u.B + t * bk, vB, b2s); }
    bf16x8 A0[4][2], B0[2][2], A1[4][2], B1[2][2];
    WAIT_V(12); BAR();
    LDB(B0, vb0, vb1, 16384); LDA(A0, 0);
#define RDA(dst, m, off) do { dst[m][0] = *(const LAS bf16x8*)(lds + (off) + aoff + (m) * 2048); dst[m][1] = *(const LAS bf16x8*)(lds + (off) + aoff + (m) * 2048 + 1024); } while (0)
#define RDB(dst, nn, v, off) do { s16x4 l0, h0, l1, h1; TRD(l0, v, (off)); TRD(h0, v, (off) + 256); TRD(l1, v, (off) + 8192); TRD(h1, v, (off) + 8448); dst[nn][0] = cat8(l0, h0); dst[nn][1] = cat8(l1, h1); } while (0)
#define MM4(Ac, Bc, m) do { SCHED(); _Pragma("unroll") for (int n = 0; n < 2; ++n) _Pragma("unroll") for (int k = 0; k < 2; ++k) acc[m][n] = mma32(Bc[n][k], Ac[m][k], acc[m][n]); SCHED(); } while (0)
#define KSTEP(i, Ac, Bc, An, Bn, v0, v1, boffn) do { \
        const int tn = min(t0 + (i) + 4, nt - 1); \
        WAIT_L(0); WAIT_V(8); BAR(); \
        MM4(Ac, Bc, 0); RDB(Bn, 0, v0, boffn); RDA(An, 0, (((i) + 1) & 3) * 32768); \
        MM4(Ac, Bc, 1); RDB(Bn, 1, v1, boffn); RDA(An, 1, (((i) + 1) & 3) * 32768); \
        MM4(Ac, Bc, 2); RDA(An, 2, (((i) + 1) & 3) * 32768); STAGE((i) * 32768, u.A + (size_t)tn * u.ak, vA, a2s); \
        MM4(Ac, Bc, 3); RDA(An, 3, (((i) + 1) & 3) * 32768); STAGE((i) * 32768 + 16384, u.B + (size_t)tn * bk, vB, b2s); } while (0)
    for (int t0 = 0; t0 < nt; t0 += 4) {
        KSTEP(0, A0, B0, A1, B1, vb0, vb1, 49152);
        KSTEP(1, A1, B1, A0, B0, vb2, vb3, 16384);
        KSTEP(2, A0, B0, A1, B1, vb2, vb3, 49152);
        KSTEP(3, A1, B1, A0, B0, vb0, vb1, 16384);
    }
    WAIT_L(0); SCHED();
#undef RDA
#undef RDB
#undef MM4
#undef KSTEP
    const rsrc_t cr = mkrsrc(u.C, 128u * u.ldc);
    if (u.nsig) {
#pragma unroll
        for (int m = 0; m < 4; ++m) store_frag(cr, (unsigned)(wr * 64) * u.ldc + (unsigned)wc * u.cw + frag_off(lane), (unsigned)(m * 16) * u.ldc, acc[m][0], acc[m][1]);
    } else {
#pragma unroll
        for (int m = 0; m < 4; ++m)
            ST16(to_rows(lds + L_SCR + wid * 1024, lane, acc[m][0], acc[m][1]), cr, (unsigned)(wr * 64 + (lane >> 2)) * u.ldc + (unsigned)(wc * 64 + (lane & 3) * 16), (unsigned)(m * 16) * u.ldc, 0);
    }
    WAIT_V(0);
    signal(ctl, u.sig, u.nsig);
    BAR();
}

#define win_off(idx) ((unsigned)(idx) * 16384u + ((((unsigned)(idx) >> 7) ^ par) & 1u) * 8192u)
#define W_MEM(s) ((s) == 0 ? 0 : 6 + 2 * (s))
#define W_PART(slot) (192 + (slot) * 64)
#define W_X(slot) (384 + (slot) * 128)
DI bool all_set(unsigned mx) { return __builtin_amdgcn_ballot_w64(mx == SENT) == 0ull; }
DI u32x4 rows_gather(unsigned v) { unsigned a = v, b = v; swap16(a, b); unsigned c = a, d = b; swap32(a, c); swap32(b, d); return (u32x4){a, b, c, d}; }
DI float max3f(float a, float b, float c) { return fmaxf(fmaxf(a, b), c); }
#define KD0 6
#define KD1 5
#define KD2 4
#define KRA 3
#define KDM 3
#define KWP 4
#define KNT1 0
#define KNT2 0
#define KPM 8
constexpr int RED_M = 144;
constexpr int L_PX = 32768, L_PM = L_PX + 512, L_ML = L_PM + 512, L_FLAG = L_ML + 256, L_PACE = L_FLAG + 16, L_VM = 34816, L_OWN = L_VM + 2048, L_X = L_OWN + 1536  ;
constexpr unsigned OOB = 0x80000000u;
#define RETRY(i)
#define SEG(i)
#define PST(i)
#define PIN_S(x) asm volatile("" : "+s"(x))
#define PIN_V(x) asm volatile("" : "+v"(x))
DI float ror8(float v) { return __uint_as_float(__builtin_amdgcn_update_dpp(0, __float_as_uint(v), 0x128, 0xf, 0xf, true)); }

template <int ROLE>
DI void chain_loop(const Params& p, const unsigned par, const int rank, const int w, ldsp lds, const bf16x16 (&W)[6][2], const bf16x16 (&WO)[2][2], unsigned long long* stamps) {
    const int lane = fresh_tid() & 63, g = lane >> 4, n = lane & 15;
    const int h = rank >> 2, j = rank & 3;
    unsigned* ctl = (unsigned*)(p.ws + WS_CTL);
    const rsrc_t xr = mkrsrc(p.ws + WS_XCH, (unsigned)XCH_BYTES);
    const rsrc_t qr = mkrsrc(p.ws + WS_QKVX + (size_t)(32 + h) * XH, 33u * XH);
    const int sidx = (n & 2) ? 0xEEEE : 0x4444;
    const unsigned l16 = (unsigned)lane * 16u;
    const int redw = (w * 1024 + (n & 1) * 512 + (n >> 1) * 8 + 2 * (g ^ (n >> 3))) * 4;
    const int redr = (w < 4 ? (n >> 3) * RED_M + (4 * w + g) * 8 + ((n & 7) ^ ((w & 1) * 2)) : 2 * RED_M + (8 * (w & 1) + 2 * g + ((n >> 1) & 1)) * 8 + ((2 * (n >> 2) + (n & 1)) ^ (g & 2))) * 4;
    const bool vown = (g >> 1) == (j & 1);
    const unsigned vg1 = (unsigned)(((j & 2) ^ 2) + (g >> 1)) * 1536u + 1024u + (unsigned)(lane & 31) * 16u;
    const unsigned vg0 = vown ? OOB : (vg1 + (unsigned)(j & 2) * 3072u - 3072u);
    const unsigned pub_off = (g == j ? XCH_KM + (unsigned)rank * 1536u : (unsigned)(4 * h + g) * 16384u + (unsigned)j * 1536u) + (unsigned)w * 256u + (unsigned)n * 16u;
    const int red = ((j + 1) & 3) * 32 + h;
    const int rho = 4 * (8 * j + w) + 2 * (g >> 1) + ((n >> 2) & 1);
    const unsigned pst_off = n < 8 ? (unsigned)(rho >> 1) * 16384u + (unsigned)(rho & 1) * 4096u + (unsigned)((h ^ rho) & 31) * 128u + (unsigned)((g & 1) * 64 + (n & 3) * 16) : OOB;
    const unsigned mem_off = lane < 8 ? (unsigned)(4 * ((n >> 1) & 1) + 2 * (n >> 2) + (n & 1)) * 16u : OOB;
    const unsigned xw0 = win_off(W_X(0)), xw1 = win_off(W_X(1));
    unsigned pso = win_off(W_PART(0)), psn = win_off(W_PART(1)), psp = win_off(W_PART(2));
    const unsigned redo = (unsigned)(red >> 1) * 16384u + (unsigned)(red & 1) * 4096u;
    const int vxr = L_X + lane * 16;
    const float csc = 0.08838834764831845f * 1.4426950408889634f;
    float ninf = -__builtin_inff(); PIN_V(ninf);
    u32x4 KX[8], VWX[2];
    auto dma_x = [&](int s) {
        if (ROLE == 0 && w < 4) {
            if ((s & 3) == 0) { wait_count(ctl, C_KV + (s >> 2) * 512 + h * 16, 16u); asm volatile("" ::: "memory"); }
#pragma unroll
            for (int i = 0; i < 2; ++i) {
                const unsigned vo = l16, so = (unsigned)s * 8192u + (unsigned)(w * 2 + i) * 1024u;
                __builtin_amdgcn_raw_ptr_buffer_load_lds(qr, (LAS void*)(lds + L_X + (s & 1) * 16384 + (w * 2 + i) * 1024), 16, vo, so + 32u * XH, 0, 16);
                __builtin_amdgcn_raw_ptr_buffer_load_lds(qr, (LAS void*)(lds + L_X + (s & 1) * 16384 + 8192 + (w * 2 + i) * 1024), 16, vo, so, 0, 16);
            }
        }
    };
    auto load_kx = [&](int s) {
#pragma unroll
        for (int kt = 0; kt < 2; ++kt)
#pragma unroll
            for (int kk = 0; kk < 4; ++kk) KX[kt * 4 + kk] = *(const LAS u32x4*)(lds + vxr + (s & 1) * 16384 + 8192 + kt * 4096 + ((j + kk) & 3) * 1024);
    };
    auto vwx_kt = [&](int s, int kt) {
        bf16x8 VA[4];
#pragma unroll
        for (int kk = 0; kk < 4; ++kk) VA[kk] = *(const LAS bf16x8*)(lds + vxr + (s & 1) * 16384 + kt * 4096 + (kk ^ (j & 2)) * 1024);
#pragma unroll
        for (int ct = 0; ct < 2; ++ct) {
            f32x4 d = {0.f, 0.f, 0.f, 0.f};
#pragma unroll
            for (int kk = 0; kk < 4; ++kk) d = mma32(VA[kk], (kk & 1) ? hi8(WO[ct][kk >> 1]) : lo8(WO[ct][kk >> 1]), d);
            VWX[ct][2 * kt] = pk_bf16(d[0], d[1]); VWX[ct][2 * kt + 1] = pk_bf16(d[2], d[3]);
        }
    };
    auto rmax = [&](float v) { float a = v, b = v; swap16f(a, b); v = max3f(a, b, ninf); a = v; b = v; swap32f(a, b); return max3f(a, b, ninf); };
    auto lo2 = [](f32x4 v) { return (f32x2){v[0], v[1]}; };
    auto hi2 = [](f32x4 v) { return (f32x2){v[2], v[3]}; };
    dma_x(0); dma_x(1);
    asm volatile("s_waitcnt vmcnt(0) lgkmcnt(0)" ::: "memory"); BAR(); asm volatile("" ::: "memory");
    PST(5);
    vwx_kt(0, 0); vwx_kt(0, 1);
    if (ROLE == 2) load_kx(0);
    __builtin_amdgcn_s_setprio(2);

    unsigned iv_t0 = 0, iv_acc = 0;
    unsigned spins = 0, pace_c = 0x7fffffffu, pace_t = 0;
    const u32x4 sv = {SENT, SENT, SENT, SENT};
    for (int s = 0;; ++s) {
        unsigned xw = (s & 1) ? xw1 : xw0;
        unsigned mso = win_off(W_MEM(s) + (w >> 2)) + (unsigned)(w & 3) * 2048u;
        unsigned pub = pub_off + (g == j ? (unsigned)s * (128u * 1536u) : xw);
        PIN_S(xw); PIN_S(mso); PIN_V(pub);
        {
            if (KD0) __builtin_amdgcn_s_sleep(KD0);
            const u32x2 pv = *(const LAS u32x2*)(lds + L_PACE);
            unsigned target = __builtin_amdgcn_readfirstlane(pv.x); const bool valid = __builtin_amdgcn_readfirstlane(pv.y) + 1u == (unsigned)s && s > 3;
            unsigned now = (unsigned)__builtin_amdgcn_s_memrealtime();
            if (ROLE == 3 && s > 2) pace_c = min(pace_c, now - pace_t);
            if (valid && (int)(target - now) < 100) { while ((int)(target - now) > 0) now = (unsigned)__builtin_amdgcn_s_memrealtime(); }
        }
        SEG(10);
        f32x4 c[6]; u32x4 a1;
        u32x4 a0;
        auto ld_a = [&]() { asm volatile("" ::: "memory"); a0 = ld16_sc1(xr, l16, mso); a1 = ld16_sc1(xr, l16 + 1024, mso); };
        auto ok_a = [&]() { return all_set(max(umax4(a0), umax4(a1))); };
        auto mm_a = [&]() {
#pragma unroll
            for (int i = 0; i < 6; ++i) { c[i] = (f32x4){0.f, 0.f, 0.f, 0.f}; PIN_V(c[i]); }
            SCHED();
            c[0] = smma(a0, W[0][0], c[0], sidx); c[1] = smma(a0, W[1][0], c[1], sidx); c[0] = smma(a1, W[0][1], c[0], sidx); c[1] = smma(a1, W[1][1], c[1], sidx);
            c[2] = smma(a0, W[2][0], c[2], sidx); c[3] = smma(a0, W[3][0], c[3], sidx);
        };
        auto put = [&](int i) { *(LAS f32x2*)(lds + redw + ((i >> 1) * RED_M + (i & 1) * 64) * 4) = lo2(c[i]) + hi2(c[i]); };
        ld_a(); SCHED();
        mm_a();
        if (__builtin_expect(!ok_a(), 0)) { bool out; do { RETRY(0); out = spin_out(spins, ctl); ld_a(); } while (!(ok_a() | out)); mm_a(); }
        SEG(0);
        c[2] = smma(a1, W[2][1], c[2], sidx); c[3] = smma(a1, W[3][1], c[3], sidx);
        put(0); put(1);
        SCHED();
        c[4] = smma(a0, W[4][0], c[4], sidx); c[5] = smma(a0, W[5][0], c[5], sidx); c[4] = smma(a1, W[4][1], c[4], sidx);
        put(2); put(3);
        c[5] = smma(a1, W[5][1], c[5], sidx);
        SCHED();
        put(4); put(5);
        SEG(1);
        LDS_BAR();
        SEG(2);
        if (ROLE <= 1) {
            f32x2 v[8];
#pragma unroll
            for (int ww = 0; ww < 8; ++ww) { const LAS float* q = (const LAS float*)(lds + redr + ww * 4096); v[ww] = (f32x2){q[0], q[512]}; }
            const f32x2 t = ((v[0] + v[1]) + (v[2] + v[3])) + ((v[4] + v[5]) + (v[6] + v[7]));
            const u32x4 o = rows_gather(min(pk_bf16(t.x, t.y), SENT - 1));
            ST16(o, xr, pub, 0, 16);
            *(LAS u32x4*)(lds + L_OWN + w * 256 + n * 16) = o;
        } else {
            if (ROLE == 3 && rank == 0) {
                pace_t = (unsigned)__builtin_amdgcn_s_memrealtime();
                if (lane == 0) { __hip_atomic_store(ctl + C_PROG, (unsigned)s, RLX_AGENT);
                    __hip_atomic_store((unsigned long long*)(ctl + C_PACE + par * 2048), (unsigned long long)(pace_t + pace_c + KPM) | ((unsigned long long)s << 32), RLX_AGENT); }
            }
        }
        SEG(3);
        if (s == NSEG - 1) break;
        LDS_BAR();
        SEG(13);
        unsigned VWM[2];
        auto vwm_pack = [&](const f32x4 (&dv)[2]) {
#pragma unroll
            for (int ct = 0; ct < 2; ++ct) { const f32x2 t = lo2(dv[ct]) + hi2(dv[ct]); VWM[ct] = pk_bf16(t.x, t.y); PIN_V(VWM[ct]); }
        };
        f32x4 dv[2] = {{0.f, 0.f, 0.f, 0.f}, {0.f, 0.f, 0.f, 0.f}}, s0, s1;
        PIN_V(dv[0]); PIN_V(dv[1]);
        u32x4 v0, v1;
        if (ROLE == 0) {
            if (KDM == 1) { WAIT_V(0); dma_x(min(s + 2, NSEG - 2)); }
            else if (KDM == 2) { dma_x(min(s + 2, NSEG - 2)); if (w < 4) WAIT_V(4); else WAIT_V(0); }
            else WAIT_V(0);
        } else {
            unsigned cw = xw + (unsigned)rank * 16384u, c1 = cw + (unsigned)((j + 1) & 3) * 1536u, c2 = cw + (unsigned)((j + 2) & 3) * 1536u, c3 = cw + (unsigned)((j + 3) & 3) * 1536u;
            PIN_S(cw); PIN_S(c1); PIN_S(c2); PIN_S(c3);
            const unsigned xl = ROLE == 2 ? (l16 & ~128u) : l16;
            u32x4 vo;
            f32x4 e0 = {0.f, 0.f, 0.f, 0.f}, e1 = e0;
            if (ROLE == 1) { vo = *(const LAS u32x4*)(lds + L_OWN + 1024 + (lane & 31) * 16); if (!vown) vo = (u32x4){0u, 0u, 0u, 0u}; PIN_V(vo); }
            else {
                const u32x4 X0 = *(const LAS u32x4*)(lds + L_OWN + xl);
                if (ROLE == 2) { e0 = mma32(asbf(KX[0]), asbf(X0), e0); e1 = mma32(asbf(KX[4]), asbf(X0), e1); } else e0 = mma32(asbf(X0), asbf(X0), e0);
                PIN_V(e0); PIN_V(e1);
            }
            if (KD1) __builtin_amdgcn_s_sleep(KD1);
            u32x4 X1, X2, X3;
            auto ld_b = [&]() {
                asm volatile("" ::: "memory");
                if (ROLE >= 2) { X1 = ld16_sc1(xr, xl, c1); X2 = ld16_sc1(xr, xl, c2); X3 = ld16_sc1(xr, xl, c3); }
                else { v0 = ld16_sc1(xr, vg0, cw); v1 = ld16_sc1(xr, vg1, cw); }
            };
            auto ok_b = [&]() { return all_set(ROLE >= 2 ? max(umax4(X1), max(umax4(X2), umax4(X3))) : max(umax4(v0), umax4(v1))); };
            auto mm_b = [&]() {
                if (ROLE == 2) {
                    s0 = mma32(asbf(KX[1]), asbf(X1), e0); s1 = mma32(asbf(KX[5]), asbf(X1), e1);
                    s0 = mma32(asbf(KX[2]), asbf(X2), s0); s1 = mma32(asbf(KX[6]), asbf(X2), s1);
                    s0 = mma32(asbf(KX[3]), asbf(X3), s0); s1 = mma32(asbf(KX[7]), asbf(X3), s1);
                } else if (ROLE == 3) {
                    s0 = mma32(asbf(X1), asbf(X1), e0); s1 = mma32(asbf(X2), asbf(X2), e1); s0 = mma32(asbf(X3), asbf(X3), s0);
                } else {
                    v0 |= vo;
                    *(LAS u32x4*)(lds + L_VM + lane * 16) = v0; *(LAS u32x4*)(lds + L_VM + 1024 + lane * 16) = v1;
                }
            };
            SEG(11);
            ld_b(); SCHED(); mm_b();
            if (__builtin_expect(!ok_b(), 0)) { bool out; do { RETRY(1); out = spin_out(spins, ctl); ld_b(); } while (!(ok_b() | out)); mm_b(); }
            SEG(4);
        }
        float m;
        f32x4 sc;
        if (ROLE >= 2) {
            sc = ROLE == 2 ? (n < 8 ? s0 : s1) : s0 + s1;
            if (ROLE == 3 && g < 2) sc = (f32x4){ninf, ninf, ninf, ninf};
            m = max3f(max3f(sc[0], sc[1], sc[2]), sc[3], ninf);
            if (ROLE == 2) m = max3f(m, ror8(m), ninf);
            m = rmax(m);
            PIN_V(m);
        }
        LDS_BAR();
        if (ROLE == 0) {
            const unsigned ro = w < 3 ? xw + (unsigned)rank * 16384u + (unsigned)w * 2048u : psp + redo + (unsigned)(w - 3) * 2048u;
            ST16(sv, xr, l16, ro, 16); ST16(sv, xr, l16 + 1024, ro, 16);
        }
        if (ROLE != 1) { v0 = *(const LAS u32x4*)(lds + L_VM + lane * 16); v1 = *(const LAS u32x4*)(lds + L_VM + 1024 + lane * 16); }
        if (ROLE >= 2) {
            const float mc = -m * csc;
#pragma unroll
            for (int i = 0; i < 4; ++i) sc[i] = __builtin_amdgcn_exp2f(fmaf(sc[i], csc, mc));
            *(LAS u32x2*)(lds + (ROLE == 2 ? L_PX + (g * 8 + (n & 7)) * 16 + (n >> 3) * 8 : L_PM + (g < 2 ? 256 : 0) + n * 16 + (g & 1) * 8)) = (u32x2){pk_bf16(sc[0], sc[1]), pk_bf16(sc[2], sc[3])};
            SCHED();
            dv[0] = smma(v0, WO[0][0], dv[0], sidx); dv[1] = smma(v0, WO[1][0], dv[1], sidx);
            float l = (sc[0] + sc[1]) + (sc[2] + sc[3]);
            if (ROLE == 2) l += ror8(l);
            dv[0] = smma(v1, WO[0][1], dv[0], sidx); dv[1] = smma(v1, WO[1][1], dv[1], sidx);
            l = rows_sum(l);
            *(LAS f32x2*)(lds + L_ML + n * 16 + (ROLE == 2 ? 0 : 8)) = (f32x2){mc, l};
        } else {
#pragma unroll
            for (int ct = 0; ct < 2; ++ct) { dv[ct] = smma(v0, WO[ct][0], dv[ct], sidx); dv[ct] = smma(v1, WO[ct][1], dv[ct], sidx); }
        }
        vwm_pack(dv);
        SEG(5);
        u32x4 am0 = {VWM[0], 0u, 0u, 0u}, am1 = {VWM[1], 0u, 0u, 0u}, pm = {0u, 0u, 0u, 0u};
        PIN_V(am0); PIN_V(am1); PIN_V(pm);
        LDS_BAR();
        SEG(6);
        {
            const f32x4 ml = *(const LAS f32x4*)(lds + L_ML + n * 16);
            const u32x4 px = *(const LAS u32x4*)(lds + L_PX + (g * 8 + (n & 7)) * 16);
            pm.x = *(const LAS unsigned*)(lds + L_PM + n * 16 + g * 4);
            const f32x4 z = {0.f, 0.f, 0.f, 0.f};
            const f32x4 dx0 = mma32(asbf(VWX[0]), asbf(px), z), dx1 = mma32(asbf(VWX[1]), asbf(px), z), dm0 = mma32(asbf(am0), asbf(pm), z), dm1 = mma32(asbf(am1), asbf(pm), z);
            const float dl = ml[0] - ml[2];
            const float e = __builtin_amdgcn_exp2f(-__builtin_fabsf(dl));
            const float ex = dl > 0.f ? e : 1.f, em = dl > 0.f ? 1.f : e;
            const float inv = __builtin_amdgcn_rcpf(fmaf(ex, ml[1], em * ml[3]));
            const f32x4 d0 = (dx0 * ex + dm0 * em) * inv, d1 = (dx1 * ex + dm1 * em) * inv;
            const u32x4 o = {min(pk_bf16(d0[0], d0[1]), SENT - 1), min(pk_bf16(d0[2], d0[3]), SENT - 1), min(pk_bf16(d1[0], d1[1]), SENT - 1), min(pk_bf16(d1[2], d1[3]), SENT - 1)};
            ST16(o, xr, pst_off, pso, 16);
        }
        SCHED();
        SEG(7);
        __builtin_amdgcn_s_setprio(ROLE == 3 ? 1 : 0);
        if (KDM == 3 && s + 2 < NSEG - 1) dma_x(s + 2);
        unsigned long long pv = 0;
        if (ROLE == 1) { pv = __hip_atomic_load((unsigned long long*)(ctl + C_PACE + par * 2048), RLX_AGENT); SCHED(); }
        vwx_kt(s + 1, 0); vwx_kt(s + 1, 1);
        SCHED();
        if (ROLE == 3) __builtin_amdgcn_s_setprio(2);
        if (ROLE == 3) {
            u32x4 SEL;
            { const unsigned one = (n & 1) ? 0x3f800000u : 0x00003f80u; const int dw = (n & 7) >> 1; SEL = (u32x4){dw == 0 ? one : 0u, dw == 1 ? one : 0u, dw == 2 ? one : 0u, dw == 3 ? one : 0u}; }
            unsigned so = pso + redo, mo = win_off(W_MEM(s + 1) + (red >> 6)) + (unsigned)(red & 63) * 128u;
            PIN_S(so); PIN_S(mo); PIN_V(SEL);
            if (KD2) __builtin_amdgcn_s_sleep(KD2);
            f32x4 ta, tb;
            u32x4 q[4];
            auto ld_c = [&]() {
                asm volatile("" ::: "memory");
#pragma unroll
                for (int i = 0; i < 4; ++i) q[i] = ld16_sc1(xr, l16 + i * 1024, so);
            };
            auto ok_c = [&]() { return all_set(max(max(umax4(q[0]), umax4(q[1])), max(umax4(q[2]), umax4(q[3])))); };
            auto mm_c = [&]() {
                ta = mma32(asbf(SEL), asbf(q[0]), (f32x4){0.f, 0.f, 0.f, 0.f}); tb = mma32(asbf(SEL), asbf(q[1]), (f32x4){0.f, 0.f, 0.f, 0.f});
                ta = mma32(asbf(SEL), asbf(q[2]), ta); tb = mma32(asbf(SEL), asbf(q[3]), tb);
            };
            SEG(12);
            ld_c(); mm_c();
            if (__builtin_expect(!ok_c(), 0)) { bool out; do { RETRY(2); out = spin_out(spins, ctl); ld_c(); } while (!(ok_c() | out)); mm_c(); }
            SEG(8);
            f32x4 t = ta + tb;
#pragma unroll
            for (int i = 0; i < 4; ++i) t[i] += ror8(t[i]);
            unsigned a0 = min(pk_bf16(t[0], t[1]), SENT - 1), a1 = min(pk_bf16(t[2], t[3]), SENT - 1);
            unsigned b0 = a0, b1 = a1; swap16(a0, b0); swap16(a1, b1);
            const u32x4 o = {a0, a1, b0, b1};
            ST16(o, xr, mem_off, mo, 16);
        } else {
            if (KDM == 0 && s + 2 < NSEG - 1) dma_x(s + 2);
            if (ROLE == 2) load_kx(s + 1);
            if (ROLE == 1) *(LAS u32x2*)(lds + L_PACE) = (u32x2){(unsigned)pv, (unsigned)(pv >> 32)};
        }
        { const unsigned t = psp; psp = pso; pso = psn; psn = t; }
        SEG(9);
        if (ROLE != 3) __builtin_amdgcn_s_setprio(2);
        LDS_BAR();
        SEG(14);
    }
    WAIT_V(0);
    __builtin_amdgcn_s_setprio(0);
}

DI void chain_role(const Params& p, int rank, ldsp lds, unsigned long long* stamps) {
    const int tid = fresh_tid(), w = __builtin_amdgcn_readfirstlane(tid >> 6), lane = tid & 63, g = lane >> 4, n = lane & 15;
    const int h = rank >> 2, j = rank & 3;
    const rsrc_t xr = mkrsrc(p.ws + WS_XCH, (unsigned)XCH_BYTES);
    unsigned* ctl = (unsigned*)(p.ws + WS_CTL);
    PST(0);
    const unsigned par = (unsigned)__builtin_amdgcn_readfirstlane(ctl[C_PAR + 1]) < (unsigned)__builtin_amdgcn_readfirstlane(ctl[C_PAR]) ? 1u : 0u;
    const int red = ((j + 1) & 3) * 32 + h;
    bf16x16 W[6][2], WO[2][2];
    {
        ldsp my = lds + w * 16384;
        const int rrow = lane >> 2, rc = (lane & 3) * 8, q4 = n >> 2;
        const int toffo = (8 * g + q4) * 64 + (n & 3) * 8;
        const int toffq = (16 * (g & 1) + 8 * (g >> 1) + 4 * (q4 >> 1) + (q4 & 1)) * 64 + (n & 3) * 8;
#define FRAG(base, st) cat16(cat8(trd((base)), trd((base) + (st))), cat8(trd((base) + 2048), trd((base) + 2048 + (st))))
#pragma unroll
        for (int i = 0; i < 8; ++i) {
            __builtin_amdgcn_global_load_lds((const unsigned*)(p.wq + (size_t)(128 * w + 16 * i + rrow) * 4096 + h * 128 + 32 * j + rc), (LAS unsigned*)(my + i * 1024), 16, 0, KNT1);
            __builtin_amdgcn_global_load_lds((const unsigned*)(p.wo + (size_t)(h * 128 + 16 * i + rrow) * 1024 + 256 * j + 32 * w + rc), (LAS unsigned*)(my + 8192 + i * 1024), 16, 0, KNT1);
        }
        {
            const u32x4 sv = {SENT, SENT, SENT, SENT};
            if (w < 6) {
                ST16(sv, xr, (unsigned)lane * 16u, win_off(W_X(0)) + (unsigned)rank * 16384u + (unsigned)w * 1024u, 16);
                ST16(sv, xr, (unsigned)lane * 16u, win_off(W_X(1)) + (unsigned)rank * 16384u + (unsigned)w * 1024u, 16);
            }
            ST16(sv, xr, (unsigned)lane * 16u, win_off(W_PART(w >> 2)) + (unsigned)(red >> 1) * 16384u + (unsigned)(red & 1) * 4096u + (unsigned)(w & 3) * 1024u, 16);
            ST16(sv, xr, (unsigned)lane * 16u, XCH_KM + (unsigned)((56 + w) * 128 + rank) * 1536u, 16);
            ST16(sv, xr, lane < 32 ? (unsigned)lane * 16u + 1024u : OOB, XCH_KM + (unsigned)((56 + w) * 128 + rank) * 1536u, 16);
            const int s = 8 * w + (lane >> 3);
            if (s > 0) ST16(sv, xr, win_off(W_MEM(s) + (red >> 6)) + (unsigned)(red & 63) * 128u + (unsigned)(lane & 7) * 16u, 0, 16);
        }
        PST(3);
        WAIT_V(0);
#pragma unroll
        for (int T = 0; T < 2; ++T)
#pragma unroll
            for (int t = 0; t < 2; ++t) { W[T][t] = FRAG(my + t * 4096 + T * 32 + toffq, 128); WO[T][t] = FRAG(my + 8192 + (t ^ (j >> 1)) * 4096 + T * 32 + toffo, 256); }
#pragma unroll
        for (int T = 0; T < 2; ++T) asm volatile("" : "+v"(W[T][0]), "+v"(W[T][1]), "+v"(WO[T][0]), "+v"(WO[T][1]));
        WAIT_L(0);
        PST(1);
#pragma unroll
        for (int i = 0; i < 8; ++i) {
            __builtin_amdgcn_global_load_lds((const unsigned*)(p.wk + (size_t)(128 * w + 16 * i + rrow) * 4096 + h * 128 + 32 * j + rc), (LAS unsigned*)(my + i * 1024), 16, 0, KNT2);
            __builtin_amdgcn_global_load_lds((const unsigned*)(p.wv + (size_t)(128 * w + 16 * i + rrow) * 4096 + h * 128 + 32 * j + rc), (LAS unsigned*)(my + 8192 + i * 1024), 16, 0, KNT2);
        }
        WAIT_V(0);
#pragma unroll
        for (int T = 0; T < 4; ++T)
#pragma unroll
            for (int t = 0; t < 2; ++t) W[2 + T][t] = FRAG(my + (T >> 1) * 8192 + t * 4096 + (T & 1) * 32 + toffq, 128);
#pragma unroll
        for (int T = 0; T < 4; ++T) asm volatile("" : "+v"(W[2 + T][0]), "+v"(W[2 + T][1]));
        WAIT_L(0);
#undef FRAG
    }
    PST(2);
    BAR();
    PST(4);
    if (w == 7 && lane < 8) {
        const unsigned* src = (const unsigned*)(p.mem0 + (4 * (red & 1) + 2 * (lane >> 2) + (lane & 1)) * DM + 32 * (red >> 2) + 8 * ((red >> 1) & 1) + 4 * ((lane >> 1) & 1));
        const u32x4 v = {min(src[0], SENT - 1), min(src[1], SENT - 1), min(src[8], SENT - 1), min(src[9], SENT - 1)};
        ST16(v, xr, (unsigned)lane * 16u + (unsigned)(red & 63) * 128u, win_off(W_MEM(0) + (red >> 6)), 16);
    }
    if (w < 5) chain_loop<0>(p, par, rank, w, lds, W, WO, stamps);
    else if (w == 5) chain_loop<1>(p, par, rank, 5, lds, W, WO, stamps);
    else if (w == 6) chain_loop<2>(p, par, rank, 6, lds, W, WO, stamps);
    else chain_loop<3>(p, par, rank, 7, lds, W, WO, stamps);
}

template <bool POLL>
DI void attn_a_item(const Params& p, int s, int h, ldsp my, int lane) {
    const int g = lane >> 4, n = lane & 15;
    const rsrc_t xr = mkrsrc(p.ws + WS_XCH, (unsigned)XCH_BYTES);
    const rsrc_t qr = mkrsrc(p.ws + WS_QKVX + (size_t)h * XH + (size_t)s * 8192, 65u * XH);
#pragma unroll
    for (int i = 0; i < 8; ++i)
        __builtin_amdgcn_raw_ptr_buffer_load_lds(qr, (LAS void*)(my + i * 1024), 16, (unsigned)(g * 256 + (n ^ ((g & 1) * 8)) * 16), 64u * XH + (unsigned)i * 1024u, 0, 16);
    u32x4 Q[2][4], K[2][4], XM[4];
    const unsigned ro = (unsigned)lane * 16u;
#pragma unroll
    for (int t = 0; t < 2; ++t)
#pragma unroll
        for (int k = 0; k < 4; ++k) { Q[t][k] = ld16_sc1(qr, ro + k * 1024, (unsigned)t * 4096u); K[t][k] = ld16_sc1(qr, ro + k * 1024, (unsigned)t * 4096u + 32u * XH); }
    const unsigned qwin = XCH_KM + (unsigned)(s * 32 + h) * 6144u;
    u32x4 VM[2];
    if (POLL) {
        wait_count<4>((unsigned*)(p.ws + WS_CTL), C_PROG, (unsigned)s + (s < 56 ? 2u : 1u));
        if (s >= 56) __builtin_amdgcn_s_sleep(16);
    }
    for (unsigned spins = 0;;) {
        asm volatile("" ::: "memory");
#pragma unroll
        for (int k = 0; k < 4; ++k) XM[k] = ld16_sc1(xr, (unsigned)lane * 16u + (k & 1) * 1536, qwin + (k >> 1) * 3072);
#pragma unroll
        for (int ts = 0; ts < 2; ++ts) VM[ts] = ld16_sc1(xr, (unsigned)(g >> 1) * 1536u + 1024u + (unsigned)(lane & 31) * 16u, qwin + ts * 3072);
        if (!POLL || s < 56) break;
        if (all_set(max(max(max(umax4(XM[0]), umax4(XM[1])), max(umax4(XM[2]), umax4(XM[3]))), max(umax4(VM[0]), umax4(VM[1]))))) break;
        __builtin_amdgcn_s_sleep(4);
        if (spin_out(spins, (unsigned*)(p.ws + WS_CTL))) break;
    }
    SCHED();
#pragma unroll
    for (int ts = 0; ts < 2; ++ts) {
        const u32x4 v = VM[ts];
        const int m = 2 * (n >> 2) + (n & 1), hf = (n >> 1) & 1;
#pragma unroll
        for (int i = 0; i < 4; ++i) { const int e = 64 * ts + 16 * g + 4 * i + 2 * hf;
            *(LAS unsigned*)(my + 8192 + (e >> 3) * 256 + (m ^ (e & 8)) * 16 + (e & 7) * 2) = v[i]; }
    }
    const float csc = 0.08838834764831845f * 1.4426950408889634f, ninf = -__builtin_inff();
    u32x4 PX[2]; u32x2 PM[2];
#pragma unroll
    for (int qt = 0; qt < 2; ++qt) {
        f32x4 sm = {0.f, 0.f, 0.f, 0.f}, s0 = sm, s1 = sm;
#pragma unroll
        for (int k = 0; k < 4; ++k) { sm = mma32(asbf(XM[k]), asbf(Q[qt][k]), sm); s0 = mma32(asbf(K[0][k]), asbf(Q[qt][k]), s0); if (qt == 1) s1 = mma32(asbf(K[1][k]), asbf(Q[qt][k]), s1); }
#pragma unroll
        for (int i = 0; i < 4; ++i) {
            if (g < 2) sm[i] = ninf;
            if (qt == 0) { if (4 * g + i > n) s0[i] = ninf; s1[i] = ninf; }
            else if (4 * g + i > n) s1[i] = ninf;
        }
        float m = fmaxf(fmaxf(fmaxf(s0[0], s0[1]), fmaxf(s0[2], s0[3])), fmaxf(fmaxf(s1[0], s1[1]), fmaxf(s1[2], s1[3])));
        m = fmaxf(m, fmaxf(fmaxf(sm[0], sm[1]), fmaxf(sm[2], sm[3])));
        m = rows_max(m);
        const float mc = -m * csc; float sum = 0.f;
#pragma unroll
        for (int i = 0; i < 4; ++i) { s0[i] = __builtin_amdgcn_exp2f(fmaf(s0[i], csc, mc)); s1[i] = __builtin_amdgcn_exp2f(fmaf(s1[i], csc, mc)); sm[i] = __builtin_amdgcn_exp2f(fmaf(sm[i], csc, mc));
            sum += (s0[i] + s1[i]) + sm[i]; }
        sum = rows_sum(sum);
        const float inv = __builtin_amdgcn_rcpf(sum);
        PX[qt] = (u32x4){pk_bf16(s0[0] * inv, s0[1] * inv), pk_bf16(s0[2] * inv, s0[3] * inv), pk_bf16(s1[0] * inv, s1[1] * inv), pk_bf16(s1[2] * inv, s1[3] * inv)};
        PM[qt] = (u32x2){pk_bf16(sm[0] * inv, sm[1] * inv), pk_bf16(sm[2] * inv, sm[3] * inv)};
    }
    asm volatile("s_waitcnt vmcnt(0) lgkmcnt(0)" ::: "memory");
    const int q4 = n >> 2, p4 = n & 3;
    const ldsp vx = my + (p4 >> 1) * 256 + ((4 * g + q4) ^ ((p4 >> 1) * 8)) * 16 + (p4 & 1) * 8, vmm = my + 8192 + (p4 >> 1) * 256 + ((4 * (g & 1) + q4) ^ ((p4 >> 1) * 8)) * 16 + (p4 & 1) * 8;
    const rsrc_t orr = mkrsrc(p.ws + WS_OA + (size_t)s * 262144 + (size_t)h * 4096, 262144u);
    const unsigned oo = frag_off(lane);
    bf16x8 VA[8]; s16x4 VMf[8];
#pragma unroll
    for (int et = 0; et < 8; ++et) { VA[et] = cat8(trd(vx + et * 512), trd(vx + 4096 + et * 512)); VMf[et] = trd(vmm + et * 512); }
    SCHED();
#pragma unroll
    for (int ep = 0; ep < 4; ++ep) {
        f32x4 d[2][2];
#pragma unroll
        for (int e2 = 0; e2 < 2; ++e2)
#pragma unroll
            for (int qt = 0; qt < 2; ++qt) d[qt][e2] = mma16(__builtin_bit_cast(u32x2, VMf[2 * ep + e2]), PM[qt], mma32(VA[2 * ep + e2], asbf(PX[qt]), (f32x4){0.f, 0.f, 0.f, 0.f}));
#pragma unroll
        for (int qt = 0; qt < 2; ++qt) store_frag(orr, oo + ep * 1024, (unsigned)qt * 131072u, d[qt][0], d[qt][1]);
    }
    asm volatile("s_waitcnt lgkmcnt(0)" ::: "memory");
}

DI void attn_b_item(const Params& p, int hq, int qb, ldsp lds) {
    const int tid = fresh_tid(), w = __builtin_amdgcn_readfirstlane(tid >> 6), lane = tid & 63, g = lane >> 4, n = lane & 15;
    const int q0 = qb * 128;
    const rsrc_t br = mkrsrc(p.ws + WS_QKVB, 96u * BH);
    const rsrc_t orr = mkrsrc(p.ws + WS_OB + (size_t)qb * 16 * 16384, 16u * 16384u);
    constexpr int BUF = 57344;
    unsigned to[3];
#pragma unroll
    for (int i = 0; i < 3; ++i) to[i] = (unsigned)max(8 * qb - 16 + w * 3 + i, 0) * 1024u;
    const unsigned l16 = (unsigned)lane * 16u, vsw = (unsigned)(g * 256 + (n ^ ((g & 1) * 8)) * 16);
    auto stage = [&](int i) {
        const unsigned so = (unsigned)(hq * 4 + i) * BH; ldsp d = lds + (i & 1) * BUF;
#pragma unroll
        for (int k = 0; k < 3; ++k) {
            __builtin_amdgcn_raw_ptr_buffer_load_lds(br, (LAS void*)(d + (w * 3 + k) * 1024), 16, l16, so + 32u * BH + to[k], 0, 16);
            __builtin_amdgcn_raw_ptr_buffer_load_lds(br, (LAS void*)(d + 24576 + (w * 3 + k) * 1024), 16, vsw, so + 64u * BH + to[k], 0, 16);
        }
        __builtin_amdgcn_raw_ptr_buffer_load_lds(br, (LAS void*)(d + 49152 + w * 1024), 16, l16, so + (unsigned)(8 * qb + w) * 1024u, 0, 16);
    };
    const float ninf = -__builtin_inff(), csc = 0.17677669529663687f * 1.4426950408889634f;
    const int tmin = (256 - q0 - 16 * w) >> 4;
    const int q4 = n >> 2, p4 = n & 3;
    const int kq = w * 1024 + lane * 16;
    const unsigned vb = (unsigned)(size_t)lds + 24576u + (unsigned)(w * 1024 + (p4 >> 1) * 256 + ((4 * g + q4) ^ ((p4 >> 1) * 8)) * 16 + (p4 & 1) * 8);
    stage(0);
#pragma unroll 1
    for (int i = 0; i < 4; ++i) {
        if (i < 3) { stage(i + 1); WAIT_V(7); } else WAIT_V(0);
        BAR(); asm volatile("" ::: "memory");
        const ldsp bp = lds + (i & 1) * BUF;
        const bf16x8 Q = *(const LAS bf16x8*)(bp + 49152 + kq);
        f32x4 sc[17]; bf16x8 KF[17];
#pragma unroll
        for (int t = 0; t < 17; ++t) KF[t] = *(const LAS bf16x8*)(bp + kq + t * 1024);
        SCHED();
#pragma unroll
        for (int t = 0; t < 17; ++t) sc[t] = mma32(KF[t], Q, (f32x4){0.f, 0.f, 0.f, 0.f});
#pragma unroll
        for (int k = 0; k < 4; ++k) { if (4 * g + k <= n) sc[0][k] = ninf; if (4 * g + k > n) sc[16][k] = ninf; }
        if (tmin > 0) {
#pragma unroll
            for (int t = 0; t < 16; ++t) if (t < tmin) sc[t] = (f32x4){ninf, ninf, ninf, ninf};
        }
        float m = max3f(sc[16][0], sc[16][1], max3f(sc[16][2], sc[16][3], ninf));
#pragma unroll
        for (int t = 0; t < 16; ++t) m = max3f(max3f(m, sc[t][0], sc[t][1]), sc[t][2], sc[t][3]);
        m = rows_max(m);
        const float mc = -m * csc;
        f32x4 sum4 = {0.f, 0.f, 0.f, 0.f};
#pragma unroll
        for (int t = 0; t < 17; ++t) {
#pragma unroll
            for (int k = 0; k < 4; ++k) sc[t][k] = __builtin_amdgcn_exp2f(fmaf(sc[t][k], csc, mc));
            sum4 += sc[t];
        }
        const float inv = __builtin_amdgcn_rcpf(rows_sum((sum4[0] + sum4[1]) + (sum4[2] + sum4[3])));
        const unsigned v0 = vb + (unsigned)((i & 1) * BUF);
        s16x4 V0[17], V1[17];
#define VRD(t) TRD(V0[t], v0, (t) * 1024); TRD(V1[t], v0, (t) * 1024 + 512)
        VRD(0); VRD(1); VRD(2); VRD(3); VRD(4); VRD(5); VRD(6); VRD(7); VRD(8); VRD(9); VRD(10); VRD(11); VRD(12); VRD(13); VRD(14); VRD(15); VRD(16);
#undef VRD
        u32x2 P[17];
#pragma unroll
        for (int t = 0; t < 17; ++t) P[t] = (u32x2){pk_bf16(sc[t][0], sc[t][1]), pk_bf16(sc[t][2], sc[t][3])};
        WAIT_L(0); SCHED();
        f32x4 d0 = {0.f, 0.f, 0.f, 0.f}, d1 = d0;
#pragma unroll
        for (int tp = 0; tp < 8; ++tp) {
            const bf16x8 PP = asbf((u32x4){P[2 * tp].x, P[2 * tp].y, P[2 * tp + 1].x, P[2 * tp + 1].y});
            d0 = mma32(cat8(V0[2 * tp], V0[2 * tp + 1]), PP, d0); d1 = mma32(cat8(V1[2 * tp], V1[2 * tp + 1]), PP, d1);
        }
        d0 = mma16(__builtin_bit_cast(u32x2, V0[16]), P[16], d0); d1 = mma16(__builtin_bit_cast(u32x2, V1[16]), P[16], d1);
        BAR();
        store_frag(orr, frag_off(lane), (unsigned)(w * 32768 + (hq * 4 + i) * 1024), d0 * inv, d1 * inv);
    }
    WAIT_V(0);
    signal((unsigned*)(p.ws + WS_CTL), C_OB + qb * 32, 1);
}

DI void out_tail(const Params& p, int c, int q, ldsp lds) {
    const int tid = fresh_tid(), w = __builtin_amdgcn_readfirstlane(tid >> 6), lane = tid & 63, g = lane >> 4, n = lane & 15;
    unsigned* ctl = (unsigned*)(p.ws + WS_CTL);
    bf16x8 WT[16][2];
    {
        ldsp my = lds + w * 16384;
        const int toff = (8 * g + (n >> 2)) * 64 + (n & 3) * 8;
#pragma unroll
        for (int r = 0; r < 2; ++r) {
#pragma unroll
            for (int i = 0; i < 16; ++i)
                __builtin_amdgcn_global_load_lds((const unsigned*)(p.wo + (size_t)(512 * w + 256 * r + 16 * i + (lane >> 2)) * 1024 + 32 * c + (lane & 3) * 8), (LAS unsigned*)(my + i * 1024), 16, 0, 0);
            WAIT_V(0);
#pragma unroll
            for (int k = 0; k < 8; ++k)
#pragma unroll
                for (int ct = 0; ct < 2; ++ct) { WT[8 * r + k][ct] = cat8(trd(my + k * 2048 + ct * 32 + toff), trd(my + k * 2048 + ct * 32 + toff + 256)); }
#pragma unroll
            for (int k = 0; k < 8; ++k) asm volatile("" : "+v"(WT[8 * r + k][0]), "+v"(WT[8 * r + k][1]));
            WAIT_L(0);
        }
    }
    BAR();
#pragma unroll 1
    for (int hs = q; hs < 32; hs += 3) {
        const int s = 48 + (hs >> 1), rt = hs & 1;
        if (w == 0) wait_count<3>(ctl, C_OS + (s - 48) * 256 + (c >> 3) * 64, 4u);
        asm volatile("" ::: "memory"); BAR(); asm volatile("" ::: "memory");
        const rsrc_t ar = mkrsrc(p.ws + WS_OA + (size_t)(2 * s + rt) * 131072 + (size_t)w * 16384, 16384u);
        const unsigned ao = (unsigned)lane * 16u;
        u32x4 A[16];
#pragma unroll
        for (int k = 0; k < 16; ++k) A[k] = ld16_sc1(ar, ao + (k & 3) * 1024, (unsigned)(k >> 2) * 4096u);
        SCHED();
#pragma unroll
        for (int ct = 0; ct < 2; ++ct) {
            f32x4 acc = {0.f, 0.f, 0.f, 0.f};
#pragma unroll
            for (int k = 0; k < 16; ++k) acc = mma32(WT[k][ct], asbf(A[k]), acc);
            *(LAS f32x4*)(lds + w * 2048 + ct * 1024 + lane * 16) = acc;
        }
        LDS_BAR();
        if (w < 2) {
            f32x4 r[8];
#pragma unroll
            for (int ww = 0; ww < 8; ++ww) r[ww] = *(const LAS f32x4*)(lds + ww * 2048 + w * 1024 + lane * 16);
            SCHED();
            const f32x4 t = ((r[0] + r[1]) + (r[2] + r[3])) + ((r[4] + r[5]) + (r[6] + r[7]));
            *(u32x2*)(p.out + (size_t)(32 * s + 16 * rt + n) * 2048 + 32 * c + 16 * w + 4 * g) = (u32x2){pk_bf16(t[0], t[1]), pk_bf16(t[2], t[3])};
        }
        WAIT_L(0);
    }
    BAR();
}

DI void big_unit(const Params& p, int id, Unit& u) {
    u.lda = DM * 2; u.nt = 16; u.ak = 128; u.ah = 128 * DM * 2;
    if (id < 384) {
        const int pm = id & 7, pn = id >> 3;
        const u16 *w0 = p.wq, *w1 = p.wk, *w2 = p.wv; const u16* W = pn < 16 ? w0 : (pn < 32 ? w1 : w2);
        u.A = (const char*)(p.ax + (size_t)pm * 256 * DM); u.B = (const char*)(W + (pn & 15) * 256); u.ldb = 4096 * 2;
        u.C = p.ws + WS_QKVX + (size_t)(2 * pn) * XH + (size_t)pm * 65536; u.ldc = 256; u.cw = 1024; u.ch = XH;
        if (pn < 16) { u.sig = C_Q + pm * 32; u.nsig = 1; } else { u.sig = C_KV + 2 * pm * 512 + (pn & 15) * 32; u.nsig = 4; }
    } else if (id < 480) {
        const int pm = (id - 384) & 7, pn = (id - 384) >> 3;
        u.A = (const char*)(p.bx + (size_t)pm * 256 * DM); u.B = (const char*)(p.bwqkv + pn * 256); u.ldb = LDB3 * 2;
        u.C = p.ws + WS_QKVB + (size_t)(8 * pn) * BH + (size_t)pm * 16384; u.ldc = 64; u.cw = BH; u.ch = 4 * BH; u.sig = C_B + pm * 32; u.nsig = 1;
    } else {
        const bool isb = id < 512; const int q = isb ? id - 480 : id - 512, pm = q >> 2, pn = q & 3;
        const u16 *w0 = p.wo, *w1 = p.bwo;
        u.nt = isb ? 16 : 64; u.lda = 0; u.ak = 2048; u.ah = (unsigned)u.nt * 16384u;
        u.A = p.ws + (isb ? WS_OB : WS_OA) + (size_t)pm * 2 * u.ah; u.B = (const char*)((isb ? w1 : w0) + pn * 256); u.ldb = DM * 2;
        u.C = (char*)(p.out + (size_t)pm * 256 * 2048 + (isb ? 1024 : 0) + pn * 256); u.ldc = 4096; u.cw = 64; u.ch = 256; u.sig = 0; u.nsig = 0;
    }
}
struct BigSched { const Params& p; int id0, id1; DI bool next(int i, Unit& u) const { const int id = i == 0 ? id0 : (i == 1 ? id1 : -1); if (id < 0) return false; big_unit(p, id, u); return true; } };

__global__ __launch_bounds__(512, 2) void k_fused(Params p) {
    extern __shared__ __attribute__((aligned(16))) unsigned char shm[];
    ldsp lds = (ldsp)shm;
    unsigned* ctl = (unsigned*)(p.ws + WS_CTL);
    const int b = (blockIdx.x & ~7) | ((blockIdx.x + __builtin_amdgcn_readfirstlane(ctl[C_PHASE])) & 7), id = (b & 3) * 32 + (b >> 3);
#define WSTAMP(code)
    WSTAMP(1);
    if ((b & 7) < 4) {
        chain_role(p, id, lds, (unsigned long long*)(p.ws + WS_END));
        BAR();
        WSTAMP(2);
    } else {
#pragma unroll 1
        for (int r = 0; r < 2; ++r) {
            const int rb = 2 * r + (id & 1), ct = id >> 1;
            const u16 *w1 = p.wk, *w2 = p.wv;
            Unit u; u.A = (const char*)(p.ax + (size_t)rb * 128 * DM); u.lda = DM * 2; u.B = (const char*)((ct < 32 ? w1 : w2) + (ct & 31) * 128); u.ldb = 4096 * 2; u.nt = 16;
            u.C = p.ws + WS_QKVX + (size_t)(32 + ct) * XH + (size_t)rb * 32768; u.ldc = 256; u.cw = 1024; u.ch = 0; u.ak = 128; u.ah = 0;
            u.sig = C_KV + rb * 512 + (ct & 31) * 16; u.nsig = 1;
            gemm_small(lds, u, ctl);
            WSTAMP(3);
        }
        {
            const int t = fresh_tid(), wv = __builtin_amdgcn_readfirstlane(t >> 6);
            const char* src; const int c = id & 7, q = id >> 3;
            if (c < 2) src = (const char*)p.ax + (size_t)(q * 2 + c) * 131072; else if (c < 4) src = (const char*)p.bx + (size_t)(q * 2 + c - 2) * 131072;
            else if (c < 7) src = (const char*)p.bwqkv + (size_t)(q * 3 + c - 4) * 131072; else src = (const char*)p.bwo + (size_t)q * 131072;
#pragma unroll
            for (int i = 0; i < 16; ++i) { __builtin_amdgcn_global_load_lds((const unsigned*)(src + i * 8192 + t * 16), (LAS unsigned*)(lds + L_SCR + wv * 1024), 16, 0, 0); if ((i & (KWP - 1)) == KWP - 1) WAIT_V(0); }
            if (KNT1) {
#pragma unroll
                for (int i = 0; i < 8; ++i) { __builtin_amdgcn_global_load_lds((const unsigned*)((const char*)p.wo + (size_t)id * 65536 + i * 8192 + t * 16), (LAS unsigned*)(lds + L_SCR + wv * 1024), 16, 0, 0); if ((i & (KWP - 1)) == KWP - 1) WAIT_V(0); }
            }
        }
    }
    volatile LAS unsigned* qw = (volatile LAS unsigned*)(lds + 131072);
    const int w = __builtin_amdgcn_readfirstlane(threadIdx.x >> 6);
    const int k = b & 3;
    bool fixed = (b & 7) >= 4;
#pragma unroll 1
    for (;;) {
        int kind, x, it = -1, x2 = -1;
        if (fixed) { const int i = id & 31; kind = 6; x = i < 24 ? (16 + 8 * k + (i & 7)) * 8 + 2 + (i >> 3) : (4 * k + (i & 3)) * 8 + ((i >> 2) & 1); x2 = x + (i < 24 ? 3 : 2); fixed = false; }
        else {
        if (threadIdx.x == 0) qw[0] = __hip_atomic_fetch_add(ctl + C_HEAD + k * 32, 1u, RLX_AGENT) + 1u;
        LDS_BAR();
        it = (int)__builtin_amdgcn_readfirstlane(qw[0]);
        LDS_BAR();
        if (it < 4) { kind = 0; x = it * 4 + k; }
        else if (it < 28) { kind = 6; x = 384 + 24 * k + it - 4; }
        else if (it < 36) { kind = 6; x = (4 * k + ((it - 28) & 3)) * 8 + 4 + ((it - 28) >> 2); }
        else if (it < 38) { kind = 6; x = 512 + (it - 36) * 4 + k; }
        else if (it < 42) { kind = 0; x = (it - 38 + 4) * 4 + k; }
        else if (it < 74) { kind = 1; x = ((it - 42) >> 1) * 8 + 2 * k + ((it - 42) & 1); }
        else if (it < 82) { kind = 6; x = (4 * k + ((it - 74) & 3)) * 8 + 6 + ((it - 74) >> 2); }
        else if (it < 86) { kind = 4; x = (it - 82) * 4 + k; }
        else if (it < 94) { kind = 6; x = 480 + (it - 86) * 4 + k; }
        else if (it < 110) { kind = 2; x = (4 + ((it - 94) >> 1)) * 8 + 2 * k + ((it - 94) & 1); }
        else if (it < 134) { kind = 5; x = ((it - 110) >> 3) * 32 + 8 * k + ((it - 110) & 7); }
        else break;
        }
        if (kind == 6) {
            if (x >= 480) { const int w0 = x < 512 ? C_OB + ((x - 480) >> 2) * 64 : C_OA + ((x - 512) >> 2) * 64; const unsigned need = x < 512 ? 64u : 32u; wg_wait(ctl, w0, need, w0 + 32, need); }
            BigSched S{p, x, x2};
            gemm_stream(lds, S, ctl);
        } else
        if (kind == 0) {
            const int s0 = (x >> 2) * 4;
            wait_count(ctl, C_Q + (s0 >> 3) * 32, 128u); wait_count(ctl, C_PROG, (unsigned)s0 + 5u);
            asm volatile("" ::: "memory");
#pragma unroll 1
            for (int s = s0; s < s0 + 4; ++s) attn_a_item<false>(p, s, (x & 3) * 8 + w, lds + w * 12288, fresh_tid() & 63);
            WAIT_V(0); signal(ctl, C_OA + (x >> 2) * 32, 1);
            BAR();
        } else if (kind == 4) {
#pragma unroll 1
            for (int s = 32 + (x >> 2); s < NSEG; s += 4) {
                wait_count(ctl, C_Q + (s >> 3) * 32, 128u); wait_count(ctl, C_KV + (s >> 2) * 512 + ((x & 3) * 8 + w) * 16, 16u);
                asm volatile("" ::: "memory");
                attn_a_item<true>(p, s, (x & 3) * 8 + w, lds + w * 12288, fresh_tid() & 63);
                WAIT_V(0);
                if (s < 48) signal(ctl, C_OA + (s >> 2) * 32, 1);
                else { BAR(); const int l = fresh_tid(); if (l < 4) __hip_atomic_fetch_add(ctl + C_OS + (s - 48) * 256 + l * 64, 1u, RLX_AGENT); }
            }
            BAR();
        } else if (kind == 1) {
            const int qb = x >> 3;
            wait_count(ctl, C_B + (qb >> 1) * 32, 96u); wait_count(ctl, C_B + max((qb >> 1) - 1, 0) * 32, 96u);
            asm volatile("" ::: "memory");
            attn_b_item(p, x & 7, qb, lds);
        } else if (kind == 5) {
            out_tail(p, x & 31, x >> 5, lds);
        } else {
            const int rb = x >> 3, ct = x & 7;
            Unit u; u.ldb = DM * 2; u.ldc = 2048 * 2; u.sig = 0; u.nsig = 0; u.lda = 0; u.ak = 2048; u.ah = 0; u.cw = 64; u.ch = 0;
            u.A = p.ws + WS_OA + (size_t)rb * 64 * 16384; u.B = (const char*)(p.wo + ct * 128); u.nt = 64; u.C = (char*)(p.out + (size_t)rb * 128 * 2048 + ct * 128);
            wg_wait(ctl, C_OA + rb * 32, rb < 8 ? 32u : 128u, C_OA + rb * 32, 32u);
            gemm_small(lds, u, ctl);
        }
        WSTAMP(16 + (kind == 6 && x >= 480 ? (x < 512 ? 7 : 8) : kind) + ((it & 0xffff) << 8));
    }
}

__global__ __launch_bounds__(512) void k_init(char* ws) {
    const rsrc_t xr = mkrsrc(ws + WS_XCH, NWIN_INIT * 16384u);
    unsigned* ctl = (unsigned*)(ws + WS_CTL);
    const u32x4 sv = {SENT, SENT, SENT, SENT};
    const unsigned xcc = (unsigned)__builtin_amdgcn_s_getreg(20 | (3 << 11)) & 7u, half = blockIdx.x >> 3, win = blockIdx.x & 7;
    if (threadIdx.x < 64 && xcc == 1) {
        unsigned b = ~0u;
        for (int i = 0; i < 5; ++i) {
            const unsigned long long t0 = __builtin_amdgcn_s_memtime();
            __builtin_amdgcn_raw_buffer_store_b32(0u, xr, (unsigned)(2 * 16384 + C_PROBE * 4) + half * 8192u + (unsigned)i * 256u, 0, 16);
            asm volatile("s_waitcnt vmcnt(0)" ::: "memory");
            b = min(b, (unsigned)(__builtin_amdgcn_s_memtime() - t0));
        }
        if (threadIdx.x == 0) ctl[C_PAR + half] = b;
    }
    __syncthreads();
    if (win < 7 && win != 5) ST16(sv, xr, win * 16384u + half * 8192u + threadIdx.x * 16u, 0, 16);
    if (blockIdx.x == 0 && threadIdx.x == 0) ctl[C_PHASE] = xcc;
}

extern "C" void kernel_launch(void* const* d_in, const int* in_sizes, int n_in, void* d_out, int out_size, void* d_ws, size_t ws_size, hipStream_t stream) {
    Params p;
    p.ax = (const u16*)d_in[0]; p.mem0 = (const u16*)d_in[1]; p.wq = (const u16*)d_in[2]; p.wk = (const u16*)d_in[3]; p.wv = (const u16*)d_in[4]; p.wo = (const u16*)d_in[5];
    p.bx = (const u16*)d_in[7]; p.bwqkv = (const u16*)d_in[8]; p.bwo = (const u16*)d_in[9];
    p.out = (u16*)d_out; p.ws = (char*)(((uintptr_t)d_ws + WS_SHIFT(ws_size) + 0x1FFFFFu) & ~(uintptr_t)0x1FFFFFu);
    (void)hipFuncSetAttribute((const void*)k_fused, hipFuncAttributeMaxDynamicSharedMemorySize, LDS_BYTES);
    k_init<<<16, 512, 0, stream>>>(p.ws);
    k_fused<<<256, 512, LDS_BYTES, stream>>>(p);
}
```

```cpp
#include <hip/hip_runtime.h>
#include <stdint.h>
#include <stdlib.h>

#define LAS __attribute__((address_space(3)))
#define DI __device__ __forceinline__
typedef unsigned short u16;
typedef __attribute__((ext_vector_type(8))) __bf16 bf16x8;
typedef __attribute__((ext_vector_type(16))) __bf16 bf16x16;
typedef __attribute__((ext_vector_type(2))) __bf16 bf16x2;
typedef __attribute__((ext_vector_type(4))) short s16x4;
typedef __attribute__((ext_vector_type(8))) short s16x8;
typedef __attribute__((ext_vector_type(16))) short s16x16;
typedef __attribute__((ext_vector_type(2))) float f32x2;
typedef __attribute__((ext_vector_type(4))) float f32x4;
typedef __attribute__((ext_vector_type(2))) unsigned u32x2;
typedef __attribute__((ext_vector_type(4))) unsigned u32x4;
typedef LAS unsigned char* ldsp;
typedef __amdgpu_buffer_rsrc_t rsrc_t;

constexpr int S_LEN = 2048, DM = 1024, NH = 32, DH = 128, NSEG = 64;
constexpr int LDB3 = 3072;
constexpr unsigned XH = S_LEN * 512, BH = S_LEN * 128;
constexpr unsigned SENT = 0xFFFFFFFFu;
constexpr size_t WS_XCH = 2u << 20;
constexpr int NWIN = 640, NWIN_INIT = 7;
constexpr size_t XCH_BYTES = (size_t)(NWIN + 2048) * 16384;
constexpr size_t WS_CTL = WS_XCH + 2 * 16384;
constexpr int C_TMO = 0, C_PAR = 13312  , C_PHASE = 13314, C_HEAD = 3072, C_PROG = 192, C_PACE = 128, C_Q = 256, C_B = C_Q + 8 * 32, C_OA = C_B + 8 * 32, C_OB = C_OA + 16 * 32, C_OS = 16384, C_KV = 4096, C_PROBE = 12288;
constexpr size_t WS_QKVX = 44u << 20;
constexpr size_t WS_QKVB = WS_QKVX + (size_t)96 * XH;
constexpr size_t WS_OA = WS_QKVB + (size_t)96 * BH;
constexpr size_t WS_OB = WS_OA + (size_t)S_LEN * 4096 * 4;
constexpr size_t WS_END = WS_OB + (size_t)S_LEN * 1024 * 4;
#define gap(L) (((L) & 8191u) | (((L) >> 13) << 14))
#define WH(off) ((off) + (size_t)((((unsigned)((off) >> 21) ^ par ^ KWH) & 1u) << 13))
#define KWH 0
#define WS_SHIFT(sz) 0
constexpr int LDS_BYTES = 131072 + 1024 + 8192;

DI unsigned pk_bf16(float a, float b) { f32x2 v = {a, b}; return __builtin_bit_cast(unsigned, __builtin_convertvector(v, bf16x2)); }
DI s16x4 trd(ldsp p) { return __builtin_amdgcn_ds_read_tr16_b64_v4i16((LAS s16x4*)p); }
DI bf16x8 cat8(s16x4 a, s16x4 b) { return __builtin_bit_cast(bf16x8, __builtin_shufflevector(a, b, 0, 1, 2, 3, 4, 5, 6, 7)); }
DI bf16x16 cat16(bf16x8 a, bf16x8 b) { return __builtin_shufflevector(a, b, 0, 1, 2, 3, 4, 5, 6, 7, 8, 9, 10, 11, 12, 13, 14, 15); }
DI bf16x8 lo8(bf16x16 v) { return __builtin_shufflevector(v, v, 0, 1, 2, 3, 4, 5, 6, 7); }
DI bf16x8 hi8(bf16x16 v) { return __builtin_shufflevector(v, v, 8, 9, 10, 11, 12, 13, 14, 15); }
DI bf16x8 asbf(u32x4 v) { return __builtin_bit_cast(bf16x8, v); }
DI f32x4 mma32(bf16x8 a, bf16x8 b, f32x4 c) { return __builtin_amdgcn_mfma_f32_16x16x32_bf16(a, b, c, 0, 0, 0); }
DI f32x4 mma16(u32x2 a, u32x2 b, f32x4 c) { return mma32(asbf((u32x4){a.x, a.y, 0u, 0u}), asbf((u32x4){b.x, b.y, 0u, 0u}), c); }
DI f32x4 smma(u32x4 a, bf16x16 b, f32x4 c, int idx) { return __builtin_amdgcn_smfmac_f32_16x16x64_bf16(asbf(a), b, c, idx, 0, 0); }
DI rsrc_t mkrsrc(const void* p, unsigned bytes) { return __builtin_amdgcn_make_buffer_rsrc((void*)p, 0, bytes, 0x00020000); }
DI u32x4 ld16_sc1(rsrc_t r, unsigned voff, unsigned soff) { return __builtin_amdgcn_raw_buffer_load_b128(r, voff, soff, 16); }
#define ST16(v, r, voff, soff, aux) do { const u32x4 st_v = (v); __builtin_amdgcn_raw_buffer_store_b128(st_v, r, voff, soff, aux); asm volatile("s_nop 1" :: "v"(st_v)); } while (0)
DI void swap16(unsigned& a, unsigned& b) { auto r = __builtin_amdgcn_permlane16_swap(a, b, false, false); a = r[0]; b = r[1]; }
DI void swap32(unsigned& a, unsigned& b) { auto r = __builtin_amdgcn_permlane32_swap(a, b, false, false); a = r[0]; b = r[1]; }
DI void swap16f(float& a, float& b) { unsigned x = __float_as_uint(a), y = __float_as_uint(b); swap16(x, y); a = __uint_as_float(x); b = __uint_as_float(y); }
DI void swap32f(float& a, float& b) { unsigned x = __float_as_uint(a), y = __float_as_uint(b); swap32(x, y); a = __uint_as_float(x); b = __uint_as_float(y); }
DI float rows_max(float v) { float a = v, b = v; swap16f(a, b); v = fmaxf(a, b); a = v; b = v; swap32f(a, b); return fmaxf(a, b); }
DI float rows_sum(float v) { float a = v, b = v; swap16f(a, b); v = a + b; a = v; b = v; swap32f(a, b); return a + b; }
DI unsigned umax4(u32x4 v) { return max(max(v.x, v.y), max(v.z, v.w)); }
#define WAIT_V(n) asm volatile("s_waitcnt vmcnt(" #n ")" ::: "memory")
#define WAIT_L(n) asm volatile("s_waitcnt lgkmcnt(" #n ")" ::: "memory")
#define BAR() __builtin_amdgcn_s_barrier()
#define SCHED() __builtin_amdgcn_sched_barrier(0)
#define LDS_BAR() do { asm volatile("s_waitcnt lgkmcnt(0)" ::: "memory"); __builtin_amdgcn_s_barrier(); asm volatile("" ::: "memory"); } while (0)

constexpr int HTB = 128 * 64 * 2;
DI int lds_byte(int r, int c) { const int st = (r >> 4) * 2 + (c >> 5), rr = r & 15, cc = c & 31, ob = rr * 64 + cc * 2; return st * 1024 + (ob ^ (((ob >> 9) & 1) << 5)); }
DI void stage_rc(int b, int& R, int& C) { const int st = b / 1024, sb = b % 1024, swz = sb ^ (((sb >> 9) & 1) << 5); R = (st >> 1) * 16 + swz / 64; C = (st & 1) * 32 + (swz % 64) / 2; }

struct Params {
    const u16 *ax, *mem0, *wq, *wk, *wv, *wo, *bx, *bwqkv, *bwo;
    u16* out; char* ws;
};
#define RLX_AGENT __ATOMIC_RELAXED, __HIP_MEMORY_SCOPE_AGENT
DI bool spin_out(unsigned& spins, unsigned* ctl) {
    spins = __builtin_amdgcn_readfirstlane(spins) + 1u;
    if ((spins & 63u) == 0u) {
        if (spins > (1u << 18)) { __hip_atomic_store(ctl + C_TMO, 1u, RLX_AGENT); return true; }
        if (__builtin_amdgcn_readfirstlane(__hip_atomic_load(ctl + C_TMO, RLX_AGENT)) != SENT) return true;
    }
    return false;
}
template <int NAP = 8>
DI void wait_count(unsigned* ctl, int word, unsigned need) {
    unsigned spins = 0;
    while (__builtin_amdgcn_readfirstlane(__hip_atomic_load(ctl + word, RLX_AGENT)) + 1u < need) { __builtin_amdgcn_s_sleep(NAP); if (spin_out(spins, ctl)) break; }
}
DI void wg_wait(unsigned* ctl, int word0, unsigned need0, int word1, unsigned need1) {
    if (threadIdx.x < 64) {
        wait_count(ctl, word0, need0); wait_count(ctl, word1, need1);
        __builtin_amdgcn_fence(__ATOMIC_ACQUIRE, "agent");
        asm volatile("s_waitcnt vmcnt(0)" ::: "memory");
    }
    asm volatile("" ::: "memory"); BAR(); asm volatile("" ::: "memory");
}
struct Unit { const char* A; const char* B; char* C; unsigned lda, ldb, ldc, cw, ch, ak, ah; int nt, sig, nsig; };
DI int fresh_tid() { int t = threadIdx.x; asm volatile("" : "+v"(t)); return t; }
DI void signal(unsigned* ctl, int sig, int nsig) { const int lane = fresh_tid() & 63; if (lane < nsig) __hip_atomic_fetch_add(ctl + sig + (lane & 1) * 16 + (lane >> 1) * 512, 1u, RLX_AGENT); }
DI u32x4 pack_pair(f32x4 v0, f32x4 v1) {
    unsigned ax = pk_bf16(v0[0], v0[1]), ay = pk_bf16(v0[2], v0[3]), bx = pk_bf16(v1[0], v1[1]), by = pk_bf16(v1[2], v1[3]);
    swap16(ax, bx); swap16(ay, by);
    return (u32x4){ax, ay, bx, by};
}
DI unsigned frag_off(int lane) { return (unsigned)((((lane >> 4) & 1) * 2 + (lane >> 5)) * 256 + (lane & 15) * 16); }
DI void store_frag(rsrc_t cr, unsigned voff, unsigned soff, f32x4 v0, f32x4 v1) { ST16(pack_pair(v0, v1), cr, voff, soff, 16); }
DI u32x4 to_rows(ldsp sc, int lane, f32x4 v0, f32x4 v1) {
    *(volatile LAS u32x4*)(sc + ((lane & 15) * 4 + ((lane >> 4) & 1) * 2 + (lane >> 5)) * 16) = pack_pair(v0, v1);
    return *(volatile LAS u32x4*)(sc + lane * 16);
}
constexpr int L_SCR = 132096;
#define STAGE(bufoff, gbase, voff, step2) do { \
        __builtin_amdgcn_global_load_lds((const unsigned*)((gbase) + (voff)), (LAS unsigned*)(lds + (bufoff) + ldsw), 16, 0, 0); \
        __builtin_amdgcn_global_load_lds((const unsigned*)((gbase) + (step2) + (voff)), (LAS unsigned*)(lds + (bufoff) + ldsw + 8192), 16, 0, 0); } while (0)
#define LDA(dst, off) do { _Pragma("unroll") for (int m = 0; m < 4; ++m) _Pragma("unroll") for (int k = 0; k < 2; ++k) dst[m][k] = *(const LAS bf16x8*)(lds + (off) + aoff + m * 2048 + k * 1024); } while (0)
#define TRD(dst, base, off) asm volatile("ds_read_b64_tr_b16 %0, %1 offset:%2" : "=v"(dst) : "v"(base), "i"(off) : "memory")
#define LDB(dst, v0, v1, off) do { s16x4 l0, h0, l1, h1, l2, h2, l3, h3; \
        TRD(l0, v0, (off)); TRD(h0, v0, (off) + 256); TRD(l1, v0, (off) + 8192); TRD(h1, v0, (off) + 8448); \
        TRD(l2, v1, (off)); TRD(h2, v1, (off) + 256); TRD(l3, v1, (off) + 8192); TRD(h3, v1, (off) + 8448); \
        dst[0][0] = cat8(l0, h0); dst[0][1] = cat8(l1, h1); dst[1][0] = cat8(l2, h2); dst[1][1] = cat8(l3, h3); } while (0)
#define MMA(accv, At, Bt) do { SCHED(); __builtin_amdgcn_s_setprio(1); _Pragma("unroll") for (int m = 0; m < 4; ++m) _Pragma("unroll") for (int n = 0; n < 2; ++n) _Pragma("unroll") for (int k = 0; k < 2; ++k) \
        accv[m][n] = mma32(Bt[n][k], At[m][k], accv[m][n]); __builtin_amdgcn_s_setprio(0); } while (0)
DI unsigned va_of(int tid, unsigned lda, int nt) { int R, C; stage_rc(tid * 16, R, C); return lda ? (unsigned)R * lda + (unsigned)C * 2u : (unsigned)((tid >> 7) * nt * 4096 + (tid & 127) * 16); }
DI unsigned vb_of(int tid, int wid, unsigned ldb) { return (unsigned)(8 * (wid >> 1) + ((tid >> 2) & 7)) * ldb + (unsigned)((2 * (wid & 1) + ((tid >> 5) & 1)) * 64 + (((tid & 3) ^ (((wid >> 1) & 1) << 1)) * 16)); }
#define GEMM_LANE_SETUP(lda0) \
    const int tid = fresh_tid(); \
    const int wid = __builtin_amdgcn_readfirstlane(tid >> 6), lane = tid & 63, wr = wid >> 2, wc = wid & 3, fr = lane & 15, fq = lane >> 4; \
    const unsigned ldsw = (unsigned)wid * 1024u; \
    const int aoff = (lda0) ? lds_byte(wr * 64 + fr, fq * 8) : wr * 8192 + lane * 16; \
    const int boff0 = fq * 2048 + wc * 512 + (fr >> 2) * 64 + (fq & 1) * 32 + (fr & 3) * 8, boff1 = boff0 ^ 32;

template <class Sched>
DI void gemm_stream(ldsp lds, const Sched& S, unsigned* ctl) {
    Unit cur, nxt; int ui = 0;
    if (!S.next(0, cur)) return;
    GEMM_LANE_SETUP(cur.lda);
#define SA(b, h) (((b) * 2 + (h)) * HTB)
#define SB(b, h) ((4 + (b) * 2 + (h)) * HTB)
    const unsigned vb0 = (unsigned)(size_t)lds + 65536u + (unsigned)boff0, vb1 = (unsigned)(size_t)lds + 65536u + (unsigned)boff1;
    f32x4 acc[2][2][4][2];
#pragma unroll
    for (int a = 0; a < 2; ++a)
#pragma unroll
        for (int b = 0; b < 2; ++b)
#pragma unroll
            for (int m = 0; m < 4; ++m)
#pragma unroll
                for (int n = 0; n < 2; ++n) acc[a][b][m][n] = (f32x4){0.f, 0.f, 0.f, 0.f};
    bf16x8 At[4][2], B0[2][2], B1[2][2];
    const unsigned vA = va_of(tid, cur.lda, cur.nt); unsigned vB = vb_of(tid, wid, cur.ldb);
    const size_t a2s = cur.lda ? (size_t)64 * cur.lda : (size_t)cur.nt * 16384;
    {
        const size_t ah = cur.ah, b2s = (size_t)32 * cur.ldb, bk = (size_t)64 * cur.ldb;
        STAGE(SB(0, 0), cur.B, vB, b2s); STAGE(SB(0, 1), cur.B + 256, vB, b2s); STAGE(SA(0, 0), cur.A, vA, a2s); STAGE(SA(0, 1), cur.A + ah, vA, a2s);
        if (wr == 1) BAR();
        WAIT_V(2); BAR();
        STAGE(SB(1, 0), cur.B + bk, vB, b2s); STAGE(SA(1, 0), cur.A + cur.ak, vA, a2s); STAGE(SB(1, 1), cur.B + bk + 256, vB, b2s);
        WAIT_V(6); BAR();
    }
    int psig = 0, pn = 0;
    for (;;) {
        const bool has_next = S.next(ui + 1, nxt);
        if (!has_next) nxt = cur;
        int nt = cur.nt; asm volatile("" : "+s"(nt));
        for (int t = 0; t < nt; t += 2) {
            const bool last = (t == nt - 2);
            const char* a1 = cur.A + (cur.lda ? (unsigned)(t + 1) * 128u : gap((unsigned)(t + 1) * 2048u)) + cur.ah;
            if (last) vB = vb_of(fresh_tid(), wid, nxt.ldb);
            const unsigned ldb = last ? nxt.ldb : cur.ldb;
            const char* a2 = last ? nxt.A : cur.A + (cur.lda ? (unsigned)(t + 2) * 128u : gap((unsigned)(t + 2) * 2048u));
            const char* b2 = last ? nxt.B : cur.B + (size_t)(t + 2) * 64 * cur.ldb;
            const size_t ah = cur.ah, b2s = (size_t)32 * ldb;
            const char* a3 = a2 + cur.ak; const char* b3 = b2 + (size_t)64 * ldb;
            LDB(B0, vb0, vb1, 0); LDB(B1, vb0, vb1, HTB); SCHED(); LDA(At, SA(0, 0)); STAGE(SA(1, 1), a1, vA, a2s);
            WAIT_V(8); WAIT_L(0); BAR(); MMA(acc[0][0], At, B0); MMA(acc[0][1], At, B1); BAR(); SCHED();
            LDA(At, SA(0, 1)); STAGE(SB(0, 0), b2, vB, b2s); STAGE(SB(0, 1), b2 + 256, vB, b2s); STAGE(SA(0, 0), a2, vA, a2s);
            WAIT_V(8);
            asm volatile("" : "+s"(pn));
            if (pn) { signal(ctl, psig, pn); pn = 0; }
            WAIT_L(0); BAR(); MMA(acc[1][0], At, B0); MMA(acc[1][1], At, B1); BAR(); SCHED();
            LDB(B0, vb0, vb1, 2 * HTB); LDB(B1, vb0, vb1, 3 * HTB); SCHED(); LDA(At, SA(1, 0)); STAGE(SA(0, 1), a2 + ah, vA, a2s);
            WAIT_V(8); WAIT_L(0); BAR(); MMA(acc[0][0], At, B0); MMA(acc[0][1], At, B1); BAR(); SCHED();
            LDA(At, SA(1, 1)); STAGE(SB(1, 0), b3, vB, b2s); STAGE(SB(1, 1), b3 + 256, vB, b2s); STAGE(SA(1, 0), a3, vA, a2s);
            WAIT_V(8); WAIT_L(0); BAR(); MMA(acc[1][0], At, B0); MMA(acc[1][1], At, B1); BAR(); SCHED();
        }
        if (wr == 0) BAR();
        {
            const rsrc_t cr = mkrsrc(cur.C, 0x1000000u);
            const int tz = fresh_tid() & 63;
            if (cur.nsig) {
                const unsigned co = (unsigned)wc * cur.cw + frag_off(tz);
#pragma unroll
                for (int ai = 0; ai < 2; ++ai)
#pragma unroll
                    for (int m = 0; m < 4; ++m)
#pragma unroll
                        for (int bj = 0; bj < 2; ++bj) store_frag(cr, co, gap((unsigned)(ai * 128 + wr * 64 + m * 16) * cur.ldc) + (unsigned)bj * cur.ch, acc[ai][bj][m][0], acc[ai][bj][m][1]);
            } else {
                const unsigned co = (unsigned)(wr * 64 + (tz >> 2)) * cur.ldc + (unsigned)(wc * 64 + (tz & 3) * 16);
#pragma unroll
                for (int ai = 0; ai < 2; ++ai)
#pragma unroll
                    for (int m = 0; m < 4; ++m)
#pragma unroll
                        for (int bj = 0; bj < 2; ++bj)
                            ST16(to_rows(lds + L_SCR + wid * 1024, tz, acc[ai][bj][m][0], acc[ai][bj][m][1]), cr, co, (unsigned)(ai * 128 + m * 16) * cur.ldc + (unsigned)bj * 256u, 0);
            }
        }
        psig = cur.sig; pn = cur.nsig;
        if (!has_next) break;
#pragma unroll
        for (int a = 0; a < 2; ++a)
#pragma unroll
            for (int b = 0; b < 2; ++b)
#pragma unroll
                for (int m = 0; m < 4; ++m)
#pragma unroll
                    for (int n = 0; n < 2; ++n) acc[a][b][m][n] = (f32x4){0.f, 0.f, 0.f, 0.f};
        cur = nxt; ++ui;
        if (wr == 1) BAR();
    }
    WAIT_V(0);
    signal(ctl, psig, pn);
    BAR();
#undef SA
#undef SB
}

DI void gemm_small(ldsp lds, const Unit u, unsigned* ctl) {
    GEMM_LANE_SETUP(u.lda);
    const unsigned vA = va_of(tid, u.lda, u.nt), vB = vb_of(tid, wid, u.ldb);
    const size_t a2s = u.lda ? (size_t)64 * u.lda : (size_t)u.nt * 16384, b2s = (size_t)32 * u.ldb, bk = (size_t)64 * u.ldb;
    f32x4 acc[4][2];
#pragma unroll
    for (int m = 0; m < 4; ++m)
#pragma unroll
        for (int n = 0; n < 2; ++n) acc[m][n] = (f32x4){0.f, 0.f, 0.f, 0.f};
    int nt = u.nt; asm volatile("" : "+s"(nt));
    const unsigned vb0 = (unsigned)(size_t)lds + (unsigned)boff0, vb1 = (unsigned)(size_t)lds + (unsigned)boff1, vb2 = vb0 + 65536u, vb3 = vb1 + 65536u;
#pragma unroll
    for (int t = 0; t < 4; ++t) { STAGE(t * 32768, u.A + (size_t)t * u.ak, vA, a2s); STAGE(t * 32768 + 16384, u.B + t * bk, vB, b2s); }
    bf16x8 A0[4][2], B0[2][2], A1[4][2], B1[2][2];
    WAIT_V(12); BAR();
    LDB(B0, vb0, vb1, 16384); LDA(A0, 0);
#define RDA(dst, m, off) do { dst[m][0] = *(const LAS bf16x8*)(lds + (off) + aoff + (m) * 2048); dst[m][1] = *(const LAS bf16x8*)(lds + (off) + aoff + (m) * 2048 + 1024); } while (0)
#define RDB(dst, nn, v, off) do { s16x4 l0, h0, l1, h1; TRD(l0, v, (off)); TRD(h0, v, (off) + 256); TRD(l1, v, (off) + 8192); TRD(h1, v, (off) + 8448); dst[nn][0] = cat8(l0, h0); dst[nn][1] = cat8(l1, h1); } while (0)
#define MM4(Ac, Bc, m) do { SCHED(); _Pragma("unroll") for (int n = 0; n < 2; ++n) _Pragma("unroll") for (int k = 0; k < 2; ++k) acc[m][n] = mma32(Bc[n][k], Ac[m][k], acc[m][n]); SCHED(); } while (0)
#define KSTEP(i, Ac, Bc, An, Bn, v0, v1, boffn) do { \
        const int tn = min(t0 + (i) + 4, nt - 1); \
        WAIT_L(0); WAIT_V(8); BAR(); \
        MM4(Ac, Bc, 0); RDB(Bn, 0, v0, boffn); RDA(An, 0, (((i) + 1) & 3) * 32768); \
        MM4(Ac, Bc, 1); RDB(Bn, 1, v1, boffn); RDA(An, 1, (((i) + 1) & 3) * 32768); \
        MM4(Ac, Bc, 2); RDA(An, 2, (((i) + 1) & 3) * 32768); STAGE((i) * 32768, u.A + (u.lda ? (unsigned)tn * 128u : gap((unsigned)tn * 2048u)), vA, a2s); \
        MM4(Ac, Bc, 3); RDA(An, 3, (((i) + 1) & 3) * 32768); STAGE((i) * 32768 + 16384, u.B + (size_t)tn * bk, vB, b2s); } while (0)
    for (int t0 = 0; t0 < nt; t0 += 4) {
        KSTEP(0, A0, B0, A1, B1, vb0, vb1, 49152);
        KSTEP(1, A1, B1, A0, B0, vb2, vb3, 16384);
        KSTEP(2, A0, B0, A1, B1, vb2, vb3, 49152);
        KSTEP(3, A1, B1, A0, B0, vb0, vb1, 16384);
    }
    WAIT_L(0); SCHED();
#undef RDA
#undef RDB
#undef MM4
#undef KSTEP
    const rsrc_t cr = mkrsrc(u.C, 256u * u.ldc);
    if (u.nsig) {
#pragma unroll
        for (int m = 0; m < 4; ++m) store_frag(cr, (unsigned)wc * u.cw + frag_off(lane), gap((unsigned)(wr * 64 + m * 16) * u.ldc), acc[m][0], acc[m][1]);
    } else {
#pragma unroll
        for (int m = 0; m < 4; ++m)
            ST16(to_rows(lds + L_SCR + wid * 1024, lane, acc[m][0], acc[m][1]), cr, (unsigned)(wr * 64 + (lane >> 2)) * u.ldc + (unsigned)(wc * 64 + (lane & 3) * 16), (unsigned)(m * 16) * u.ldc, 0);
    }
    WAIT_V(0);
    signal(ctl, u.sig, u.nsig);
    BAR();
}

#define win_off(idx) ((unsigned)(idx) * 16384u + ((((unsigned)(idx) >> 7) ^ par) & 1u) * 8192u)
#define KKM 8192u
#define km_win(s, h) (win_off(NWIN + (s) * 32 + (h)) ^ KKM)
#define W_MEM(s) ((s) == 0 ? 0 : 6 + 2 * (s))
#define W_PART(slot) (192 + (slot) * 64)
#define W_X(slot) (384 + (slot) * 128)
DI bool all_set(unsigned mx) { return __builtin_amdgcn_ballot_w64(mx == SENT) == 0ull; }
DI u32x4 rows_gather(unsigned v) { unsigned a = v, b = v; swap16(a, b); unsigned c = a, d = b; swap32(a, c); swap32(b, d); return (u32x4){a, b, c, d}; }
DI float max3f(float a, float b, float c) { return fmaxf(fmaxf(a, b), c); }
#define KD0 6
#define KD1 6
#define KD2 3
#define KRA 3
#define KDM 3
#define KWP 4
#define KNT1 0
#define KNT2 0
#define KPM 8
constexpr int RED_M = 144;
constexpr int L_PX = 32768, L_PM = L_PX + 512, L_ML = L_PM + 512, L_FLAG = L_ML + 256, L_PACE = L_FLAG + 16, L_VM = 34816, L_OWN = L_VM + 2048, L_X = L_OWN + 1536  ;
constexpr unsigned OOB = 0x80000000u;
#define RETRY(i)
#define SEG(i)
#define PST(i)
#define PIN_S(x) asm volatile("" : "+s"(x))
#define PIN_V(x) asm volatile("" : "+v"(x))
DI float ror8(float v) { return __uint_as_float(__builtin_amdgcn_update_dpp(0, __float_as_uint(v), 0x128, 0xf, 0xf, true)); }

template <int ROLE>
DI void chain_loop(const Params& p, const unsigned par, const int rank, const int w, ldsp lds, const bf16x16 (&W)[6][2], const bf16x16 (&WO)[2][2], unsigned long long* stamps) {
    const int lane = fresh_tid() & 63, g = lane >> 4, n = lane & 15;
    const int h = rank >> 2, j = rank & 3;
    unsigned* ctl = (unsigned*)(p.ws + WS_CTL);
    const rsrc_t xr = mkrsrc(p.ws + WS_XCH, (unsigned)XCH_BYTES);
    const rsrc_t qr = mkrsrc(p.ws + WH(WS_QKVX + (size_t)(32 + h) * XH), 33u * XH);
    const int sidx = (n & 2) ? 0xEEEE : 0x4444;
    const unsigned l16 = (unsigned)lane * 16u;
    const int redw = (w * 1024 + (n & 1) * 512 + (n >> 1) * 8 + 2 * (g ^ (n >> 3))) * 4;
    const int redr = (w < 4 ? (n >> 3) * RED_M + (4 * w + g) * 8 + ((n & 7) ^ ((w & 1) * 2)) : 2 * RED_M + (8 * (w & 1) + 2 * g + ((n >> 1) & 1)) * 8 + ((2 * (n >> 2) + (n & 1)) ^ (g & 2))) * 4;
    const bool vown = (g >> 1) == (j & 1);
    const unsigned vg1 = (unsigned)(((j & 2) ^ 2) + (g >> 1)) * 1536u + 1024u + (unsigned)(lane & 31) * 16u;
    const unsigned vg0 = vown ? OOB : (vg1 + (unsigned)(j & 2) * 3072u - 3072u);
    const unsigned pub_off = (g == j ? 0u : (unsigned)(4 * h + g) * 16384u) + (unsigned)j * 1536u + (unsigned)w * 256u + (unsigned)n * 16u;
    const int red = ((j + 1) & 3) * 32 + h;
    const int rho = 4 * (8 * j + w) + 2 * (g >> 1) + ((n >> 2) & 1);
    const unsigned pst_off = n < 8 ? (unsigned)(rho >> 1) * 16384u + (unsigned)(rho & 1) * 4096u + (unsigned)((h ^ rho) & 31) * 128u + (unsigned)((g & 1) * 64 + (n & 3) * 16) : OOB;
    const unsigned mem_off = lane < 8 ? (unsigned)(4 * ((n >> 1) & 1) + 2 * (n >> 2) + (n & 1)) * 16u : OOB;
    const unsigned xw0 = win_off(W_X(0)), xw1 = win_off(W_X(1));
    unsigned pso = win_off(W_PART(0)), psn = win_off(W_PART(1)), psp = win_off(W_PART(2));
    const unsigned redo = (unsigned)(red >> 1) * 16384u + (unsigned)(red & 1) * 4096u;
    const int vxr = L_X + lane * 16;
    const float csc = 0.08838834764831845f * 1.4426950408889634f;
    float ninf = -__builtin_inff(); PIN_V(ninf);
    u32x4 KX[8], VWX[2];
    auto dma_x = [&](int s) {
        if (ROLE == 0 && w < 4) {
            if ((s & 3) == 0) { wait_count(ctl, C_KV + (s >> 2) * 512 + h * 16, 16u); asm volatile("" ::: "memory"); }
#pragma unroll
            for (int i = 0; i < 2; ++i) {
                const unsigned vo = l16, so = (unsigned)s * 16384u + (unsigned)(w * 2 + i) * 1024u;
                __builtin_amdgcn_raw_ptr_buffer_load_lds(qr, (LAS void*)(lds + L_X + (s & 1) * 16384 + (w * 2 + i) * 1024), 16, vo, so + 32u * XH, 0, 16);
                __builtin_amdgcn_raw_ptr_buffer_load_lds(qr, (LAS void*)(lds + L_X + (s & 1) * 16384 + 8192 + (w * 2 + i) * 1024), 16, vo, so, 0, 16);
            }
        }
    };
    auto load_kx = [&](int s) {
#pragma unroll
        for (int kt = 0; kt < 2; ++kt)
#pragma unroll
            for (int kk = 0; kk < 4; ++kk) KX[kt * 4 + kk] = *(const LAS u32x4*)(lds + vxr + (s & 1) * 16384 + 8192 + kt * 4096 + ((j + kk) & 3) * 1024);
    };
    auto vwx_kt = [&](int s, int kt) {
        bf16x8 VA[4];
#pragma unroll
        for (int kk = 0; kk < 4; ++kk) VA[kk] = *(const LAS bf16x8*)(lds + vxr + (s & 1) * 16384 + kt * 4096 + (kk ^ (j & 2)) * 1024);
#pragma unroll
        for (int ct = 0; ct < 2; ++ct) {
            f32x4 d = {0.f, 0.f, 0.f, 0.f};
#pragma unroll
            for (int kk = 0; kk < 4; ++kk) d = mma32(VA[kk], (kk & 1) ? hi8(WO[ct][kk >> 1]) : lo8(WO[ct][kk >> 1]), d);
            VWX[ct][2 * kt] = pk_bf16(d[0], d[1]); VWX[ct][2 * kt + 1] = pk_bf16(d[2], d[3]);
        }
    };
    auto rmax = [&](float v) { float a = v, b = v; swap16f(a, b); v = max3f(a, b, ninf); a = v; b = v; swap32f(a, b); return max3f(a, b, ninf); };
    auto lo2 = [](f32x4 v) { return (f32x2){v[0], v[1]}; };
    auto hi2 = [](f32x4 v) { return (f32x2){v[2], v[3]}; };
    dma_x(0); dma_x(1);
    asm volatile("s_waitcnt vmcnt(0) lgkmcnt(0)" ::: "memory"); BAR(); asm volatile("" ::: "memory");
    PST(5);
    vwx_kt(0, 0); vwx_kt(0, 1);
    if (ROLE == 2) load_kx(0);
    __builtin_amdgcn_s_setprio(2);

    unsigned iv_t0 = 0, iv_acc = 0;
    unsigned spins = 0, pace_c = 0x7fffffffu, pace_t = 0;
    const u32x4 sv = {SENT, SENT, SENT, SENT};
    for (int s = 0;; ++s) {
        unsigned xw = (s & 1) ? xw1 : xw0;
        unsigned mso = win_off(W_MEM(s) + (w >> 2)) + (unsigned)(w & 3) * 2048u;
        unsigned pub = pub_off + (g == j ? km_win(s, h) : xw);
        PIN_S(xw); PIN_S(mso); PIN_V(pub);
        {
            if (KD0) __builtin_amdgcn_s_sleep(KD0);
            const u32x2 pv = *(const LAS u32x2*)(lds + L_PACE);
            unsigned target = __builtin_amdgcn_readfirstlane(pv.x); const bool valid = __builtin_amdgcn_readfirstlane(pv.y) + 1u == (unsigned)s && s > 3;
            unsigned now = (unsigned)__builtin_amdgcn_s_memrealtime();
            if (ROLE == 3 && s > 2) pace_c = min(pace_c, now - pace_t);
            if (valid && (int)(target - now) < 100) { while ((int)(target - now) > 0) now = (unsigned)__builtin_amdgcn_s_memrealtime(); }
        }
        SEG(10);
        f32x4 c[6]; u32x4 a1;
        u32x4 a0;
        auto ld_a = [&]() { asm volatile("" ::: "memory"); a0 = ld16_sc1(xr, l16, mso); a1 = ld16_sc1(xr, l16 + 1024, mso); };
        auto ok_a = [&]() { return all_set(max(umax4(a0), umax4(a1))); };
        auto mm_a = [&]() {
#pragma unroll
            for (int i = 0; i < 6; ++i) { c[i] = (f32x4){0.f, 0.f, 0.f, 0.f}; PIN_V(c[i]); }
            SCHED();
            c[0] = smma(a0, W[0][0], c[0], sidx); c[1] = smma(a0, W[1][0], c[1], sidx); c[0] = smma(a1, W[0][1], c[0], sidx); c[1] = smma(a1, W[1][1], c[1], sidx);
            c[2] = smma(a0, W[2][0], c[2], sidx); c[3] = smma(a0, W[3][0], c[3], sidx);
        };
        auto put = [&](int i) { *(LAS f32x2*)(lds + redw + ((i >> 1) * RED_M + (i & 1) * 64) * 4) = lo2(c[i]) + hi2(c[i]); };
        ld_a(); SCHED();
        mm_a();
        if (__builtin_expect(!ok_a(), 0)) { bool out; do { RETRY(0); out = spin_out(spins, ctl); ld_a(); } while (!(ok_a() | out)); mm_a(); }
        SEG(0);
        c[2] = smma(a1, W[2][1], c[2], sidx); c[3] = smma(a1, W[3][1], c[3], sidx);
        put(0); put(1);
        SCHED();
        c[4] = smma(a0, W[4][0], c[4], sidx); c[5] = smma(a0, W[5][0], c[5], sidx); c[4] = smma(a1, W[4][1], c[4], sidx);
        put(2); put(3);
        c[5] = smma(a1, W[5][1], c[5], sidx);
        SCHED();
        put(4); put(5);
        SEG(1);
        LDS_BAR();
        SEG(2);
        if (ROLE <= 1) {
            f32x2 v[8];
#pragma unroll
            for (int ww = 0; ww < 8; ++ww) { const LAS float* q = (const LAS float*)(lds + redr + ww * 4096); v[ww] = (f32x2){q[0], q[512]}; }
            const f32x2 t = ((v[0] + v[1]) + (v[2] + v[3])) + ((v[4] + v[5]) + (v[6] + v[7]));
            const u32x4 o = rows_gather(min(pk_bf16(t.x, t.y), SENT - 1));
            ST16(o, xr, pub, 0, 16);
            *(LAS u32x4*)(lds + L_OWN + w * 256 + n * 16) = o;
        } else {
            if (ROLE == 3 && rank == 0) {
                pace_t = (unsigned)__builtin_amdgcn_s_memrealtime();
                if (lane == 0) { __hip_atomic_store(ctl + C_PROG, (unsigned)s, RLX_AGENT);
                    __hip_atomic_store((unsigned long long*)(ctl + C_PACE + par * 2048), (unsigned long long)(pace_t + pace_c + KPM) | ((unsigned long long)s << 32), RLX_AGENT); }
            }
        }
        SEG(3);
        if (s == NSEG - 1) break;
        LDS_BAR();
        SEG(13);
        unsigned VWM[2];
        auto vwm_pack = [&](const f32x4 (&dv)[2]) {
#pragma unroll
            for (int ct = 0; ct < 2; ++ct) { const f32x2 t = lo2(dv[ct]) + hi2(dv[ct]); VWM[ct] = pk_bf16(t.x, t.y); PIN_V(VWM[ct]); }
        };
        f32x4 dv[2] = {{0.f, 0.f, 0.f, 0.f}, {0.f, 0.f, 0.f, 0.f}}, s0, s1;
        PIN_V(dv[0]); PIN_V(dv[1]);
        u32x4 v0, v1;
        if (ROLE == 0) {
            if (KDM == 1) { WAIT_V(0); dma_x(min(s + 2, NSEG - 2)); }
            else if (KDM == 2) { dma_x(min(s + 2, NSEG - 2)); if (w < 4) WAIT_V(4); else WAIT_V(0); }
            else WAIT_V(0);
        } else {
            unsigned cw = xw + (unsigned)rank * 16384u, c1 = cw + (unsigned)((j + 1) & 3) * 1536u, c2 = cw + (unsigned)((j + 2) & 3) * 1536u, c3 = cw + (unsigned)((j + 3) & 3) * 1536u;
            PIN_S(cw); PIN_S(c1); PIN_S(c2); PIN_S(c3);
            const unsigned xl = ROLE == 2 ? (l16 & ~128u) : l16;
            u32x4 vo;
            f32x4 e0 = {0.f, 0.f, 0.f, 0.f}, e1 = e0;
            if (ROLE == 1) { vo = *(const LAS u32x4*)(lds + L_OWN + 1024 + (lane & 31) * 16); if (!vown) vo = (u32x4){0u, 0u, 0u, 0u}; PIN_V(vo); }
            else {
                const u32x4 X0 = *(const LAS u32x4*)(lds + L_OWN + xl);
                if (ROLE == 2) { e0 = mma32(asbf(KX[0]), asbf(X0), e0); e1 = mma32(asbf(KX[4]), asbf(X0), e1); } else e0 = mma32(asbf(X0), asbf(X0), e0);
                PIN_V(e0); PIN_V(e1);
            }
            if (KD1) __builtin_amdgcn_s_sleep(KD1);
            u32x4 X1, X2, X3;
            auto ld_b = [&]() {
                asm volatile("" ::: "memory");
                if (ROLE >= 2) { X1 = ld16_sc1(xr, xl, c1); X2 = ld16_sc1(xr, xl, c2); X3 = ld16_sc1(xr, xl, c3); }
                else { v0 = ld16_sc1(xr, vg0, cw); v1 = ld16_sc1(xr, vg1, cw); }
            };
            auto ok_b = [&]() { return all_set(ROLE >= 2 ? max(umax4(X1), max(umax4(X2), umax4(X3))) : max(umax4(v0), umax4(v1))); };
            auto mm_b = [&]() {
                if (ROLE == 2) {
                    s0 = mma32(asbf(KX[1]), asbf(X1), e0); s1 = mma32(asbf(KX[5]), asbf(X1), e1);
                    s0 = mma32(asbf(KX[2]), asbf(X2), s0); s1 = mma32(asbf(KX[6]), asbf(X2), s1);
                    s0 = mma32(asbf(KX[3]), asbf(X3), s0); s1 = mma32(asbf(KX[7]), asbf(X3), s1);
                } else if (ROLE == 3) {
                    s0 = mma32(asbf(X1), asbf(X1), e0); s1 = mma32(asbf(X2), asbf(X2), e1); s0 = mma32(asbf(X3), asbf(X3), s0);
                } else {
                    v0 |= vo;
                    *(LAS u32x4*)(lds + L_VM + lane * 16) = v0; *(LAS u32x4*)(lds + L_VM + 1024 + lane * 16) = v1;
                }
            };
            SEG(11);
            ld_b(); SCHED(); mm_b();
            if (__builtin_expect(!ok_b(), 0)) { bool out; do { RETRY(1); out = spin_out(spins, ctl); ld_b(); } while (!(ok_b() | out)); mm_b(); }
            SEG(4);
        }
        float m;
        f32x4 sc;
        if (ROLE >= 2) {
            sc = ROLE == 2 ? (n < 8 ? s0 : s1) : s0 + s1;
            if (ROLE == 3 && g < 2) sc = (f32x4){ninf, ninf, ninf, ninf};
            m = max3f(max3f(sc[0], sc[1], sc[2]), sc[3], ninf);
            if (ROLE == 2) m = max3f(m, ror8(m), ninf);
            m = rmax(m);
            PIN_V(m);
        }
        LDS_BAR();
        if (ROLE == 0) {
            const unsigned ro = w < 3 ? xw + (unsigned)rank * 16384u + (unsigned)w * 2048u : psp + redo + (unsigned)(w - 3) * 2048u;
            ST16(sv, xr, l16, ro, 16); ST16(sv, xr, l16 + 1024, ro, 16);
        }
        if (ROLE != 1) { v0 = *(const LAS u32x4*)(lds + L_VM + lane * 16); v1 = *(const LAS u32x4*)(lds + L_VM + 1024 + lane * 16); }
        if (ROLE >= 2) {
            const float mc = -m * csc;
#pragma unroll
            for (int i = 0; i < 4; ++i) sc[i] = __builtin_amdgcn_exp2f(fmaf(sc[i], csc, mc));
            *(LAS u32x2*)(lds + (ROLE == 2 ? L_PX + (g * 8 + (n & 7)) * 16 + (n >> 3) * 8 : L_PM + (g < 2 ? 256 : 0) + n * 16 + (g & 1) * 8)) = (u32x2){pk_bf16(sc[0], sc[1]), pk_bf16(sc[2], sc[3])};
            SCHED();
            dv[0] = smma(v0, WO[0][0], dv[0], sidx); dv[1] = smma(v0, WO[1][0], dv[1], sidx);
            float l = (sc[0] + sc[1]) + (sc[2] + sc[3]);
            if (ROLE == 2) l += ror8(l);
            dv[0] = smma(v1, WO[0][1], dv[0], sidx); dv[1] = smma(v1, WO[1][1], dv[1], sidx);
            l = rows_sum(l);
            *(LAS f32x2*)(lds + L_ML + n * 16 + (ROLE == 2 ? 0 : 8)) = (f32x2){mc, l};
        } else {
#pragma unroll
            for (int ct = 0; ct < 2; ++ct) { dv[ct] = smma(v0, WO[ct][0], dv[ct], sidx); dv[ct] = smma(v1, WO[ct][1], dv[ct], sidx); }
        }
        vwm_pack(dv);
        SEG(5);
        u32x4 am0 = {VWM[0], 0u, 0u, 0u}, am1 = {VWM[1], 0u, 0u, 0u}, pm = {0u, 0u, 0u, 0u};
        PIN_V(am0); PIN_V(am1); PIN_V(pm);
        LDS_BAR();
        SEG(6);
        {
            const f32x4 ml = *(const LAS f32x4*)(lds + L_ML + n * 16);
            const u32x4 px = *(const LAS u32x4*)(lds + L_PX + (g * 8 + (n & 7)) * 16);
            pm.x = *(const LAS unsigned*)(lds + L_PM + n * 16 + g * 4);
            const f32x4 z = {0.f, 0.f, 0.f, 0.f};
            const f32x4 dx0 = mma32(asbf(VWX[0]), asbf(px), z), dx1 = mma32(asbf(VWX[1]), asbf(px), z), dm0 = mma32(asbf(am0), asbf(pm), z), dm1 = mma32(asbf(am1), asbf(pm), z);
            const float dl = ml[0] - ml[2];
            const float e = __builtin_amdgcn_exp2f(-__builtin_fabsf(dl));
            const float ex = dl > 0.f ? e : 1.f, em = dl > 0.f ? 1.f : e;
            const float inv = __builtin_amdgcn_rcpf(fmaf(ex, ml[1], em * ml[3]));
            const f32x4 d0 = (dx0 * ex + dm0 * em) * inv, d1 = (dx1 * ex + dm1 * em) * inv;
            const u32x4 o = {min(pk_bf16(d0[0], d0[1]), SENT - 1), min(pk_bf16(d0[2], d0[3]), SENT - 1), min(pk_bf16(d1[0], d1[1]), SENT - 1), min(pk_bf16(d1[2], d1[3]), SENT - 1)};
            ST16(o, xr, pst_off, pso, 16);
        }
        SCHED();
        SEG(7);
        __builtin_amdgcn_s_setprio(ROLE == 3 ? 1 : 0);
        if (KDM == 3 && s + 2 < NSEG - 1) dma_x(s + 2);
        unsigned long long pv = 0;
        if (ROLE == 1) { pv = __hip_atomic_load((unsigned long long*)(ctl + C_PACE + par * 2048), RLX_AGENT); SCHED(); }
        vwx_kt(s + 1, 0); vwx_kt(s + 1, 1);
        SCHED();
        if (ROLE == 3) __builtin_amdgcn_s_setprio(2);
        if (ROLE == 3) {
            u32x4 SEL;
            { const unsigned one = (n & 1) ? 0x3f800000u : 0x00003f80u; const int dw = (n & 7) >> 1; SEL = (u32x4){dw == 0 ? one : 0u, dw == 1 ? one : 0u, dw == 2 ? one : 0u, dw == 3 ? one : 0u}; }
            unsigned so = pso + redo, mo = win_off(W_MEM(s + 1) + (red >> 6)) + (unsigned)(red & 63) * 128u;
            PIN_S(so); PIN_S(mo); PIN_V(SEL);
            if (KD2) __builtin_amdgcn_s_sleep(KD2);
            f32x4 ta, tb;
            u32x4 q[4];
            auto ld_c = [&]() {
                asm volatile("" ::: "memory");
#pragma unroll
                for (int i = 0; i < 4; ++i) q[i] = ld16_sc1(xr, l16 + i * 1024, so);
            };
            auto ok_c = [&]() { return all_set(max(max(umax4(q[0]), umax4(q[1])), max(umax4(q[2]), umax4(q[3])))); };
            auto mm_c = [&]() {
                ta = mma32(asbf(SEL), asbf(q[0]), (f32x4){0.f, 0.f, 0.f, 0.f}); tb = mma32(asbf(SEL), asbf(q[1]), (f32x4){0.f, 0.f, 0.f, 0.f});
                ta = mma32(asbf(SEL), asbf(q[2]), ta); tb = mma32(asbf(SEL), asbf(q[3]), tb);
            };
            SEG(12);
            ld_c(); mm_c();
            if (__builtin_expect(!ok_c(), 0)) { bool out; do { RETRY(2); out = spin_out(spins, ctl); ld_c(); } while (!(ok_c() | out)); mm_c(); }
            SEG(8);
            f32x4 t = ta + tb;
#pragma unroll
            for (int i = 0; i < 4; ++i) t[i] += ror8(t[i]);
            unsigned a0 = min(pk_bf16(t[0], t[1]), SENT - 1), a1 = min(pk_bf16(t[2], t[3]), SENT - 1);
            unsigned b0 = a0, b1 = a1; swap16(a0, b0); swap16(a1, b1);
            const u32x4 o = {a0, a1, b0, b1};
            ST16(o, xr, mem_off, mo, 16);
        } else {
            if (KDM == 0 && s + 2 < NSEG - 1) dma_x(s + 2);
            if (ROLE == 2) load_kx(s + 1);
            if (ROLE == 1) *(LAS u32x2*)(lds + L_PACE) = (u32x2){(unsigned)pv, (unsigned)(pv >> 32)};
        }
        { const unsigned t = psp; psp = pso; pso = psn; psn = t; }
        SEG(9);
        if (ROLE != 3) __builtin_amdgcn_s_setprio(2);
        LDS_BAR();
        SEG(14);
    }
    WAIT_V(0);
    __builtin_amdgcn_s_setprio(0);
}

DI void chain_role(const Params& p, unsigned par, int rank, ldsp lds, unsigned long long* stamps) {
    const int tid = fresh_tid(), w = __builtin_amdgcn_readfirstlane(tid >> 6), lane = tid & 63, g = lane >> 4, n = lane & 15;
    const int h = rank >> 2, j = rank & 3;
    const rsrc_t xr = mkrsrc(p.ws + WS_XCH, (unsigned)XCH_BYTES);
    unsigned* ctl = (unsigned*)(p.ws + WS_CTL);
    PST(0);
    const int red = ((j + 1) & 3) * 32 + h;
    bf16x16 W[6][2], WO[2][2];
    {
        ldsp my = lds + w * 16384;
        const int rrow = lane >> 2, rc = (lane & 3) * 8, q4 = n >> 2;
        const int toffo = (8 * g + q4) * 64 + (n & 3) * 8;
        const int toffq = (16 * (g & 1) + 8 * (g >> 1) + 4 * (q4 >> 1) + (q4 & 1)) * 64 + (n & 3) * 8;
#define FRAG(base, st) cat16(cat8(trd((base)), trd((base) + (st))), cat8(trd((base) + 2048), trd((base) + 2048 + (st))))
#pragma unroll
        for (int i = 0; i < 8; ++i) {
            __builtin_amdgcn_global_load_lds((const unsigned*)(p.wq + (size_t)(128 * w + 16 * i + rrow) * 4096 + h * 128 + 32 * j + rc), (LAS unsigned*)(my + i * 1024), 16, 0, KNT1);
            __builtin_amdgcn_global_load_lds((const unsigned*)(p.wo + (size_t)(h * 128 + 16 * i + rrow) * 1024 + 256 * j + 32 * w + rc), (LAS unsigned*)(my + 8192 + i * 1024), 16, 0, KNT1);
        }
        {
            const u32x4 sv = {SENT, SENT, SENT, SENT};
            if (w < 6) {
                ST16(sv, xr, (unsigned)lane * 16u, win_off(W_X(0)) + (unsigned)rank * 16384u + (unsigned)w * 1024u, 16);
                ST16(sv, xr, (unsigned)lane * 16u, win_off(W_X(1)) + (unsigned)rank * 16384u + (unsigned)w * 1024u, 16);
            }
            ST16(sv, xr, (unsigned)lane * 16u, win_off(W_PART(w >> 2)) + (unsigned)(red >> 1) * 16384u + (unsigned)(red & 1) * 4096u + (unsigned)(w & 3) * 1024u, 16);
            ST16(sv, xr, (unsigned)lane * 16u, km_win(56 + w, h) + (unsigned)j * 1536u, 16);
            ST16(sv, xr, lane < 32 ? (unsigned)lane * 16u + 1024u : OOB, km_win(56 + w, h) + (unsigned)j * 1536u, 16);
            const int s = 8 * w + (lane >> 3);
            if (s > 0) ST16(sv, xr, win_off(W_MEM(s) + (red >> 6)) + (unsigned)(red & 63) * 128u + (unsigned)(lane & 7) * 16u, 0, 16);
        }
        PST(3);
        WAIT_V(0);
#pragma unroll
        for (int T = 0; T < 2; ++T)
#pragma unroll
            for (int t = 0; t < 2; ++t) { W[T][t] = FRAG(my + t * 4096 + T * 32 + toffq, 128); WO[T][t] = FRAG(my + 8192 + (t ^ (j >> 1)) * 4096 + T * 32 + toffo, 256); }
#pragma unroll
        for (int T = 0; T < 2; ++T) asm volatile("" : "+v"(W[T][0]), "+v"(W[T][1]), "+v"(WO[T][0]), "+v"(WO[T][1]));
        WAIT_L(0);
        PST(1);
#pragma unroll
        for (int i = 0; i < 8; ++i) {
            __builtin_amdgcn_global_load_lds((const unsigned*)(p.wk + (size_t)(128 * w + 16 * i + rrow) * 4096 + h * 128 + 32 * j + rc), (LAS unsigned*)(my + i * 1024), 16, 0, KNT2);
            __builtin_amdgcn_global_load_lds((const unsigned*)(p.wv + (size_t)(128 * w + 16 * i + rrow) * 4096 + h * 128 + 32 * j + rc), (LAS unsigned*)(my + 8192 + i * 1024), 16, 0, KNT2);
        }
        WAIT_V(0);
#pragma unroll
        for (int T = 0; T < 4; ++T)
#pragma unroll
            for (int t = 0; t < 2; ++t) W[2 + T][t] = FRAG(my + (T >> 1) * 8192 + t * 4096 + (T & 1) * 32 + toffq, 128);
#pragma unroll
        for (int T = 0; T < 4; ++T) asm volatile("" : "+v"(W[2 + T][0]), "+v"(W[2 + T][1]));
        WAIT_L(0);
#undef FRAG
    }
    PST(2);
    BAR();
    PST(4);
    if (w == 7 && lane < 8) {
        const unsigned* src = (const unsigned*)(p.mem0 + (4 * (red & 1) + 2 * (lane >> 2) + (lane & 1)) * DM + 32 * (red >> 2) + 8 * ((red >> 1) & 1) + 4 * ((lane >> 1) & 1));
        const u32x4 v = {min(src[0], SENT - 1), min(src[1], SENT - 1), min(src[8], SENT - 1), min(src[9], SENT - 1)};
        ST16(v, xr, (unsigned)lane * 16u + (unsigned)(red & 63) * 128u, win_off(W_MEM(0) + (red >> 6)), 16);
    }
    if (w < 5) chain_loop<0>(p, par, rank, w, lds, W, WO, stamps);
    else if (w == 5) chain_loop<1>(p, par, rank, 5, lds, W, WO, stamps);
    else if (w == 6) chain_loop<2>(p, par, rank, 6, lds, W, WO, stamps);
    else chain_loop<3>(p, par, rank, 7, lds, W, WO, stamps);
}

template <bool POLL>
DI void attn_a_item(const Params& p, unsigned par, int s, int h, ldsp my, int lane) {
    const int g = lane >> 4, n = lane & 15;
    const rsrc_t xr = mkrsrc(p.ws + WS_XCH, (unsigned)XCH_BYTES);
    const rsrc_t qr = mkrsrc(p.ws + WH(WS_QKVX + (size_t)h * XH + (size_t)s * 16384), 65u * XH);
#pragma unroll
    for (int i = 0; i < 8; ++i)
        __builtin_amdgcn_raw_ptr_buffer_load_lds(qr, (LAS void*)(my + i * 1024), 16, (unsigned)(g * 256 + (n ^ ((g & 1) * 8)) * 16), 64u * XH + (unsigned)i * 1024u, 0, 16);
    u32x4 Q[2][4], K[2][4], XM[4];
    const unsigned ro = (unsigned)lane * 16u;
#pragma unroll
    for (int t = 0; t < 2; ++t)
#pragma unroll
        for (int k = 0; k < 4; ++k) { Q[t][k] = ld16_sc1(qr, ro + k * 1024, (unsigned)t * 4096u); K[t][k] = ld16_sc1(qr, ro + k * 1024, (unsigned)t * 4096u + 32u * XH); }
    const unsigned qwin = km_win(s, h);
    u32x4 VM[2];
    if (POLL) {
        wait_count<4>((unsigned*)(p.ws + WS_CTL), C_PROG, (unsigned)s + (s < 56 ? 2u : 1u));
        if (s >= 56) __builtin_amdgcn_s_sleep(16);
    }
    for (unsigned spins = 0;;) {
        asm volatile("" ::: "memory");
#pragma unroll
        for (int k = 0; k < 4; ++k) XM[k] = ld16_sc1(xr, (unsigned)lane * 16u + (k & 1) * 1536, qwin + (k >> 1) * 3072);
#pragma unroll
        for (int ts = 0; ts < 2; ++ts) VM[ts] = ld16_sc1(xr, (unsigned)(g >> 1) * 1536u + 1024u + (unsigned)(lane & 31) * 16u, qwin + ts * 3072);
        if (!POLL || s < 56) break;
        if (all_set(max(max(max(umax4(XM[0]), umax4(XM[1])), max(umax4(XM[2]), umax4(XM[3]))), max(umax4(VM[0]), umax4(VM[1]))))) break;
        __builtin_amdgcn_s_sleep(4);
        if (spin_out(spins, (unsigned*)(p.ws + WS_CTL))) break;
    }
    SCHED();
#pragma unroll
    for (int ts = 0; ts < 2; ++ts) {
        const u32x4 v = VM[ts];
        const int m = 2 * (n >> 2) + (n & 1), hf = (n >> 1) & 1;
#pragma unroll
        for (int i = 0; i < 4; ++i) { const int e = 64 * ts + 16 * g + 4 * i + 2 * hf;
            *(LAS unsigned*)(my + 8192 + (e >> 3) * 256 + (m ^ (e & 8)) * 16 + (e & 7) * 2) = v[i]; }
    }
    const float csc = 0.08838834764831845f * 1.4426950408889634f, ninf = -__builtin_inff();
    u32x4 PX[2]; u32x2 PM[2];
#pragma unroll
    for (int qt = 0; qt < 2; ++qt) {
        f32x4 sm = {0.f, 0.f, 0.f, 0.f}, s0 = sm, s1 = sm;
#pragma unroll
        for (int k = 0; k < 4; ++k) { sm = mma32(asbf(XM[k]), asbf(Q[qt][k]), sm); s0 = mma32(asbf(K[0][k]), asbf(Q[qt][k]), s0); if (qt == 1) s1 = mma32(asbf(K[1][k]), asbf(Q[qt][k]), s1); }
#pragma unroll
        for (int i = 0; i < 4; ++i) {
            if (g < 2) sm[i] = ninf;
            if (qt == 0) { if (4 * g + i > n) s0[i] = ninf; s1[i] = ninf; }
            else if (4 * g + i > n) s1[i] = ninf;
        }
        float m = fmaxf(fmaxf(fmaxf(s0[0], s0[1]), fmaxf(s0[2], s0[3])), fmaxf(fmaxf(s1[0], s1[1]), fmaxf(s1[2], s1[3])));
        m = fmaxf(m, fmaxf(fmaxf(sm[0], sm[1]), fmaxf(sm[2], sm[3])));
        m = rows_max(m);
        const float mc = -m * csc; float sum = 0.f;
#pragma unroll
        for (int i = 0; i < 4; ++i) { s0[i] = __builtin_amdgcn_exp2f(fmaf(s0[i], csc, mc)); s1[i] = __builtin_amdgcn_exp2f(fmaf(s1[i], csc, mc)); sm[i] = __builtin_amdgcn_exp2f(fmaf(sm[i], csc, mc));
            sum += (s0[i] + s1[i]) + sm[i]; }
        sum = rows_sum(sum);
        const float inv = __builtin_amdgcn_rcpf(sum);
        PX[qt] = (u32x4){pk_bf16(s0[0] * inv, s0[1] * inv), pk_bf16(s0[2] * inv, s0[3] * inv), pk_bf16(s1[0] * inv, s1[1] * inv), pk_bf16(s1[2] * inv, s1[3] * inv)};
        PM[qt] = (u32x2){pk_bf16(sm[0] * inv, sm[1] * inv), pk_bf16(sm[2] * inv, sm[3] * inv)};
    }
    asm volatile("s_waitcnt vmcnt(0) lgkmcnt(0)" ::: "memory");
    const int q4 = n >> 2, p4 = n & 3;
    const ldsp vx = my + (p4 >> 1) * 256 + ((4 * g + q4) ^ ((p4 >> 1) * 8)) * 16 + (p4 & 1) * 8, vmm = my + 8192 + (p4 >> 1) * 256 + ((4 * (g & 1) + q4) ^ ((p4 >> 1) * 8)) * 16 + (p4 & 1) * 8;
    const rsrc_t orr = mkrsrc(p.ws + WH(WS_OA + (size_t)s * 524288 + gap((unsigned)h * 4096u)), 524288u);
    const unsigned oo = frag_off(lane);
    bf16x8 VA[8]; s16x4 VMf[8];
#pragma unroll
    for (int et = 0; et < 8; ++et) { VA[et] = cat8(trd(vx + et * 512), trd(vx + 4096 + et * 512)); VMf[et] = trd(vmm + et * 512); }
    SCHED();
#pragma unroll
    for (int ep = 0; ep < 4; ++ep) {
        f32x4 d[2][2];
#pragma unroll
        for (int e2 = 0; e2 < 2; ++e2)
#pragma unroll
            for (int qt = 0; qt < 2; ++qt) d[qt][e2] = mma16(__builtin_bit_cast(u32x2, VMf[2 * ep + e2]), PM[qt], mma32(VA[2 * ep + e2], asbf(PX[qt]), (f32x4){0.f, 0.f, 0.f, 0.f}));
#pragma unroll
        for (int qt = 0; qt < 2; ++qt) store_frag(orr, oo + ep * 1024, (unsigned)qt * 262144u, d[qt][0], d[qt][1]);
    }
    asm volatile("s_waitcnt lgkmcnt(0)" ::: "memory");
}

DI void attn_b_item(const Params& p, unsigned par, int hq, int qb, ldsp lds) {
    const int tid = fresh_tid(), w = __builtin_amdgcn_readfirstlane(tid >> 6), lane = tid & 63, g = lane >> 4, n = lane & 15;
    const int q0 = qb * 128;
    const rsrc_t br = mkrsrc(p.ws + WH(WS_QKVB + (size_t)hq * 4 * BH), 68u * BH);
    const rsrc_t orr = mkrsrc(p.ws + WH(WS_OB + (size_t)qb * 524288), 524288u);
    constexpr int BUF = 57344;
    unsigned to[3];
#pragma unroll
    for (int i = 0; i < 3; ++i) to[i] = gap((unsigned)max(8 * qb - 16 + w * 3 + i, 0) * 1024u);
    const unsigned l16 = (unsigned)lane * 16u, vsw = (unsigned)(g * 256 + (n ^ ((g & 1) * 8)) * 16);
    auto stage = [&](int i) {
        const unsigned so = (unsigned)i * BH; ldsp d = lds + (i & 1) * BUF;
#pragma unroll
        for (int k = 0; k < 3; ++k) {
            __builtin_amdgcn_raw_ptr_buffer_load_lds(br, (LAS void*)(d + (w * 3 + k) * 1024), 16, l16, so + 32u * BH + to[k], 0, 16);
            __builtin_amdgcn_raw_ptr_buffer_load_lds(br, (LAS void*)(d + 24576 + (w * 3 + k) * 1024), 16, vsw, so + 64u * BH + to[k], 0, 16);
        }
        __builtin_amdgcn_raw_ptr_buffer_load_lds(br, (LAS void*)(d + 49152 + w * 1024), 16, l16, so + (unsigned)(qb * 16384 + w * 1024), 0, 16);
    };
    const float ninf = -__builtin_inff(), csc = 0.17677669529663687f * 1.4426950408889634f;
    const int tmin = (256 - q0 - 16 * w) >> 4;
    const int q4 = n >> 2, p4 = n & 3;
    const int kq = w * 1024 + lane * 16;
    const unsigned vb = (unsigned)(size_t)lds + 24576u + (unsigned)(w * 1024 + (p4 >> 1) * 256 + ((4 * g + q4) ^ ((p4 >> 1) * 8)) * 16 + (p4 & 1) * 8);
    stage(0);
#pragma unroll 1
    for (int i = 0; i < 4; ++i) {
        if (i < 3) { stage(i + 1); WAIT_V(7); } else WAIT_V(0);
        BAR(); asm volatile("" ::: "memory");
        const ldsp bp = lds + (i & 1) * BUF;
        const bf16x8 Q = *(const LAS bf16x8*)(bp + 49152 + kq);
        f32x4 sc[17]; bf16x8 KF[17];
#pragma unroll
        for (int t = 0; t < 17; ++t) KF[t] = *(const LAS bf16x8*)(bp + kq + t * 1024);
        SCHED();
#pragma unroll
        for (int t = 0; t < 17; ++t) sc[t] = mma32(KF[t], Q, (f32x4){0.f, 0.f, 0.f, 0.f});
#pragma unroll
        for (int k = 0; k < 4; ++k) { if (4 * g + k <= n) sc[0][k] = ninf; if (4 * g + k > n) sc[16][k] = ninf; }
        if (tmin > 0) {
#pragma unroll
            for (int t = 0; t < 16; ++t) if (t < tmin) sc[t] = (f32x4){ninf, ninf, ninf, ninf};
        }
        float m = max3f(sc[16][0], sc[16][1], max3f(sc[16][2], sc[16][3], ninf));
#pragma unroll
        for (int t = 0; t < 16; ++t) m = max3f(max3f(m, sc[t][0], sc[t][1]), sc[t][2], sc[t][3]);
        m = rows_max(m);
        const float mc = -m * csc;
        f32x4 sum4 = {0.f, 0.f, 0.f, 0.f};
#pragma unroll
        for (int t = 0; t < 17; ++t) {
#pragma unroll
            for (int k = 0; k < 4; ++k) sc[t][k] = __builtin_amdgcn_exp2f(fmaf(sc[t][k], csc, mc));
            sum4 += sc[t];
        }
        const float inv = __builtin_amdgcn_rcpf(rows_sum((sum4[0] + sum4[1]) + (sum4[2] + sum4[3])));
        const unsigned v0 = vb + (unsigned)((i & 1) * BUF);
        s16x4 V0[17], V1[17];
#define VRD(t) TRD(V0[t], v0, (t) * 1024); TRD(V1[t], v0, (t) * 1024 + 512)
        VRD(0); VRD(1); VRD(2); VRD(3); VRD(4); VRD(5); VRD(6); VRD(7); VRD(8); VRD(9); VRD(10); VRD(11); VRD(12); VRD(13); VRD(14); VRD(15); VRD(16);
#undef VRD
        u32x2 P[17];
#pragma unroll
        for (int t = 0; t < 17; ++t) P[t] = (u32x2){pk_bf16(sc[t][0], sc[t][1]), pk_bf16(sc[t][2], sc[t][3])};
        WAIT_L(0); SCHED();
        f32x4 d0 = {0.f, 0.f, 0.f, 0.f}, d1 = d0;
#pragma unroll
        for (int tp = 0; tp < 8; ++tp) {
            const bf16x8 PP = asbf((u32x4){P[2 * tp].x, P[2 * tp].y, P[2 * tp + 1].x, P[2 * tp + 1].y});
            d0 = mma32(cat8(V0[2 * tp], V0[2 * tp + 1]), PP, d0); d1 = mma32(cat8(V1[2 * tp], V1[2 * tp + 1]), PP, d1);
        }
        d0 = mma16(__builtin_bit_cast(u32x2, V0[16]), P[16], d0); d1 = mma16(__builtin_bit_cast(u32x2, V1[16]), P[16], d1);
        BAR();
        store_frag(orr, frag_off(lane), (unsigned)w * 65536u + gap((unsigned)(hq * 4 + i) * 1024u), d0 * inv, d1 * inv);
    }
    WAIT_V(0);
    signal((unsigned*)(p.ws + WS_CTL), C_OB + qb * 32, 1);
}

DI void out_tail(const Params& p, unsigned par, int c, int q, ldsp lds) {
    const int tid = fresh_tid(), w = __builtin_amdgcn_readfirstlane(tid >> 6), lane = tid & 63, g = lane >> 4, n = lane & 15;
    unsigned* ctl = (unsigned*)(p.ws + WS_CTL);
    bf16x8 WT[16][2];
    {
        ldsp my = lds + w * 16384;
        const int toff = (8 * g + (n >> 2)) * 64 + (n & 3) * 8;
#pragma unroll
        for (int r = 0; r < 2; ++r) {
#pragma unroll
            for (int i = 0; i < 16; ++i)
                __builtin_amdgcn_global_load_lds((const unsigned*)(p.wo + (size_t)(512 * w + 256 * r + 16 * i + (lane >> 2)) * 1024 + 32 * c + (lane & 3) * 8), (LAS unsigned*)(my + i * 1024), 16, 0, 0);
            WAIT_V(0);
#pragma unroll
            for (int k = 0; k < 8; ++k)
#pragma unroll
                for (int ct = 0; ct < 2; ++ct) { WT[8 * r + k][ct] = cat8(trd(my + k * 2048 + ct * 32 + toff), trd(my + k * 2048 + ct * 32 + toff + 256)); }
#pragma unroll
            for (int k = 0; k < 8; ++k) asm volatile("" : "+v"(WT[8 * r + k][0]), "+v"(WT[8 * r + k][1]));
            WAIT_L(0);
        }
    }
    BAR();
#pragma unroll 1
    for (int hs = q; hs < 32; hs += 3) {
        const int s = 48 + (hs >> 1), rt = hs & 1;
        if (w == 0) wait_count<3>(ctl, C_OS + (s - 48) * 256 + (c >> 3) * 64, 4u);
        asm volatile("" ::: "memory"); BAR(); asm volatile("" ::: "memory");
        const rsrc_t ar = mkrsrc(p.ws + WH(WS_OA + (size_t)(2 * s + rt) * 262144 + (size_t)w * 32768), 32768u);
        const unsigned ao = (unsigned)lane * 16u;
        u32x4 A[16];
#pragma unroll
        for (int k = 0; k < 16; ++k) A[k] = ld16_sc1(ar, ao + (k & 3) * 1024, gap((unsigned)(k >> 2) * 4096u));
        SCHED();
#pragma unroll
        for (int ct = 0; ct < 2; ++ct) {
            f32x4 acc = {0.f, 0.f, 0.f, 0.f};
#pragma unroll
            for (int k = 0; k < 16; ++k) acc = mma32(WT[k][ct], asbf(A[k]), acc);
            *(LAS f32x4*)(lds + w * 2048 + ct * 1024 + lane * 16) = acc;
        }
        LDS_BAR();
        if (w < 2) {
            f32x4 r[8];
#pragma unroll
            for (int ww = 0; ww < 8; ++ww) r[ww] = *(const LAS f32x4*)(lds + ww * 2048 + w * 1024 + lane * 16);
            SCHED();
            const f32x4 t = ((r[0] + r[1]) + (r[2] + r[3])) + ((r[4] + r[5]) + (r[6] + r[7]));
            *(u32x2*)(p.out + (size_t)(32 * s + 16 * rt + n) * 2048 + 32 * c + 16 * w + 4 * g) = (u32x2){pk_bf16(t[0], t[1]), pk_bf16(t[2], t[3])};
        }
        WAIT_L(0);
    }
    BAR();
}

DI void big_unit(const Params& p, unsigned par, int id, Unit& u) {
    u.lda = DM * 2; u.nt = 16; u.ak = 128; u.ah = 128 * DM * 2;
    if (id < 384) {
        const int pm = id & 7, pn = id >> 3;
        const u16 *w0 = p.wq, *w1 = p.wk, *w2 = p.wv; const u16* W = pn < 16 ? w0 : (pn < 32 ? w1 : w2);
        u.A = (const char*)(p.ax + (size_t)pm * 256 * DM); u.B = (const char*)(W + (pn & 15) * 256); u.ldb = 4096 * 2;
        u.C = p.ws + WH(WS_QKVX + (size_t)(2 * pn) * XH + (size_t)pm * 131072); u.ldc = 256; u.cw = 1024; u.ch = XH;
        if (pn < 16) { u.sig = C_Q + pm * 32; u.nsig = 1; } else { u.sig = C_KV + 2 * pm * 512 + (pn & 15) * 32; u.nsig = 4; }
    } else if (id < 480) {
        const int pm = (id - 384) & 7, pn = (id - 384) >> 3;
        u.A = (const char*)(p.bx + (size_t)pm * 256 * DM); u.B = (const char*)(p.bwqkv + pn * 256); u.ldb = LDB3 * 2;
        u.C = p.ws + WH(WS_QKVB + (size_t)(8 * pn) * BH + (size_t)pm * 32768); u.ldc = 64; u.cw = BH; u.ch = 4 * BH; u.sig = C_B + pm * 32; u.nsig = 1;
    } else {
        const bool isb = id < 512; const int q = isb ? id - 480 : id - 512, pm = q >> 2, pn = q & 3;
        const u16 *w0 = p.wo, *w1 = p.bwo;
        u.nt = isb ? 16 : 64; u.lda = 0; u.ak = 2048;
        const size_t a0 = (isb ? WS_OB : WS_OA) + (size_t)pm * u.nt * 65536, a1 = a0 + (size_t)u.nt * 32768;
        u.A = p.ws + WH(a0); u.ah = (unsigned)(WH(a1) - WH(a0)); u.B = (const char*)((isb ? w1 : w0) + pn * 256); u.ldb = DM * 2;
        u.C = (char*)(p.out + (size_t)pm * 256 * 2048 + (isb ? 1024 : 0) + pn * 256); u.ldc = 4096; u.cw = 64; u.ch = 256; u.sig = 0; u.nsig = 0;
    }
}
struct BigSched { const Params& p; unsigned par; int id0, id1; DI bool next(int i, Unit& u) const { const int id = i == 0 ? id0 : (i == 1 ? id1 : -1); if (id < 0) return false; big_unit(p, par, id, u); return true; } };

__global__ __launch_bounds__(512, 2) void k_fused(Params p) {
    extern __shared__ __attribute__((aligned(16))) unsigned char shm[];
    ldsp lds = (ldsp)shm;
    unsigned* ctl = (unsigned*)(p.ws + WS_CTL);
    const int b = (blockIdx.x & ~7) | ((blockIdx.x + __builtin_amdgcn_readfirstlane(ctl[C_PHASE])) & 7), id = (b & 3) * 32 + (b >> 3);
#define WSTAMP(code)
    const unsigned par = (unsigned)__builtin_amdgcn_readfirstlane(ctl[C_PAR + 1]) < (unsigned)__builtin_amdgcn_readfirstlane(ctl[C_PAR]) ? 1u : 0u;
    WSTAMP(1);
    if ((b & 7) < 4) {
        chain_role(p, par, id, lds, (unsigned long long*)(p.ws + WS_END));
        BAR();
        WSTAMP(2);
    } else {
#pragma unroll 1
        for (int r = 0; r < 2; ++r) {
            const int rb = 2 * r + (id & 1), ct = id >> 1;
            const u16 *w1 = p.wk, *w2 = p.wv;
            Unit u; u.A = (const char*)(p.ax + (size_t)rb * 128 * DM); u.lda = DM * 2; u.B = (const char*)((ct < 32 ? w1 : w2) + (ct & 31) * 128); u.ldb = 4096 * 2; u.nt = 16;
            u.C = p.ws + WH(WS_QKVX + (size_t)(32 + ct) * XH + (size_t)rb * 65536); u.ldc = 256; u.cw = 1024; u.ch = 0; u.ak = 128; u.ah = 0;
            u.sig = C_KV + rb * 512 + (ct & 31) * 16; u.nsig = 1;
            gemm_small(lds, u, ctl);
            WSTAMP(3);
        }
        {
            const int t = fresh_tid(), wv = __builtin_amdgcn_readfirstlane(t >> 6);
            const char* src; const int c = id & 7, q = id >> 3;
            if (c < 2) src = (const char*)p.ax + (size_t)(q * 2 + c) * 131072; else if (c < 4) src = (const char*)p.bx + (size_t)(q * 2 + c - 2) * 131072;
            else if (c < 7) src = (const char*)p.bwqkv + (size_t)(q * 3 + c - 4) * 131072; else src = (const char*)p.bwo + (size_t)q * 131072;
#pragma unroll
            for (int i = 0; i < 16; ++i) { __builtin_amdgcn_global_load_lds((const unsigned*)(src + i * 8192 + t * 16), (LAS unsigned*)(lds + L_SCR + wv * 1024), 16, 0, 0); if ((i & (KWP - 1)) == KWP - 1) WAIT_V(0); }
            if (KNT1) {
#pragma unroll
                for (int i = 0; i < 8; ++i) { __builtin_amdgcn_global_load_lds((const unsigned*)((const char*)p.wo + (size_t)id * 65536 + i * 8192 + t * 16), (LAS unsigned*)(lds + L_SCR + wv * 1024), 16, 0, 0); if ((i & (KWP - 1)) == KWP - 1) WAIT_V(0); }
            }
        }
    }
    volatile LAS unsigned* qw = (volatile LAS unsigned*)(lds + 131072);
    const int w = __builtin_amdgcn_readfirstlane(threadIdx.x >> 6);
    const int k = b & 3;
    bool fixed = (b & 7) >= 4;
#pragma unroll 1
    for (;;) {
        int kind, x, it = -1, x2 = -1;
        if (fixed) { const int i = id & 31; kind = 6; x = i < 24 ? (16 + 8 * k + (i & 7)) * 8 + 2 + (i >> 3) : (4 * k + (i & 3)) * 8 + ((i >> 2) & 1); x2 = x + (i < 24 ? 3 : 2); fixed = false; }
        else {
        if (threadIdx.x == 0) qw[0] = __hip_atomic_fetch_add(ctl + C_HEAD + k * 32, 1u, RLX_AGENT) + 1u;
        LDS_BAR();
        it = (int)__builtin_amdgcn_readfirstlane(qw[0]);
        LDS_BAR();
        if (it < 4) { kind = 0; x = it * 4 + k; }
        else if (it < 28) { kind = 6; x = 384 + 24 * k + it - 4; }
        else if (it < 36) { kind = 6; x = (4 * k + ((it - 28) & 3)) * 8 + 4 + ((it - 28) >> 2); }
        else if (it < 38) { kind = 6; x = 512 + (it - 36) * 4 + k; }
        else if (it < 42) { kind = 0; x = (it - 38 + 4) * 4 + k; }
        else if (it < 74) { kind = 1; x = ((it - 42) >> 1) * 8 + 2 * k + ((it - 42) & 1); }
        else if (it < 82) { kind = 6; x = (4 * k + ((it - 74) & 3)) * 8 + 6 + ((it - 74) >> 2); }
        else if (it < 86) { kind = 4; x = (it - 82) * 4 + k; }
        else if (it < 94) { kind = 6; x = 480 + (it - 86) * 4 + k; }
        else if (it < 110) { kind = 2; x = (4 + ((it - 94) >> 1)) * 8 + 2 * k + ((it - 94) & 1); }
        else if (it < 134) { kind = 5; x = ((it - 110) >> 3) * 32 + 8 * k + ((it - 110) & 7); }
        else break;
        }
        if (kind == 6) {
            if (x >= 480) { const int w0 = x < 512 ? C_OB + ((x - 480) >> 2) * 64 : C_OA + ((x - 512) >> 2) * 64; const unsigned need = x < 512 ? 64u : 32u; wg_wait(ctl, w0, need, w0 + 32, need); }
            BigSched S{p, par, x, x2};
            gemm_stream(lds, S, ctl);
        } else
        if (kind == 0) {
            const int s0 = (x >> 2) * 4;
            wait_count(ctl, C_Q + (s0 >> 3) * 32, 128u); wait_count(ctl, C_PROG, (unsigned)s0 + 5u);
            asm volatile("" ::: "memory");
#pragma unroll 1
            for (int s = s0; s < s0 + 4; ++s) attn_a_item<false>(p, par, s, (x & 3) * 8 + w, lds + w * 12288, fresh_tid() & 63);
            WAIT_V(0); signal(ctl, C_OA + (x >> 2) * 32, 1);
            BAR();
        } else if (kind == 4) {
#pragma unroll 1
            for (int s = 32 + (x >> 2); s < NSEG; s += 4) {
                wait_count(ctl, C_Q + (s >> 3) * 32, 128u); wait_count(ctl, C_KV + (s >> 2) * 512 + ((x & 3) * 8 + w) * 16, 16u);
                asm volatile("" ::: "memory");
                attn_a_item<true>(p, par, s, (x & 3) * 8 + w, lds + w * 12288, fresh_tid() & 63);
                WAIT_V(0);
                if (s < 48) signal(ctl, C_OA + (s >> 2) * 32, 1);
                else { BAR(); const int l = fresh_tid(); if (l < 4) __hip_atomic_fetch_add(ctl + C_OS + (s - 48) * 256 + l * 64, 1u, RLX_AGENT); }
            }
            BAR();
        } else if (kind == 1) {
            const int qb = x >> 3;
            wait_count(ctl, C_B + (qb >> 1) * 32, 96u); wait_count(ctl, C_B + max((qb >> 1) - 1, 0) * 32, 96u);
            asm volatile("" ::: "memory");
            attn_b_item(p, par, x & 7, qb, lds);
        } else if (kind == 5) {
            out_tail(p, par, x & 31, x >> 5, lds);
        } else {
            const int rb = x >> 3, ct = x & 7;
            Unit u; u.ldb = DM * 2; u.ldc = 2048 * 2; u.sig = 0; u.nsig = 0; u.lda = 0; u.ak = 2048; u.ah = 0; u.cw = 64; u.ch = 0;
            u.A = p.ws + WH(WS_OA + (size_t)rb * 2097152); u.B = (const char*)(p.wo + ct * 128); u.nt = 64; u.C = (char*)(p.out + (size_t)rb * 128 * 2048 + ct * 128);
            wg_wait(ctl, C_OA + rb * 32, rb < 8 ? 32u : 128u, C_OA + rb * 32, 32u);
            gemm_small(lds, u, ctl);
        }
        WSTAMP(16 + (kind == 6 && x >= 480 ? (x < 512 ? 7 : 8) : kind) + ((it & 0xffff) << 8));
    }
}

__global__ __launch_bounds__(512) void k_init(char* ws) {
    const rsrc_t xr = mkrsrc(ws + WS_XCH, NWIN_INIT * 16384u);
    unsigned* ctl = (unsigned*)(ws + WS_CTL);
    const u32x4 sv = {SENT, SENT, SENT, SENT};
    const unsigned xcc = (unsigned)__builtin_amdgcn_s_getreg(20 | (3 << 11)) & 7u, half = blockIdx.x >> 3, win = blockIdx.x & 7;
    if (threadIdx.x < 64 && xcc == 1) {
        unsigned b = ~0u;
        for (int i = 0; i < 5; ++i) {
            const unsigned long long t0 = __builtin_amdgcn_s_memtime();
            __builtin_amdgcn_raw_buffer_store_b32(0u, xr, (unsigned)(2 * 16384 + C_PROBE * 4) + half * 8192u + (unsigned)i * 256u, 0, 16);
            asm volatile("s_waitcnt vmcnt(0)" ::: "memory");
            b = min(b, (unsigned)(__builtin_amdgcn_s_memtime() - t0));
        }
        if (threadIdx.x == 0) ctl[C_PAR + half] = b;
    }
    __syncthreads();
    if (win < 7 && win != 5) ST16(sv, xr, win * 16384u + half * 8192u + threadIdx.x * 16u, 0, 16);
    if (blockIdx.x == 0 && threadIdx.x == 0) ctl[C_PHASE] = xcc;
}

extern "C" void kernel_launch(void* const* d_in, const int* in_sizes, int n_in, void* d_out, int out_size, void* d_ws, size_t ws_size, hipStream_t stream) {
    Params p;
    p.ax = (const u16*)d_in[0]; p.mem0 = (const u16*)d_in[1]; p.wq = (const u16*)d_in[2]; p.wk = (const u16*)d_in[3]; p.wv = (const u16*)d_in[4]; p.wo = (const u16*)d_in[5];
    p.bx = (const u16*)d_in[7]; p.bwqkv = (const u16*)d_in[8]; p.bwo = (const u16*)d_in[9];
    p.out = (u16*)d_out; p.ws = (char*)(((uintptr_t)d_ws + WS_SHIFT(ws_size) + 0x1FFFFFu) & ~(uintptr_t)0x1FFFFFu);
    (void)hipFuncSetAttribute((const void*)k_fused, hipFuncAttributeMaxDynamicSharedMemorySize, LDS_BYTES);
    k_init<<<16, 512, 0, stream>>>(p.ws);
    k_fused<<<256, 512, LDS_BYTES, stream>>>(p);
}
```

```cpp
#include <hip/hip_runtime.h>
#include <stdint.h>
#include <stdlib.h>

#define LAS __attribute__((address_space(3)))
#define DI __device__ __forceinline__
typedef unsigned short u16;
typedef __attribute__((ext_vector_type(8))) __bf16 bf16x8;
typedef __attribute__((ext_vector_type(16))) __bf16 bf16x16;
typedef __attribute__((ext_vector_type(2))) __bf16 bf16x2;
typedef __attribute__((ext_vector_type(4))) short s16x4;
typedef __attribute__((ext_vector_type(8))) short s16x8;
typedef __attribute__((ext_vector_type(16))) short s16x16;
typedef __attribute__((ext_vector_type(2))) float f32x2;
typedef __attribute__((ext_vector_type(4))) float f32x4;
typedef __attribute__((ext_vector_type(2))) unsigned u32x2;
typedef __attribute__((ext_vector_type(4))) unsigned u32x4;
typedef LAS unsigned char* ldsp;
typedef __amdgpu_buffer_rsrc_t rsrc_t;

constexpr int S_LEN = 2048, DM = 1024, NH = 32, DH = 128, NSEG = 64;
constexpr int LDB3 = 3072;
constexpr unsigned XH = S_LEN * 512, BH = S_LEN * 128;
constexpr unsigned SENT = 0xFFFFFFFFu;
constexpr size_t WS_XCH = 2u << 20;
constexpr int NWIN = 640, NWIN_INIT = 7;
constexpr size_t XCH_BYTES = (size_t)(NWIN + 2048 + 512) * 16384;
constexpr size_t WS_CTL = WS_XCH + 2 * 16384;
constexpr int C_TMO = 0, C_PAR = 13312  , C_PHASE = 13314, C_HEAD = 3072, C_PROG = 192, C_PACE = 128, C_Q = 256, C_B = C_Q + 8 * 32, C_OA = C_B + 8 * 32, C_OB = C_OA + 16 * 32, C_OS = 16384, C_KV = 4096, C_PROBE = 12288;
constexpr size_t WS_QKVX = 52u << 20;
constexpr size_t WS_QKVB = WS_QKVX + (size_t)96 * XH;
constexpr size_t WS_OA = WS_QKVB + (size_t)96 * BH;
constexpr size_t WS_OB = WS_OA + (size_t)S_LEN * 4096 * 4;
constexpr size_t WS_END = WS_OB + (size_t)S_LEN * 1024 * 4;
#define gap(L) (((L) & 8191u) | (((L) >> 13) << 14))
#define WH(off) ((off) + (size_t)((((unsigned)((off) >> 21) ^ par ^ KWH) & 1u) << 13))
#define KWH 0
#define WS_SHIFT(sz) 0
constexpr int LDS_BYTES = 131072 + 1024 + 8192;

DI unsigned pk_bf16(float a, float b) { f32x2 v = {a, b}; return __builtin_bit_cast(unsigned, __builtin_convertvector(v, bf16x2)); }
DI s16x4 trd(ldsp p) { return __builtin_amdgcn_ds_read_tr16_b64_v4i16((LAS s16x4*)p); }
DI bf16x8 cat8(s16x4 a, s16x4 b) { return __builtin_bit_cast(bf16x8, __builtin_shufflevector(a, b, 0, 1, 2, 3, 4, 5, 6, 7)); }
DI bf16x16 cat16(bf16x8 a, bf16x8 b) { return __builtin_shufflevector(a, b, 0, 1, 2, 3, 4, 5, 6, 7, 8, 9, 10, 11, 12, 13, 14, 15); }
DI bf16x8 lo8(bf16x16 v) { return __builtin_shufflevector(v, v, 0, 1, 2, 3, 4, 5, 6, 7); }
DI bf16x8 hi8(bf16x16 v) { return __builtin_shufflevector(v, v, 8, 9, 10, 11, 12, 13, 14, 15); }
DI bf16x8 asbf(u32x4 v) { return __builtin_bit_cast(bf16x8, v); }
DI f32x4 mma32(bf16x8 a, bf16x8 b, f32x4 c) { return __builtin_amdgcn_mfma_f32_16x16x32_bf16(a, b, c, 0, 0, 0); }
DI f32x4 mma16(u32x2 a, u32x2 b, f32x4 c) { return mma32(asbf((u32x4){a.x, a.y, 0u, 0u}), asbf((u32x4){b.x, b.y, 0u, 0u}), c); }
DI f32x4 smma(u32x4 a, bf16x16 b, f32x4 c, int idx) { return __builtin_amdgcn_smfmac_f32_16x16x64_bf16(asbf(a), b, c, idx, 0, 0); }
DI rsrc_t mkrsrc(const void* p, unsigned bytes) { return __builtin_amdgcn_make_buffer_rsrc((void*)p, 0, bytes, 0x00020000); }
DI u32x4 ld16_sc1(rsrc_t r, unsigned voff, unsigned soff) { return __builtin_amdgcn_raw_buffer_load_b128(r, voff, soff, 16); }
#define ST16(v, r, voff, soff, aux) do { const u32x4 st_v = (v); __builtin_amdgcn_raw_buffer_store_b128(st_v, r, voff, soff, aux); asm volatile("s_nop 1" :: "v"(st_v)); } while (0)
DI void swap16(unsigned& a, unsigned& b) { auto r = __builtin_amdgcn_permlane16_swap(a, b, false, false); a = r[0]; b = r[1]; }
DI void swap32(unsigned& a, unsigned& b) { auto r = __builtin_amdgcn_permlane32_swap(a, b, false, false); a = r[0]; b = r[1]; }
DI void swap16f(float& a, float& b) { unsigned x = __float_as_uint(a), y = __float_as_uint(b); swap16(x, y); a = __uint_as_float(x); b = __uint_as_float(y); }
DI void swap32f(float& a, float& b) { unsigned x = __float_as_uint(a), y = __float_as_uint(b); swap32(x, y); a = __uint_as_float(x); b = __uint_as_float(y); }
DI float rows_max(float v) { float a = v, b = v; swap16f(a, b); v = fmaxf(a, b); a = v; b = v; swap32f(a, b); return fmaxf(a, b); }
DI float rows_sum(float v) { float a = v, b = v; swap16f(a, b); v = a + b; a = v; b = v; swap32f(a, b); return a + b; }
DI unsigned umax4(u32x4 v) { return max(max(v.x, v.y), max(v.z, v.w)); }
#define WAIT_V(n) asm volatile("s_waitcnt vmcnt(" #n ")" ::: "memory")
#define WAIT_L(n) asm volatile("s_waitcnt lgkmcnt(" #n ")" ::: "memory")
#define BAR() __builtin_amdgcn_s_barrier()
#define SCHED() __builtin_amdgcn_sched_barrier(0)
#define LDS_BAR() do { asm volatile("s_waitcnt lgkmcnt(0)" ::: "memory"); __builtin_amdgcn_s_barrier(); asm volatile("" ::: "memory"); } while (0)

constexpr int HTB = 128 * 64 * 2;
DI int lds_byte(int r, int c) { const int st = (r >> 4) * 2 + (c >> 5), rr = r & 15, cc = c & 31, ob = rr * 64 + cc * 2; return st * 1024 + (ob ^ (((ob >> 9) & 1) << 5)); }
DI void stage_rc(int b, int& R, int& C) { const int st = b / 1024, sb = b % 1024, swz = sb ^ (((sb >> 9) & 1) << 5); R = (st >> 1) * 16 + swz / 64; C = (st & 1) * 32 + (swz % 64) / 2; }

struct Params {
    const u16 *ax, *mem0, *wq, *wk, *wv, *wo, *bx, *bwqkv, *bwo;
    u16* out; char* ws;
};
#define RLX_AGENT __ATOMIC_RELAXED, __HIP_MEMORY_SCOPE_AGENT
DI bool spin_out(unsigned& spins, unsigned* ctl) {
    spins = __builtin_amdgcn_readfirstlane(spins) + 1u;
    if ((spins & 63u) == 0u) {
        if (spins > (1u << 18)) { __hip_atomic_store(ctl + C_TMO, 1u, RLX_AGENT); return true; }
        if (__builtin_amdgcn_readfirstlane(__hip_atomic_load(ctl + C_TMO, RLX_AGENT)) != SENT) return true;
    }
    return false;
}
template <int NAP = 8>
DI void wait_count(unsigned* ctl, int word, unsigned need) {
    unsigned spins = 0;
    while (__builtin_amdgcn_readfirstlane(__hip_atomic_load(ctl + word, RLX_AGENT)) + 1u < need) { __builtin_amdgcn_s_sleep(NAP); if (spin_out(spins, ctl)) break; }
}
DI void wg_wait(unsigned* ctl, int word0, unsigned need0, int word1, unsigned need1) {
    if (threadIdx.x < 64) {
        wait_count(ctl, word0, need0); wait_count(ctl, word1, need1);
        __builtin_amdgcn_fence(__ATOMIC_ACQUIRE, "agent");
        asm volatile("s_waitcnt vmcnt(0)" ::: "memory");
    }
    asm volatile("" ::: "memory"); BAR(); asm volatile("" ::: "memory");
}
struct Unit { const char* A; const char* B; char* C; unsigned lda, ldb, ldc, cw, ch, ak, ah; int nt, sig, nsig; };
DI int fresh_tid() { int t = threadIdx.x; asm volatile("" : "+v"(t)); return t; }
DI void signal(unsigned* ctl, int sig, int nsig) { const int lane = fresh_tid() & 63; if (lane < nsig) __hip_atomic_fetch_add(ctl + sig + (lane & 1) * 16 + (lane >> 1) * 512, 1u, RLX_AGENT); }
DI u32x4 pack_pair(f32x4 v0, f32x4 v1) {
    unsigned ax = pk_bf16(v0[0], v0[1]), ay = pk_bf16(v0[2], v0[3]), bx = pk_bf16(v1[0], v1[1]), by = pk_bf16(v1[2], v1[3]);
    swap16(ax, bx); swap16(ay, by);
    return (u32x4){ax, ay, bx, by};
}
DI unsigned frag_off(int lane) { return (unsigned)((((lane >> 4) & 1) * 2 + (lane >> 5)) * 256 + (lane & 15) * 16); }
DI void store_frag(rsrc_t cr, unsigned voff, unsigned soff, f32x4 v0, f32x4 v1) { ST16(pack_pair(v0, v1), cr, voff, soff, 16); }
DI u32x4 to_rows(ldsp sc, int lane, f32x4 v0, f32x4 v1) {
    *(volatile LAS u32x4*)(sc + ((lane & 15) * 4 + ((lane >> 4) & 1) * 2 + (lane >> 5)) * 16) = pack_pair(v0, v1);
    return *(volatile LAS u32x4*)(sc + lane * 16);
}
constexpr int L_SCR = 132096;
#define STAGE(bufoff, gbase, voff, step2) do { \
        __builtin_amdgcn_global_load_lds((const unsigned*)((gbase) + (voff)), (LAS unsigned*)(lds + (bufoff) + ldsw), 16, 0, 0); \
        __builtin_amdgcn_global_load_lds((const unsigned*)((gbase) + (step2) + (voff)), (LAS unsigned*)(lds + (bufoff) + ldsw + 8192), 16, 0, 0); } while (0)
#define LDA(dst, off) do { _Pragma("unroll") for (int m = 0; m < 4; ++m) _Pragma("unroll") for (int k = 0; k < 2; ++k) dst[m][k] = *(const LAS bf16x8*)(lds + (off) + aoff + m * 2048 + k * 1024); } while (0)
#define TRD(dst, base, off) asm volatile("ds_read_b64_tr_b16 %0, %1 offset:%2" : "=v"(dst) : "v"(base), "i"(off) : "memory")
#define LDB(dst, v0, v1, off) do { s16x4 l0, h0, l1, h1, l2, h2, l3, h3; \
        TRD(l0, v0, (off)); TRD(h0, v0, (off) + 256); TRD(l1, v0, (off) + 8192); TRD(h1, v0, (off) + 8448); \
        TRD(l2, v1, (off)); TRD(h2, v1, (off) + 256); TRD(l3, v1, (off) + 8192); TRD(h3, v1, (off) + 8448); \
        dst[0][0] = cat8(l0, h0); dst[0][1] = cat8(l1, h1); dst[1][0] = cat8(l2, h2); dst[1][1] = cat8(l3, h3); } while (0)
#define MMA(accv, At, Bt) do { SCHED(); __builtin_amdgcn_s_setprio(1); _Pragma("unroll") for (int m = 0; m < 4; ++m) _Pragma("unroll") for (int n = 0; n < 2; ++n) _Pragma("unroll") for (int k = 0; k < 2; ++k) \
        accv[m][n] = mma32(Bt[n][k], At[m][k], accv[m][n]); __builtin_amdgcn_s_setprio(0); } while (0)
DI unsigned va_of(int tid, unsigned lda, int nt) { int R, C; stage_rc(tid * 16, R, C); return lda ? (unsigned)R * lda + (unsigned)C * 2u : (unsigned)((tid >> 7) * nt * 4096 + (tid & 127) * 16); }
DI unsigned vb_of(int tid, int wid, unsigned ldb) { return (unsigned)(8 * (wid >> 1) + ((tid >> 2) & 7)) * ldb + (unsigned)((2 * (wid & 1) + ((tid >> 5) & 1)) * 64 + (((tid & 3) ^ (((wid >> 1) & 1) << 1)) * 16)); }
#define GEMM_LANE_SETUP(lda0) \
    const int tid = fresh_tid(); \
    const int wid = __builtin_amdgcn_readfirstlane(tid >> 6), lane = tid & 63, wr = wid >> 2, wc = wid & 3, fr = lane & 15, fq = lane >> 4; \
    const unsigned ldsw = (unsigned)wid * 1024u; \
    const int aoff = (lda0) ? lds_byte(wr * 64 + fr, fq * 8) : wr * 8192 + lane * 16; \
    const int boff0 = fq * 2048 + wc * 512 + (fr >> 2) * 64 + (fq & 1) * 32 + (fr & 3) * 8, boff1 = boff0 ^ 32;

template <class Sched>
DI void gemm_stream(ldsp lds, const Sched& S, unsigned* ctl) {
    Unit cur, nxt; int ui = 0;
    if (!S.next(0, cur)) return;
    GEMM_LANE_SETUP(cur.lda);
#define SA(b, h) (((b) * 2 + (h)) * HTB)
#define SB(b, h) ((4 + (b) * 2 + (h)) * HTB)
    const unsigned vb0 = (unsigned)(size_t)lds + 65536u + (unsigned)boff0, vb1 = (unsigned)(size_t)lds + 65536u + (unsigned)boff1;
    f32x4 acc[2][2][4][2];
#pragma unroll
    for (int a = 0; a < 2; ++a)
#pragma unroll
        for (int b = 0; b < 2; ++b)
#pragma unroll
            for (int m = 0; m < 4; ++m)
#pragma unroll
                for (int n = 0; n < 2; ++n) acc[a][b][m][n] = (f32x4){0.f, 0.f, 0.f, 0.f};
    bf16x8 At[4][2], B0[2][2], B1[2][2];
    const unsigned vA = va_of(tid, cur.lda, cur.nt); unsigned vB = vb_of(tid, wid, cur.ldb);
    const size_t a2s = cur.lda ? (size_t)64 * cur.lda : (size_t)cur.nt * 16384;
    {
        const size_t ah = cur.ah, b2s = (size_t)32 * cur.ldb, bk = (size_t)64 * cur.ldb;
        STAGE(SB(0, 0), cur.B, vB, b2s); STAGE(SB(0, 1), cur.B + 256, vB, b2s); STAGE(SA(0, 0), cur.A, vA, a2s); STAGE(SA(0, 1), cur.A + ah, vA, a2s);
        if (wr == 1) BAR();
        WAIT_V(2); BAR();
        STAGE(SB(1, 0), cur.B + bk, vB, b2s); STAGE(SA(1, 0), cur.A + cur.ak, vA, a2s); STAGE(SB(1, 1), cur.B + bk + 256, vB, b2s);
        WAIT_V(6); BAR();
    }
    int psig = 0, pn = 0;
    for (;;) {
        const bool has_next = S.next(ui + 1, nxt);
        if (!has_next) nxt = cur;
        int nt = cur.nt; asm volatile("" : "+s"(nt));
        for (int t = 0; t < nt; t += 2) {
            const bool last = (t == nt - 2);
            const char* a1 = cur.A + (cur.lda ? (unsigned)(t + 1) * 128u : gap((unsigned)(t + 1) * 2048u)) + cur.ah;
            if (last) vB = vb_of(fresh_tid(), wid, nxt.ldb);
            const unsigned ldb = last ? nxt.ldb : cur.ldb;
            const char* a2 = last ? nxt.A : cur.A + (cur.lda ? (unsigned)(t + 2) * 128u : gap((unsigned)(t + 2) * 2048u));
            const char* b2 = last ? nxt.B : cur.B + (size_t)(t + 2) * 64 * cur.ldb;
            const size_t ah = cur.ah, b2s = (size_t)32 * ldb;
            const char* a3 = a2 + cur.ak; const char* b3 = b2 + (size_t)64 * ldb;
            LDB(B0, vb0, vb1, 0); LDB(B1, vb0, vb1, HTB); SCHED(); LDA(At, SA(0, 0)); STAGE(SA(1, 1), a1, vA, a2s);
            WAIT_V(8); WAIT_L(0); BAR(); MMA(acc[0][0], At, B0); MMA(acc[0][1], At, B1); BAR(); SCHED();
            LDA(At, SA(0, 1)); STAGE(SB(0, 0), b2, vB, b2s); STAGE(SB(0, 1), b2 + 256, vB, b2s); STAGE(SA(0, 0), a2, vA, a2s);
            WAIT_V(8);
            asm volatile("" : "+s"(pn));
            if (pn) { signal(ctl, psig, pn); pn = 0; }
            WAIT_L(0); BAR(); MMA(acc[1][0], At, B0); MMA(acc[1][1], At, B1); BAR(); SCHED();
            LDB(B0, vb0, vb1, 2 * HTB); LDB(B1, vb0, vb1, 3 * HTB); SCHED(); LDA(At, SA(1, 0)); STAGE(SA(0, 1), a2 + ah, vA, a2s);
            WAIT_V(8); WAIT_L(0); BAR(); MMA(acc[0][0], At, B0); MMA(acc[0][1], At, B1); BAR(); SCHED();
            LDA(At, SA(1, 1)); STAGE(SB(1, 0), b3, vB, b2s); STAGE(SB(1, 1), b3 + 256, vB, b2s); STAGE(SA(1, 0), a3, vA, a2s);
            WAIT_V(8); WAIT_L(0); BAR(); MMA(acc[1][0], At, B0); MMA(acc[1][1], At, B1); BAR(); SCHED();
        }
        if (wr == 0) BAR();
        {
            const rsrc_t cr = mkrsrc(cur.C, 0x1000000u);
            const int tz = fresh_tid() & 63;
            if (cur.nsig) {
                const unsigned co = (unsigned)wc * cur.cw + frag_off(tz);
#pragma unroll
                for (int ai = 0; ai < 2; ++ai)
#pragma unroll
                    for (int m = 0; m < 4; ++m)
#pragma unroll
                        for (int bj = 0; bj < 2; ++bj) store_frag(cr, co, gap((unsigned)(ai * 128 + wr * 64 + m * 16) * cur.ldc) + (unsigned)bj * cur.ch, acc[ai][bj][m][0], acc[ai][bj][m][1]);
            } else {
                const unsigned co = (unsigned)(wr * 64 + (tz >> 2)) * cur.ldc + (unsigned)(wc * 64 + (tz & 3) * 16);
#pragma unroll
                for (int ai = 0; ai < 2; ++ai)
#pragma unroll
                    for (int m = 0; m < 4; ++m)
#pragma unroll
                        for (int bj = 0; bj < 2; ++bj)
                            ST16(to_rows(lds + L_SCR + wid * 1024, tz, acc[ai][bj][m][0], acc[ai][bj][m][1]), cr, co, (unsigned)(ai * 128 + m * 16) * cur.ldc + (unsigned)bj * 256u, 0);
            }
        }
        psig = cur.sig; pn = cur.nsig;
        if (!has_next) break;
#pragma unroll
        for (int a = 0; a < 2; ++a)
#pragma unroll
            for (int b = 0; b < 2; ++b)
#pragma unroll
                for (int m = 0; m < 4; ++m)
#pragma unroll
                    for (int n = 0; n < 2; ++n) acc[a][b][m][n] = (f32x4){0.f, 0.f, 0.f, 0.f};
        cur = nxt; ++ui;
        if (wr == 1) BAR();
    }
    WAIT_V(0);
    signal(ctl, psig, pn);
    BAR();
#undef SA
#undef SB
}

DI void gemm_small(ldsp lds, const Unit u, unsigned* ctl) {
    GEMM_LANE_SETUP(u.lda);
    const unsigned vA = va_of(tid, u.lda, u.nt), vB = vb_of(tid, wid, u.ldb);
    const size_t a2s = u.lda ? (size_t)64 * u.lda : (size_t)u.nt * 16384, b2s = (size_t)32 * u.ldb, bk = (size_t)64 * u.ldb;
    f32x4 acc[4][2];
#pragma unroll
    for (int m = 0; m < 4; ++m)
#pragma unroll
        for (int n = 0; n < 2; ++n) acc[m][n] = (f32x4){0.f, 0.f, 0.f, 0.f};
    int nt = u.nt; asm volatile("" : "+s"(nt));
    const unsigned vb0 = (unsigned)(size_t)lds + (unsigned)boff0, vb1 = (unsigned)(size_t)lds + (unsigned)boff1, vb2 = vb0 + 65536u, vb3 = vb1 + 65536u;
#pragma unroll
    for (int t = 0; t < 4; ++t) { STAGE(t * 32768, u.A + (size_t)t * u.ak, vA, a2s); STAGE(t * 32768 + 16384, u.B + t * bk, vB, b2s); }
    bf16x8 A0[4][2], B0[2][2], A1[4][2], B1[2][2];
    WAIT_V(12); BAR();
    LDB(B0, vb0, vb1, 16384); LDA(A0, 0);
#define RDA(dst, m, off) do { dst[m][0] = *(const LAS bf16x8*)(lds + (off) + aoff + (m) * 2048); dst[m][1] = *(const LAS bf16x8*)(lds + (off) + aoff + (m) * 2048 + 1024); } while (0)
#define RDB(dst, nn, v, off) do { s16x4 l0, h0, l1, h1; TRD(l0, v, (off)); TRD(h0, v, (off) + 256); TRD(l1, v, (off) + 8192); TRD(h1, v, (off) + 8448); dst[nn][0] = cat8(l0, h0); dst[nn][1] = cat8(l1, h1); } while (0)
#define MM4(Ac, Bc, m) do { SCHED(); _Pragma("unroll") for (int n = 0; n < 2; ++n) _Pragma("unroll") for (int k = 0; k < 2; ++k) acc[m][n] = mma32(Bc[n][k], Ac[m][k], acc[m][n]); SCHED(); } while (0)
#define KSTEP(i, Ac, Bc, An, Bn, v0, v1, boffn) do { \
        const int tn = min(t0 + (i) + 4, nt - 1); \
        WAIT_L(0); WAIT_V(8); BAR(); \
        MM4(Ac, Bc, 0); RDB(Bn, 0, v0, boffn); RDA(An, 0, (((i) + 1) & 3) * 32768); \
        MM4(Ac, Bc, 1); RDB(Bn, 1, v1, boffn); RDA(An, 1, (((i) + 1) & 3) * 32768); \
        MM4(Ac, Bc, 2); RDA(An, 2, (((i) + 1) & 3) * 32768); STAGE((i) * 32768, u.A + (u.lda ? (unsigned)tn * 128u : gap((unsigned)tn * 2048u)), vA, a2s); \
        MM4(Ac, Bc, 3); RDA(An, 3, (((i) + 1) & 3) * 32768); STAGE((i) * 32768 + 16384, u.B + (size_t)tn * bk, vB, b2s); } while (0)
    for (int t0 = 0; t0 < nt; t0 += 4) {
        KSTEP(0, A0, B0, A1, B1, vb0, vb1, 49152);
        KSTEP(1, A1, B1, A0, B0, vb2, vb3, 16384);
        KSTEP(2, A0, B0, A1, B1, vb2, vb3, 49152);
        KSTEP(3, A1, B1, A0, B0, vb0, vb1, 16384);
    }
    WAIT_L(0); SCHED();
#undef RDA
#undef RDB
#undef MM4
#undef KSTEP
    const rsrc_t cr = mkrsrc(u.C, 256u * u.ldc);
    if (u.nsig) {
#pragma unroll
        for (int m = 0; m < 4; ++m) store_frag(cr, (unsigned)wc * u.cw + frag_off(lane), gap((unsigned)(wr * 64 + m * 16) * u.ldc), acc[m][0], acc[m][1]);
    } else {
#pragma unroll
        for (int m = 0; m < 4; ++m)
            ST16(to_rows(lds + L_SCR + wid * 1024, lane, acc[m][0], acc[m][1]), cr, (unsigned)(wr * 64 + (lane >> 2)) * u.ldc + (unsigned)(wc * 64 + (lane & 3) * 16), (unsigned)(m * 16) * u.ldc, 0);
    }
    WAIT_V(0);
    signal(ctl, u.sig, u.nsig);
    BAR();
}

#define win_off(idx) ((unsigned)(idx) * 16384u + ((((unsigned)(idx) >> 7) ^ par) & 1u) * 8192u)
#define KKM 8192u
#define km_win(s, h) (win_off(NWIN + (s) * 32 + (h)) ^ KKM)
#define W_MEM(s, x) ((s) == 0 ? 0 : NWIN + 2048 + 8 * (s) + 2 * (x))
#define W_PART(slot) (192 + (slot) * 64)
#define W_X(slot) (384 + (slot) * 128)
DI bool all_set(unsigned mx) { return __builtin_amdgcn_ballot_w64(mx == SENT) == 0ull; }
DI u32x4 rows_gather(unsigned v) { unsigned a = v, b = v; swap16(a, b); unsigned c = a, d = b; swap32(a, c); swap32(b, d); return (u32x4){a, b, c, d}; }
DI float max3f(float a, float b, float c) { return fmaxf(fmaxf(a, b), c); }
#define KD0 3
#define KD1 6
#define KD2 4
#define KRA 3
#define KDM 3
#define KWP 4
#define KNT1 0
#define KNT2 0
#define KPM 4
constexpr int RED_M = 144;
constexpr int L_PX = 32768, L_PM = L_PX + 512, L_ML = L_PM + 512, L_FLAG = L_ML + 256, L_PACE = L_FLAG + 16, L_VM = 34816, L_OWN = L_VM + 2048, L_X = L_OWN + 1536  ;
constexpr unsigned OOB = 0x80000000u;
#define RETRY(i)
#define SEG(i)
#define PST(i)
#define PIN_S(x) asm volatile("" : "+s"(x))
#define PIN_V(x) asm volatile("" : "+v"(x))
DI float ror8(float v) { return __uint_as_float(__builtin_amdgcn_update_dpp(0, __float_as_uint(v), 0x128, 0xf, 0xf, true)); }

template <int ROLE>
DI void chain_loop(const Params& p, const unsigned par, const int rank, const int w, ldsp lds, const bf16x16 (&W)[6][2], const bf16x16 (&WO)[2][2], unsigned long long* stamps) {
    const int lane = fresh_tid() & 63, g = lane >> 4, n = lane & 15;
    const int h = rank >> 2, j = rank & 3;
    unsigned* ctl = (unsigned*)(p.ws + WS_CTL);
    const rsrc_t xr = mkrsrc(p.ws + WS_XCH, (unsigned)XCH_BYTES);
    const rsrc_t qr = mkrsrc(p.ws + WH(WS_QKVX + (size_t)(32 + h) * XH), 33u * XH);
    const int sidx = (n & 2) ? 0xEEEE : 0x4444;
    const unsigned l16 = (unsigned)lane * 16u;
    const int redw = (w * 1024 + (n & 1) * 512 + (n >> 1) * 8 + 2 * (g ^ (n >> 3))) * 4;
    const int redr = (w < 4 ? (n >> 3) * RED_M + (4 * w + g) * 8 + ((n & 7) ^ ((w & 1) * 2)) : 2 * RED_M + (8 * (w & 1) + 2 * g + ((n >> 1) & 1)) * 8 + ((2 * (n >> 2) + (n & 1)) ^ (g & 2))) * 4;
    const bool vown = (g >> 1) == (j & 1);
    const unsigned vg1 = (unsigned)(((j & 2) ^ 2) + (g >> 1)) * 1536u + 1024u + (unsigned)(lane & 31) * 16u;
    const unsigned vg0 = vown ? OOB : (vg1 + (unsigned)(j & 2) * 3072u - 3072u);
    const unsigned pub_off = (g == j ? 0u : (unsigned)(4 * h + g) * 16384u) + (unsigned)j * 1536u + (unsigned)w * 256u + (unsigned)n * 16u;
    const int red = ((j + 1) & 3) * 32 + h;
    const int rho = 4 * (8 * j + w) + 2 * (g >> 1) + ((n >> 2) & 1);
    const unsigned pst_off = n < 8 ? (unsigned)(rho >> 1) * 16384u + (unsigned)(rho & 1) * 4096u + (unsigned)((h ^ rho) & 31) * 128u + (unsigned)((g & 1) * 64 + (n & 3) * 16) : OOB;
    const unsigned mem_off = lane < 32 ? (unsigned)(4 * ((n >> 1) & 1) + 2 * ((n >> 2) & 1) + (n & 1)) * 16u + (unsigned)(lane >> 3) * 32768u : OOB;
    const unsigned xw0 = win_off(W_X(0)), xw1 = win_off(W_X(1));
    unsigned pso = win_off(W_PART(0)), psn = win_off(W_PART(1)), psp = win_off(W_PART(2));
    const unsigned redo = (unsigned)(red >> 1) * 16384u + (unsigned)(red & 1) * 4096u;
    const int vxr = L_X + lane * 16;
    const float csc = 0.08838834764831845f * 1.4426950408889634f;
    float ninf = -__builtin_inff(); PIN_V(ninf);
    u32x4 KX[8], VWX[2];
    auto dma_x = [&](int s) {
        if (ROLE == 0 && w < 4) {
            if ((s & 3) == 0) { wait_count(ctl, C_KV + (s >> 2) * 512 + h * 16, 16u); asm volatile("" ::: "memory"); }
#pragma unroll
            for (int i = 0; i < 2; ++i) {
                const unsigned vo = l16, so = (unsigned)s * 16384u + (unsigned)(w * 2 + i) * 1024u;
                __builtin_amdgcn_raw_ptr_buffer_load_lds(qr, (LAS void*)(lds + L_X + (s & 1) * 16384 + (w * 2 + i) * 1024), 16, vo, so + 32u * XH, 0, 16);
                __builtin_amdgcn_raw_ptr_buffer_load_lds(qr, (LAS void*)(lds + L_X + (s & 1) * 16384 + 8192 + (w * 2 + i) * 1024), 16, vo, so, 0, 16);
            }
        }
    };
    auto load_kx = [&](int s) {
#pragma unroll
        for (int kt = 0; kt < 2; ++kt)
#pragma unroll
            for (int kk = 0; kk < 4; ++kk) KX[kt * 4 + kk] = *(const LAS u32x4*)(lds + vxr + (s & 1) * 16384 + 8192 + kt * 4096 + ((j + kk) & 3) * 1024);
    };
    auto vwx_kt = [&](int s, int kt) {
        bf16x8 VA[4];
#pragma unroll
        for (int kk = 0; kk < 4; ++kk) VA[kk] = *(const LAS bf16x8*)(lds + vxr + (s & 1) * 16384 + kt * 4096 + (kk ^ (j & 2)) * 1024);
#pragma unroll
        for (int ct = 0; ct < 2; ++ct) {
            f32x4 d = {0.f, 0.f, 0.f, 0.f};
#pragma unroll
            for (int kk = 0; kk < 4; ++kk) d = mma32(VA[kk], (kk & 1) ? hi8(WO[ct][kk >> 1]) : lo8(WO[ct][kk >> 1]), d);
            VWX[ct][2 * kt] = pk_bf16(d[0], d[1]); VWX[ct][2 * kt + 1] = pk_bf16(d[2], d[3]);
        }
    };
    auto rmax = [&](float v) { float a = v, b = v; swap16f(a, b); v = max3f(a, b, ninf); a = v; b = v; swap32f(a, b); return max3f(a, b, ninf); };
    auto lo2 = [](f32x4 v) { return (f32x2){v[0], v[1]}; };
    auto hi2 = [](f32x4 v) { return (f32x2){v[2], v[3]}; };
    dma_x(0); dma_x(1);
    asm volatile("s_waitcnt vmcnt(0) lgkmcnt(0)" ::: "memory"); BAR(); asm volatile("" ::: "memory");
    PST(5);
    vwx_kt(0, 0); vwx_kt(0, 1);
    if (ROLE == 2) load_kx(0);
    __builtin_amdgcn_s_setprio(2);

    unsigned iv_t0 = 0, iv_acc = 0;
    unsigned spins = 0, pace_c = 0x7fffffffu, pace_t = 0;
    const u32x4 sv = {SENT, SENT, SENT, SENT};
    for (int s = 0;; ++s) {
        unsigned xw = (s & 1) ? xw1 : xw0;
        unsigned mso = win_off(W_MEM(s, rank >> 5) + (w >> 2)) + (unsigned)(w & 3) * 2048u;
        unsigned pub = pub_off + (g == j ? km_win(s, h) : xw);
        PIN_S(xw); PIN_S(mso); PIN_V(pub);
        {
            if (KD0) __builtin_amdgcn_s_sleep(KD0);
            const u32x2 pv = *(const LAS u32x2*)(lds + L_PACE);
            unsigned target = __builtin_amdgcn_readfirstlane(pv.x); const bool valid = __builtin_amdgcn_readfirstlane(pv.y) + 1u == (unsigned)s && s > 3;
            unsigned now = (unsigned)__builtin_amdgcn_s_memrealtime();
            if (ROLE == 3 && s > 2) pace_c = min(pace_c, now - pace_t);
            if (valid && (int)(target - now) < 100) { while ((int)(target - now) > 0) now = (unsigned)__builtin_amdgcn_s_memrealtime(); }
        }
        SEG(10);
        f32x4 c[6]; u32x4 a1;
        u32x4 a0;
        auto ld_a = [&]() { asm volatile("" ::: "memory"); a0 = ld16_sc1(xr, l16, mso); a1 = ld16_sc1(xr, l16 + 1024, mso); };
        auto ok_a = [&]() { return all_set(max(umax4(a0), umax4(a1))); };
        auto mm_a = [&]() {
#pragma unroll
            for (int i = 0; i < 6; ++i) { c[i] = (f32x4){0.f, 0.f, 0.f, 0.f}; PIN_V(c[i]); }
            SCHED();
            c[0] = smma(a0, W[0][0], c[0], sidx); c[1] = smma(a0, W[1][0], c[1], sidx); c[0] = smma(a1, W[0][1], c[0], sidx); c[1] = smma(a1, W[1][1], c[1], sidx);
            c[2] = smma(a0, W[2][0], c[2], sidx); c[3] = smma(a0, W[3][0], c[3], sidx);
        };
        auto put = [&](int i) { *(LAS f32x2*)(lds + redw + ((i >> 1) * RED_M + (i & 1) * 64) * 4) = lo2(c[i]) + hi2(c[i]); };
        ld_a(); SCHED();
        mm_a();
        if (__builtin_expect(!ok_a(), 0)) { bool out; do { RETRY(0); out = spin_out(spins, ctl); ld_a(); } while (!(ok_a() | out)); mm_a(); }
        SEG(0);
        c[2] = smma(a1, W[2][1], c[2], sidx); c[3] = smma(a1, W[3][1], c[3], sidx);
        put(0); put(1);
        SCHED();
        c[4] = smma(a0, W[4][0], c[4], sidx); c[5] = smma(a0, W[5][0], c[5], sidx); c[4] = smma(a1, W[4][1], c[4], sidx);
        put(2); put(3);
        c[5] = smma(a1, W[5][1], c[5], sidx);
        SCHED();
        put(4); put(5);
        SEG(1);
        LDS_BAR();
        SEG(2);
        if (ROLE <= 1) {
            f32x2 v[8];
#pragma unroll
            for (int ww = 0; ww < 8; ++ww) { const LAS float* q = (const LAS float*)(lds + redr + ww * 4096); v[ww] = (f32x2){q[0], q[512]}; }
            const f32x2 t = ((v[0] + v[1]) + (v[2] + v[3])) + ((v[4] + v[5]) + (v[6] + v[7]));
            const u32x4 o = rows_gather(min(pk_bf16(t.x, t.y), SENT - 1));
            ST16(o, xr, pub, 0, 16);
            *(LAS u32x4*)(lds + L_OWN + w * 256 + n * 16) = o;
        } else {
            if (ROLE == 3 && rank == 0) {
                pace_t = (unsigned)__builtin_amdgcn_s_memrealtime();
                if (lane == 0) { __hip_atomic_store(ctl + C_PROG, (unsigned)s, RLX_AGENT);
                    __hip_atomic_store((unsigned long long*)(ctl + C_PACE + par * 2048), (unsigned long long)(pace_t + pace_c + KPM) | ((unsigned long long)s << 32), RLX_AGENT); }
            }
        }
        SEG(3);
        if (s == NSEG - 1) break;
        LDS_BAR();
        SEG(13);
        unsigned VWM[2];
        auto vwm_pack = [&](const f32x4 (&dv)[2]) {
#pragma unroll
            for (int ct = 0; ct < 2; ++ct) { const f32x2 t = lo2(dv[ct]) + hi2(dv[ct]); VWM[ct] = pk_bf16(t.x, t.y); PIN_V(VWM[ct]); }
        };
        f32x4 dv[2] = {{0.f, 0.f, 0.f, 0.f}, {0.f, 0.f, 0.f, 0.f}}, s0, s1;
        PIN_V(dv[0]); PIN_V(dv[1]);
        u32x4 v0, v1;
        if (ROLE == 0) {
            if (KDM == 1) { WAIT_V(0); dma_x(min(s + 2, NSEG - 2)); }
            else if (KDM == 2) { dma_x(min(s + 2, NSEG - 2)); if (w < 4) WAIT_V(4); else WAIT_V(0); }
            else WAIT_V(0);
        } else {
            unsigned cw = xw + (unsigned)rank * 16384u, c1 = cw + (unsigned)((j + 1) & 3) * 1536u, c2 = cw + (unsigned)((j + 2) & 3) * 1536u, c3 = cw + (unsigned)((j + 3) & 3) * 1536u;
            PIN_S(cw); PIN_S(c1); PIN_S(c2); PIN_S(c3);
            const unsigned xl = ROLE == 2 ? (l16 & ~128u) : l16;
            u32x4 vo;
            f32x4 e0 = {0.f, 0.f, 0.f, 0.f}, e1 = e0;
            if (ROLE == 1) { vo = *(const LAS u32x4*)(lds + L_OWN + 1024 + (lane & 31) * 16); if (!vown) vo = (u32x4){0u, 0u, 0u, 0u}; PIN_V(vo); }
            else {
                const u32x4 X0 = *(const LAS u32x4*)(lds + L_OWN + xl);
                if (ROLE == 2) { e0 = mma32(asbf(KX[0]), asbf(X0), e0); e1 = mma32(asbf(KX[4]), asbf(X0), e1); } else e0 = mma32(asbf(X0), asbf(X0), e0);
                PIN_V(e0); PIN_V(e1);
            }
            if (KD1) __builtin_amdgcn_s_sleep(KD1);
            u32x4 X1, X2, X3;
            auto ld_b = [&]() {
                asm volatile("" ::: "memory");
                if (ROLE >= 2) { X1 = ld16_sc1(xr, xl, c1); X2 = ld16_sc1(xr, xl, c2); X3 = ld16_sc1(xr, xl, c3); }
                else { v0 = ld16_sc1(xr, vg0, cw); v1 = ld16_sc1(xr, vg1, cw); }
            };
            auto ok_b = [&]() { return all_set(ROLE >= 2 ? max(umax4(X1), max(umax4(X2), umax4(X3))) : max(umax4(v0), umax4(v1))); };
            auto mm_b = [&]() {
                if (ROLE == 2) {
                    s0 = mma32(asbf(KX[1]), asbf(X1), e0); s1 = mma32(asbf(KX[5]), asbf(X1), e1);
                    s0 = mma32(asbf(KX[2]), asbf(X2), s0); s1 = mma32(asbf(KX[6]), asbf(X2), s1);
                    s0 = mma32(asbf(KX[3]), asbf(X3), s0); s1 = mma32(asbf(KX[7]), asbf(X3), s1);
                } else if (ROLE == 3) {
                    s0 = mma32(asbf(X1), asbf(X1), e0); s1 = mma32(asbf(X2), asbf(X2), e1); s0 = mma32(asbf(X3), asbf(X3), s0);
                } else {
                    v0 |= vo;
                    *(LAS u32x4*)(lds + L_VM + lane * 16) = v0; *(LAS u32x4*)(lds + L_VM + 1024 + lane * 16) = v1;
                }
            };
            SEG(11);
            ld_b(); SCHED(); mm_b();
            if (__builtin_expect(!ok_b(), 0)) { bool out; do { RETRY(1); out = spin_out(spins, ctl); ld_b(); } while (!(ok_b() | out)); mm_b(); }
            SEG(4);
        }
        float m;
        f32x4 sc;
        if (ROLE >= 2) {
            sc = ROLE == 2 ? (n < 8 ? s0 : s1) : s0 + s1;
            if (ROLE == 3 && g < 2) sc = (f32x4){ninf, ninf, ninf, ninf};
            m = max3f(max3f(sc[0], sc[1], sc[2]), sc[3], ninf);
            if (ROLE == 2) m = max3f(m, ror8(m), ninf);
            m = rmax(m);
            PIN_V(m);
        }
        LDS_BAR();
        if (ROLE == 0) {
            const unsigned ro = w < 3 ? xw + (unsigned)rank * 16384u + (unsigned)w * 2048u : psp + redo + (unsigned)(w - 3) * 2048u;
            ST16(sv, xr, l16, ro, 16); ST16(sv, xr, l16 + 1024, ro, 16);
        }
        if (ROLE != 1) { v0 = *(const LAS u32x4*)(lds + L_VM + lane * 16); v1 = *(const LAS u32x4*)(lds + L_VM + 1024 + lane * 16); }
        if (ROLE >= 2) {
            const float mc = -m * csc;
#pragma unroll
            for (int i = 0; i < 4; ++i) sc[i] = __builtin_amdgcn_exp2f(fmaf(sc[i], csc, mc));
            *(LAS u32x2*)(lds + (ROLE == 2 ? L_PX + (g * 8 + (n & 7)) * 16 + (n >> 3) * 8 : L_PM + (g < 2 ? 256 : 0) + n * 16 + (g & 1) * 8)) = (u32x2){pk_bf16(sc[0], sc[1]), pk_bf16(sc[2], sc[3])};
            SCHED();
            dv[0] = smma(v0, WO[0][0], dv[0], sidx); dv[1] = smma(v0, WO[1][0], dv[1], sidx);
            float l = (sc[0] + sc[1]) + (sc[2] + sc[3]);
            if (ROLE == 2) l += ror8(l);
            dv[0] = smma(v1, WO[0][1], dv[0], sidx); dv[1] = smma(v1, WO[1][1], dv[1], sidx);
            l = rows_sum(l);
            *(LAS f32x2*)(lds + L_ML + n * 16 + (ROLE == 2 ? 0 : 8)) = (f32x2){mc, l};
        } else {
#pragma unroll
            for (int ct = 0; ct < 2; ++ct) { dv[ct] = smma(v0, WO[ct][0], dv[ct], sidx); dv[ct] = smma(v1, WO[ct][1], dv[ct], sidx); }
        }
        vwm_pack(dv);
        SEG(5);
        u32x4 am0 = {VWM[0], 0u, 0u, 0u}, am1 = {VWM[1], 0u, 0u, 0u}, pm = {0u, 0u, 0u, 0u};
        PIN_V(am0); PIN_V(am1); PIN_V(pm);
        LDS_BAR();
        SEG(6);
        {
            const f32x4 ml = *(const LAS f32x4*)(lds + L_ML + n * 16);
            const u32x4 px = *(const LAS u32x4*)(lds + L_PX + (g * 8 + (n & 7)) * 16);
            pm.x = *(const LAS unsigned*)(lds + L_PM + n * 16 + g * 4);
            const f32x4 z = {0.f, 0.f, 0.f, 0.f};
            const f32x4 dx0 = mma32(asbf(VWX[0]), asbf(px), z), dx1 = mma32(asbf(VWX[1]), asbf(px), z), dm0 = mma32(asbf(am0), asbf(pm), z), dm1 = mma32(asbf(am1), asbf(pm), z);
            const float dl = ml[0] - ml[2];
            const float e = __builtin_amdgcn_exp2f(-__builtin_fabsf(dl));
            const float ex = dl > 0.f ? e : 1.f, em = dl > 0.f ? 1.f : e;
            const float inv = __builtin_amdgcn_rcpf(fmaf(ex, ml[1], em * ml[3]));
            const f32x4 d0 = (dx0 * ex + dm0 * em) * inv, d1 = (dx1 * ex + dm1 * em) * inv;
            const u32x4 o = {min(pk_bf16(d0[0], d0[1]), SENT - 1), min(pk_bf16(d0[2], d0[3]), SENT - 1), min(pk_bf16(d1[0], d1[1]), SENT - 1), min(pk_bf16(d1[2], d1[3]), SENT - 1)};
            ST16(o, xr, pst_off, pso, 16);
        }
        SCHED();
        SEG(7);
        __builtin_amdgcn_s_setprio(ROLE == 3 ? 1 : 0);
        if (KDM == 3 && s + 2 < NSEG - 1) dma_x(s + 2);
        unsigned long long pv = 0;
        if (ROLE == 1) { pv = __hip_atomic_load((unsigned long long*)(ctl + C_PACE + par * 2048), RLX_AGENT); SCHED(); }
        vwx_kt(s + 1, 0); vwx_kt(s + 1, 1);
        SCHED();
        if (ROLE == 3) __builtin_amdgcn_s_setprio(2);
        if (ROLE == 3) {
            u32x4 SEL;
            { const unsigned one = (n & 1) ? 0x3f800000u : 0x00003f80u; const int dw = (n & 7) >> 1; SEL = (u32x4){dw == 0 ? one : 0u, dw == 1 ? one : 0u, dw == 2 ? one : 0u, dw == 3 ? one : 0u}; }
            unsigned so = pso + redo, mo = win_off(W_MEM(s + 1, 0) + (red >> 6)) + (unsigned)(red & 63) * 128u;
            PIN_S(so); PIN_S(mo); PIN_V(SEL);
            if (KD2) __builtin_amdgcn_s_sleep(KD2);
            f32x4 ta, tb;
            u32x4 q[4];
            auto ld_c = [&]() {
                asm volatile("" ::: "memory");
#pragma unroll
                for (int i = 0; i < 4; ++i) q[i] = ld16_sc1(xr, l16 + i * 1024, so);
            };
            auto ok_c = [&]() { return all_set(max(max(umax4(q[0]), umax4(q[1])), max(umax4(q[2]), umax4(q[3])))); };
            auto mm_c = [&]() {
                ta = mma32(asbf(SEL), asbf(q[0]), (f32x4){0.f, 0.f, 0.f, 0.f}); tb = mma32(asbf(SEL), asbf(q[1]), (f32x4){0.f, 0.f, 0.f, 0.f});
                ta = mma32(asbf(SEL), asbf(q[2]), ta); tb = mma32(asbf(SEL), asbf(q[3]), tb);
            };
            SEG(12);
            ld_c(); mm_c();
            if (__builtin_expect(!ok_c(), 0)) { bool out; do { RETRY(2); out = spin_out(spins, ctl); ld_c(); } while (!(ok_c() | out)); mm_c(); }
            SEG(8);
            f32x4 t = ta + tb;
#pragma unroll
            for (int i = 0; i < 4; ++i) t[i] += ror8(t[i]);
            unsigned a0 = min(pk_bf16(t[0], t[1]), SENT - 1), a1 = min(pk_bf16(t[2], t[3]), SENT - 1);
            unsigned b0 = a0, b1 = a1; swap16(a0, b0); swap16(a1, b1);
            const u32x4 o = {a0, a1, b0, b1};
            ST16(o, xr, mem_off, mo, 16);
        } else {
            if (KDM == 0 && s + 2 < NSEG - 1) dma_x(s + 2);
            if (ROLE == 2) load_kx(s + 1);
            if (ROLE == 1) *(LAS u32x2*)(lds + L_PACE) = (u32x2){(unsigned)pv, (unsigned)(pv >> 32)};
        }
        { const unsigned t = psp; psp = pso; pso = psn; psn = t; }
        SEG(9);
        if (ROLE != 3) __builtin_amdgcn_s_setprio(2);
        LDS_BAR();
        SEG(14);
    }
    WAIT_V(0);
    __builtin_amdgcn_s_setprio(0);
}

DI void chain_role(const Params& p, unsigned par, int rank, ldsp lds, unsigned long long* stamps) {
    const int tid = fresh_tid(), w = __builtin_amdgcn_readfirstlane(tid >> 6), lane = tid & 63, g = lane >> 4, n = lane & 15;
    const int h = rank >> 2, j = rank & 3;
    const rsrc_t xr = mkrsrc(p.ws + WS_XCH, (unsigned)XCH_BYTES);
    unsigned* ctl = (unsigned*)(p.ws + WS_CTL);
    PST(0);
    const int red = ((j + 1) & 3) * 32 + h;
    bf16x16 W[6][2], WO[2][2];
    {
        ldsp my = lds + w * 16384;
        const int rrow = lane >> 2, rc = (lane & 3) * 8, q4 = n >> 2;
        const int toffo = (8 * g + q4) * 64 + (n & 3) * 8;
        const int toffq = (16 * (g & 1) + 8 * (g >> 1) + 4 * (q4 >> 1) + (q4 & 1)) * 64 + (n & 3) * 8;
#define FRAG(base, st) cat16(cat8(trd((base)), trd((base) + (st))), cat8(trd((base) + 2048), trd((base) + 2048 + (st))))
#pragma unroll
        for (int i = 0; i < 8; ++i) {
            __builtin_amdgcn_global_load_lds((const unsigned*)(p.wq + (size_t)(128 * w + 16 * i + rrow) * 4096 + h * 128 + 32 * j + rc), (LAS unsigned*)(my + i * 1024), 16, 0, KNT1);
            __builtin_amdgcn_global_load_lds((const unsigned*)(p.wo + (size_t)(h * 128 + 16 * i + rrow) * 1024 + 256 * j + 32 * w + rc), (LAS unsigned*)(my + 8192 + i * 1024), 16, 0, KNT1);
        }
        {
            const u32x4 sv = {SENT, SENT, SENT, SENT};
            if (w < 6) {
                ST16(sv, xr, (unsigned)lane * 16u, win_off(W_X(0)) + (unsigned)rank * 16384u + (unsigned)w * 1024u, 16);
                ST16(sv, xr, (unsigned)lane * 16u, win_off(W_X(1)) + (unsigned)rank * 16384u + (unsigned)w * 1024u, 16);
            }
            ST16(sv, xr, (unsigned)lane * 16u, win_off(W_PART(w >> 2)) + (unsigned)(red >> 1) * 16384u + (unsigned)(red & 1) * 4096u + (unsigned)(w & 3) * 1024u, 16);
            ST16(sv, xr, (unsigned)lane * 16u, km_win(56 + w, h) + (unsigned)j * 1536u, 16);
            ST16(sv, xr, lane < 32 ? (unsigned)lane * 16u + 1024u : OOB, km_win(56 + w, h) + (unsigned)j * 1536u, 16);
            const int s = 8 * w + (lane >> 3);
#pragma unroll
            for (int x = 0; x < 4; ++x) if (s > 0) ST16(sv, xr, win_off(W_MEM(s, x) + (red >> 6)) + (unsigned)(red & 63) * 128u + (unsigned)(lane & 7) * 16u, 0, 16);
        }
        PST(3);
        WAIT_V(0);
#pragma unroll
        for (int T = 0; T < 2; ++T)
#pragma unroll
            for (int t = 0; t < 2; ++t) { W[T][t] = FRAG(my + t * 4096 + T * 32 + toffq, 128); WO[T][t] = FRAG(my + 8192 + (t ^ (j >> 1)) * 4096 + T * 32 + toffo, 256); }
#pragma unroll
        for (int T = 0; T < 2; ++T) asm volatile("" : "+v"(W[T][0]), "+v"(W[T][1]), "+v"(WO[T][0]), "+v"(WO[T][1]));
        WAIT_L(0);
        PST(1);
#pragma unroll
        for (int i = 0; i < 8; ++i) {
            __builtin_amdgcn_global_load_lds((const unsigned*)(p.wk + (size_t)(128 * w + 16 * i + rrow) * 4096 + h * 128 + 32 * j + rc), (LAS unsigned*)(my + i * 1024), 16, 0, KNT2);
            __builtin_amdgcn_global_load_lds((const unsigned*)(p.wv + (size_t)(128 * w + 16 * i + rrow) * 4096 + h * 128 + 32 * j + rc), (LAS unsigned*)(my + 8192 + i * 1024), 16, 0, KNT2);
        }
        WAIT_V(0);
#pragma unroll
        for (int T = 0; T < 4; ++T)
#pragma unroll
            for (int t = 0; t < 2; ++t) W[2 + T][t] = FRAG(my + (T >> 1) * 8192 + t * 4096 + (T & 1) * 32 + toffq, 128);
#pragma unroll
        for (int T = 0; T < 4; ++T) asm volatile("" : "+v"(W[2 + T][0]), "+v"(W[2 + T][1]));
        WAIT_L(0);
#undef FRAG
    }
    PST(2);
    BAR();
    PST(4);
    if (w == 7 && lane < 8) {
        const unsigned* src = (const unsigned*)(p.mem0 + (4 * (red & 1) + 2 * (lane >> 2) + (lane & 1)) * DM + 32 * (red >> 2) + 8 * ((red >> 1) & 1) + 4 * ((lane >> 1) & 1));
        const u32x4 v = {min(src[0], SENT - 1), min(src[1], SENT - 1), min(src[8], SENT - 1), min(src[9], SENT - 1)};
        ST16(v, xr, (unsigned)lane * 16u + (unsigned)(red & 63) * 128u, win_off(W_MEM(0, 0) + (red >> 6)), 16);
    }
    if (w < 5) chain_loop<0>(p, par, rank, w, lds, W, WO, stamps);
    else if (w == 5) chain_loop<1>(p, par, rank, 5, lds, W, WO, stamps);
    else if (w == 6) chain_loop<2>(p, par, rank, 6, lds, W, WO, stamps);
    else chain_loop<3>(p, par, rank, 7, lds, W, WO, stamps);
}

template <bool POLL>
DI void attn_a_item(const Params& p, unsigned par, int s, int h, ldsp my, int lane) {
    const int g = lane >> 4, n = lane & 15;
    const rsrc_t xr = mkrsrc(p.ws + WS_XCH, (unsigned)XCH_BYTES);
    const rsrc_t qr = mkrsrc(p.ws + WH(WS_QKVX + (size_t)h * XH + (size_t)s * 16384), 65u * XH);
#pragma unroll
    for (int i = 0; i < 8; ++i)
        __builtin_amdgcn_raw_ptr_buffer_load_lds(qr, (LAS void*)(my + i * 1024), 16, (unsigned)(g * 256 + (n ^ ((g & 1) * 8)) * 16), 64u * XH + (unsigned)i * 1024u, 0, 16);
    u32x4 Q[2][4], K[2][4], XM[4];
    const unsigned ro = (unsigned)lane * 16u;
#pragma unroll
    for (int t = 0; t < 2; ++t)
#pragma unroll
        for (int k = 0; k < 4; ++k) { Q[t][k] = ld16_sc1(qr, ro + k * 1024, (unsigned)t * 4096u); K[t][k] = ld16_sc1(qr, ro + k * 1024, (unsigned)t * 4096u + 32u * XH); }
    const unsigned qwin = km_win(s, h);
    u32x4 VM[2];
    if (POLL) {
        wait_count<4>((unsigned*)(p.ws + WS_CTL), C_PROG, (unsigned)s + (s < 56 ? 2u : 1u));
        if (s >= 56) __builtin_amdgcn_s_sleep(16);
    }
    for (unsigned spins = 0;;) {
        asm volatile("" ::: "memory");
#pragma unroll
        for (int k = 0; k < 4; ++k) XM[k] = ld16_sc1(xr, (unsigned)lane * 16u + (k & 1) * 1536, qwin + (k >> 1) * 3072);
#pragma unroll
        for (int ts = 0; ts < 2; ++ts) VM[ts] = ld16_sc1(xr, (unsigned)(g >> 1) * 1536u + 1024u + (unsigned)(lane & 31) * 16u, qwin + ts * 3072);
        if (!POLL || s < 56) break;
        if (all_set(max(max(max(umax4(XM[0]), umax4(XM[1])), max(umax4(XM[2]), umax4(XM[3]))), max(umax4(VM[0]), umax4(VM[1]))))) break;
        __builtin_amdgcn_s_sleep(4);
        if (spin_out(spins, (unsigned*)(p.ws + WS_CTL))) break;
    }
    SCHED();
#pragma unroll
    for (int ts = 0; ts < 2; ++ts) {
        const u32x4 v = VM[ts];
        const int m = 2 * (n >> 2) + (n & 1), hf = (n >> 1) & 1;
#pragma unroll
        for (int i = 0; i < 4; ++i) { const int e = 64 * ts + 16 * g + 4 * i + 2 * hf;
            *(LAS unsigned*)(my + 8192 + (e >> 3) * 256 + (m ^ (e & 8)) * 16 + (e & 7) * 2) = v[i]; }
    }
    const float csc = 0.08838834764831845f * 1.4426950408889634f, ninf = -__builtin_inff();
    u32x4 PX[2]; u32x2 PM[2];
#pragma unroll
    for (int qt = 0; qt < 2; ++qt) {
        f32x4 sm = {0.f, 0.f, 0.f, 0.f}, s0 = sm, s1 = sm;
#pragma unroll
        for (int k = 0; k < 4; ++k) { sm = mma32(asbf(XM[k]), asbf(Q[qt][k]), sm); s0 = mma32(asbf(K[0][k]), asbf(Q[qt][k]), s0); if (qt == 1) s1 = mma32(asbf(K[1][k]), asbf(Q[qt][k]), s1); }
#pragma unroll
        for (int i = 0; i < 4; ++i) {
            if (g < 2) sm[i] = ninf;
            if (qt == 0) { if (4 * g + i > n) s0[i] = ninf; s1[i] = ninf; }
            else if (4 * g + i > n) s1[i] = ninf;
        }
        float m = fmaxf(fmaxf(fmaxf(s0[0], s0[1]), fmaxf(s0[2], s0[3])), fmaxf(fmaxf(s1[0], s1[1]), fmaxf(s1[2], s1[3])));
        m = fmaxf(m, fmaxf(fmaxf(sm[0], sm[1]), fmaxf(sm[2], sm[3])));
        m = rows_max(m);
        const float mc = -m * csc; float sum = 0.f;
#pragma unroll
        for (int i = 0; i < 4; ++i) { s0[i] = __builtin_amdgcn_exp2f(fmaf(s0[i], csc, mc)); s1[i] = __builtin_amdgcn_exp2f(fmaf(s1[i], csc, mc)); sm[i] = __builtin_amdgcn_exp2f(fmaf(sm[i], csc, mc));
            sum += (s0[i] + s1[i]) + sm[i]; }
        sum = rows_sum(sum);
        const float inv = __builtin_amdgcn_rcpf(sum);
        PX[qt] = (u32x4){pk_bf16(s0[0] * inv, s0[1] * inv), pk_bf16(s0[2] * inv, s0[3] * inv), pk_bf16(s1[0] * inv, s1[1] * inv), pk_bf16(s1[2] * inv, s1[3] * inv)};
        PM[qt] = (u32x2){pk_bf16(sm[0] * inv, sm[1] * inv), pk_bf16(sm[2] * inv, sm[3] * inv)};
    }
    asm volatile("s_waitcnt vmcnt(0) lgkmcnt(0)" ::: "memory");
    const int q4 = n >> 2, p4 = n & 3;
    const ldsp vx = my + (p4 >> 1) * 256 + ((4 * g + q4) ^ ((p4 >> 1) * 8)) * 16 + (p4 & 1) * 8, vmm = my + 8192 + (p4 >> 1) * 256 + ((4 * (g & 1) + q4) ^ ((p4 >> 1) * 8)) * 16 + (p4 & 1) * 8;
    const rsrc_t orr = mkrsrc(p.ws + WH(WS_OA + (size_t)s * 524288 + gap((unsigned)h * 4096u)), 524288u);
    const unsigned oo = frag_off(lane);
    bf16x8 VA[8]; s16x4 VMf[8];
#pragma unroll
    for (int et = 0; et < 8; ++et) { VA[et] = cat8(trd(vx + et * 512), trd(vx + 4096 + et * 512)); VMf[et] = trd(vmm + et * 512); }
    SCHED();
#pragma unroll
    for (int ep = 0; ep < 4; ++ep) {
        f32x4 d[2][2];
#pragma unroll
        for (int e2 = 0; e2 < 2; ++e2)
#pragma unroll
            for (int qt = 0; qt < 2; ++qt) d[qt][e2] = mma16(__builtin_bit_cast(u32x2, VMf[2 * ep + e2]), PM[qt], mma32(VA[2 * ep + e2], asbf(PX[qt]), (f32x4){0.f, 0.f, 0.f, 0.f}));
#pragma unroll
        for (int qt = 0; qt < 2; ++qt) store_frag(orr, oo + ep * 1024, (unsigned)qt * 262144u, d[qt][0], d[qt][1]);
    }
    asm volatile("s_waitcnt lgkmcnt(0)" ::: "memory");
}

DI void attn_b_item(const Params& p, unsigned par, int hq, int qb, ldsp lds) {
    const int tid = fresh_tid(), w = __builtin_amdgcn_readfirstlane(tid >> 6), lane = tid & 63, g = lane >> 4, n = lane & 15;
    const int q0 = qb * 128;
    const rsrc_t br = mkrsrc(p.ws + WH(WS_QKVB + (size_t)hq * 4 * BH), 68u * BH);
    const rsrc_t orr = mkrsrc(p.ws + WH(WS_OB + (size_t)qb * 524288), 524288u);
    constexpr int BUF = 57344;
    unsigned to[3];
#pragma unroll
    for (int i = 0; i < 3; ++i) to[i] = gap((unsigned)max(8 * qb - 16 + w * 3 + i, 0) * 1024u);
    const unsigned l16 = (unsigned)lane * 16u, vsw = (unsigned)(g * 256 + (n ^ ((g & 1) * 8)) * 16);
    auto stage = [&](int i) {
        const unsigned so = (unsigned)i * BH; ldsp d = lds + (i & 1) * BUF;
#pragma unroll
        for (int k = 0; k < 3; ++k) {
            __builtin_amdgcn_raw_ptr_buffer_load_lds(br, (LAS void*)(d + (w * 3 + k) * 1024), 16, l16, so + 32u * BH + to[k], 0, 16);
            __builtin_amdgcn_raw_ptr_buffer_load_lds(br, (LAS void*)(d + 24576 + (w * 3 + k) * 1024), 16, vsw, so + 64u * BH + to[k], 0, 16);
        }
        __builtin_amdgcn_raw_ptr_buffer_load_lds(br, (LAS void*)(d + 49152 + w * 1024), 16, l16, so + (unsigned)(qb * 16384 + w * 1024), 0, 16);
    };
    const float ninf = -__builtin_inff(), csc = 0.17677669529663687f * 1.4426950408889634f;
    const int tmin = (256 - q0 - 16 * w) >> 4;
    const int q4 = n >> 2, p4 = n & 3;
    const int kq = w * 1024 + lane * 16;
    const unsigned vb = (unsigned)(size_t)lds + 24576u + (unsigned)(w * 1024 + (p4 >> 1) * 256 + ((4 * g + q4) ^ ((p4 >> 1) * 8)) * 16 + (p4 & 1) * 8);
    stage(0);
#pragma unroll 1
    for (int i = 0; i < 4; ++i) {
        if (i < 3) { stage(i + 1); WAIT_V(7); } else WAIT_V(0);
        BAR(); asm volatile("" ::: "memory");
        const ldsp bp = lds + (i & 1) * BUF;
        const bf16x8 Q = *(const LAS bf16x8*)(bp + 49152 + kq);
        f32x4 sc[17]; bf16x8 KF[17];
#pragma unroll
        for (int t = 0; t < 17; ++t) KF[t] = *(const LAS bf16x8*)(bp + kq + t * 1024);
        SCHED();
#pragma unroll
        for (int t = 0; t < 17; ++t) sc[t] = mma32(KF[t], Q, (f32x4){0.f, 0.f, 0.f, 0.f});
#pragma unroll
        for (int k = 0; k < 4; ++k) { if (4 * g + k <= n) sc[0][k] = ninf; if (4 * g + k > n) sc[16][k] = ninf; }
        if (tmin > 0) {
#pragma unroll
            for (int t = 0; t < 16; ++t) if (t < tmin) sc[t] = (f32x4){ninf, ninf, ninf, ninf};
        }
        float m = max3f(sc[16][0], sc[16][1], max3f(sc[16][2], sc[16][3], ninf));
#pragma unroll
        for (int t = 0; t < 16; ++t) m = max3f(max3f(m, sc[t][0], sc[t][1]), sc[t][2], sc[t][3]);
        m = rows_max(m);
        const float mc = -m * csc;
        f32x4 sum4 = {0.f, 0.f, 0.f, 0.f};
#pragma unroll
        for (int t = 0; t < 17; ++t) {
#pragma unroll
            for (int k = 0; k < 4; ++k) sc[t][k] = __builtin_amdgcn_exp2f(fmaf(sc[t][k], csc, mc));
            sum4 += sc[t];
        }
        const float inv = __builtin_amdgcn_rcpf(rows_sum((sum4[0] + sum4[1]) + (sum4[2] + sum4[3])));
        const unsigned v0 = vb + (unsigned)((i & 1) * BUF);
        s16x4 V0[17], V1[17];
#define VRD(t) TRD(V0[t], v0, (t) * 1024); TRD(V1[t], v0, (t) * 1024 + 512)
        VRD(0); VRD(1); VRD(2); VRD(3); VRD(4); VRD(5); VRD(6); VRD(7); VRD(8); VRD(9); VRD(10); VRD(11); VRD(12); VRD(13); VRD(14); VRD(15); VRD(16);
#undef VRD
        u32x2 P[17];
#pragma unroll
        for (int t = 0; t < 17; ++t) P[t] = (u32x2){pk_bf16(sc[t][0], sc[t][1]), pk_bf16(sc[t][2], sc[t][3])};
        WAIT_L(0); SCHED();
        f32x4 d0 = {0.f, 0.f, 0.f, 0.f}, d1 = d0;
#pragma unroll
        for (int tp = 0; tp < 8; ++tp) {
            const bf16x8 PP = asbf((u32x4){P[2 * tp].x, P[2 * tp].y, P[2 * tp + 1].x, P[2 * tp + 1].y});
            d0 = mma32(cat8(V0[2 * tp], V0[2 * tp + 1]), PP, d0); d1 = mma32(cat8(V1[2 * tp], V1[2 * tp + 1]), PP, d1);
        }
        d0 = mma16(__builtin_bit_cast(u32x2, V0[16]), P[16], d0); d1 = mma16(__builtin_bit_cast(u32x2, V1[16]), P[16], d1);
        BAR();
        store_frag(orr, frag_off(lane), (unsigned)w * 65536u + gap((unsigned)(hq * 4 + i) * 1024u), d0 * inv, d1 * inv);
    }
    WAIT_V(0);
    signal((unsigned*)(p.ws + WS_CTL), C_OB + qb * 32, 1);
}

DI void out_tail(const Params& p, unsigned par, int c, int q, ldsp lds) {
    const int tid = fresh_tid(), w = __builtin_amdgcn_readfirstlane(tid >> 6), lane = tid & 63, g = lane >> 4, n = lane & 15;
    unsigned* ctl = (unsigned*)(p.ws + WS_CTL);
    bf16x8 WT[16][2];
    {
        ldsp my = lds + w * 16384;
        const int toff = (8 * g + (n >> 2)) * 64 + (n & 3) * 8;
#pragma unroll
        for (int r = 0; r < 2; ++r) {
#pragma unroll
            for (int i = 0; i < 16; ++i)
                __builtin_amdgcn_global_load_lds((const unsigned*)(p.wo + (size_t)(512 * w + 256 * r + 16 * i + (lane >> 2)) * 1024 + 32 * c + (lane & 3) * 8), (LAS unsigned*)(my + i * 1024), 16, 0, 0);
            WAIT_V(0);
#pragma unroll
            for (int k = 0; k < 8; ++k)
#pragma unroll
                for (int ct = 0; ct < 2; ++ct) { WT[8 * r + k][ct] = cat8(trd(my + k * 2048 + ct * 32 + toff), trd(my + k * 2048 + ct * 32 + toff + 256)); }
#pragma unroll
            for (int k = 0; k < 8; ++k) asm volatile("" : "+v"(WT[8 * r + k][0]), "+v"(WT[8 * r + k][1]));
            WAIT_L(0);
        }
    }
    BAR();
#pragma unroll 1
    for (int hs = q; hs < 32; hs += 3) {
        const int s = 48 + (hs >> 1), rt = hs & 1;
        if (w == 0) wait_count<3>(ctl, C_OS + (s - 48) * 256 + (c >> 3) * 64, 4u);
        asm volatile("" ::: "memory"); BAR(); asm volatile("" ::: "memory");
        const rsrc_t ar = mkrsrc(p.ws + WH(WS_OA + (size_t)(2 * s + rt) * 262144 + (size_t)w * 32768), 32768u);
        const unsigned ao = (unsigned)lane * 16u;
        u32x4 A[16];
#pragma unroll
        for (int k = 0; k < 16; ++k) A[k] = ld16_sc1(ar, ao + (k & 3) * 1024, gap((unsigned)(k >> 2) * 4096u));
        SCHED();
#pragma unroll
        for (int ct = 0; ct < 2; ++ct) {
            f32x4 acc = {0.f, 0.f, 0.f, 0.f};
#pragma unroll
            for (int k = 0; k < 16; ++k) acc = mma32(WT[k][ct], asbf(A[k]), acc);
            *(LAS f32x4*)(lds + w * 2048 + ct * 1024 + lane * 16) = acc;
        }
        LDS_BAR();
        if (w < 2) {
            f32x4 r[8];
#pragma unroll
            for (int ww = 0; ww < 8; ++ww) r[ww] = *(const LAS f32x4*)(lds + ww * 2048 + w * 1024 + lane * 16);
            SCHED();
            const f32x4 t = ((r[0] + r[1]) + (r[2] + r[3])) + ((r[4] + r[5]) + (r[6] + r[7]));
            *(u32x2*)(p.out + (size_t)(32 * s + 16 * rt + n) * 2048 + 32 * c + 16 * w + 4 * g) = (u32x2){pk_bf16(t[0], t[1]), pk_bf16(t[2], t[3])};
        }
        WAIT_L(0);
    }
    BAR();
}

DI void big_unit(const Params& p, unsigned par, int id, Unit& u) {
    u.lda = DM * 2; u.nt = 16; u.ak = 128; u.ah = 128 * DM * 2;
    if (id < 384) {
        const int pm = id & 7, pn = id >> 3;
        const u16 *w0 = p.wq, *w1 = p.wk, *w2 = p.wv; const u16* W = pn < 16 ? w0 : (pn < 32 ? w1 : w2);
        u.A = (const char*)(p.ax + (size_t)pm * 256 * DM); u.B = (const char*)(W + (pn & 15) * 256); u.ldb = 4096 * 2;
        u.C = p.ws + WH(WS_QKVX + (size_t)(2 * pn) * XH + (size_t)pm * 131072); u.ldc = 256; u.cw = 1024; u.ch = XH;
        if (pn < 16) { u.sig = C_Q + pm * 32; u.nsig = 1; } else { u.sig = C_KV + 2 * pm * 512 + (pn & 15) * 32; u.nsig = 4; }
    } else if (id < 480) {
        const int pm = (id - 384) & 7, pn = (id - 384) >> 3;
        u.A = (const char*)(p.bx + (size_t)pm * 256 * DM); u.B = (const char*)(p.bwqkv + pn * 256); u.ldb = LDB3 * 2;
        u.C = p.ws + WH(WS_QKVB + (size_t)(8 * pn) * BH + (size_t)pm * 32768); u.ldc = 64; u.cw = BH; u.ch = 4 * BH; u.sig = C_B + pm * 32; u.nsig = 1;
    } else {
        const bool isb = id < 512; const int q = isb ? id - 480 : id - 512, pm = q >> 2, pn = q & 3;
        const u16 *w0 = p.wo, *w1 = p.bwo;
        u.nt = isb ? 16 : 64; u.lda = 0; u.ak = 2048;
        const size_t a0 = (isb ? WS_OB : WS_OA) + (size_t)pm * u.nt * 65536, a1 = a0 + (size_t)u.nt * 32768;
        u.A = p.ws + WH(a0); u.ah = (unsigned)(WH(a1) - WH(a0)); u.B = (const char*)((isb ? w1 : w0) + pn * 256); u.ldb = DM * 2;
        u.C = (char*)(p.out + (size_t)pm * 256 * 2048 + (isb ? 1024 : 0) + pn * 256); u.ldc = 4096; u.cw = 64; u.ch = 256; u.sig = 0; u.nsig = 0;
    }
}
struct BigSched { const Params& p; unsigned par; int id0, id1; DI bool next(int i, Unit& u) const { const int id = i == 0 ? id0 : (i == 1 ? id1 : -1); if (id < 0) return false; big_unit(p, par, id, u); return true; } };

__global__ __launch_bounds__(512, 2) void k_fused(Params p) {
    extern __shared__ __attribute__((aligned(16))) unsigned char shm[];
    ldsp lds = (ldsp)shm;
    unsigned* ctl = (unsigned*)(p.ws + WS_CTL);
    const int b = (blockIdx.x & ~7) | ((blockIdx.x + __builtin_amdgcn_readfirstlane(ctl[C_PHASE])) & 7), id = (b & 3) * 32 + (b >> 3);
#define WSTAMP(code)
    const unsigned par = (unsigned)__builtin_amdgcn_readfirstlane(ctl[C_PAR + 1]) < (unsigned)__builtin_amdgcn_readfirstlane(ctl[C_PAR]) ? 1u : 0u;
    WSTAMP(1);
    if ((b & 7) < 4) {
        chain_role(p, par, id, lds, (unsigned long long*)(p.ws + WS_END));
        BAR();
        WSTAMP(2);
    } else {
#pragma unroll 1
        for (int r = 0; r < 2; ++r) {
            const int rb = 2 * r + (id & 1), ct = id >> 1;
            const u16 *w1 = p.wk, *w2 = p.wv;
            Unit u; u.A = (const char*)(p.ax + (size_t)rb * 128 * DM); u.lda = DM * 2; u.B = (const char*)((ct < 32 ? w1 : w2) + (ct & 31) * 128); u.ldb = 4096 * 2; u.nt = 16;
            u.C = p.ws + WH(WS_QKVX + (size_t)(32 + ct) * XH + (size_t)rb * 65536); u.ldc = 256; u.cw = 1024; u.ch = 0; u.ak = 128; u.ah = 0;
            u.sig = C_KV + rb * 512 + (ct & 31) * 16; u.nsig = 1;
            gemm_small(lds, u, ctl);
            WSTAMP(3);
        }
        {
            const int t = fresh_tid(), wv = __builtin_amdgcn_readfirstlane(t >> 6);
            const char* src; const int c = id & 7, q = id >> 3;
            if (c < 2) src = (const char*)p.ax + (size_t)(q * 2 + c) * 131072; else if (c < 4) src = (const char*)p.bx + (size_t)(q * 2 + c - 2) * 131072;
            else if (c < 7) src = (const char*)p.bwqkv + (size_t)(q * 3 + c - 4) * 131072; else src = (const char*)p.bwo + (size_t)q * 131072;
#pragma unroll
            for (int i = 0; i < 16; ++i) { __builtin_amdgcn_global_load_lds((const unsigned*)(src + i * 8192 + t * 16), (LAS unsigned*)(lds + L_SCR + wv * 1024), 16, 0, 0); if ((i & (KWP - 1)) == KWP - 1) WAIT_V(0); }
            if (KNT1) {
#pragma unroll
                for (int i = 0; i < 8; ++i) { __builtin_amdgcn_global_load_lds((const unsigned*)((const char*)p.wo + (size_t)id * 65536 + i * 8192 + t * 16), (LAS unsigned*)(lds + L_SCR + wv * 1024), 16, 0, 0); if ((i & (KWP - 1)) == KWP - 1) WAIT_V(0); }
            }
        }
    }
    volatile LAS unsigned* qw = (volatile LAS unsigned*)(lds + 131072);
    const int w = __builtin_amdgcn_readfirstlane(threadIdx.x >> 6);
    const int k = b & 3;
    bool fixed = (b & 7) >= 4;
#pragma unroll 1
    for (;;) {
        int kind, x, it = -1, x2 = -1;
        if (fixed) { const int i = id & 31; kind = 6; x = i < 24 ? (16 + 8 * k + (i & 7)) * 8 + 2 + (i >> 3) : (4 * k + (i & 3)) * 8 + ((i >> 2) & 1); x2 = x + (i < 24 ? 3 : 2); fixed = false; }
        else {
        if (threadIdx.x == 0) qw[0] = __hip_atomic_fetch_add(ctl + C_HEAD + k * 32, 1u, RLX_AGENT) + 1u;
        LDS_BAR();
        it = (int)__builtin_amdgcn_readfirstlane(qw[0]);
        LDS_BAR();
        if (it < 4) { kind = 0; x = it * 4 + k; }
        else if (it < 28) { kind = 6; x = 384 + 24 * k + it - 4; }
        else if (it < 36) { kind = 6; x = (4 * k + ((it - 28) & 3)) * 8 + 4 + ((it - 28) >> 2); }
        else if (it < 38) { kind = 6; x = 512 + (it - 36) * 4 + k; }
        else if (it < 42) { kind = 0; x = (it - 38 + 4) * 4 + k; }
        else if (it < 74) { kind = 1; x = ((it - 42) >> 1) * 8 + 2 * k + ((it - 42) & 1); }
        else if (it < 82) { kind = 6; x = (4 * k + ((it - 74) & 3)) * 8 + 6 + ((it - 74) >> 2); }
        else if (it < 86) { kind = 4; x = (it - 82) * 4 + k; }
        else if (it < 94) { kind = 6; x = 480 + (it - 86) * 4 + k; }
        else if (it < 110) { kind = 2; x = (4 + ((it - 94) >> 1)) * 8 + 2 * k + ((it - 94) & 1); }
        else if (it < 134) { kind = 5; x = ((it - 110) >> 3) * 32 + 8 * k + ((it - 110) & 7); }
        else break;
        }
        if (kind == 6) {
            if (x >= 480) { const int w0 = x < 512 ? C_OB + ((x - 480) >> 2) * 64 : C_OA + ((x - 512) >> 2) * 64; const unsigned need = x < 512 ? 64u : 32u; wg_wait(ctl, w0, need, w0 + 32, need); }
            BigSched S{p, par, x, x2};
            gemm_stream(lds, S, ctl);
        } else
        if (kind == 0) {
            const int s0 = (x >> 2) * 4;
            wait_count(ctl, C_Q + (s0 >> 3) * 32, 128u); wait_count(ctl, C_PROG, (unsigned)s0 + 5u);
            asm volatile("" ::: "memory");
#pragma unroll 1
            for (int s = s0; s < s0 + 4; ++s) attn_a_item<false>(p, par, s, (x & 3) * 8 + w, lds + w * 12288, fresh_tid() & 63);
            WAIT_V(0); signal(ctl, C_OA + (x >> 2) * 32, 1);
            BAR();
        } else if (kind == 4) {
#pragma unroll 1
            for (int s = 32 + (x >> 2); s < NSEG; s += 4) {
                wait_count(ctl, C_Q + (s >> 3) * 32, 128u); wait_count(ctl, C_KV + (s >> 2) * 512 + ((x & 3) * 8 + w) * 16, 16u);
                asm volatile("" ::: "memory");
                attn_a_item<true>(p, par, s, (x & 3) * 8 + w, lds + w * 12288, fresh_tid() & 63);
                WAIT_V(0);
                if (s < 48) signal(ctl, C_OA + (s >> 2) * 32, 1);
                else { BAR(); const int l = fresh_tid(); if (l < 4) __hip_atomic_fetch_add(ctl + C_OS + (s - 48) * 256 + l * 64, 1u, RLX_AGENT); }
            }
            BAR();
        } else if (kind == 1) {
            const int qb = x >> 3;
            wait_count(ctl, C_B + (qb >> 1) * 32, 96u); wait_count(ctl, C_B + max((qb >> 1) - 1, 0) * 32, 96u);
            asm volatile("" ::: "memory");
            attn_b_item(p, par, x & 7, qb, lds);
        } else if (kind == 5) {
            out_tail(p, par, x & 31, x >> 5, lds);
        } else {
            const int rb = x >> 3, ct = x & 7;
            Unit u; u.ldb = DM * 2; u.ldc = 2048 * 2; u.sig = 0; u.nsig = 0; u.lda = 0; u.ak = 2048; u.ah = 0; u.cw = 64; u.ch = 0;
            u.A = p.ws + WH(WS_OA + (size_t)rb * 2097152); u.B = (const char*)(p.wo + ct * 128); u.nt = 64; u.C = (char*)(p.out + (size_t)rb * 128 * 2048 + ct * 128);
            wg_wait(ctl, C_OA + rb * 32, rb < 8 ? 32u : 128u, C_OA + rb * 32, 32u);
            gemm_small(lds, u, ctl);
        }
        WSTAMP(16 + (kind == 6 && x >= 480 ? (x < 512 ? 7 : 8) : kind) + ((it & 0xffff) << 8));
    }
}

__global__ __launch_bounds__(512) void k_init(char* ws) {
    const rsrc_t xr = mkrsrc(ws + WS_XCH, NWIN_INIT * 16384u);
    unsigned* ctl = (unsigned*)(ws + WS_CTL);
    const u32x4 sv = {SENT, SENT, SENT, SENT};
    const unsigned xcc = (unsigned)__builtin_amdgcn_s_getreg(20 | (3 << 11)) & 7u, half = blockIdx.x >> 3, win = blockIdx.x & 7;
    if (threadIdx.x < 64 && xcc == 1) {
        unsigned b = ~0u;
        for (int i = 0; i < 5; ++i) {
            const unsigned long long t0 = __builtin_amdgcn_s_memtime();
            __builtin_amdgcn_raw_buffer_store_b32(0u, xr, (unsigned)(2 * 16384 + C_PROBE * 4) + half * 8192u + (unsigned)i * 256u, 0, 16);
            asm volatile("s_waitcnt vmcnt(0)" ::: "memory");
            b = min(b, (unsigned)(__builtin_amdgcn_s_memtime() - t0));
        }
        if (threadIdx.x == 0) ctl[C_PAR + half] = b;
    }
    __syncthreads();
    if (win < 7 && win != 5) ST16(sv, xr, win * 16384u + half * 8192u + threadIdx.x * 16u, 0, 16);
    if (blockIdx.x == 0 && threadIdx.x == 0) ctl[C_PHASE] = xcc;
}

extern "C" void kernel_launch(void* const* d_in, const int* in_sizes, int n_in, void* d_out, int out_size, void* d_ws, size_t ws_size, hipStream_t stream) {
    Params p;
    p.ax = (const u16*)d_in[0]; p.mem0 = (const u16*)d_in[1]; p.wq = (const u16*)d_in[2]; p.wk = (const u16*)d_in[3]; p.wv = (const u16*)d_in[4]; p.wo = (const u16*)d_in[5];
    p.bx = (const u16*)d_in[7]; p.bwqkv = (const u16*)d_in[8]; p.bwo = (const u16*)d_in[9];
    p.out = (u16*)d_out; p.ws = (char*)(((uintptr_t)d_ws + WS_SHIFT(ws_size) + 0x1FFFFFu) & ~(uintptr_t)0x1FFFFFu);
    (void)hipFuncSetAttribute((const void*)k_fused, hipFuncAttributeMaxDynamicSharedMemorySize, LDS_BYTES);
    k_init<<<16, 512, 0, stream>>>(p.ws);
    k_fused<<<256, 512, LDS_BYTES, stream>>>(p);
}
```

```cpp
#include <hip/hip_runtime.h>
#include <stdint.h>
#include <stdlib.h>

#define LAS __attribute__((address_space(3)))
#define DI __device__ __forceinline__
typedef unsigned short u16;
typedef __attribute__((ext_vector_type(8))) __bf16 bf16x8;
typedef __attribute__((ext_vector_type(16))) __bf16 bf16x16;
typedef __attribute__((ext_vector_type(2))) __bf16 bf16x2;
typedef __attribute__((ext_vector_type(4))) short s16x4;
typedef __attribute__((ext_vector_type(8))) short s16x8;
typedef __attribute__((ext_vector_type(16))) short s16x16;
typedef __attribute__((ext_vector_type(2))) float f32x2;
typedef __attribute__((ext_vector_type(4))) float f32x4;
typedef __attribute__((ext_vector_type(2))) unsigned u32x2;
typedef __attribute__((ext_vector_type(4))) unsigned u32x4;
typedef LAS unsigned char* ldsp;
typedef __amdgpu_buffer_rsrc_t rsrc_t;

constexpr int S_LEN = 2048, DM = 1024, NH = 32, DH = 128, NSEG = 64;
constexpr int LDB3 = 3072;
constexpr unsigned XH = S_LEN * 512, BH = S_LEN * 128;
constexpr unsigned SENT = 0xFFFFFFFFu;
constexpr size_t WS_XCH = 2u << 20;
constexpr int NWIN = 640, NWIN_INIT = 7;
constexpr size_t XCH_BYTES = (size_t)(NWIN + 2048 + 512) * 16384;
constexpr size_t WS_CTL = WS_XCH + 2 * 16384;
constexpr int C_TMO = 0, C_PAR = 13312  , C_PHASE = 13314, C_HEAD = 3072, C_PROG = 192, C_PACE = 128, C_Q = 256, C_B = C_Q + 8 * 32, C_OA = C_B + 8 * 32, C_OB = C_OA + 16 * 32, C_OS = 16384, C_KV = 4096, C_PROBE = 12288;
constexpr size_t WS_QKVX = 52u << 20;
constexpr size_t WS_QKVB = WS_QKVX + (size_t)96 * XH;
constexpr size_t WS_OA = WS_QKVB + (size_t)96 * BH;
constexpr size_t WS_OB = WS_OA + (size_t)S_LEN * 4096 * 4;
constexpr size_t WS_END = WS_OB + (size_t)S_LEN * 1024 * 4;
#define C_KVC(rb, h) (C_KV + (h) * 32 + (rb))
#define gap(L) (((L) & 8191u) | (((L) >> 13) << 14))
#define WH(off) ((off) + (size_t)((((unsigned)((off) >> 21) ^ par ^ KWH) & 1u) << 13))
#define KWH 0
#define WS_SHIFT(sz) 0
constexpr int LDS_BYTES = 131072 + 1024 + 8192;

DI unsigned pk_bf16(float a, float b) { f32x2 v = {a, b}; return __builtin_bit_cast(unsigned, __builtin_convertvector(v, bf16x2)); }
DI s16x4 trd(ldsp p) { return __builtin_amdgcn_ds_read_tr16_b64_v4i16((LAS s16x4*)p); }
DI bf16x8 cat8(s16x4 a, s16x4 b) { return __builtin_bit_cast(bf16x8, __builtin_shufflevector(a, b, 0, 1, 2, 3, 4, 5, 6, 7)); }
DI bf16x16 cat16(bf16x8 a, bf16x8 b) { return __builtin_shufflevector(a, b, 0, 1, 2, 3, 4, 5, 6, 7, 8, 9, 10, 11, 12, 13, 14, 15); }
DI bf16x8 lo8(bf16x16 v) { return __builtin_shufflevector(v, v, 0, 1, 2, 3, 4, 5, 6, 7); }
DI bf16x8 hi8(bf16x16 v) { return __builtin_shufflevector(v, v, 8, 9, 10, 11, 12, 13, 14, 15); }
DI bf16x8 asbf(u32x4 v) { return __builtin_bit_cast(bf16x8, v); }
DI f32x4 mma32(bf16x8 a, bf16x8 b, f32x4 c) { return __builtin_amdgcn_mfma_f32_16x16x32_bf16(a, b, c, 0, 0, 0); }
DI f32x4 mma16(u32x2 a, u32x2 b, f32x4 c) { return mma32(asbf((u32x4){a.x, a.y, 0u, 0u}), asbf((u32x4){b.x, b.y, 0u, 0u}), c); }
DI f32x4 smma(u32x4 a, bf16x16 b, f32x4 c, int idx) { return __builtin_amdgcn_smfmac_f32_16x16x64_bf16(asbf(a), b, c, idx, 0, 0); }
DI rsrc_t mkrsrc(const void* p, unsigned bytes) { return __builtin_amdgcn_make_buffer_rsrc((void*)p, 0, bytes, 0x00020000); }
DI u32x4 ld16_sc1(rsrc_t r, unsigned voff, unsigned soff) { return __builtin_amdgcn_raw_buffer_load_b128(r, voff, soff, 16); }
#define ST16(v, r, voff, soff, aux) do { const u32x4 st_v = (v); __builtin_amdgcn_raw_buffer_store_b128(st_v, r, voff, soff, aux); asm volatile("s_nop 1" :: "v"(st_v)); } while (0)
DI void swap16(unsigned& a, unsigned& b) { auto r = __builtin_amdgcn_permlane16_swap(a, b, false, false); a = r[0]; b = r[1]; }
DI void swap32(unsigned& a, unsigned& b) { auto r = __builtin_amdgcn_permlane32_swap(a, b, false, false); a = r[0]; b = r[1]; }
DI void swap16f(float& a, float& b) { unsigned x = __float_as_uint(a), y = __float_as_uint(b); swap16(x, y); a = __uint_as_float(x); b = __uint_as_float(y); }
DI void swap32f(float& a, float& b) { unsigned x = __float_as_uint(a), y = __float_as_uint(b); swap32(x, y); a = __uint_as_float(x); b = __uint_as_float(y); }
DI float rows_max(float v) { float a = v, b = v; swap16f(a, b); v = fmaxf(a, b); a = v; b = v; swap32f(a, b); return fmaxf(a, b); }
DI float rows_sum(float v) { float a = v, b = v; swap16f(a, b); v = a + b; a = v; b = v; swap32f(a, b); return a + b; }
DI unsigned umax4(u32x4 v) { return max(max(v.x, v.y), max(v.z, v.w)); }
#define WAIT_V(n) asm volatile("s_waitcnt vmcnt(" #n ")" ::: "memory")
#define WAIT_L(n) asm volatile("s_waitcnt lgkmcnt(" #n ")" ::: "memory")
#define BAR() __builtin_amdgcn_s_barrier()
#define SCHED() __builtin_amdgcn_sched_barrier(0)
#define LDS_BAR() do { asm volatile("s_waitcnt lgkmcnt(0)" ::: "memory"); __builtin_amdgcn_s_barrier(); asm volatile("" ::: "memory"); } while (0)

constexpr int HTB = 128 * 64 * 2;
DI int lds_byte(int r, int c) { const int st = (r >> 4) * 2 + (c >> 5), rr = r & 15, cc = c & 31, ob = rr * 64 + cc * 2; return st * 1024 + (ob ^ (((ob >> 9) & 1) << 5)); }
DI void stage_rc(int b, int& R, int& C) { const int st = b / 1024, sb = b % 1024, swz = sb ^ (((sb >> 9) & 1) << 5); R = (st >> 1) * 16 + swz / 64; C = (st & 1) * 32 + (swz % 64) / 2; }

struct Params {
    const u16 *ax, *mem0, *wq, *wk, *wv, *wo, *bx, *bwqkv, *bwo;
    u16* out; char* ws;
};
#define RLX_AGENT __ATOMIC_RELAXED, __HIP_MEMORY_SCOPE_AGENT
DI bool spin_out(unsigned& spins, unsigned* ctl) {
    spins = __builtin_amdgcn_readfirstlane(spins) + 1u;
    if ((spins & 63u) == 0u) {
        if (spins > (1u << 18)) { __hip_atomic_store(ctl + C_TMO, 1u, RLX_AGENT); return true; }
        if (__builtin_amdgcn_readfirstlane(__hip_atomic_load(ctl + C_TMO, RLX_AGENT)) != SENT) return true;
    }
    return false;
}
template <int NAP = 8>
DI void wait_count(unsigned* ctl, int word, unsigned need) {
    unsigned spins = 0;
    while (__builtin_amdgcn_readfirstlane(__hip_atomic_load(ctl + word, RLX_AGENT)) + 1u < need) { __builtin_amdgcn_s_sleep(NAP); if (spin_out(spins, ctl)) break; }
}
DI void wg_wait(unsigned* ctl, int word0, unsigned need0, int word1, unsigned need1) {
    if (threadIdx.x < 64) {
        wait_count(ctl, word0, need0); wait_count(ctl, word1, need1);
        __builtin_amdgcn_fence(__ATOMIC_ACQUIRE, "agent");
        asm volatile("s_waitcnt vmcnt(0)" ::: "memory");
    }
    asm volatile("" ::: "memory"); BAR(); asm volatile("" ::: "memory");
}
struct Unit { const char* A; const char* B; char* C; unsigned lda, ldb, ldc, cw, ch, ak, ah; int nt, sig, nsig; };
DI int fresh_tid() { int t = threadIdx.x; asm volatile("" : "+v"(t)); return t; }
DI void signal(unsigned* ctl, int sig, int nsig) { const int lane = fresh_tid() & 63; if (lane < nsig) __hip_atomic_fetch_add(ctl + sig + (lane & 1) * 32 + (lane >> 1), 1u, RLX_AGENT); }
DI u32x4 pack_pair(f32x4 v0, f32x4 v1) {
    unsigned ax = pk_bf16(v0[0], v0[1]), ay = pk_bf16(v0[2], v0[3]), bx = pk_bf16(v1[0], v1[1]), by = pk_bf16(v1[2], v1[3]);
    swap16(ax, bx); swap16(ay, by);
    return (u32x4){ax, ay, bx, by};
}
DI unsigned frag_off(int lane) { return (unsigned)((((lane >> 4) & 1) * 2 + (lane >> 5)) * 256 + (lane & 15) * 16); }
DI void store_frag(rsrc_t cr, unsigned voff, unsigned soff, f32x4 v0, f32x4 v1) { ST16(pack_pair(v0, v1), cr, voff, soff, 16); }
DI u32x4 to_rows(ldsp sc, int lane, f32x4 v0, f32x4 v1) {
    *(volatile LAS u32x4*)(sc + ((lane & 15) * 4 + ((lane >> 4) & 1) * 2 + (lane >> 5)) * 16) = pack_pair(v0, v1);
    return *(volatile LAS u32x4*)(sc + lane * 16);
}
constexpr int L_SCR = 132096;
#define STAGE(bufoff, gbase, voff, step2) do { \
        __builtin_amdgcn_global_load_lds((const unsigned*)((gbase) + (voff)), (LAS unsigned*)(lds + (bufoff) + ldsw), 16, 0, 0); \
        __builtin_amdgcn_global_load_lds((const unsigned*)((gbase) + (step2) + (voff)), (LAS unsigned*)(lds + (bufoff) + ldsw + 8192), 16, 0, 0); } while (0)
#define LDA(dst, off) do { _Pragma("unroll") for (int m = 0; m < 4; ++m) _Pragma("unroll") for (int k = 0; k < 2; ++k) dst[m][k] = *(const LAS bf16x8*)(lds + (off) + aoff + m * 2048 + k * 1024); } while (0)
#define TRD(dst, base, off) asm volatile("ds_read_b64_tr_b16 %0, %1 offset:%2" : "=v"(dst) : "v"(base), "i"(off) : "memory")
#define LDB(dst, v0, v1, off) do { s16x4 l0, h0, l1, h1, l2, h2, l3, h3; \
        TRD(l0, v0, (off)); TRD(h0, v0, (off) + 256); TRD(l1, v0, (off) + 8192); TRD(h1, v0, (off) + 8448); \
        TRD(l2, v1, (off)); TRD(h2, v1, (off) + 256); TRD(l3, v1, (off) + 8192); TRD(h3, v1, (off) + 8448); \
        dst[0][0] = cat8(l0, h0); dst[0][1] = cat8(l1, h1); dst[1][0] = cat8(l2, h2); dst[1][1] = cat8(l3, h3); } while (0)
#define MMA(accv, At, Bt) do { SCHED(); __builtin_amdgcn_s_setprio(1); _Pragma("unroll") for (int m = 0; m < 4; ++m) _Pragma("unroll") for (int n = 0; n < 2; ++n) _Pragma("unroll") for (int k = 0; k < 2; ++k) \
        accv[m][n] = mma32(Bt[n][k], At[m][k], accv[m][n]); __builtin_amdgcn_s_setprio(0); } while (0)
DI unsigned va_of(int tid, unsigned lda, int nt) { int R, C; stage_rc(tid * 16, R, C); return lda ? (unsigned)R * lda + (unsigned)C * 2u : (unsigned)((tid >> 7) * nt * 4096 + (tid & 127) * 16); }
DI unsigned vb_of(int tid, int wid, unsigned ldb) { return (unsigned)(8 * (wid >> 1) + ((tid >> 2) & 7)) * ldb + (unsigned)((2 * (wid & 1) + ((tid >> 5) & 1)) * 64 + (((tid & 3) ^ (((wid >> 1) & 1) << 1)) * 16)); }
#define GEMM_LANE_SETUP(lda0) \
    const int tid = fresh_tid(); \
    const int wid = __builtin_amdgcn_readfirstlane(tid >> 6), lane = tid & 63, wr = wid >> 2, wc = wid & 3, fr = lane & 15, fq = lane >> 4; \
    const unsigned ldsw = (unsigned)wid * 1024u; \
    const int aoff = (lda0) ? lds_byte(wr * 64 + fr, fq * 8) : wr * 8192 + lane * 16; \
    const int boff0 = fq * 2048 + wc * 512 + (fr >> 2) * 64 + (fq & 1) * 32 + (fr & 3) * 8, boff1 = boff0 ^ 32;

template <class Sched>
DI void gemm_stream(ldsp lds, const Sched& S, unsigned* ctl) {
    Unit cur, nxt; int ui = 0;
    if (!S.next(0, cur)) return;
    GEMM_LANE_SETUP(cur.lda);
#define SA(b, h) (((b) * 2 + (h)) * HTB)
#define SB(b, h) ((4 + (b) * 2 + (h)) * HTB)
    const unsigned vb0 = (unsigned)(size_t)lds + 65536u + (unsigned)boff0, vb1 = (unsigned)(size_t)lds + 65536u + (unsigned)boff1;
    f32x4 acc[2][2][4][2];
#pragma unroll
    for (int a = 0; a < 2; ++a)
#pragma unroll
        for (int b = 0; b < 2; ++b)
#pragma unroll
            for (int m = 0; m < 4; ++m)
#pragma unroll
                for (int n = 0; n < 2; ++n) acc[a][b][m][n] = (f32x4){0.f, 0.f, 0.f, 0.f};
    bf16x8 At[4][2], B0[2][2], B1[2][2];
    const unsigned vA = va_of(tid, cur.lda, cur.nt); unsigned vB = vb_of(tid, wid, cur.ldb);
    const size_t a2s = cur.lda ? (size_t)64 * cur.lda : (size_t)cur.nt * 16384;
    {
        const size_t ah = cur.ah, b2s = (size_t)32 * cur.ldb, bk = (size_t)64 * cur.ldb;
        STAGE(SB(0, 0), cur.B, vB, b2s); STAGE(SB(0, 1), cur.B + 256, vB, b2s); STAGE(SA(0, 0), cur.A, vA, a2s); STAGE(SA(0, 1), cur.A + ah, vA, a2s);
        if (wr == 1) BAR();
        WAIT_V(2); BAR();
        STAGE(SB(1, 0), cur.B + bk, vB, b2s); STAGE(SA(1, 0), cur.A + cur.ak, vA, a2s); STAGE(SB(1, 1), cur.B + bk + 256, vB, b2s);
        WAIT_V(6); BAR();
    }
    int psig = 0, pn = 0;
    for (;;) {
        const bool has_next = S.next(ui + 1, nxt);
        if (!has_next) nxt = cur;
        int nt = cur.nt; asm volatile("" : "+s"(nt));
        for (int t = 0; t < nt; t += 2) {
            const bool last = (t == nt - 2);
            const char* a1 = cur.A + (cur.lda ? (unsigned)(t + 1) * 128u : gap((unsigned)(t + 1) * 2048u)) + cur.ah;
            if (last) vB = vb_of(fresh_tid(), wid, nxt.ldb);
            const unsigned ldb = last ? nxt.ldb : cur.ldb;
            const char* a2 = last ? nxt.A : cur.A + (cur.lda ? (unsigned)(t + 2) * 128u : gap((unsigned)(t + 2) * 2048u));
            const char* b2 = last ? nxt.B : cur.B + (size_t)(t + 2) * 64 * cur.ldb;
            const size_t ah = cur.ah, b2s = (size_t)32 * ldb;
            const char* a3 = a2 + cur.ak; const char* b3 = b2 + (size_t)64 * ldb;
            LDB(B0, vb0, vb1, 0); LDB(B1, vb0, vb1, HTB); SCHED(); LDA(At, SA(0, 0)); STAGE(SA(1, 1), a1, vA, a2s);
            WAIT_V(8); WAIT_L(0); BAR(); MMA(acc[0][0], At, B0); MMA(acc[0][1], At, B1); BAR(); SCHED();
            LDA(At, SA(0, 1)); STAGE(SB(0, 0), b2, vB, b2s); STAGE(SB(0, 1), b2 + 256, vB, b2s); STAGE(SA(0, 0), a2, vA, a2s);
            WAIT_V(8);
            asm volatile("" : "+s"(pn));
            if (pn) { signal(ctl, psig, pn); pn = 0; }
            WAIT_L(0); BAR(); MMA(acc[1][0], At, B0); MMA(acc[1][1], At, B1); BAR(); SCHED();
            LDB(B0, vb0, vb1, 2 * HTB); LDB(B1, vb0, vb1, 3 * HTB); SCHED(); LDA(At, SA(1, 0)); STAGE(SA(0, 1), a2 + ah, vA, a2s);
            WAIT_V(8); WAIT_L(0); BAR(); MMA(acc[0][0], At, B0); MMA(acc[0][1], At, B1); BAR(); SCHED();
            LDA(At, SA(1, 1)); STAGE(SB(1, 0), b3, vB, b2s); STAGE(SB(1, 1), b3 + 256, vB, b2s); STAGE(SA(1, 0), a3, vA, a2s);
            WAIT_V(8); WAIT_L(0); BAR(); MMA(acc[1][0], At, B0); MMA(acc[1][1], At, B1); BAR(); SCHED();
        }
        if (wr == 0) BAR();
        {
            const rsrc_t cr = mkrsrc(cur.C, 0x1000000u);
            const int tz = fresh_tid() & 63;
            if (cur.nsig) {
                const unsigned co = (unsigned)wc * cur.cw + frag_off(tz);
#pragma unroll
                for (int ai = 0; ai < 2; ++ai)
#pragma unroll
                    for (int m = 0; m < 4; ++m)
#pragma unroll
                        for (int bj = 0; bj < 2; ++bj) store_frag(cr, co, gap((unsigned)(ai * 128 + wr * 64 + m * 16) * cur.ldc) + (unsigned)bj * cur.ch, acc[ai][bj][m][0], acc[ai][bj][m][1]);
            } else {
                const unsigned co = (unsigned)(wr * 64 + (tz >> 2)) * cur.ldc + (unsigned)(wc * 64 + (tz & 3) * 16);
#pragma unroll
                for (int ai = 0; ai < 2; ++ai)
#pragma unroll
                    for (int m = 0; m < 4; ++m)
#pragma unroll
                        for (int bj = 0; bj < 2; ++bj)
                            ST16(to_rows(lds + L_SCR + wid * 1024, tz, acc[ai][bj][m][0], acc[ai][bj][m][1]), cr, co, (unsigned)(ai * 128 + m * 16) * cur.ldc + (unsigned)bj * 256u, 0);
            }
        }
        psig = cur.sig; pn = cur.nsig;
        if (!has_next) break;
#pragma unroll
        for (int a = 0; a < 2; ++a)
#pragma unroll
            for (int b = 0; b < 2; ++b)
#pragma unroll
                for (int m = 0; m < 4; ++m)
#pragma unroll
                    for (int n = 0; n < 2; ++n) acc[a][b][m][n] = (f32x4){0.f, 0.f, 0.f, 0.f};
        cur = nxt; ++ui;
        if (wr == 1) BAR();
    }
    WAIT_V(0);
    signal(ctl, psig, pn);
    BAR();
#undef SA
#undef SB
}

DI void gemm_small(ldsp lds, const Unit u, unsigned* ctl) {
    GEMM_LANE_SETUP(u.lda);
    const unsigned vA = va_of(tid, u.lda, u.nt), vB = vb_of(tid, wid, u.ldb);
    const size_t a2s = u.lda ? (size_t)64 * u.lda : (size_t)u.nt * 16384, b2s = (size_t)32 * u.ldb, bk = (size_t)64 * u.ldb;
    f32x4 acc[4][2];
#pragma unroll
    for (int m = 0; m < 4; ++m)
#pragma unroll
        for (int n = 0; n < 2; ++n) acc[m][n] = (f32x4){0.f, 0.f, 0.f, 0.f};
    int nt = u.nt; asm volatile("" : "+s"(nt));
    const unsigned vb0 = (unsigned)(size_t)lds + (unsigned)boff0, vb1 = (unsigned)(size_t)lds + (unsigned)boff1, vb2 = vb0 + 65536u, vb3 = vb1 + 65536u;
#pragma unroll
    for (int t = 0; t < 4; ++t) { STAGE(t * 32768, u.A + (size_t)t * u.ak, vA, a2s); STAGE(t * 32768 + 16384, u.B + t * bk, vB, b2s); }
    bf16x8 A0[4][2], B0[2][2], A1[4][2], B1[2][2];
    WAIT_V(12); BAR();
    LDB(B0, vb0, vb1, 16384); LDA(A0, 0);
#define RDA(dst, m, off) do { dst[m][0] = *(const LAS bf16x8*)(lds + (off) + aoff + (m) * 2048); dst[m][1] = *(const LAS bf16x8*)(lds + (off) + aoff + (m) * 2048 + 1024); } while (0)
#define RDB(dst, nn, v, off) do { s16x4 l0, h0, l1, h1; TRD(l0, v, (off)); TRD(h0, v, (off) + 256); TRD(l1, v, (off) + 8192); TRD(h1, v, (off) + 8448); dst[nn][0] = cat8(l0, h0); dst[nn][1] = cat8(l1, h1); } while (0)
#define MM4(Ac, Bc, m) do { SCHED(); _Pragma("unroll") for (int n = 0; n < 2; ++n) _Pragma("unroll") for (int k = 0; k < 2; ++k) acc[m][n] = mma32(Bc[n][k], Ac[m][k], acc[m][n]); SCHED(); } while (0)
#define KSTEP(i, Ac, Bc, An, Bn, v0, v1, boffn) do { \
        const int tn = min(t0 + (i) + 4, nt - 1); \
        WAIT_L(0); WAIT_V(8); BAR(); \
        MM4(Ac, Bc, 0); RDB(Bn, 0, v0, boffn); RDA(An, 0, (((i) + 1) & 3) * 32768); \
        MM4(Ac, Bc, 1); RDB(Bn, 1, v1, boffn); RDA(An, 1, (((i) + 1) & 3) * 32768); \
        MM4(Ac, Bc, 2); RDA(An, 2, (((i) + 1) & 3) * 32768); STAGE((i) * 32768, u.A + (u.lda ? (unsigned)tn * 128u : gap((unsigned)tn * 2048u)), vA, a2s); \
        MM4(Ac, Bc, 3); RDA(An, 3, (((i) + 1) & 3) * 32768); STAGE((i) * 32768 + 16384, u.B + (size_t)tn * bk, vB, b2s); } while (0)
    for (int t0 = 0; t0 < nt; t0 += 4) {
        KSTEP(0, A0, B0, A1, B1, vb0, vb1, 49152);
        KSTEP(1, A1, B1, A0, B0, vb2, vb3, 16384);
        KSTEP(2, A0, B0, A1, B1, vb2, vb3, 49152);
        KSTEP(3, A1, B1, A0, B0, vb0, vb1, 16384);
    }
    WAIT_L(0); SCHED();
#undef RDA
#undef RDB
#undef MM4
#undef KSTEP
    const rsrc_t cr = mkrsrc(u.C, 256u * u.ldc);
    if (u.nsig) {
#pragma unroll
        for (int m = 0; m < 4; ++m) store_frag(cr, (unsigned)wc * u.cw + frag_off(lane), gap((unsigned)(wr * 64 + m * 16) * u.ldc), acc[m][0], acc[m][1]);
    } else {
#pragma unroll
        for (int m = 0; m < 4; ++m)
            ST16(to_rows(lds + L_SCR + wid * 1024, lane, acc[m][0], acc[m][1]), cr, (unsigned)(wr * 64 + (lane >> 2)) * u.ldc + (unsigned)(wc * 64 + (lane & 3) * 16), (unsigned)(m * 16) * u.ldc, 0);
    }
    WAIT_V(0);
    signal(ctl, u.sig, u.nsig);
    BAR();
}

#define win_off(idx) ((unsigned)(idx) * 16384u + ((((unsigned)(idx) >> 7) ^ par) & 1u) * 8192u)
#define KKM 8192u
#define km_win(s, h) (win_off(NWIN + (s) * 32 + (h)) ^ KKM)
#define W_MEM(s, x) ((s) == 0 ? 0 : NWIN + 2048 + 8 * (s) + 2 * (x))
#define W_PART(slot) (192 + (slot) * 64)
#define W_X(slot) (384 + (slot) * 128)
DI bool all_set(unsigned mx) { return __builtin_amdgcn_ballot_w64(mx == SENT) == 0ull; }
DI u32x4 rows_gather(unsigned v) { unsigned a = v, b = v; swap16(a, b); unsigned c = a, d = b; swap32(a, c); swap32(b, d); return (u32x4){a, b, c, d}; }
DI float max3f(float a, float b, float c) { return fmaxf(fmaxf(a, b), c); }
#define KD0 2
#define KD1 6
#define KD2 5
#define KRA 3
#define KDM 1
#define KWP 4
#define KNT1 0
#define KNT2 0
#define KPM 4
constexpr int RED_M = 144;
constexpr int L_PX = 32768, L_PM = L_PX + 512, L_ML = L_PM + 512, L_FLAG = L_ML + 256, L_PACE = L_FLAG + 16, L_VM = 34816, L_OWN = L_VM + 2048, L_X = L_OWN + 1536  ;
constexpr unsigned OOB = 0x80000000u;
#define RETRY(i)
#define SEG(i)
#define PST(i)
#define PIN_S(x) asm volatile("" : "+s"(x))
#define PIN_V(x) asm volatile("" : "+v"(x))
DI float ror8(float v) { return __uint_as_float(__builtin_amdgcn_update_dpp(0, __float_as_uint(v), 0x128, 0xf, 0xf, true)); }

template <int ROLE>
DI void chain_loop(const Params& p, const unsigned par, const int rank, const int w, ldsp lds, const bf16x16 (&W)[6][2], const bf16x16 (&WO)[2][2], unsigned long long* stamps) {
    const int lane = fresh_tid() & 63, g = lane >> 4, n = lane & 15;
    const int h = rank >> 2, j = rank & 3;
    unsigned* ctl = (unsigned*)(p.ws + WS_CTL);
    const rsrc_t xr = mkrsrc(p.ws + WS_XCH, (unsigned)XCH_BYTES);
    const rsrc_t qr = mkrsrc(p.ws + WH(WS_QKVX + (size_t)(32 + h) * XH), 33u * XH);
    const int sidx = (n & 2) ? 0xEEEE : 0x4444;
    const unsigned l16 = (unsigned)lane * 16u;
    const int redw = (w * 1024 + (n & 1) * 512 + (n >> 1) * 8 + 2 * (g ^ (n >> 3))) * 4;
    const int redr = (w < 4 ? (n >> 3) * RED_M + (4 * w + g) * 8 + ((n & 7) ^ ((w & 1) * 2)) : 2 * RED_M + (8 * (w & 1) + 2 * g + ((n >> 1) & 1)) * 8 + ((2 * (n >> 2) + (n & 1)) ^ (g & 2))) * 4;
    const bool vown = (g >> 1) == (j & 1);
    const unsigned vg1 = (unsigned)(((j & 2) ^ 2) + (g >> 1)) * 1536u + 1024u + (unsigned)(lane & 31) * 16u;
    const unsigned vg0 = vown ? OOB : (vg1 + (unsigned)(j & 2) * 3072u - 3072u);
    const unsigned pub_off = (g == j ? 0u : (unsigned)(4 * h + g) * 16384u) + (unsigned)j * 1536u + (unsigned)w * 256u + (unsigned)n * 16u;
    const int red = ((j + 1) & 3) * 32 + h;
    const int rho = 4 * (8 * j + w) + 2 * (g >> 1) + ((n >> 2) & 1);
    const unsigned pst_off = n < 8 ? (unsigned)(rho >> 1) * 16384u + (unsigned)(rho & 1) * 4096u + (unsigned)((h ^ rho) & 31) * 128u + (unsigned)((g & 1) * 64 + (n & 3) * 16) : OOB;
    const unsigned mem_off = lane < 32 ? (unsigned)(4 * ((n >> 1) & 1) + 2 * ((n >> 2) & 1) + (n & 1)) * 16u + (unsigned)(lane >> 3) * 32768u : OOB;
    const unsigned xw0 = win_off(W_X(0)), xw1 = win_off(W_X(1));
    unsigned pso = win_off(W_PART(0)), psn = win_off(W_PART(1)), psp = win_off(W_PART(2));
    const unsigned redo = (unsigned)(red >> 1) * 16384u + (unsigned)(red & 1) * 4096u;
    const int vxr = L_X + lane * 16;
    const float csc = 0.08838834764831845f * 1.4426950408889634f;
    float ninf = -__builtin_inff(); PIN_V(ninf);
    u32x4 KX[8], VWX[2];
    unsigned long long kv_ok = 0;
    auto dma_x = [&](int s) {
        if (ROLE == 0 && w < 4) {
            if (!((kv_ok >> (s >> 2)) & 1ull)) {
                kv_ok = __builtin_amdgcn_ballot_w64(__hip_atomic_load(ctl + C_KVC(n, h), RLX_AGENT) + 1u >= 16u);
                if (!((kv_ok >> (s >> 2)) & 1ull)) { wait_count(ctl, C_KVC(s >> 2, h), 16u); kv_ok |= 1ull << (s >> 2); }
                asm volatile("" ::: "memory");
            }
#pragma unroll
            for (int i = 0; i < 2; ++i) {
                const unsigned vo = l16, so = (unsigned)s * 16384u + (unsigned)(w * 2 + i) * 1024u;
                __builtin_amdgcn_raw_ptr_buffer_load_lds(qr, (LAS void*)(lds + L_X + (s & 1) * 16384 + (w * 2 + i) * 1024), 16, vo, so + 32u * XH, 0, 16);
                __builtin_amdgcn_raw_ptr_buffer_load_lds(qr, (LAS void*)(lds + L_X + (s & 1) * 16384 + 8192 + (w * 2 + i) * 1024), 16, vo, so, 0, 16);
            }
        }
    };
    auto load_kx = [&](int s) {
#pragma unroll
        for (int kt = 0; kt < 2; ++kt)
#pragma unroll
            for (int kk = 0; kk < 4; ++kk) KX[kt * 4 + kk] = *(const LAS u32x4*)(lds + vxr + (s & 1) * 16384 + 8192 + kt * 4096 + ((j + kk) & 3) * 1024);
    };
    auto vwx_kt = [&](int s, int kt) {
        bf16x8 VA[4];
#pragma unroll
        for (int kk = 0; kk < 4; ++kk) VA[kk] = *(const LAS bf16x8*)(lds + vxr + (s & 1) * 16384 + kt * 4096 + (kk ^ (j & 2)) * 1024);
#pragma unroll
        for (int ct = 0; ct < 2; ++ct) {
            f32x4 d = {0.f, 0.f, 0.f, 0.f};
#pragma unroll
            for (int kk = 0; kk < 4; ++kk) d = mma32(VA[kk], (kk & 1) ? hi8(WO[ct][kk >> 1]) : lo8(WO[ct][kk >> 1]), d);
            VWX[ct][2 * kt] = pk_bf16(d[0], d[1]); VWX[ct][2 * kt + 1] = pk_bf16(d[2], d[3]);
        }
    };
    auto rmax = [&](float v) { float a = v, b = v; swap16f(a, b); v = max3f(a, b, ninf); a = v; b = v; swap32f(a, b); return max3f(a, b, ninf); };
    auto lo2 = [](f32x4 v) { return (f32x2){v[0], v[1]}; };
    auto hi2 = [](f32x4 v) { return (f32x2){v[2], v[3]}; };
    dma_x(0); dma_x(1);
    asm volatile("s_waitcnt vmcnt(0) lgkmcnt(0)" ::: "memory"); BAR(); asm volatile("" ::: "memory");
    PST(5);
    vwx_kt(0, 0); vwx_kt(0, 1);
    if (ROLE == 2) load_kx(0);
    __builtin_amdgcn_s_setprio(2);

    unsigned iv_t0 = 0, iv_acc = 0;
    unsigned spins = 0, pace_c = 0x7fffffffu, pace_t = 0;
    const u32x4 sv = {SENT, SENT, SENT, SENT};
    for (int s = 0;; ++s) {
        unsigned xw = (s & 1) ? xw1 : xw0;
        unsigned mso = win_off(W_MEM(s, rank >> 5) + (w >> 2)) + (unsigned)(w & 3) * 2048u;
        unsigned pub = pub_off + (g == j ? km_win(s, h) : xw);
        PIN_S(xw); PIN_S(mso); PIN_V(pub);
        {
            if (KD0) __builtin_amdgcn_s_sleep(KD0);
            const u32x2 pv = *(const LAS u32x2*)(lds + L_PACE);
            unsigned target = __builtin_amdgcn_readfirstlane(pv.x); const bool valid = __builtin_amdgcn_readfirstlane(pv.y) + 1u == (unsigned)s && s > 3;
            unsigned now = (unsigned)__builtin_amdgcn_s_memrealtime();
            if (ROLE == 3 && s > 2) pace_c = min(pace_c, now - pace_t);
            if (valid && (int)(target - now) < 100) { while ((int)(target - now) > 0) now = (unsigned)__builtin_amdgcn_s_memrealtime(); }
        }
        SEG(10);
        f32x4 c[6]; u32x4 a1;
        u32x4 a0;
        auto ld_a = [&]() { asm volatile("" ::: "memory"); a0 = ld16_sc1(xr, l16, mso); a1 = ld16_sc1(xr, l16 + 1024, mso); };
        auto ok_a = [&]() { return all_set(max(umax4(a0), umax4(a1))); };
        auto mm_a = [&]() {
#pragma unroll
            for (int i = 0; i < 6; ++i) { c[i] = (f32x4){0.f, 0.f, 0.f, 0.f}; PIN_V(c[i]); }
            SCHED();
            c[0] = smma(a0, W[0][0], c[0], sidx); c[1] = smma(a0, W[1][0], c[1], sidx); c[0] = smma(a1, W[0][1], c[0], sidx); c[1] = smma(a1, W[1][1], c[1], sidx);
            c[2] = smma(a0, W[2][0], c[2], sidx); c[3] = smma(a0, W[3][0], c[3], sidx);
        };
        auto put = [&](int i) { *(LAS f32x2*)(lds + redw + ((i >> 1) * RED_M + (i & 1) * 64) * 4) = lo2(c[i]) + hi2(c[i]); };
        ld_a(); SCHED();
        mm_a();
        if (__builtin_expect(!ok_a(), 0)) { bool out; do { RETRY(0); out = spin_out(spins, ctl); ld_a(); } while (!(ok_a() | out)); mm_a(); }
        SEG(0);
        c[2] = smma(a1, W[2][1], c[2], sidx); c[3] = smma(a1, W[3][1], c[3], sidx);
        put(0); put(1);
        SCHED();
        c[4] = smma(a0, W[4][0], c[4], sidx); c[5] = smma(a0, W[5][0], c[5], sidx); c[4] = smma(a1, W[4][1], c[4], sidx);
        put(2); put(3);
        c[5] = smma(a1, W[5][1], c[5], sidx);
        SCHED();
        put(4); put(5);
        SEG(1);
        LDS_BAR();
        SEG(2);
        if (ROLE <= 1) {
            f32x2 v[8];
#pragma unroll
            for (int ww = 0; ww < 8; ++ww) { const LAS float* q = (const LAS float*)(lds + redr + ww * 4096); v[ww] = (f32x2){q[0], q[512]}; }
            const f32x2 t = ((v[0] + v[1]) + (v[2] + v[3])) + ((v[4] + v[5]) + (v[6] + v[7]));
            const u32x4 o = rows_gather(min(pk_bf16(t.x, t.y), SENT - 1));
            ST16(o, xr, pub, 0, 16);
            *(LAS u32x4*)(lds + L_OWN + w * 256 + n * 16) = o;
        } else {
            if (ROLE == 3 && rank == 0) {
                pace_t = (unsigned)__builtin_amdgcn_s_memrealtime();
                if (lane == 0) { __hip_atomic_store(ctl + C_PROG, (unsigned)s, RLX_AGENT);
                    __hip_atomic_store((unsigned long long*)(ctl + C_PACE + par * 2048), (unsigned long long)(pace_t + pace_c + KPM) | ((unsigned long long)s << 32), RLX_AGENT); }
            }
        }
        SEG(3);
        if (s == NSEG - 1) break;
        LDS_BAR();
        SEG(13);
        unsigned VWM[2];
        auto vwm_pack = [&](const f32x4 (&dv)[2]) {
#pragma unroll
            for (int ct = 0; ct < 2; ++ct) { const f32x2 t = lo2(dv[ct]) + hi2(dv[ct]); VWM[ct] = pk_bf16(t.x, t.y); PIN_V(VWM[ct]); }
        };
        f32x4 dv[2] = {{0.f, 0.f, 0.f, 0.f}, {0.f, 0.f, 0.f, 0.f}}, s0, s1;
        PIN_V(dv[0]); PIN_V(dv[1]);
        u32x4 v0, v1;
        if (ROLE == 0) {
            if (KDM == 1) { WAIT_V(0); dma_x(min(s + 2, NSEG - 2)); }
            else if (KDM == 2) { dma_x(min(s + 2, NSEG - 2)); if (w < 4) WAIT_V(4); else WAIT_V(0); }
            else WAIT_V(0);
        } else {
            unsigned cw = xw + (unsigned)rank * 16384u, c1 = cw + (unsigned)((j + 1) & 3) * 1536u, c2 = cw + (unsigned)((j + 2) & 3) * 1536u, c3 = cw + (unsigned)((j + 3) & 3) * 1536u;
            PIN_S(cw); PIN_S(c1); PIN_S(c2); PIN_S(c3);
            const unsigned xl = ROLE == 2 ? (l16 & ~128u) : l16;
            u32x4 vo;
            f32x4 e0 = {0.f, 0.f, 0.f, 0.f}, e1 = e0;
            if (ROLE == 1) { vo = *(const LAS u32x4*)(lds + L_OWN + 1024 + (lane & 31) * 16); if (!vown) vo = (u32x4){0u, 0u, 0u, 0u}; PIN_V(vo); }
            else {
                const u32x4 X0 = *(const LAS u32x4*)(lds + L_OWN + xl);
                if (ROLE == 2) { e0 = mma32(asbf(KX[0]), asbf(X0), e0); e1 = mma32(asbf(KX[4]), asbf(X0), e1); } else e0 = mma32(asbf(X0), asbf(X0), e0);
                PIN_V(e0); PIN_V(e1);
            }
            if (KD1) __builtin_amdgcn_s_sleep(KD1);
            u32x4 X1, X2, X3;
            auto ld_b = [&]() {
                asm volatile("" ::: "memory");
                if (ROLE >= 2) { X1 = ld16_sc1(xr, xl, c1); X2 = ld16_sc1(xr, xl, c2); X3 = ld16_sc1(xr, xl, c3); }
                else { v0 = ld16_sc1(xr, vg0, cw); v1 = ld16_sc1(xr, vg1, cw); }
            };
            auto ok_b = [&]() { return all_set(ROLE >= 2 ? max(umax4(X1), max(umax4(X2), umax4(X3))) : max(umax4(v0), umax4(v1))); };
            auto mm_b = [&]() {
                if (ROLE == 2) {
                    s0 = mma32(asbf(KX[1]), asbf(X1), e0); s1 = mma32(asbf(KX[5]), asbf(X1), e1);
                    s0 = mma32(asbf(KX[2]), asbf(X2), s0); s1 = mma32(asbf(KX[6]), asbf(X2), s1);
                    s0 = mma32(asbf(KX[3]), asbf(X3), s0); s1 = mma32(asbf(KX[7]), asbf(X3), s1);
                } else if (ROLE == 3) {
                    s0 = mma32(asbf(X1), asbf(X1), e0); s1 = mma32(asbf(X2), asbf(X2), e1); s0 = mma32(asbf(X3), asbf(X3), s0);
                } else {
                    v0 |= vo;
                    *(LAS u32x4*)(lds + L_VM + lane * 16) = v0; *(LAS u32x4*)(lds + L_VM + 1024 + lane * 16) = v1;
                }
            };
            SEG(11);
            ld_b(); SCHED(); mm_b();
            if (__builtin_expect(!ok_b(), 0)) { bool out; do { RETRY(1); out = spin_out(spins, ctl); ld_b(); } while (!(ok_b() | out)); mm_b(); }
            SEG(4);
        }
        float m;
        f32x4 sc;
        if (ROLE >= 2) {
            sc = ROLE == 2 ? (n < 8 ? s0 : s1) : s0 + s1;
            if (ROLE == 3 && g < 2) sc = (f32x4){ninf, ninf, ninf, ninf};
            m = max3f(max3f(sc[0], sc[1], sc[2]), sc[3], ninf);
            if (ROLE == 2) m = max3f(m, ror8(m), ninf);
            m = rmax(m);
            PIN_V(m);
        }
        LDS_BAR();
        if (ROLE == 0) {
            const unsigned ro = w < 3 ? xw + (unsigned)rank * 16384u + (unsigned)w * 2048u : psp + redo + (unsigned)(w - 3) * 2048u;
            if (KDM == 4 && s + 2 < NSEG - 1) dma_x(s + 2);
            ST16(sv, xr, l16, ro, 16); ST16(sv, xr, l16 + 1024, ro, 16);
            if (KDM == 5 && s + 2 < NSEG - 1) dma_x(s + 2);
        }
        if (ROLE != 1) { v0 = *(const LAS u32x4*)(lds + L_VM + lane * 16); v1 = *(const LAS u32x4*)(lds + L_VM + 1024 + lane * 16); }
        if (ROLE >= 2) {
            const float mc = -m * csc;
#pragma unroll
            for (int i = 0; i < 4; ++i) sc[i] = __builtin_amdgcn_exp2f(fmaf(sc[i], csc, mc));
            *(LAS u32x2*)(lds + (ROLE == 2 ? L_PX + (g * 8 + (n & 7)) * 16 + (n >> 3) * 8 : L_PM + (g < 2 ? 256 : 0) + n * 16 + (g & 1) * 8)) = (u32x2){pk_bf16(sc[0], sc[1]), pk_bf16(sc[2], sc[3])};
            SCHED();
            dv[0] = smma(v0, WO[0][0], dv[0], sidx); dv[1] = smma(v0, WO[1][0], dv[1], sidx);
            float l = (sc[0] + sc[1]) + (sc[2] + sc[3]);
            if (ROLE == 2) l += ror8(l);
            dv[0] = smma(v1, WO[0][1], dv[0], sidx); dv[1] = smma(v1, WO[1][1], dv[1], sidx);
            l = rows_sum(l);
            *(LAS f32x2*)(lds + L_ML + n * 16 + (ROLE == 2 ? 0 : 8)) = (f32x2){mc, l};
        } else {
#pragma unroll
            for (int ct = 0; ct < 2; ++ct) { dv[ct] = smma(v0, WO[ct][0], dv[ct], sidx); dv[ct] = smma(v1, WO[ct][1], dv[ct], sidx); }
        }
        vwm_pack(dv);
        SEG(5);
        u32x4 am0 = {VWM[0], 0u, 0u, 0u}, am1 = {VWM[1], 0u, 0u, 0u}, pm = {0u, 0u, 0u, 0u};
        PIN_V(am0); PIN_V(am1); PIN_V(pm);
        LDS_BAR();
        SEG(6);
        {
            const f32x4 ml = *(const LAS f32x4*)(lds + L_ML + n * 16);
            const u32x4 px = *(const LAS u32x4*)(lds + L_PX + (g * 8 + (n & 7)) * 16);
            pm.x = *(const LAS unsigned*)(lds + L_PM + n * 16 + g * 4);
            const f32x4 z = {0.f, 0.f, 0.f, 0.f};
            const f32x4 dx0 = mma32(asbf(VWX[0]), asbf(px), z), dx1 = mma32(asbf(VWX[1]), asbf(px), z), dm0 = mma32(asbf(am0), asbf(pm), z), dm1 = mma32(asbf(am1), asbf(pm), z);
            const float dl = ml[0] - ml[2];
            const float e = __builtin_amdgcn_exp2f(-__builtin_fabsf(dl));
            const float ex = dl > 0.f ? e : 1.f, em = dl > 0.f ? 1.f : e;
            const float inv = __builtin_amdgcn_rcpf(fmaf(ex, ml[1], em * ml[3]));
            const f32x4 d0 = (dx0 * ex + dm0 * em) * inv, d1 = (dx1 * ex + dm1 * em) * inv;
            const u32x4 o = {min(pk_bf16(d0[0], d0[1]), SENT - 1), min(pk_bf16(d0[2], d0[3]), SENT - 1), min(pk_bf16(d1[0], d1[1]), SENT - 1), min(pk_bf16(d1[2], d1[3]), SENT - 1)};
            ST16(o, xr, pst_off, pso, 16);
        }
        SCHED();
        SEG(7);
        __builtin_amdgcn_s_setprio(ROLE == 3 ? 1 : 0);
        if (KDM == 3 && s + 2 < NSEG - 1) dma_x(s + 2);
        unsigned long long pv = 0;
        if (ROLE == 1) { pv = __hip_atomic_load((unsigned long long*)(ctl + C_PACE + par * 2048), RLX_AGENT); SCHED(); }
        vwx_kt(s + 1, 0); vwx_kt(s + 1, 1);
        SCHED();
        if (ROLE == 3) __builtin_amdgcn_s_setprio(2);
        if (ROLE == 3) {
            u32x4 SEL;
            { const unsigned one = (n & 1) ? 0x3f800000u : 0x00003f80u; const int dw = (n & 7) >> 1; SEL = (u32x4){dw == 0 ? one : 0u, dw == 1 ? one : 0u, dw == 2 ? one : 0u, dw == 3 ? one : 0u}; }
            unsigned so = pso + redo, mo = win_off(W_MEM(s + 1, 0) + (red >> 6)) + (unsigned)(red & 63) * 128u;
            PIN_S(so); PIN_S(mo); PIN_V(SEL);
            if (KD2) __builtin_amdgcn_s_sleep(KD2);
            f32x4 ta, tb;
            u32x4 q[4];
            auto ld_c = [&]() {
                asm volatile("" ::: "memory");
#pragma unroll
                for (int i = 0; i < 4; ++i) q[i] = ld16_sc1(xr, l16 + i * 1024, so);
            };
            auto ok_c = [&]() { return all_set(max(max(umax4(q[0]), umax4(q[1])), max(umax4(q[2]), umax4(q[3])))); };
            auto mm_c = [&]() {
                ta = mma32(asbf(SEL), asbf(q[0]), (f32x4){0.f, 0.f, 0.f, 0.f}); tb = mma32(asbf(SEL), asbf(q[1]), (f32x4){0.f, 0.f, 0.f, 0.f});
                ta = mma32(asbf(SEL), asbf(q[2]), ta); tb = mma32(asbf(SEL), asbf(q[3]), tb);
            };
            SEG(12);
            ld_c(); mm_c();
            if (__builtin_expect(!ok_c(), 0)) { bool out; do { RETRY(2); out = spin_out(spins, ctl); ld_c(); } while (!(ok_c() | out)); mm_c(); }
            SEG(8);
            f32x4 t = ta + tb;
#pragma unroll
            for (int i = 0; i < 4; ++i) t[i] += ror8(t[i]);
            unsigned a0 = min(pk_bf16(t[0], t[1]), SENT - 1), a1 = min(pk_bf16(t[2], t[3]), SENT - 1);
            unsigned b0 = a0, b1 = a1; swap16(a0, b0); swap16(a1, b1);
            const u32x4 o = {a0, a1, b0, b1};
            ST16(o, xr, mem_off, mo, 16);
        } else {
            if (KDM == 0 && s + 2 < NSEG - 1) dma_x(s + 2);
            if (ROLE == 2) load_kx(s + 1);
            if (ROLE == 1) *(LAS u32x2*)(lds + L_PACE) = (u32x2){(unsigned)pv, (unsigned)(pv >> 32)};
        }
        { const unsigned t = psp; psp = pso; pso = psn; psn = t; }
        SEG(9);
        if (ROLE != 3) __builtin_amdgcn_s_setprio(2);
        LDS_BAR();
        SEG(14);
    }
    WAIT_V(0);
    __builtin_amdgcn_s_setprio(0);
}

DI void chain_role(const Params& p, unsigned par, int rank, ldsp lds, unsigned long long* stamps) {
    const int tid = fresh_tid(), w = __builtin_amdgcn_readfirstlane(tid >> 6), lane = tid & 63, g = lane >> 4, n = lane & 15;
    const int h = rank >> 2, j = rank & 3;
    const rsrc_t xr = mkrsrc(p.ws + WS_XCH, (unsigned)XCH_BYTES);
    unsigned* ctl = (unsigned*)(p.ws + WS_CTL);
    PST(0);
    const int red = ((j + 1) & 3) * 32 + h;
    bf16x16 W[6][2], WO[2][2];
    {
        ldsp my = lds + w * 16384;
        const int rrow = lane >> 2, rc = (lane & 3) * 8, q4 = n >> 2;
        const int toffo = (8 * g + q4) * 64 + (n & 3) * 8;
        const int toffq = (16 * (g & 1) + 8 * (g >> 1) + 4 * (q4 >> 1) + (q4 & 1)) * 64 + (n & 3) * 8;
#define FRAG(base, st) cat16(cat8(trd((base)), trd((base) + (st))), cat8(trd((base) + 2048), trd((base) + 2048 + (st))))
#pragma unroll
        for (int i = 0; i < 8; ++i) {
            __builtin_amdgcn_global_load_lds((const unsigned*)(p.wq + (size_t)(128 * w + 16 * i + rrow) * 4096 + h * 128 + 32 * j + rc), (LAS unsigned*)(my + i * 1024), 16, 0, KNT1);
            __builtin_amdgcn_global_load_lds((const unsigned*)(p.wo + (size_t)(h * 128 + 16 * i + rrow) * 1024 + 256 * j + 32 * w + rc), (LAS unsigned*)(my + 8192 + i * 1024), 16, 0, KNT1);
        }
        {
            const u32x4 sv = {SENT, SENT, SENT, SENT};
            if (w < 6) {
                ST16(sv, xr, (unsigned)lane * 16u, win_off(W_X(0)) + (unsigned)rank * 16384u + (unsigned)w * 1024u, 16);
                ST16(sv, xr, (unsigned)lane * 16u, win_off(W_X(1)) + (unsigned)rank * 16384u + (unsigned)w * 1024u, 16);
            }
            ST16(sv, xr, (unsigned)lane * 16u, win_off(W_PART(w >> 2)) + (unsigned)(red >> 1) * 16384u + (unsigned)(red & 1) * 4096u + (unsigned)(w & 3) * 1024u, 16);
            ST16(sv, xr, (unsigned)lane * 16u, km_win(56 + w, h) + (unsigned)j * 1536u, 16);
            ST16(sv, xr, lane < 32 ? (unsigned)lane * 16u + 1024u : OOB, km_win(56 + w, h) + (unsigned)j * 1536u, 16);
            const int s = 8 * w + (lane >> 3);
#pragma unroll
            for (int x = 0; x < 4; ++x) if (s > 0) ST16(sv, xr, win_off(W_MEM(s, x) + (red >> 6)) + (unsigned)(red & 63) * 128u + (unsigned)(lane & 7) * 16u, 0, 16);
        }
        PST(3);
        WAIT_V(0);
#pragma unroll
        for (int T = 0; T < 2; ++T)
#pragma unroll
            for (int t = 0; t < 2; ++t) { W[T][t] = FRAG(my + t * 4096 + T * 32 + toffq, 128); WO[T][t] = FRAG(my + 8192 + (t ^ (j >> 1)) * 4096 + T * 32 + toffo, 256); }
#pragma unroll
        for (int T = 0; T < 2; ++T) asm volatile("" : "+v"(W[T][0]), "+v"(W[T][1]), "+v"(WO[T][0]), "+v"(WO[T][1]));
        WAIT_L(0);
        PST(1);
#pragma unroll
        for (int i = 0; i < 8; ++i) {
            __builtin_amdgcn_global_load_lds((const unsigned*)(p.wk + (size_t)(128 * w + 16 * i + rrow) * 4096 + h * 128 + 32 * j + rc), (LAS unsigned*)(my + i * 1024), 16, 0, KNT2);
            __builtin_amdgcn_global_load_lds((const unsigned*)(p.wv + (size_t)(128 * w + 16 * i + rrow) * 4096 + h * 128 + 32 * j + rc), (LAS unsigned*)(my + 8192 + i * 1024), 16, 0, KNT2);
        }
        WAIT_V(0);
#pragma unroll
        for (int T = 0; T < 4; ++T)
#pragma unroll
            for (int t = 0; t < 2; ++t) W[2 + T][t] = FRAG(my + (T >> 1) * 8192 + t * 4096 + (T & 1) * 32 + toffq, 128);
#pragma unroll
        for (int T = 0; T < 4; ++T) asm volatile("" : "+v"(W[2 + T][0]), "+v"(W[2 + T][1]));
        WAIT_L(0);
#undef FRAG
    }
    PST(2);
    BAR();
    PST(4);
    if (w == 7 && lane < 8) {
        const unsigned* src = (const unsigned*)(p.mem0 + (4 * (red & 1) + 2 * (lane >> 2) + (lane & 1)) * DM + 32 * (red >> 2) + 8 * ((red >> 1) & 1) + 4 * ((lane >> 1) & 1));
        const u32x4 v = {min(src[0], SENT - 1), min(src[1], SENT - 1), min(src[8], SENT - 1), min(src[9], SENT - 1)};
        ST16(v, xr, (unsigned)lane * 16u + (unsigned)(red & 63) * 128u, win_off(W_MEM(0, 0) + (red >> 6)), 16);
    }
    if (w < 5) chain_loop<0>(p, par, rank, w, lds, W, WO, stamps);
    else if (w == 5) chain_loop<1>(p, par, rank, 5, lds, W, WO, stamps);
    else if (w == 6) chain_loop<2>(p, par, rank, 6, lds, W, WO, stamps);
    else chain_loop<3>(p, par, rank, 7, lds, W, WO, stamps);
}

template <bool POLL>
DI void attn_a_item(const Params& p, unsigned par, int s, int h, ldsp my, int lane) {
    const int g = lane >> 4, n = lane & 15;
    const rsrc_t xr = mkrsrc(p.ws + WS_XCH, (unsigned)XCH_BYTES);
    const rsrc_t qr = mkrsrc(p.ws + WH(WS_QKVX + (size_t)h * XH + (size_t)s * 16384), 65u * XH);
#pragma unroll
    for (int i = 0; i < 8; ++i)
        __builtin_amdgcn_raw_ptr_buffer_load_lds(qr, (LAS void*)(my + i * 1024), 16, (unsigned)(g * 256 + (n ^ ((g & 1) * 8)) * 16), 64u * XH + (unsigned)i * 1024u, 0, 16);
    u32x4 Q[2][4], K[2][4], XM[4];
    const unsigned ro = (unsigned)lane * 16u;
#pragma unroll
    for (int t = 0; t < 2; ++t)
#pragma unroll
        for (int k = 0; k < 4; ++k) { Q[t][k] = ld16_sc1(qr, ro + k * 1024, (unsigned)t * 4096u); K[t][k] = ld16_sc1(qr, ro + k * 1024, (unsigned)t * 4096u + 32u * XH); }
    const unsigned qwin = km_win(s, h);
    u32x4 VM[2];
    if (POLL) {
        wait_count<4>((unsigned*)(p.ws + WS_CTL), C_PROG, (unsigned)s + (s < 56 ? 2u : 1u));
        if (s >= 56) __builtin_amdgcn_s_sleep(16);
    }
    for (unsigned spins = 0;;) {
        asm volatile("" ::: "memory");
#pragma unroll
        for (int k = 0; k < 4; ++k) XM[k] = ld16_sc1(xr, (unsigned)lane * 16u + (k & 1) * 1536, qwin + (k >> 1) * 3072);
#pragma unroll
        for (int ts = 0; ts < 2; ++ts) VM[ts] = ld16_sc1(xr, (unsigned)(g >> 1) * 1536u + 1024u + (unsigned)(lane & 31) * 16u, qwin + ts * 3072);
        if (!POLL || s < 56) break;
        if (all_set(max(max(max(umax4(XM[0]), umax4(XM[1])), max(umax4(XM[2]), umax4(XM[3]))), max(umax4(VM[0]), umax4(VM[1]))))) break;
        __builtin_amdgcn_s_sleep(4);
        if (spin_out(spins, (unsigned*)(p.ws + WS_CTL))) break;
    }
    SCHED();
#pragma unroll
    for (int ts = 0; ts < 2; ++ts) {
        const u32x4 v = VM[ts];
        const int m = 2 * (n >> 2) + (n & 1), hf = (n >> 1) & 1;
#pragma unroll
        for (int i = 0; i < 4; ++i) { const int e = 64 * ts + 16 * g + 4 * i + 2 * hf;
            *(LAS unsigned*)(my + 8192 + (e >> 3) * 256 + (m ^ (e & 8)) * 16 + (e & 7) * 2) = v[i]; }
    }
    const float csc = 0.08838834764831845f * 1.4426950408889634f, ninf = -__builtin_inff();
    u32x4 PX[2]; u32x2 PM[2];
#pragma unroll
    for (int qt = 0; qt < 2; ++qt) {
        f32x4 sm = {0.f, 0.f, 0.f, 0.f}, s0 = sm, s1 = sm;
#pragma unroll
        for (int k = 0; k < 4; ++k) { sm = mma32(asbf(XM[k]), asbf(Q[qt][k]), sm); s0 = mma32(asbf(K[0][k]), asbf(Q[qt][k]), s0); if (qt == 1) s1 = mma32(asbf(K[1][k]), asbf(Q[qt][k]), s1); }
#pragma unroll
        for (int i = 0; i < 4; ++i) {
            if (g < 2) sm[i] = ninf;
            if (qt == 0) { if (4 * g + i > n) s0[i] = ninf; s1[i] = ninf; }
            else if (4 * g + i > n) s1[i] = ninf;
        }
        float m = fmaxf(fmaxf(fmaxf(s0[0], s0[1]), fmaxf(s0[2], s0[3])), fmaxf(fmaxf(s1[0], s1[1]), fmaxf(s1[2], s1[3])));
        m = fmaxf(m, fmaxf(fmaxf(sm[0], sm[1]), fmaxf(sm[2], sm[3])));
        m = rows_max(m);
        const float mc = -m * csc; float sum = 0.f;
#pragma unroll
        for (int i = 0; i < 4; ++i) { s0[i] = __builtin_amdgcn_exp2f(fmaf(s0[i], csc, mc)); s1[i] = __builtin_amdgcn_exp2f(fmaf(s1[i], csc, mc)); sm[i] = __builtin_amdgcn_exp2f(fmaf(sm[i], csc, mc));
            sum += (s0[i] + s1[i]) + sm[i]; }
        sum = rows_sum(sum);
        const float inv = __builtin_amdgcn_rcpf(sum);
        PX[qt] = (u32x4){pk_bf16(s0[0] * inv, s0[1] * inv), pk_bf16(s0[2] * inv, s0[3] * inv), pk_bf16(s1[0] * inv, s1[1] * inv), pk_bf16(s1[2] * inv, s1[3] * inv)};
        PM[qt] = (u32x2){pk_bf16(sm[0] * inv, sm[1] * inv), pk_bf16(sm[2] * inv, sm[3] * inv)};
    }
    asm volatile("s_waitcnt vmcnt(0) lgkmcnt(0)" ::: "memory");
    const int q4 = n >> 2, p4 = n & 3;
    const ldsp vx = my + (p4 >> 1) * 256 + ((4 * g + q4) ^ ((p4 >> 1) * 8)) * 16 + (p4 & 1) * 8, vmm = my + 8192 + (p4 >> 1) * 256 + ((4 * (g & 1) + q4) ^ ((p4 >> 1) * 8)) * 16 + (p4 & 1) * 8;
    const rsrc_t orr = mkrsrc(p.ws + WH(WS_OA + (size_t)s * 524288 + gap((unsigned)h * 4096u)), 524288u);
    const unsigned oo = frag_off(lane);
    bf16x8 VA[8]; s16x4 VMf[8];
#pragma unroll
    for (int et = 0; et < 8; ++et) { VA[et] = cat8(trd(vx + et * 512), trd(vx + 4096 + et * 512)); VMf[et] = trd(vmm + et * 512); }
    SCHED();
#pragma unroll
    for (int ep = 0; ep < 4; ++ep) {
        f32x4 d[2][2];
#pragma unroll
        for (int e2 = 0; e2 < 2; ++e2)
#pragma unroll
            for (int qt = 0; qt < 2; ++qt) d[qt][e2] = mma16(__builtin_bit_cast(u32x2, VMf[2 * ep + e2]), PM[qt], mma32(VA[2 * ep + e2], asbf(PX[qt]), (f32x4){0.f, 0.f, 0.f, 0.f}));
#pragma unroll
        for (int qt = 0; qt < 2; ++qt) store_frag(orr, oo + ep * 1024, (unsigned)qt * 262144u, d[qt][0], d[qt][1]);
    }
    asm volatile("s_waitcnt lgkmcnt(0)" ::: "memory");
}

DI void attn_b_item(const Params& p, unsigned par, int hq, int qb, ldsp lds) {
    const int tid = fresh_tid(), w = __builtin_amdgcn_readfirstlane(tid >> 6), lane = tid & 63, g = lane >> 4, n = lane & 15;
    const int q0 = qb * 128;
    const rsrc_t br = mkrsrc(p.ws + WH(WS_QKVB + (size_t)hq * 4 * BH), 68u * BH);
    const rsrc_t orr = mkrsrc(p.ws + WH(WS_OB + (size_t)qb * 524288), 524288u);
    constexpr int BUF = 57344;
    unsigned to[3];
#pragma unroll
    for (int i = 0; i < 3; ++i) to[i] = gap((unsigned)max(8 * qb - 16 + w * 3 + i, 0) * 1024u);
    const unsigned l16 = (unsigned)lane * 16u, vsw = (unsigned)(g * 256 + (n ^ ((g & 1) * 8)) * 16);
    auto stage = [&](int i) {
        const unsigned so = (unsigned)i * BH; ldsp d = lds + (i & 1) * BUF;
#pragma unroll
        for (int k = 0; k < 3; ++k) {
            __builtin_amdgcn_raw_ptr_buffer_load_lds(br, (LAS void*)(d + (w * 3 + k) * 1024), 16, l16, so + 32u * BH + to[k], 0, 16);
            __builtin_amdgcn_raw_ptr_buffer_load_lds(br, (LAS void*)(d + 24576 + (w * 3 + k) * 1024), 16, vsw, so + 64u * BH + to[k], 0, 16);
        }
        __builtin_amdgcn_raw_ptr_buffer_load_lds(br, (LAS void*)(d + 49152 + w * 1024), 16, l16, so + (unsigned)(qb * 16384 + w * 1024), 0, 16);
    };
    const float ninf = -__builtin_inff(), csc = 0.17677669529663687f * 1.4426950408889634f;
    const int tmin = (256 - q0 - 16 * w) >> 4;
    const int q4 = n >> 2, p4 = n & 3;
    const int kq = w * 1024 + lane * 16;
    const unsigned vb = (unsigned)(size_t)lds + 24576u + (unsigned)(w * 1024 + (p4 >> 1) * 256 + ((4 * g + q4) ^ ((p4 >> 1) * 8)) * 16 + (p4 & 1) * 8);
    stage(0);
#pragma unroll 1
    for (int i = 0; i < 4; ++i) {
        if (i < 3) { stage(i + 1); WAIT_V(7); } else WAIT_V(0);
        BAR(); asm volatile("" ::: "memory");
        const ldsp bp = lds + (i & 1) * BUF;
        const bf16x8 Q = *(const LAS bf16x8*)(bp + 49152 + kq);
        f32x4 sc[17]; bf16x8 KF[17];
#pragma unroll
        for (int t = 0; t < 17; ++t) KF[t] = *(const LAS bf16x8*)(bp + kq + t * 1024);
        SCHED();
#pragma unroll
        for (int t = 0; t < 17; ++t) sc[t] = mma32(KF[t], Q, (f32x4){0.f, 0.f, 0.f, 0.f});
#pragma unroll
        for (int k = 0; k < 4; ++k) { if (4 * g + k <= n) sc[0][k] = ninf; if (4 * g + k > n) sc[16][k] = ninf; }
        if (tmin > 0) {
#pragma unroll
            for (int t = 0; t < 16; ++t) if (t < tmin) sc[t] = (f32x4){ninf, ninf, ninf, ninf};
        }
        float mq[4];
#pragma unroll
        for (int k = 0; k < 4; ++k) {
            mq[k] = max3f(sc[16][k], sc[k][0], sc[k][1]); mq[k] = max3f(mq[k], sc[k][2], sc[k][3]);
#pragma unroll
            for (int t = 4 + k; t < 16; t += 4) mq[k] = max3f(max3f(mq[k], sc[t][0], sc[t][1]), sc[t][2], sc[t][3]);
        }
        const float m = rows_max(max3f(max3f(mq[0], mq[1], mq[2]), mq[3], mq[3]));
        const float mc = -m * csc;
        f32x4 sum4 = {0.f, 0.f, 0.f, 0.f};
#pragma unroll
        for (int t = 0; t < 17; ++t) {
            sc[t] = sc[t] * csc + mc;
#pragma unroll
            for (int k = 0; k < 4; ++k) sc[t][k] = __builtin_amdgcn_exp2f(sc[t][k]);
            sum4 += sc[t];
        }
        const float inv = __builtin_amdgcn_rcpf(rows_sum((sum4[0] + sum4[1]) + (sum4[2] + sum4[3])));
        const unsigned v0 = vb + (unsigned)((i & 1) * BUF);
        s16x4 V0[17], V1[17];
#define VRD(t) TRD(V0[t], v0, (t) * 1024); TRD(V1[t], v0, (t) * 1024 + 512)
        VRD(0); VRD(1); VRD(2); VRD(3); VRD(4); VRD(5); VRD(6); VRD(7); VRD(8); VRD(9); VRD(10); VRD(11); VRD(12); VRD(13); VRD(14); VRD(15); VRD(16);
#undef VRD
        u32x2 P[17];
#pragma unroll
        for (int t = 0; t < 17; ++t) P[t] = (u32x2){pk_bf16(sc[t][0], sc[t][1]), pk_bf16(sc[t][2], sc[t][3])};
        WAIT_L(0); SCHED();
        f32x4 d0 = {0.f, 0.f, 0.f, 0.f}, d1 = d0;
#pragma unroll
        for (int tp = 0; tp < 8; ++tp) {
            const bf16x8 PP = asbf((u32x4){P[2 * tp].x, P[2 * tp].y, P[2 * tp + 1].x, P[2 * tp + 1].y});
            d0 = mma32(cat8(V0[2 * tp], V0[2 * tp + 1]), PP, d0); d1 = mma32(cat8(V1[2 * tp], V1[2 * tp + 1]), PP, d1);
        }
        d0 = mma16(__builtin_bit_cast(u32x2, V0[16]), P[16], d0); d1 = mma16(__builtin_bit_cast(u32x2, V1[16]), P[16], d1);
        BAR();
        store_frag(orr, frag_off(lane), (unsigned)w * 65536u + gap((unsigned)(hq * 4 + i) * 1024u), d0 * inv, d1 * inv);
    }
    WAIT_V(0);
    signal((unsigned*)(p.ws + WS_CTL), C_OB + qb * 32, 1);
}

DI void out_tail(const Params& p, unsigned par, int c, int q, ldsp lds) {
    const int tid = fresh_tid(), w = __builtin_amdgcn_readfirstlane(tid >> 6), lane = tid & 63, g = lane >> 4, n = lane & 15;
    unsigned* ctl = (unsigned*)(p.ws + WS_CTL);
    bf16x8 WT[16][2];
    {
        ldsp my = lds + w * 16384;
        const int toff = (8 * g + (n >> 2)) * 64 + (n & 3) * 8;
#pragma unroll
        for (int r = 0; r < 2; ++r) {
#pragma unroll
            for (int i = 0; i < 16; ++i)
                __builtin_amdgcn_global_load_lds((const unsigned*)(p.wo + (size_t)(512 * w + 256 * r + 16 * i + (lane >> 2)) * 1024 + 32 * c + (lane & 3) * 8), (LAS unsigned*)(my + i * 1024), 16, 0, 0);
            WAIT_V(0);
#pragma unroll
            for (int k = 0; k < 8; ++k)
#pragma unroll
                for (int ct = 0; ct < 2; ++ct) { WT[8 * r + k][ct] = cat8(trd(my + k * 2048 + ct * 32 + toff), trd(my + k * 2048 + ct * 32 + toff + 256)); }
#pragma unroll
            for (int k = 0; k < 8; ++k) asm volatile("" : "+v"(WT[8 * r + k][0]), "+v"(WT[8 * r + k][1]));
            WAIT_L(0);
        }
    }
    BAR();
#pragma unroll 1
    for (int hs = q; hs < 32; hs += 3) {
        const int s = 48 + (hs >> 1), rt = hs & 1;
        if (w == 0) wait_count<3>(ctl, C_OS + (s - 48) * 256 + (c >> 3) * 64, 4u);
        asm volatile("" ::: "memory"); BAR(); asm volatile("" ::: "memory");
        const rsrc_t ar = mkrsrc(p.ws + WH(WS_OA + (size_t)(2 * s + rt) * 262144 + (size_t)w * 32768), 32768u);
        const unsigned ao = (unsigned)lane * 16u;
        u32x4 A[16];
#pragma unroll
        for (int k = 0; k < 16; ++k) A[k] = ld16_sc1(ar, ao + (k & 3) * 1024, gap((unsigned)(k >> 2) * 4096u));
        SCHED();
#pragma unroll
        for (int ct = 0; ct < 2; ++ct) {
            f32x4 acc = {0.f, 0.f, 0.f, 0.f};
#pragma unroll
            for (int k = 0; k < 16; ++k) acc = mma32(WT[k][ct], asbf(A[k]), acc);
            *(LAS f32x4*)(lds + w * 2048 + ct * 1024 + lane * 16) = acc;
        }
        LDS_BAR();
        if (w < 2) {
            f32x4 r[8];
#pragma unroll
            for (int ww = 0; ww < 8; ++ww) r[ww] = *(const LAS f32x4*)(lds + ww * 2048 + w * 1024 + lane * 16);
            SCHED();
            const f32x4 t = ((r[0] + r[1]) + (r[2] + r[3])) + ((r[4] + r[5]) + (r[6] + r[7]));
            *(u32x2*)(p.out + (size_t)(32 * s + 16 * rt + n) * 2048 + 32 * c + 16 * w + 4 * g) = (u32x2){pk_bf16(t[0], t[1]), pk_bf16(t[2], t[3])};
        }
        WAIT_L(0);
    }
    BAR();
}

DI void big_unit(const Params& p, unsigned par, int id, Unit& u) {
    u.lda = DM * 2; u.nt = 16; u.ak = 128; u.ah = 128 * DM * 2;
    if (id < 384) {
        const int pm = id & 7, pn = id >> 3;
        const u16 *w0 = p.wq, *w1 = p.wk, *w2 = p.wv; const u16* W = pn < 16 ? w0 : (pn < 32 ? w1 : w2);
        u.A = (const char*)(p.ax + (size_t)pm * 256 * DM); u.B = (const char*)(W + (pn & 15) * 256); u.ldb = 4096 * 2;
        u.C = p.ws + WH(WS_QKVX + (size_t)(2 * pn) * XH + (size_t)pm * 131072); u.ldc = 256; u.cw = 1024; u.ch = XH;
        if (pn < 16) { u.sig = C_Q + pm * 32; u.nsig = 1; } else { u.sig = C_KVC(2 * pm, 2 * (pn & 15)); u.nsig = 4; }
    } else if (id < 480) {
        const int pm = (id - 384) & 7, pn = (id - 384) >> 3;
        u.A = (const char*)(p.bx + (size_t)pm * 256 * DM); u.B = (const char*)(p.bwqkv + pn * 256); u.ldb = LDB3 * 2;
        u.C = p.ws + WH(WS_QKVB + (size_t)(8 * pn) * BH + (size_t)pm * 32768); u.ldc = 64; u.cw = BH; u.ch = 4 * BH; u.sig = C_B + pm * 32; u.nsig = 1;
    } else {
        const int pm = (id - 480) >> 2, pn = id & 3;
        u.lda = 0; u.ak = 2048; u.ah = 524288u;
        u.A = p.ws + WH(WS_OB + (size_t)pm * 1048576); u.B = (const char*)(p.bwo + pn * 256); u.ldb = DM * 2;
        u.C = (char*)(p.out + (size_t)pm * 256 * 2048 + 1024 + pn * 256); u.ldc = 4096; u.cw = 64; u.ch = 256; u.sig = 0; u.nsig = 0;
    }
}
struct BigSched { const Params& p; unsigned par; int id0, id1, id2; DI bool next(int i, Unit& u) const { const int id = i == 0 ? id0 : (i == 1 ? id1 : (i == 2 ? id2 : -1)); if (id < 0) return false; big_unit(p, par, id, u); return true; } };

__global__ __launch_bounds__(512, 2) void k_fused(Params p) {
    extern __shared__ __attribute__((aligned(16))) unsigned char shm[];
    ldsp lds = (ldsp)shm;
    unsigned* ctl = (unsigned*)(p.ws + WS_CTL);
    const int b = (blockIdx.x & ~7) | ((blockIdx.x + __builtin_amdgcn_readfirstlane(ctl[C_PHASE])) & 7), id = (b & 3) * 32 + (b >> 3);
#define WSTAMP(code)
    const unsigned par = (unsigned)__builtin_amdgcn_readfirstlane(ctl[C_PAR + 1]) < (unsigned)__builtin_amdgcn_readfirstlane(ctl[C_PAR]) ? 1u : 0u;
    WSTAMP(1);
    if ((b & 7) < 4) {
        chain_role(p, par, id, lds, (unsigned long long*)(p.ws + WS_END));
        BAR();
        WSTAMP(2);
    } else {
#pragma unroll 1
        for (int r = 0; r < 2; ++r) {
            const int rb = 2 * r + (id & 1), ct = id >> 1;
            const u16 *w1 = p.wk, *w2 = p.wv;
            Unit u; u.A = (const char*)(p.ax + (size_t)rb * 128 * DM); u.lda = DM * 2; u.B = (const char*)((ct < 32 ? w1 : w2) + (ct & 31) * 128); u.ldb = 4096 * 2; u.nt = 16;
            u.C = p.ws + WH(WS_QKVX + (size_t)(32 + ct) * XH + (size_t)rb * 65536); u.ldc = 256; u.cw = 1024; u.ch = 0; u.ak = 128; u.ah = 0;
            u.sig = C_KVC(rb, ct & 31); u.nsig = 1;
            gemm_small(lds, u, ctl);
            WSTAMP(3);
        }
        {
            const int t = fresh_tid(), wv = __builtin_amdgcn_readfirstlane(t >> 6);
            const char* src; const int c = id & 7, q = id >> 3;
            if (c < 2) src = (const char*)p.ax + (size_t)(q * 2 + c) * 131072; else if (c < 4) src = (const char*)p.bx + (size_t)(q * 2 + c - 2) * 131072;
            else if (c < 7) src = (const char*)p.bwqkv + (size_t)(q * 3 + c - 4) * 131072; else src = (const char*)p.bwo + (size_t)q * 131072;
#pragma unroll
            for (int i = 0; i < 16; ++i) { __builtin_amdgcn_global_load_lds((const unsigned*)(src + i * 8192 + t * 16), (LAS unsigned*)(lds + L_SCR + wv * 1024), 16, 0, 0); if ((i & (KWP - 1)) == KWP - 1) WAIT_V(0); }
            if (KNT1) {
#pragma unroll
                for (int i = 0; i < 8; ++i) { __builtin_amdgcn_global_load_lds((const unsigned*)((const char*)p.wo + (size_t)id * 65536 + i * 8192 + t * 16), (LAS unsigned*)(lds + L_SCR + wv * 1024), 16, 0, 0); if ((i & (KWP - 1)) == KWP - 1) WAIT_V(0); }
            }
        }
    }
    volatile LAS unsigned* qw = (volatile LAS unsigned*)(lds + 131072);
    const int w = __builtin_amdgcn_readfirstlane(threadIdx.x >> 6);
    const int k = b & 3;
    bool fixed = (b & 7) >= 4;
#pragma unroll 1
    for (;;) {
        int kind, x, it = -1, x2 = -1, x3 = -1;
        if (fixed) { const int i = id & 31; kind = 6; x = i < 24 ? (16 + 8 * k + (i & 7)) * 8 + 2 + (i >> 3) : (4 * k + (i & 3)) * 8 + ((i >> 2) & 1); x2 = x + (i < 24 ? 3 : 2); x3 = i < 24 ? 384 + 24 * k + i : x + 4; fixed = false; }
        else {
        if (threadIdx.x == 0) qw[0] = __hip_atomic_fetch_add(ctl + C_HEAD + k * 32, 1u, RLX_AGENT) + 1u;
        LDS_BAR();
        it = (int)__builtin_amdgcn_readfirstlane(qw[0]);
        LDS_BAR();
        if (it < 6) { kind = 0; x = it * 4 + k; }
        else if (it < 38) { kind = 1; x = ((it - 6) >> 1) * 8 + 2 * k + ((it - 6) & 1); }
        else if (it < 40) { kind = 0; x = (it - 38 + 6) * 4 + k; }
        else if (it < 44) { kind = 4; x = (it - 40) * 4 + k; }
        else if (it < 52) { kind = 6; x = (4 * k + ((it - 44) & 3)) * 8 + 6 + ((it - 44) >> 2); }
        else if (it < 64) { kind = 2; x = ((it - 52) >> 1) * 8 + 2 * k + ((it - 52) & 1); }
        else if (it < 72) { kind = 6; x = 480 + (it - 64) * 4 + k; }
        else if (it < 84) { kind = 2; x = (6 + ((it - 72) >> 1)) * 8 + 2 * k + ((it - 72) & 1); }
        else if (it < 108) { kind = 5; x = ((it - 84) >> 3) * 32 + 8 * k + ((it - 84) & 7); }
        else break;
        }
        if (kind == 6) {
            if (x >= 480) { const int w0 = C_OB + ((x - 480) >> 2) * 64; wg_wait(ctl, w0, 64u, w0 + 32, 64u); }
            BigSched S{p, par, x, x2, x3};
            gemm_stream(lds, S, ctl);
        } else
        if (kind == 0) {
            const int s0 = (x >> 2) * 4;
            wait_count(ctl, C_Q + (s0 >> 3) * 32, 128u); wait_count(ctl, C_PROG, (unsigned)s0 + 5u);
            asm volatile("" ::: "memory");
#pragma unroll 1
            for (int s = s0; s < s0 + 4; ++s) attn_a_item<false>(p, par, s, (x & 3) * 8 + w, lds + w * 12288, fresh_tid() & 63);
            WAIT_V(0); signal(ctl, C_OA + (x >> 2) * 32, 1);
            BAR();
        } else if (kind == 4) {
#pragma unroll 1
            for (int s = 32 + (x >> 2); s < NSEG; s += 4) {
                wait_count(ctl, C_Q + (s >> 3) * 32, 128u); wait_count(ctl, C_KVC(s >> 2, (x & 3) * 8 + w), 16u);
                asm volatile("" ::: "memory");
                attn_a_item<true>(p, par, s, (x & 3) * 8 + w, lds + w * 12288, fresh_tid() & 63);
                WAIT_V(0);
                if (s < 48) signal(ctl, C_OA + (s >> 2) * 32, 1);
                else { BAR(); const int l = fresh_tid(); if (l < 4) __hip_atomic_fetch_add(ctl + C_OS + (s - 48) * 256 + l * 64, 1u, RLX_AGENT); }
            }
            BAR();
        } else if (kind == 1) {
            const int qb = x >> 3;
            wait_count(ctl, C_B + (qb >> 1) * 32, 96u); wait_count(ctl, C_B + max((qb >> 1) - 1, 0) * 32, 96u);
            asm volatile("" ::: "memory");
            attn_b_item(p, par, x & 7, qb, lds);
        } else if (kind == 5) {
            out_tail(p, par, x & 31, x >> 5, lds);
        } else {
            const int rb = x >> 3, ct = x & 7;
            Unit u; u.ldb = DM * 2; u.ldc = 2048 * 2; u.sig = 0; u.nsig = 0; u.lda = 0; u.ak = 2048; u.ah = 0; u.cw = 64; u.ch = 0;
            u.A = p.ws + WH(WS_OA + (size_t)rb * 2097152); u.B = (const char*)(p.wo + ct * 128); u.nt = 64; u.C = (char*)(p.out + (size_t)rb * 128 * 2048 + ct * 128);
            wg_wait(ctl, C_OA + rb * 32, rb < 8 ? 32u : 128u, C_OA + rb * 32, 32u);
            gemm_small(lds, u, ctl);
        }
        WSTAMP(16 + (kind == 6 && x >= 480 ? (x < 512 ? 7 : 8) : kind) + ((it & 0xffff) << 8));
    }
}

__global__ __launch_bounds__(512) void k_init(char* ws) {
    const rsrc_t xr = mkrsrc(ws + WS_XCH, NWIN_INIT * 16384u);
    unsigned* ctl = (unsigned*)(ws + WS_CTL);
    const u32x4 sv = {SENT, SENT, SENT, SENT};
    const unsigned xcc = (unsigned)__builtin_amdgcn_s_getreg(20 | (3 << 11)) & 7u, half = blockIdx.x >> 3, win = blockIdx.x & 7;
    if (threadIdx.x < 64 && xcc == 1) {
        unsigned b = ~0u;
        for (int i = 0; i < 5; ++i) {
            const unsigned long long t0 = __builtin_amdgcn_s_memtime();
            __builtin_amdgcn_raw_buffer_store_b32(0u, xr, (unsigned)(2 * 16384 + C_PROBE * 4) + half * 8192u + (unsigned)i * 256u, 0, 16);
            asm volatile("s_waitcnt vmcnt(0)" ::: "memory");
            b = min(b, (unsigned)(__builtin_amdgcn_s_memtime() - t0));
        }
        if (threadIdx.x == 0) ctl[C_PAR + half] = b;
    }
    __syncthreads();
    if (win < 7 && win != 5) ST16(sv, xr, win * 16384u + half * 8192u + threadIdx.x * 16u, 0, 16);
    if (blockIdx.x == 0 && threadIdx.x == 0) ctl[C_PHASE] = xcc;
}

extern "C" void kernel_launch(void* const* d_in, const int* in_sizes, int n_in, void* d_out, int out_size, void* d_ws, size_t ws_size, hipStream_t stream) {
    Params p;
    p.ax = (const u16*)d_in[0]; p.mem0 = (const u16*)d_in[1]; p.wq = (const u16*)d_in[2]; p.wk = (const u16*)d_in[3]; p.wv = (const u16*)d_in[4]; p.wo = (const u16*)d_in[5];
    p.bx = (const u16*)d_in[7]; p.bwqkv = (const u16*)d_in[8]; p.bwo = (const u16*)d_in[9];
    p.out = (u16*)d_out; p.ws = (char*)(((uintptr_t)d_ws + WS_SHIFT(ws_size) + 0x1FFFFFu) & ~(uintptr_t)0x1FFFFFu);
    (void)hipFuncSetAttribute((const void*)k_fused, hipFuncAttributeMaxDynamicSharedMemorySize, LDS_BYTES);
    k_init<<<16, 512, 0, stream>>>(p.ws);
    k_fused<<<256, 512, LDS_BYTES, stream>>>(p);
}
```

```cpp
#include <hip/hip_runtime.h>
#include <stdint.h>
#include <stdlib.h>

#define LAS __attribute__((address_space(3)))
#define DI __device__ __forceinline__
typedef unsigned short u16;
typedef __attribute__((ext_vector_type(8))) __bf16 bf16x8;
typedef __attribute__((ext_vector_type(16))) __bf16 bf16x16;
typedef __attribute__((ext_vector_type(2))) __bf16 bf16x2;
typedef __attribute__((ext_vector_type(4))) short s16x4;
typedef __attribute__((ext_vector_type(8))) short s16x8;
typedef __attribute__((ext_vector_type(16))) short s16x16;
typedef __attribute__((ext_vector_type(2))) float f32x2;
typedef __attribute__((ext_vector_type(4))) float f32x4;
typedef __attribute__((ext_vector_type(2))) unsigned u32x2;
typedef __attribute__((ext_vector_type(4))) unsigned u32x4;
typedef LAS unsigned char* ldsp;
typedef __amdgpu_buffer_rsrc_t rsrc_t;

constexpr int S_LEN = 2048, DM = 1024, NH = 32, DH = 128, NSEG = 64;
constexpr int LDB3 = 3072, S_TAIL = 40;
constexpr unsigned XH = S_LEN * 512, BH = S_LEN * 128;
constexpr unsigned SENT = 0xFFFFFFFFu;
constexpr size_t WS_XCH = 2u << 20;
constexpr int NWIN = 640, NWIN_INIT = 7;
constexpr size_t XCH_BYTES = (size_t)(NWIN + 2048 + 512) * 16384;
constexpr size_t WS_CTL = WS_XCH + 2 * 16384;
constexpr int C_TMO = 0, C_PAR = 13312  , C_PHASE = 13314, C_HEAD = 3072, C_PROG = 192, C_PACE = 128, C_Q = 256, C_B = C_Q + 8 * 32, C_OA = C_B + 8 * 32, C_OB = C_OA + 16 * 32, C_OS = 16384, C_KV = 4096, C_PROBE = 12288;
constexpr size_t WS_QKVX = 52u << 20;
constexpr size_t WS_QKVB = WS_QKVX + (size_t)96 * XH;
constexpr size_t WS_OA = WS_QKVB + (size_t)96 * BH;
constexpr size_t WS_OB = WS_OA + (size_t)S_LEN * 4096 * 4;
constexpr size_t WS_END = WS_OB + (size_t)S_LEN * 1024 * 4;
#define C_KVC(rb, h) (C_KV + (h) * 32 + (rb))
#define gap(L) (((L) & 8191u) | (((L) >> 13) << 14))
#define WH(off) ((off) + (size_t)((((unsigned)((off) >> 21) ^ par ^ KWH) & 1u) << 13))
#define KWH 0
#define WS_SHIFT(sz) 0
constexpr int L_QW = 147456, LDS_BYTES = L_QW + 1024 + 8192;

DI unsigned pk_bf16(float a, float b) { f32x2 v = {a, b}; return __builtin_bit_cast(unsigned, __builtin_convertvector(v, bf16x2)); }
DI s16x4 trd(ldsp p) { return __builtin_amdgcn_ds_read_tr16_b64_v4i16((LAS s16x4*)p); }
DI bf16x8 cat8(s16x4 a, s16x4 b) { return __builtin_bit_cast(bf16x8, __builtin_shufflevector(a, b, 0, 1, 2, 3, 4, 5, 6, 7)); }
DI bf16x16 cat16(bf16x8 a, bf16x8 b) { return __builtin_shufflevector(a, b, 0, 1, 2, 3, 4, 5, 6, 7, 8, 9, 10, 11, 12, 13, 14, 15); }
DI bf16x8 lo8(bf16x16 v) { return __builtin_shufflevector(v, v, 0, 1, 2, 3, 4, 5, 6, 7); }
DI bf16x8 hi8(bf16x16 v) { return __builtin_shufflevector(v, v, 8, 9, 10, 11, 12, 13, 14, 15); }
DI bf16x8 asbf(u32x4 v) { return __builtin_bit_cast(bf16x8, v); }
DI f32x4 mma32(bf16x8 a, bf16x8 b, f32x4 c) { return __builtin_amdgcn_mfma_f32_16x16x32_bf16(a, b, c, 0, 0, 0); }
DI f32x4 mma16(u32x2 a, u32x2 b, f32x4 c) { return mma32(asbf((u32x4){a.x, a.y, 0u, 0u}), asbf((u32x4){b.x, b.y, 0u, 0u}), c); }
DI f32x4 smma(u32x4 a, bf16x16 b, f32x4 c, int idx) { return __builtin_amdgcn_smfmac_f32_16x16x64_bf16(asbf(a), b, c, idx, 0, 0); }
DI rsrc_t mkrsrc(const void* p, unsigned bytes) { return __builtin_amdgcn_make_buffer_rsrc((void*)p, 0, bytes, 0x00020000); }
DI u32x4 ld16_sc1(rsrc_t r, unsigned voff, unsigned soff) { return __builtin_amdgcn_raw_buffer_load_b128(r, voff, soff, 16); }
#define ST16(v, r, voff, soff, aux) do { const u32x4 st_v = (v); __builtin_amdgcn_raw_buffer_store_b128(st_v, r, voff, soff, aux); asm volatile("s_nop 1" :: "v"(st_v)); } while (0)
DI void swap16(unsigned& a, unsigned& b) { auto r = __builtin_amdgcn_permlane16_swap(a, b, false, false); a = r[0]; b = r[1]; }
DI void swap32(unsigned& a, unsigned& b) { auto r = __builtin_amdgcn_permlane32_swap(a, b, false, false); a = r[0]; b = r[1]; }
DI void swap16f(float& a, float& b) { unsigned x = __float_as_uint(a), y = __float_as_uint(b); swap16(x, y); a = __uint_as_float(x); b = __uint_as_float(y); }
DI void swap32f(float& a, float& b) { unsigned x = __float_as_uint(a), y = __float_as_uint(b); swap32(x, y); a = __uint_as_float(x); b = __uint_as_float(y); }
DI float rows_max(float v) { float a = v, b = v; swap16f(a, b); v = fmaxf(a, b); a = v; b = v; swap32f(a, b); return fmaxf(a, b); }
DI float rows_sum(float v) { float a = v, b = v; swap16f(a, b); v = a + b; a = v; b = v; swap32f(a, b); return a + b; }
DI unsigned umax4(u32x4 v) { return max(max(v.x, v.y), max(v.z, v.w)); }
#define WAIT_V(n) asm volatile("s_waitcnt vmcnt(" #n ")" ::: "memory")
#define WAIT_L(n) asm volatile("s_waitcnt lgkmcnt(" #n ")" ::: "memory")
#define BAR() __builtin_amdgcn_s_barrier()
#define SCHED() __builtin_amdgcn_sched_barrier(0)
#define LDS_BAR() do { asm volatile("s_waitcnt lgkmcnt(0)" ::: "memory"); __builtin_amdgcn_s_barrier(); asm volatile("" ::: "memory"); } while (0)

constexpr int HTB = 128 * 64 * 2;
DI int lds_byte(int r, int c) { const int st = (r >> 4) * 2 + (c >> 5), rr = r & 15, cc = c & 31, ob = rr * 64 + cc * 2; return st * 1024 + (ob ^ (((ob >> 9) & 1) << 5)); }
DI void stage_rc(int b, int& R, int& C) { const int st = b / 1024, sb = b % 1024, swz = sb ^ (((sb >> 9) & 1) << 5); R = (st >> 1) * 16 + swz / 64; C = (st & 1) * 32 + (swz % 64) / 2; }

struct Params {
    const u16 *ax, *mem0, *wq, *wk, *wv, *wo, *bx, *bwqkv, *bwo;
    u16* out; char* ws;
};
#define RLX_AGENT __ATOMIC_RELAXED, __HIP_MEMORY_SCOPE_AGENT
DI bool spin_out(unsigned& spins, unsigned* ctl) {
    spins = __builtin_amdgcn_readfirstlane(spins) + 1u;
    if ((spins & 63u) == 0u) {
        if (spins > (1u << 18)) { __hip_atomic_store(ctl + C_TMO, 1u, RLX_AGENT); return true; }
        if (__builtin_amdgcn_readfirstlane(__hip_atomic_load(ctl + C_TMO, RLX_AGENT)) != SENT) return true;
    }
    return false;
}
template <int NAP = 8>
DI void wait_count(unsigned* ctl, int word, unsigned need) {
    unsigned spins = 0;
    while (__builtin_amdgcn_readfirstlane(__hip_atomic_load(ctl + word, RLX_AGENT)) + 1u < need) { __builtin_amdgcn_s_sleep(NAP); if (spin_out(spins, ctl)) break; }
}
DI void wg_wait(unsigned* ctl, int word0, unsigned need0, int word1, unsigned need1) {
    if (threadIdx.x < 64) {
        wait_count(ctl, word0, need0); wait_count(ctl, word1, need1);
        __builtin_amdgcn_fence(__ATOMIC_ACQUIRE, "agent");
        asm volatile("s_waitcnt vmcnt(0)" ::: "memory");
    }
    asm volatile("" ::: "memory"); BAR(); asm volatile("" ::: "memory");
}
struct Unit { const char* A; const char* B; char* C; unsigned lda, ldb, ldc, cw, ch, ak, ah; int nt, sig, nsig; };
DI int fresh_tid() { int t = threadIdx.x; asm volatile("" : "+v"(t)); return t; }
DI void signal(unsigned* ctl, int sig, int nsig) { const int lane = fresh_tid() & 63; if (lane < nsig) __hip_atomic_fetch_add(ctl + sig + (lane & 1) * 32 + (lane >> 1), 1u, RLX_AGENT); }
DI u32x4 pack_pair(f32x4 v0, f32x4 v1) {
    unsigned ax = pk_bf16(v0[0], v0[1]), ay = pk_bf16(v0[2], v0[3]), bx = pk_bf16(v1[0], v1[1]), by = pk_bf16(v1[2], v1[3]);
    swap16(ax, bx); swap16(ay, by);
    return (u32x4){ax, ay, bx, by};
}
DI unsigned frag_off(int lane) { return (unsigned)((((lane >> 4) & 1) * 2 + (lane >> 5)) * 256 + (lane & 15) * 16); }
DI void store_frag(rsrc_t cr, unsigned voff, unsigned soff, f32x4 v0, f32x4 v1) { ST16(pack_pair(v0, v1), cr, voff, soff, 16); }
DI u32x4 to_rows(ldsp sc, int lane, f32x4 v0, f32x4 v1) {
    *(volatile LAS u32x4*)(sc + ((lane & 15) * 4 + ((lane >> 4) & 1) * 2 + (lane >> 5)) * 16) = pack_pair(v0, v1);
    return *(volatile LAS u32x4*)(sc + lane * 16);
}
constexpr int L_SCR = L_QW + 1024;
#define STAGE(bufoff, gbase, voff, step2) do { \
        __builtin_amdgcn_global_load_lds((const unsigned*)((gbase) + (voff)), (LAS unsigned*)(lds + (bufoff) + ldsw), 16, 0, 0); \
        __builtin_amdgcn_global_load_lds((const unsigned*)((gbase) + (step2) + (voff)), (LAS unsigned*)(lds + (bufoff) + ldsw + 8192), 16, 0, 0); } while (0)
#define LDA(dst, off) do { _Pragma("unroll") for (int m = 0; m < 4; ++m) _Pragma("unroll") for (int k = 0; k < 2; ++k) dst[m][k] = *(const LAS bf16x8*)(lds + (off) + aoff + m * 2048 + k * 1024); } while (0)
#define TRD(dst, base, off) asm volatile("ds_read_b64_tr_b16 %0, %1 offset:%2" : "=v"(dst) : "v"(base), "i"(off) : "memory")
#define LDB(dst, v0, v1, off) do { s16x4 l0, h0, l1, h1, l2, h2, l3, h3; \
        TRD(l0, v0, (off)); TRD(h0, v0, (off) + 256); TRD(l1, v0, (off) + 8192); TRD(h1, v0, (off) + 8448); \
        TRD(l2, v1, (off)); TRD(h2, v1, (off) + 256); TRD(l3, v1, (off) + 8192); TRD(h3, v1, (off) + 8448); \
        dst[0][0] = cat8(l0, h0); dst[0][1] = cat8(l1, h1); dst[1][0] = cat8(l2, h2); dst[1][1] = cat8(l3, h3); } while (0)
#define MMA(accv, At, Bt) do { SCHED(); __builtin_amdgcn_s_setprio(1); _Pragma("unroll") for (int m = 0; m < 4; ++m) _Pragma("unroll") for (int n = 0; n < 2; ++n) _Pragma("unroll") for (int k = 0; k < 2; ++k) \
        accv[m][n] = mma32(Bt[n][k], At[m][k], accv[m][n]); __builtin_amdgcn_s_setprio(0); } while (0)
DI unsigned va_of(int tid, unsigned lda, int nt) { int R, C; stage_rc(tid * 16, R, C); return lda ? (unsigned)R * lda + (unsigned)C * 2u : (unsigned)((tid >> 7) * nt * 4096 + (tid & 127) * 16); }
DI unsigned vb_of(int tid, int wid, unsigned ldb) { return (unsigned)(8 * (wid >> 1) + ((tid >> 2) & 7)) * ldb + (unsigned)((2 * (wid & 1) + ((tid >> 5) & 1)) * 64 + (((tid & 3) ^ (((wid >> 1) & 1) << 1)) * 16)); }
#define GEMM_LANE_SETUP(lda0) \
    const int tid = fresh_tid(); \
    const int wid = __builtin_amdgcn_readfirstlane(tid >> 6), lane = tid & 63, wr = wid >> 2, wc = wid & 3, fr = lane & 15, fq = lane >> 4; \
    const unsigned ldsw = (unsigned)wid * 1024u; \
    const int aoff = (lda0) ? lds_byte(wr * 64 + fr, fq * 8) : wr * 8192 + lane * 16; \
    const int boff0 = fq * 2048 + wc * 512 + (fr >> 2) * 64 + (fq & 1) * 32 + (fr & 3) * 8, boff1 = boff0 ^ 32;

template <class Sched>
DI void gemm_stream(ldsp lds, const Sched& S, unsigned* ctl) {
    Unit cur, nxt; int ui = 0;
    if (!S.next(0, cur)) return;
    GEMM_LANE_SETUP(cur.lda);
#define SA(b, h) (((b) * 2 + (h)) * HTB)
#define SB(b, h) ((4 + (b) * 2 + (h)) * HTB)
    const unsigned vb0 = (unsigned)(size_t)lds + 65536u + (unsigned)boff0, vb1 = (unsigned)(size_t)lds + 65536u + (unsigned)boff1;
    f32x4 acc[2][2][4][2];
#pragma unroll
    for (int a = 0; a < 2; ++a)
#pragma unroll
        for (int b = 0; b < 2; ++b)
#pragma unroll
            for (int m = 0; m < 4; ++m)
#pragma unroll
                for (int n = 0; n < 2; ++n) acc[a][b][m][n] = (f32x4){0.f, 0.f, 0.f, 0.f};
    bf16x8 At[4][2], B0[2][2], B1[2][2];
    const unsigned vA = va_of(tid, cur.lda, cur.nt); unsigned vB = vb_of(tid, wid, cur.ldb);
    const size_t a2s = cur.lda ? (size_t)64 * cur.lda : (size_t)cur.nt * 16384;
    {
        const size_t ah = cur.ah, b2s = (size_t)32 * cur.ldb, bk = (size_t)64 * cur.ldb;
        STAGE(SB(0, 0), cur.B, vB, b2s); STAGE(SB(0, 1), cur.B + 256, vB, b2s); STAGE(SA(0, 0), cur.A, vA, a2s); STAGE(SA(0, 1), cur.A + ah, vA, a2s);
        if (wr == 1) BAR();
        WAIT_V(2); BAR();
        STAGE(SB(1, 0), cur.B + bk, vB, b2s); STAGE(SA(1, 0), cur.A + cur.ak, vA, a2s); STAGE(SB(1, 1), cur.B + bk + 256, vB, b2s);
        WAIT_V(6); BAR();
    }
    int psig = 0, pn = 0;
    for (;;) {
        const bool has_next = S.next(ui + 1, nxt);
        if (!has_next) nxt = cur;
        int nt = cur.nt; asm volatile("" : "+s"(nt));
        for (int t = 0; t < nt; t += 2) {
            const bool last = (t == nt - 2);
            const char* a1 = cur.A + (cur.lda ? (unsigned)(t + 1) * 128u : gap((unsigned)(t + 1) * 2048u)) + cur.ah;
            if (last) vB = vb_of(fresh_tid(), wid, nxt.ldb);
            const unsigned ldb = last ? nxt.ldb : cur.ldb;
            const char* a2 = last ? nxt.A : cur.A + (cur.lda ? (unsigned)(t + 2) * 128u : gap((unsigned)(t + 2) * 2048u));
            const char* b2 = last ? nxt.B : cur.B + (size_t)(t + 2) * 64 * cur.ldb;
            const size_t ah = cur.ah, b2s = (size_t)32 * ldb;
            const char* a3 = a2 + cur.ak; const char* b3 = b2 + (size_t)64 * ldb;
            LDB(B0, vb0, vb1, 0); LDB(B1, vb0, vb1, HTB); SCHED(); LDA(At, SA(0, 0)); STAGE(SA(1, 1), a1, vA, a2s);
            WAIT_V(8); WAIT_L(0); BAR(); MMA(acc[0][0], At, B0); MMA(acc[0][1], At, B1); BAR(); SCHED();
            LDA(At, SA(0, 1)); STAGE(SB(0, 0), b2, vB, b2s); STAGE(SB(0, 1), b2 + 256, vB, b2s); STAGE(SA(0, 0), a2, vA, a2s);
            WAIT_V(8);
            asm volatile("" : "+s"(pn));
            if (pn) { signal(ctl, psig, pn); pn = 0; }
            WAIT_L(0); BAR(); MMA(acc[1][0], At, B0); MMA(acc[1][1], At, B1); BAR(); SCHED();
            LDB(B0, vb0, vb1, 2 * HTB); LDB(B1, vb0, vb1, 3 * HTB); SCHED(); LDA(At, SA(1, 0)); STAGE(SA(0, 1), a2 + ah, vA, a2s);
            WAIT_V(8); WAIT_L(0); BAR(); MMA(acc[0][0], At, B0); MMA(acc[0][1], At, B1); BAR(); SCHED();
            LDA(At, SA(1, 1)); STAGE(SB(1, 0), b3, vB, b2s); STAGE(SB(1, 1), b3 + 256, vB, b2s); STAGE(SA(1, 0), a3, vA, a2s);
            WAIT_V(8); WAIT_L(0); BAR(); MMA(acc[1][0], At, B0); MMA(acc[1][1], At, B1); BAR(); SCHED();
        }
        if (wr == 0) BAR();
        {
            const rsrc_t cr = mkrsrc(cur.C, 0x1000000u);
            const int tz = fresh_tid() & 63;
            if (cur.nsig) {
                const unsigned co = (unsigned)wc * cur.cw + frag_off(tz);
#pragma unroll
                for (int ai = 0; ai < 2; ++ai)
#pragma unroll
                    for (int m = 0; m < 4; ++m)
#pragma unroll
                        for (int bj = 0; bj < 2; ++bj) store_frag(cr, co, gap((unsigned)(ai * 128 + wr * 64 + m * 16) * cur.ldc) + (unsigned)bj * cur.ch, acc[ai][bj][m][0], acc[ai][bj][m][1]);
            } else {
                const unsigned co = (unsigned)(wr * 64 + (tz >> 2)) * cur.ldc + (unsigned)(wc * 64 + (tz & 3) * 16);
#pragma unroll
                for (int ai = 0; ai < 2; ++ai)
#pragma unroll
                    for (int m = 0; m < 4; ++m)
#pragma unroll
                        for (int bj = 0; bj < 2; ++bj)
                            ST16(to_rows(lds + L_SCR + wid * 1024, tz, acc[ai][bj][m][0], acc[ai][bj][m][1]), cr, co, (unsigned)(ai * 128 + m * 16) * cur.ldc + (unsigned)bj * 256u, 0);
            }
        }
        psig = cur.sig; pn = cur.nsig;
        if (!has_next) break;
#pragma unroll
        for (int a = 0; a < 2; ++a)
#pragma unroll
            for (int b = 0; b < 2; ++b)
#pragma unroll
                for (int m = 0; m < 4; ++m)
#pragma unroll
                    for (int n = 0; n < 2; ++n) acc[a][b][m][n] = (f32x4){0.f, 0.f, 0.f, 0.f};
        cur = nxt; ++ui;
        if (wr == 1) BAR();
    }
    WAIT_V(0);
    signal(ctl, psig, pn);
    BAR();
#undef SA
#undef SB
}

DI void gemm_small(ldsp lds, const Unit u, unsigned* ctl) {
    GEMM_LANE_SETUP(u.lda);
    const unsigned vA = va_of(tid, u.lda, u.nt), vB = vb_of(tid, wid, u.ldb);
    const size_t a2s = u.lda ? (size_t)64 * u.lda : (size_t)u.nt * 16384, b2s = (size_t)32 * u.ldb, bk = (size_t)64 * u.ldb;
    f32x4 acc[4][2];
#pragma unroll
    for (int m = 0; m < 4; ++m)
#pragma unroll
        for (int n = 0; n < 2; ++n) acc[m][n] = (f32x4){0.f, 0.f, 0.f, 0.f};
    int nt = u.nt; asm volatile("" : "+s"(nt));
    const unsigned vb0 = (unsigned)(size_t)lds + (unsigned)boff0, vb1 = (unsigned)(size_t)lds + (unsigned)boff1, vb2 = vb0 + 65536u, vb3 = vb1 + 65536u;
#pragma unroll
    for (int t = 0; t < 4; ++t) { STAGE(t * 32768, u.A + (size_t)t * u.ak, vA, a2s); STAGE(t * 32768 + 16384, u.B + t * bk, vB, b2s); }
    bf16x8 A0[4][2], B0[2][2], A1[4][2], B1[2][2];
    WAIT_V(12); BAR();
    LDB(B0, vb0, vb1, 16384); LDA(A0, 0);
#define RDA(dst, m, off) do { dst[m][0] = *(const LAS bf16x8*)(lds + (off) + aoff + (m) * 2048); dst[m][1] = *(const LAS bf16x8*)(lds + (off) + aoff + (m) * 2048 + 1024); } while (0)
#define RDB(dst, nn, v, off) do { s16x4 l0, h0, l1, h1; TRD(l0, v, (off)); TRD(h0, v, (off) + 256); TRD(l1, v, (off) + 8192); TRD(h1, v, (off) + 8448); dst[nn][0] = cat8(l0, h0); dst[nn][1] = cat8(l1, h1); } while (0)
#define MM4(Ac, Bc, m) do { SCHED(); _Pragma("unroll") for (int n = 0; n < 2; ++n) _Pragma("unroll") for (int k = 0; k < 2; ++k) acc[m][n] = mma32(Bc[n][k], Ac[m][k], acc[m][n]); SCHED(); } while (0)
#define KSTEP(i, Ac, Bc, An, Bn, v0, v1, boffn) do { \
        const int tn = min(t0 + (i) + 4, nt - 1); \
        WAIT_L(0); WAIT_V(8); BAR(); \
        MM4(Ac, Bc, 0); RDB(Bn, 0, v0, boffn); RDA(An, 0, (((i) + 1) & 3) * 32768); \
        MM4(Ac, Bc, 1); RDB(Bn, 1, v1, boffn); RDA(An, 1, (((i) + 1) & 3) * 32768); \
        MM4(Ac, Bc, 2); RDA(An, 2, (((i) + 1) & 3) * 32768); STAGE((i) * 32768, u.A + (u.lda ? (unsigned)tn * 128u : gap((unsigned)tn * 2048u)), vA, a2s); \
        MM4(Ac, Bc, 3); RDA(An, 3, (((i) + 1) & 3) * 32768); STAGE((i) * 32768 + 16384, u.B + (size_t)tn * bk, vB, b2s); } while (0)
    for (int t0 = 0; t0 < nt; t0 += 4) {
        KSTEP(0, A0, B0, A1, B1, vb0, vb1, 49152);
        KSTEP(1, A1, B1, A0, B0, vb2, vb3, 16384);
        KSTEP(2, A0, B0, A1, B1, vb2, vb3, 49152);
        KSTEP(3, A1, B1, A0, B0, vb0, vb1, 16384);
    }
    WAIT_L(0); SCHED();
#undef RDA
#undef RDB
#undef MM4
#undef KSTEP
    const rsrc_t cr = mkrsrc(u.C, 256u * u.ldc);
#pragma unroll
    for (int m = 0; m < 4; ++m) store_frag(cr, (unsigned)wc * u.cw + frag_off(lane), gap((unsigned)(wr * 64 + m * 16) * u.ldc), acc[m][0], acc[m][1]);
    WAIT_V(0);
    signal(ctl, u.sig, u.nsig);
    BAR();
}

DI void gemm_wide(ldsp lds, const char* A, const char* B, char* C, int nt) {
    GEMM_LANE_SETUP(0);
    const unsigned vA = va_of(tid, 0, nt), vB = vb_of(tid, wid, DM * 2);
    const size_t a2s = (size_t)nt * 16384, b2s = 32 * DM * 2, bk = 64 * DM * 2;
    f32x4 acc[2][4][2];
#pragma unroll
    for (int b = 0; b < 2; ++b)
#pragma unroll
        for (int m = 0; m < 4; ++m)
#pragma unroll
            for (int n = 0; n < 2; ++n) acc[b][m][n] = (f32x4){0.f, 0.f, 0.f, 0.f};
    asm volatile("" : "+s"(nt));
    const unsigned vb0 = (unsigned)(size_t)lds + (unsigned)boff0, vb1 = (unsigned)(size_t)lds + (unsigned)boff1;
#define STG3(slot, t) do { STAGE((slot), A + gap((unsigned)(t) * 2048u), vA, a2s); STAGE((slot) + 16384, B + (size_t)(t) * bk, vB, b2s); STAGE((slot) + 32768, B + (size_t)(t) * bk + 256, vB, b2s); } while (0)
    STG3(0, 0); STG3(49152, 1); STG3(98304, 2);
    bf16x8 A0[4][2], P0[2][2], Q0[2][2], A1[4][2], P1[2][2], Q1[2][2];
    WAIT_V(12); BAR();
    LDB(P0, vb0, vb1, 16384); LDB(Q0, vb0, vb1, 32768); LDA(A0, 0);
    int sc = 0, sn = 49152;
#define RDA(dst, m) do { dst[m][0] = *(const LAS bf16x8*)(lds + sn + aoff + (m) * 2048); dst[m][1] = *(const LAS bf16x8*)(lds + sn + aoff + (m) * 2048 + 1024); } while (0)
#define RDB(dst, nn, v, off) do { s16x4 l0, h0, l1, h1; TRD(l0, v, (off)); TRD(h0, v, (off) + 256); TRD(l1, v, (off) + 8192); TRD(h1, v, (off) + 8448); dst[nn][0] = cat8(l0, h0); dst[nn][1] = cat8(l1, h1); } while (0)
#define MM4(b, Ac, Bc, m) do { SCHED(); _Pragma("unroll") for (int n = 0; n < 2; ++n) _Pragma("unroll") for (int k = 0; k < 2; ++k) acc[b][m][n] = mma32(Bc[n][k], Ac[m][k], acc[b][m][n]); SCHED(); } while (0)
#define KSTEP(t, Ac, Pc, Qc, An, Pn, Qn) do { \
        const int tn = min((t) + 3, nt - 1); const unsigned u0 = vb0 + (unsigned)sn, u1 = vb1 + (unsigned)sn; \
        WAIT_L(0); WAIT_V(6); BAR(); \
        MM4(0, Ac, Pc, 0); RDB(Pn, 0, u0, 16384); \
        MM4(1, Ac, Qc, 0); RDB(Pn, 1, u1, 16384); \
        MM4(0, Ac, Pc, 1); RDB(Qn, 0, u0, 32768); \
        MM4(1, Ac, Qc, 1); RDB(Qn, 1, u1, 32768); \
        MM4(0, Ac, Pc, 2); RDA(An, 0); STAGE(sc, A + gap((unsigned)tn * 2048u), vA, a2s); \
        MM4(1, Ac, Qc, 2); RDA(An, 1); STAGE(sc + 16384, B + (size_t)tn * bk, vB, b2s); \
        MM4(0, Ac, Pc, 3); RDA(An, 2); STAGE(sc + 32768, B + (size_t)tn * bk + 256, vB, b2s); \
        MM4(1, Ac, Qc, 3); RDA(An, 3); \
        sc = sn; sn = sn == 98304 ? 0 : sn + 49152; } while (0)
    for (int t0 = 0; t0 < nt; t0 += 2) { KSTEP(t0, A0, P0, Q0, A1, P1, Q1); KSTEP(t0 + 1, A1, P1, Q1, A0, P0, Q0); }
    WAIT_L(0); SCHED();
#undef STG3
#undef RDA
#undef RDB
#undef MM4
#undef KSTEP
    const rsrc_t cr = mkrsrc(C, 128u * 4096u);
#pragma unroll
    for (int m = 0; m < 4; ++m)
#pragma unroll
        for (int b = 0; b < 2; ++b)
            ST16(to_rows(lds + L_SCR + wid * 1024, lane, acc[b][m][0], acc[b][m][1]), cr, (unsigned)(wr * 64 + (lane >> 2)) * 4096u + (unsigned)(wc * 64 + (lane & 3) * 16), (unsigned)(m * 65536 + b * 256), 0);
    WAIT_V(0);
    BAR();
}

#define win_off(idx) ((unsigned)(idx) * 16384u + ((((unsigned)(idx) >> 7) ^ par) & 1u) * 8192u)
#define KKM 8192u
#define km_win(s, h) (win_off(NWIN + (s) * 32 + (h)) ^ KKM)
#define W_MEM(s, x) ((s) == 0 ? 0 : NWIN + 2048 + 8 * (s) + 2 * (x))
#define W_PART(slot) (192 + (slot) * 64)
#define W_X(slot) (384 + (slot) * 128)
DI bool all_set(unsigned mx) { return __builtin_amdgcn_ballot_w64(mx == SENT) == 0ull; }
DI u32x4 rows_gather(unsigned v) { unsigned a = v, b = v; swap16(a, b); unsigned c = a, d = b; swap32(a, c); swap32(b, d); return (u32x4){a, b, c, d}; }
DI float max3f(float a, float b, float c) { return fmaxf(fmaxf(a, b), c); }
#define KD0 3
#define KD1 6
#define KD2 4
#define KRA 3
#define KDM 1
#define KWP 4
#define KNT1 0
#define KNT2 0
#define KPM 4
constexpr int RED_M = 144;
constexpr int L_PX = 32768, L_PM = L_PX + 512, L_ML = L_PM + 512, L_FLAG = L_ML + 256, L_PACE = L_FLAG + 16, L_VM = 34816, L_OWN = L_VM + 2048, L_X = L_OWN + 1536  ;
constexpr unsigned OOB = 0x80000000u;
#define RETRY(i)
#define SEG(i)
#define PST(i)
#define PIN_S(x) asm volatile("" : "+s"(x))
#define PIN_V(x) asm volatile("" : "+v"(x))
DI float ror8(float v) { return __uint_as_float(__builtin_amdgcn_update_dpp(0, __float_as_uint(v), 0x128, 0xf, 0xf, true)); }

template <int ROLE>
DI void chain_loop(const Params& p, const unsigned par, const int rank, const int w, ldsp lds, const bf16x16 (&W)[6][2], const bf16x16 (&WO)[2][2], unsigned long long* stamps) {
    const int lane = fresh_tid() & 63, g = lane >> 4, n = lane & 15;
    const int h = rank >> 2, j = rank & 3;
    unsigned* ctl = (unsigned*)(p.ws + WS_CTL);
    const rsrc_t xr = mkrsrc(p.ws + WS_XCH, (unsigned)XCH_BYTES);
    const rsrc_t qr = mkrsrc(p.ws + WH(WS_QKVX + (size_t)(32 + h) * XH), 33u * XH);
    const int sidx = (n & 2) ? 0xEEEE : 0x4444;
    const unsigned l16 = (unsigned)lane * 16u;
    const int redw = (w * 1024 + (n & 1) * 512 + (n >> 1) * 8 + 2 * (g ^ (n >> 3))) * 4;
    const int redr = (w < 4 ? (n >> 3) * RED_M + (4 * w + g) * 8 + ((n & 7) ^ ((w & 1) * 2)) : 2 * RED_M + (8 * (w & 1) + 2 * g + ((n >> 1) & 1)) * 8 + ((2 * (n >> 2) + (n & 1)) ^ (g & 2))) * 4;
    const bool vown = (g >> 1) == (j & 1);
    const unsigned vg1 = (unsigned)(((j & 2) ^ 2) + (g >> 1)) * 1536u + 1024u + (unsigned)(lane & 31) * 16u;
    const unsigned vg0 = vown ? OOB : (vg1 + (unsigned)(j & 2) * 3072u - 3072u);
    const unsigned pub_off = (g == j ? 0u : (unsigned)(4 * h + g) * 16384u) + (unsigned)j * 1536u + (unsigned)w * 256u + (unsigned)n * 16u;
    const int red = ((j + 1) & 3) * 32 + h;
    const int rho = 4 * (8 * j + w) + 2 * (g >> 1) + ((n >> 2) & 1);
    const unsigned pst_off = n < 8 ? (unsigned)(rho >> 1) * 16384u + (unsigned)(rho & 1) * 4096u + (unsigned)((h ^ rho) & 31) * 128u + (unsigned)((g & 1) * 64 + (n & 3) * 16) : OOB;
    const unsigned mem_off = lane < 32 ? (unsigned)(4 * ((n >> 1) & 1) + 2 * ((n >> 2) & 1) + (n & 1)) * 16u + (unsigned)(lane >> 3) * 32768u : OOB;
    const unsigned xw0 = win_off(W_X(0)), xw1 = win_off(W_X(1));
    unsigned pso = win_off(W_PART(0)), psn = win_off(W_PART(1)), psp = win_off(W_PART(2));
    const unsigned redo = (unsigned)(red >> 1) * 16384u + (unsigned)(red & 1) * 4096u;
    const int vxr = L_X + lane * 16;
    const float csc = 0.08838834764831845f * 1.4426950408889634f;
    float ninf = -__builtin_inff(); PIN_V(ninf);
    u32x4 KX[8], VWX[2];
    unsigned long long kv_ok = 0;
    auto dma_x = [&](int s) {
        if (ROLE == 0 && w < 4) {
            if (!((kv_ok >> (s >> 2)) & 1ull)) {
                kv_ok = __builtin_amdgcn_ballot_w64(__hip_atomic_load(ctl + C_KVC(n, h), RLX_AGENT) + 1u >= 16u);
                if (!((kv_ok >> (s >> 2)) & 1ull)) { wait_count(ctl, C_KVC(s >> 2, h), 16u); kv_ok |= 1ull << (s >> 2); }
                asm volatile("" ::: "memory");
            }
#pragma unroll
            for (int i = 0; i < 2; ++i) {
                const unsigned vo = l16, so = (unsigned)s * 16384u + (unsigned)(w * 2 + i) * 1024u;
                __builtin_amdgcn_raw_ptr_buffer_load_lds(qr, (LAS void*)(lds + L_X + (s & 1) * 16384 + (w * 2 + i) * 1024), 16, vo, so + 32u * XH, 0, 16);
                __builtin_amdgcn_raw_ptr_buffer_load_lds(qr, (LAS void*)(lds + L_X + (s & 1) * 16384 + 8192 + (w * 2 + i) * 1024), 16, vo, so, 0, 16);
            }
        }
    };
    auto load_kx = [&](int s) {
#pragma unroll
        for (int kt = 0; kt < 2; ++kt)
#pragma unroll
            for (int kk = 0; kk < 4; ++kk) KX[kt * 4 + kk] = *(const LAS u32x4*)(lds + vxr + (s & 1) * 16384 + 8192 + kt * 4096 + ((j + kk) & 3) * 1024);
    };
    auto vwx_kt = [&](int s, int kt) {
        bf16x8 VA[4];
#pragma unroll
        for (int kk = 0; kk < 4; ++kk) VA[kk] = *(const LAS bf16x8*)(lds + vxr + (s & 1) * 16384 + kt * 4096 + (kk ^ (j & 2)) * 1024);
#pragma unroll
        for (int ct = 0; ct < 2; ++ct) {
            f32x4 d = {0.f, 0.f, 0.f, 0.f};
#pragma unroll
            for (int kk = 0; kk < 4; ++kk) d = mma32(VA[kk], (kk & 1) ? hi8(WO[ct][kk >> 1]) : lo8(WO[ct][kk >> 1]), d);
            VWX[ct][2 * kt] = pk_bf16(d[0], d[1]); VWX[ct][2 * kt + 1] = pk_bf16(d[2], d[3]);
        }
    };
    auto rmax = [&](float v) { float a = v, b = v; swap16f(a, b); v = max3f(a, b, ninf); a = v; b = v; swap32f(a, b); return max3f(a, b, ninf); };
    auto lo2 = [](f32x4 v) { return (f32x2){v[0], v[1]}; };
    auto hi2 = [](f32x4 v) { return (f32x2){v[2], v[3]}; };
    dma_x(0); dma_x(1);
    asm volatile("s_waitcnt vmcnt(0) lgkmcnt(0)" ::: "memory"); BAR(); asm volatile("" ::: "memory");
    PST(5);
    vwx_kt(0, 0); vwx_kt(0, 1);
    if (ROLE == 2) load_kx(0);
    __builtin_amdgcn_s_setprio(2);

    unsigned iv_t0 = 0, iv_acc = 0;
    unsigned spins = 0, pace_c = 0x7fffffffu, pace_t = 0;
    const u32x4 sv = {SENT, SENT, SENT, SENT};
    for (int s = 0;; ++s) {
        unsigned xw = (s & 1) ? xw1 : xw0;
        unsigned mso = win_off(W_MEM(s, rank >> 5) + (w >> 2)) + (unsigned)(w & 3) * 2048u;
        unsigned pub = pub_off + (g == j ? km_win(s, h) : xw);
        PIN_S(xw); PIN_S(mso); PIN_V(pub);
        {
            if (KD0) __builtin_amdgcn_s_sleep(KD0);
            const u32x2 pv = *(const LAS u32x2*)(lds + L_PACE);
            unsigned target = __builtin_amdgcn_readfirstlane(pv.x); const bool valid = __builtin_amdgcn_readfirstlane(pv.y) + 1u == (unsigned)s && s > 3;
            unsigned now = (unsigned)__builtin_amdgcn_s_memrealtime();
            if (ROLE == 3 && s > 2) pace_c = min(pace_c, now - pace_t);
            if (valid && (int)(target - now) < 100) { while ((int)(target - now) > 0) now = (unsigned)__builtin_amdgcn_s_memrealtime(); }
        }
        SEG(10);
        f32x4 c[6]; u32x4 a1;
        u32x4 a0;
        auto ld_a = [&]() { asm volatile("" ::: "memory"); a0 = ld16_sc1(xr, l16, mso); a1 = ld16_sc1(xr, l16 + 1024, mso); };
        auto ok_a = [&]() { return all_set(max(umax4(a0), umax4(a1))); };
        auto mm_a = [&]() {
#pragma unroll
            for (int i = 0; i < 6; ++i) { c[i] = (f32x4){0.f, 0.f, 0.f, 0.f}; PIN_V(c[i]); }
            SCHED();
            c[0] = smma(a0, W[0][0], c[0], sidx); c[1] = smma(a0, W[1][0], c[1], sidx); c[0] = smma(a1, W[0][1], c[0], sidx); c[1] = smma(a1, W[1][1], c[1], sidx);
            c[2] = smma(a0, W[2][0], c[2], sidx); c[3] = smma(a0, W[3][0], c[3], sidx);
        };
        auto put = [&](int i) { *(LAS f32x2*)(lds + redw + ((i >> 1) * RED_M + (i & 1) * 64) * 4) = lo2(c[i]) + hi2(c[i]); };
        ld_a(); SCHED();
        mm_a();
        if (__builtin_expect(!ok_a(), 0)) { bool out; do { RETRY(0); out = spin_out(spins, ctl); ld_a(); } while (!(ok_a() | out)); mm_a(); }
        SEG(0);
        c[2] = smma(a1, W[2][1], c[2], sidx); c[3] = smma(a1, W[3][1], c[3], sidx);
        put(0); put(1);
        SCHED();
        c[4] = smma(a0, W[4][0], c[4], sidx); c[5] = smma(a0, W[5][0], c[5], sidx); c[4] = smma(a1, W[4][1], c[4], sidx);
        put(2); put(3);
        c[5] = smma(a1, W[5][1], c[5], sidx);
        SCHED();
        put(4); put(5);
        SEG(1);
        LDS_BAR();
        SEG(2);
        if (ROLE <= 1) {
            f32x2 v[8];
#pragma unroll
            for (int ww = 0; ww < 8; ++ww) { const LAS float* q = (const LAS float*)(lds + redr + ww * 4096); v[ww] = (f32x2){q[0], q[512]}; }
            const f32x2 t = ((v[0] + v[1]) + (v[2] + v[3])) + ((v[4] + v[5]) + (v[6] + v[7]));
            const u32x4 o = rows_gather(min(pk_bf16(t.x, t.y), SENT - 1));
            ST16(o, xr, pub, 0, 16);
            *(LAS u32x4*)(lds + L_OWN + w * 256 + n * 16) = o;
        } else {
            if (ROLE == 3 && rank == 0) {
                pace_t = (unsigned)__builtin_amdgcn_s_memrealtime();
                if (lane == 0) { __hip_atomic_store(ctl + C_PROG, (unsigned)s, RLX_AGENT);
                    __hip_atomic_store((unsigned long long*)(ctl + C_PACE + par * 2048), (unsigned long long)(pace_t + pace_c + KPM) | ((unsigned long long)s << 32), RLX_AGENT); }
            }
        }
        SEG(3);
        if (s == NSEG - 1) break;
        LDS_BAR();
        SEG(13);
        unsigned VWM[2];
        auto vwm_pack = [&](const f32x4 (&dv)[2]) {
#pragma unroll
            for (int ct = 0; ct < 2; ++ct) { const f32x2 t = lo2(dv[ct]) + hi2(dv[ct]); VWM[ct] = pk_bf16(t.x, t.y); PIN_V(VWM[ct]); }
        };
        f32x4 dv[2] = {{0.f, 0.f, 0.f, 0.f}, {0.f, 0.f, 0.f, 0.f}}, s0, s1;
        PIN_V(dv[0]); PIN_V(dv[1]);
        u32x4 v0, v1;
        if (ROLE == 0) {
            if (KDM == 1) { WAIT_V(0); dma_x(min(s + 2, NSEG - 2)); }
            else if (KDM == 2) { dma_x(min(s + 2, NSEG - 2)); if (w < 4) WAIT_V(4); else WAIT_V(0); }
            else WAIT_V(0);
        } else {
            unsigned cw = xw + (unsigned)rank * 16384u, c1 = cw + (unsigned)((j + 1) & 3) * 1536u, c2 = cw + (unsigned)((j + 2) & 3) * 1536u, c3 = cw + (unsigned)((j + 3) & 3) * 1536u;
            PIN_S(cw); PIN_S(c1); PIN_S(c2); PIN_S(c3);
            const unsigned xl = ROLE == 2 ? (l16 & ~128u) : l16;
            u32x4 vo;
            f32x4 e0 = {0.f, 0.f, 0.f, 0.f}, e1 = e0;
            if (ROLE == 1) { vo = *(const LAS u32x4*)(lds + L_OWN + 1024 + (lane & 31) * 16); if (!vown) vo = (u32x4){0u, 0u, 0u, 0u}; PIN_V(vo); }
            else {
                const u32x4 X0 = *(const LAS u32x4*)(lds + L_OWN + xl);
                if (ROLE == 2) { e0 = mma32(asbf(KX[0]), asbf(X0), e0); e1 = mma32(asbf(KX[4]), asbf(X0), e1); } else e0 = mma32(asbf(X0), asbf(X0), e0);
                PIN_V(e0); PIN_V(e1);
            }
            if (KD1) __builtin_amdgcn_s_sleep(KD1);
            u32x4 X1, X2, X3;
            auto ld_b = [&]() {
                asm volatile("" ::: "memory");
                if (ROLE >= 2) { X1 = ld16_sc1(xr, xl, c1); X2 = ld16_sc1(xr, xl, c2); X3 = ld16_sc1(xr, xl, c3); }
                else { v0 = ld16_sc1(xr, vg0, cw); v1 = ld16_sc1(xr, vg1, cw); }
            };
            auto ok_b = [&]() { return all_set(ROLE >= 2 ? max(umax4(X1), max(umax4(X2), umax4(X3))) : max(umax4(v0), umax4(v1))); };
            auto mm_b = [&]() {
                if (ROLE == 2) {
                    s0 = mma32(asbf(KX[1]), asbf(X1), e0); s1 = mma32(asbf(KX[5]), asbf(X1), e1);
                    s0 = mma32(asbf(KX[2]), asbf(X2), s0); s1 = mma32(asbf(KX[6]), asbf(X2), s1);
                    s0 = mma32(asbf(KX[3]), asbf(X3), s0); s1 = mma32(asbf(KX[7]), asbf(X3), s1);
                } else if (ROLE == 3) {
                    s0 = mma32(asbf(X1), asbf(X1), e0); s1 = mma32(asbf(X2), asbf(X2), e1); s0 = mma32(asbf(X3), asbf(X3), s0);
                } else {
                    v0 |= vo;
                    *(LAS u32x4*)(lds + L_VM + lane * 16) = v0; *(LAS u32x4*)(lds + L_VM + 1024 + lane * 16) = v1;
                }
            };
            SEG(11);
            ld_b(); SCHED(); mm_b();
            if (__builtin_expect(!ok_b(), 0)) { bool out; do { RETRY(1); out = spin_out(spins, ctl); ld_b(); } while (!(ok_b() | out)); mm_b(); }
            SEG(4);
        }
        float m;
        f32x4 sc;
        if (ROLE >= 2) {
            sc = ROLE == 2 ? (n < 8 ? s0 : s1) : s0 + s1;
            if (ROLE == 3 && g < 2) sc = (f32x4){ninf, ninf, ninf, ninf};
            m = max3f(max3f(sc[0], sc[1], sc[2]), sc[3], ninf);
            if (ROLE == 2) m = max3f(m, ror8(m), ninf);
            m = rmax(m);
            PIN_V(m);
        }
        LDS_BAR();
        if (ROLE == 0) {
            const unsigned ro = w < 3 ? xw + (unsigned)rank * 16384u + (unsigned)w * 2048u : psp + redo + (unsigned)(w - 3) * 2048u;
            if (KDM == 4 && s + 2 < NSEG - 1) dma_x(s + 2);
            ST16(sv, xr, l16, ro, 16); ST16(sv, xr, l16 + 1024, ro, 16);
            if (KDM == 5 && s + 2 < NSEG - 1) dma_x(s + 2);
        }
        if (ROLE != 1) { v0 = *(const LAS u32x4*)(lds + L_VM + lane * 16); v1 = *(const LAS u32x4*)(lds + L_VM + 1024 + lane * 16); }
        if (ROLE >= 2) {
            const float mc = -m * csc;
#pragma unroll
            for (int i = 0; i < 4; ++i) sc[i] = __builtin_amdgcn_exp2f(fmaf(sc[i], csc, mc));
            *(LAS u32x2*)(lds + (ROLE == 2 ? L_PX + (g * 8 + (n & 7)) * 16 + (n >> 3) * 8 : L_PM + (g < 2 ? 256 : 0) + n * 16 + (g & 1) * 8)) = (u32x2){pk_bf16(sc[0], sc[1]), pk_bf16(sc[2], sc[3])};
            SCHED();
            dv[0] = smma(v0, WO[0][0], dv[0], sidx); dv[1] = smma(v0, WO[1][0], dv[1], sidx);
            float l = (sc[0] + sc[1]) + (sc[2] + sc[3]);
            if (ROLE == 2) l += ror8(l);
            dv[0] = smma(v1, WO[0][1], dv[0], sidx); dv[1] = smma(v1, WO[1][1], dv[1], sidx);
            l = rows_sum(l);
            *(LAS f32x2*)(lds + L_ML + n * 16 + (ROLE == 2 ? 0 : 8)) = (f32x2){mc, l};
        } else {
#pragma unroll
            for (int ct = 0; ct < 2; ++ct) { dv[ct] = smma(v0, WO[ct][0], dv[ct], sidx); dv[ct] = smma(v1, WO[ct][1], dv[ct], sidx); }
        }
        vwm_pack(dv);
        SEG(5);
        u32x4 am0 = {VWM[0], 0u, 0u, 0u}, am1 = {VWM[1], 0u, 0u, 0u}, pm = {0u, 0u, 0u, 0u};
        PIN_V(am0); PIN_V(am1); PIN_V(pm);
        LDS_BAR();
        SEG(6);
        {
            const f32x4 ml = *(const LAS f32x4*)(lds + L_ML + n * 16);
            const u32x4 px = *(const LAS u32x4*)(lds + L_PX + (g * 8 + (n & 7)) * 16);
            pm.x = *(const LAS unsigned*)(lds + L_PM + n * 16 + g * 4);
            const f32x4 z = {0.f, 0.f, 0.f, 0.f};
            const f32x4 dx0 = mma32(asbf(VWX[0]), asbf(px), z), dx1 = mma32(asbf(VWX[1]), asbf(px), z), dm0 = mma32(asbf(am0), asbf(pm), z), dm1 = mma32(asbf(am1), asbf(pm), z);
            const float dl = ml[0] - ml[2];
            const float e = __builtin_amdgcn_exp2f(-__builtin_fabsf(dl));
            const float ex = dl > 0.f ? e : 1.f, em = dl > 0.f ? 1.f : e;
            const float inv = __builtin_amdgcn_rcpf(fmaf(ex, ml[1], em * ml[3]));
            const f32x4 d0 = (dx0 * ex + dm0 * em) * inv, d1 = (dx1 * ex + dm1 * em) * inv;
            const u32x4 o = {min(pk_bf16(d0[0], d0[1]), SENT - 1), min(pk_bf16(d0[2], d0[3]), SENT - 1), min(pk_bf16(d1[0], d1[1]), SENT - 1), min(pk_bf16(d1[2], d1[3]), SENT - 1)};
            ST16(o, xr, pst_off, pso, 16);
        }
        SCHED();
        SEG(7);
        __builtin_amdgcn_s_setprio(ROLE == 3 ? 1 : 0);
        if (KDM == 3 && s + 2 < NSEG - 1) dma_x(s + 2);
        unsigned long long pv = 0;
        if (ROLE == 1) { pv = __hip_atomic_load((unsigned long long*)(ctl + C_PACE + par * 2048), RLX_AGENT); SCHED(); }
        vwx_kt(s + 1, 0); vwx_kt(s + 1, 1);
        SCHED();
        if (ROLE == 3) __builtin_amdgcn_s_setprio(2);
        if (ROLE == 3) {
            u32x4 SEL;
            { const unsigned one = (n & 1) ? 0x3f800000u : 0x00003f80u; const int dw = (n & 7) >> 1; SEL = (u32x4){dw == 0 ? one : 0u, dw == 1 ? one : 0u, dw == 2 ? one : 0u, dw == 3 ? one : 0u}; }
            unsigned so = pso + redo, mo = win_off(W_MEM(s + 1, 0) + (red >> 6)) + (unsigned)(red & 63) * 128u;
            PIN_S(so); PIN_S(mo); PIN_V(SEL);
            if (KD2) __builtin_amdgcn_s_sleep(KD2);
            f32x4 ta, tb;
            u32x4 q[4];
            auto ld_c = [&]() {
                asm volatile("" ::: "memory");
#pragma unroll
                for (int i = 0; i < 4; ++i) q[i] = ld16_sc1(xr, l16 + i * 1024, so);
            };
            auto ok_c = [&]() { return all_set(max(max(umax4(q[0]), umax4(q[1])), max(umax4(q[2]), umax4(q[3])))); };
            auto mm_c = [&]() {
                ta = mma32(asbf(SEL), asbf(q[0]), (f32x4){0.f, 0.f, 0.f, 0.f}); tb = mma32(asbf(SEL), asbf(q[1]), (f32x4){0.f, 0.f, 0.f, 0.f});
                ta = mma32(asbf(SEL), asbf(q[2]), ta); tb = mma32(asbf(SEL), asbf(q[3]), tb);
            };
            SEG(12);
            ld_c(); mm_c();
            if (__builtin_expect(!ok_c(), 0)) { bool out; do { RETRY(2); out = spin_out(spins, ctl); ld_c(); } while (!(ok_c() | out)); mm_c(); }
            SEG(8);
            f32x4 t = ta + tb;
#pragma unroll
            for (int i = 0; i < 4; ++i) t[i] += ror8(t[i]);
            unsigned a0 = min(pk_bf16(t[0], t[1]), SENT - 1), a1 = min(pk_bf16(t[2], t[3]), SENT - 1);
            unsigned b0 = a0, b1 = a1; swap16(a0, b0); swap16(a1, b1);
            const u32x4 o = {a0, a1, b0, b1};
            ST16(o, xr, mem_off, mo, 16);
        } else {
            if (KDM == 0 && s + 2 < NSEG - 1) dma_x(s + 2);
            if (ROLE == 2) load_kx(s + 1);
            if (ROLE == 1) *(LAS u32x2*)(lds + L_PACE) = (u32x2){(unsigned)pv, (unsigned)(pv >> 32)};
        }
        { const unsigned t = psp; psp = pso; pso = psn; psn = t; }
        SEG(9);
        if (ROLE != 3) __builtin_amdgcn_s_setprio(2);
        LDS_BAR();
        SEG(14);
    }
    WAIT_V(0);
    __builtin_amdgcn_s_setprio(0);
}

DI void chain_role(const Params& p, unsigned par, int rank, ldsp lds, unsigned long long* stamps) {
    const int tid = fresh_tid(), w = __builtin_amdgcn_readfirstlane(tid >> 6), lane = tid & 63, g = lane >> 4, n = lane & 15;
    const int h = rank >> 2, j = rank & 3;
    const rsrc_t xr = mkrsrc(p.ws + WS_XCH, (unsigned)XCH_BYTES);
    unsigned* ctl = (unsigned*)(p.ws + WS_CTL);
    PST(0);
    const int red = ((j + 1) & 3) * 32 + h;
    bf16x16 W[6][2], WO[2][2];
    {
        ldsp my = lds + w * 16384;
        const int rrow = lane >> 2, rc = (lane & 3) * 8, q4 = n >> 2;
        const int toffo = (8 * g + q4) * 64 + (n & 3) * 8;
        const int toffq = (16 * (g & 1) + 8 * (g >> 1) + 4 * (q4 >> 1) + (q4 & 1)) * 64 + (n & 3) * 8;
#define FRAG(base, st) cat16(cat8(trd((base)), trd((base) + (st))), cat8(trd((base) + 2048), trd((base) + 2048 + (st))))
#pragma unroll
        for (int i = 0; i < 8; ++i) {
            __builtin_amdgcn_global_load_lds((const unsigned*)(p.wq + (size_t)(128 * w + 16 * i + rrow) * 4096 + h * 128 + 32 * j + rc), (LAS unsigned*)(my + i * 1024), 16, 0, KNT1);
            __builtin_amdgcn_global_load_lds((const unsigned*)(p.wo + (size_t)(h * 128 + 16 * i + rrow) * 1024 + 256 * j + 32 * w + rc), (LAS unsigned*)(my + 8192 + i * 1024), 16, 0, KNT1);
        }
        {
            const u32x4 sv = {SENT, SENT, SENT, SENT};
            if (w < 6) {
                ST16(sv, xr, (unsigned)lane * 16u, win_off(W_X(0)) + (unsigned)rank * 16384u + (unsigned)w * 1024u, 16);
                ST16(sv, xr, (unsigned)lane * 16u, win_off(W_X(1)) + (unsigned)rank * 16384u + (unsigned)w * 1024u, 16);
            }
            ST16(sv, xr, (unsigned)lane * 16u, win_off(W_PART(w >> 2)) + (unsigned)(red >> 1) * 16384u + (unsigned)(red & 1) * 4096u + (unsigned)(w & 3) * 1024u, 16);
            ST16(sv, xr, (unsigned)lane * 16u, km_win(56 + w, h) + (unsigned)j * 1536u, 16);
            ST16(sv, xr, lane < 32 ? (unsigned)lane * 16u + 1024u : OOB, km_win(56 + w, h) + (unsigned)j * 1536u, 16);
            const int s = 8 * w + (lane >> 3);
#pragma unroll
            for (int x = 0; x < 4; ++x) if (s > 0) ST16(sv, xr, win_off(W_MEM(s, x) + (red >> 6)) + (unsigned)(red & 63) * 128u + (unsigned)(lane & 7) * 16u, 0, 16);
        }
        PST(3);
        WAIT_V(0);
#pragma unroll
        for (int T = 0; T < 2; ++T)
#pragma unroll
            for (int t = 0; t < 2; ++t) { W[T][t] = FRAG(my + t * 4096 + T * 32 + toffq, 128); WO[T][t] = FRAG(my + 8192 + (t ^ (j >> 1)) * 4096 + T * 32 + toffo, 256); }
#pragma unroll
        for (int T = 0; T < 2; ++T) asm volatile("" : "+v"(W[T][0]), "+v"(W[T][1]), "+v"(WO[T][0]), "+v"(WO[T][1]));
        WAIT_L(0);
        PST(1);
#pragma unroll
        for (int i = 0; i < 8; ++i) {
            __builtin_amdgcn_global_load_lds((const unsigned*)(p.wk + (size_t)(128 * w + 16 * i + rrow) * 4096 + h * 128 + 32 * j + rc), (LAS unsigned*)(my + i * 1024), 16, 0, KNT2);
            __builtin_amdgcn_global_load_lds((const unsigned*)(p.wv + (size_t)(128 * w + 16 * i + rrow) * 4096 + h * 128 + 32 * j + rc), (LAS unsigned*)(my + 8192 + i * 1024), 16, 0, KNT2);
        }
        WAIT_V(0);
#pragma unroll
        for (int T = 0; T < 4; ++T)
#pragma unroll
            for (int t = 0; t < 2; ++t) W[2 + T][t] = FRAG(my + (T >> 1) * 8192 + t * 4096 + (T & 1) * 32 + toffq, 128);
#pragma unroll
        for (int T = 0; T < 4; ++T) asm volatile("" : "+v"(W[2 + T][0]), "+v"(W[2 + T][1]));
        WAIT_L(0);
#undef FRAG
    }
    PST(2);
    BAR();
    PST(4);
    if (w == 7 && lane < 8) {
        const unsigned* src = (const unsigned*)(p.mem0 + (4 * (red & 1) + 2 * (lane >> 2) + (lane & 1)) * DM + 32 * (red >> 2) + 8 * ((red >> 1) & 1) + 4 * ((lane >> 1) & 1));
        const u32x4 v = {min(src[0], SENT - 1), min(src[1], SENT - 1), min(src[8], SENT - 1), min(src[9], SENT - 1)};
        ST16(v, xr, (unsigned)lane * 16u + (unsigned)(red & 63) * 128u, win_off(W_MEM(0, 0) + (red >> 6)), 16);
    }
    if (w < 5) chain_loop<0>(p, par, rank, w, lds, W, WO, stamps);
    else if (w == 5) chain_loop<1>(p, par, rank, 5, lds, W, WO, stamps);
    else if (w == 6) chain_loop<2>(p, par, rank, 6, lds, W, WO, stamps);
    else chain_loop<3>(p, par, rank, 7, lds, W, WO, stamps);
}

template <bool POLL>
DI void attn_a_item(const Params& p, unsigned par, int s, int h, ldsp my, int lane) {
    const int g = lane >> 4, n = lane & 15;
    const rsrc_t xr = mkrsrc(p.ws + WS_XCH, (unsigned)XCH_BYTES);
    const rsrc_t qr = mkrsrc(p.ws + WH(WS_QKVX + (size_t)h * XH + (size_t)s * 16384), 65u * XH);
#pragma unroll
    for (int i = 0; i < 8; ++i)
        __builtin_amdgcn_raw_ptr_buffer_load_lds(qr, (LAS void*)(my + i * 1024), 16, (unsigned)(g * 256 + (n ^ ((g & 1) * 8)) * 16), 64u * XH + (unsigned)i * 1024u, 0, 16);
    u32x4 Q[2][4], K[2][4], XM[4];
    const unsigned ro = (unsigned)lane * 16u;
#pragma unroll
    for (int t = 0; t < 2; ++t)
#pragma unroll
        for (int k = 0; k < 4; ++k) { Q[t][k] = ld16_sc1(qr, ro + k * 1024, (unsigned)t * 4096u); K[t][k] = ld16_sc1(qr, ro + k * 1024, (unsigned)t * 4096u + 32u * XH); }
    const unsigned qwin = km_win(s, h);
    u32x4 VM[2];
    if (POLL) {
        wait_count<4>((unsigned*)(p.ws + WS_CTL), C_PROG, (unsigned)s + (s < 56 ? 2u : 1u));
        if (s >= 56) __builtin_amdgcn_s_sleep(16);
    }
    for (unsigned spins = 0;;) {
        asm volatile("" ::: "memory");
#pragma unroll
        for (int k = 0; k < 4; ++k) XM[k] = ld16_sc1(xr, (unsigned)lane * 16u + (k & 1) * 1536, qwin + (k >> 1) * 3072);
#pragma unroll
        for (int ts = 0; ts < 2; ++ts) VM[ts] = ld16_sc1(xr, (unsigned)(g >> 1) * 1536u + 1024u + (unsigned)(lane & 31) * 16u, qwin + ts * 3072);
        if (!POLL || s < 56) break;
        if (all_set(max(max(max(umax4(XM[0]), umax4(XM[1])), max(umax4(XM[2]), umax4(XM[3]))), max(umax4(VM[0]), umax4(VM[1]))))) break;
        __builtin_amdgcn_s_sleep(4);
        if (spin_out(spins, (unsigned*)(p.ws + WS_CTL))) break;
    }
    SCHED();
#pragma unroll
    for (int ts = 0; ts < 2; ++ts) {
        const u32x4 v = VM[ts];
        const int m = 2 * (n >> 2) + (n & 1), hf = (n >> 1) & 1;
#pragma unroll
        for (int i = 0; i < 4; ++i) { const int e = 64 * ts + 16 * g + 4 * i + 2 * hf;
            *(LAS unsigned*)(my + 8192 + (e >> 3) * 256 + (m ^ (e & 8)) * 16 + (e & 7) * 2) = v[i]; }
    }
    const float csc = 0.08838834764831845f * 1.4426950408889634f, ninf = -__builtin_inff();
    u32x4 PX[2]; u32x2 PM[2];
#pragma unroll
    for (int qt = 0; qt < 2; ++qt) {
        f32x4 sm = {0.f, 0.f, 0.f, 0.f}, s0 = sm, s1 = sm;
#pragma unroll
        for (int k = 0; k < 4; ++k) { sm = mma32(asbf(XM[k]), asbf(Q[qt][k]), sm); s0 = mma32(asbf(K[0][k]), asbf(Q[qt][k]), s0); if (qt == 1) s1 = mma32(asbf(K[1][k]), asbf(Q[qt][k]), s1); }
#pragma unroll
        for (int i = 0; i < 4; ++i) {
            if (g < 2) sm[i] = ninf;
            if (qt == 0) { if (4 * g + i > n) s0[i] = ninf; s1[i] = ninf; }
            else if (4 * g + i > n) s1[i] = ninf;
        }
        float m = fmaxf(fmaxf(fmaxf(s0[0], s0[1]), fmaxf(s0[2], s0[3])), fmaxf(fmaxf(s1[0], s1[1]), fmaxf(s1[2], s1[3])));
        m = fmaxf(m, fmaxf(fmaxf(sm[0], sm[1]), fmaxf(sm[2], sm[3])));
        m = rows_max(m);
        const float mc = -m * csc; float sum = 0.f;
#pragma unroll
        for (int i = 0; i < 4; ++i) { s0[i] = __builtin_amdgcn_exp2f(fmaf(s0[i], csc, mc)); s1[i] = __builtin_amdgcn_exp2f(fmaf(s1[i], csc, mc)); sm[i] = __builtin_amdgcn_exp2f(fmaf(sm[i], csc, mc));
            sum += (s0[i] + s1[i]) + sm[i]; }
        sum = rows_sum(sum);
        const float inv = __builtin_amdgcn_rcpf(sum);
        PX[qt] = (u32x4){pk_bf16(s0[0] * inv, s0[1] * inv), pk_bf16(s0[2] * inv, s0[3] * inv), pk_bf16(s1[0] * inv, s1[1] * inv), pk_bf16(s1[2] * inv, s1[3] * inv)};
        PM[qt] = (u32x2){pk_bf16(sm[0] * inv, sm[1] * inv), pk_bf16(sm[2] * inv, sm[3] * inv)};
    }
    asm volatile("s_waitcnt vmcnt(0) lgkmcnt(0)" ::: "memory");
    const int q4 = n >> 2, p4 = n & 3;
    const ldsp vx = my + (p4 >> 1) * 256 + ((4 * g + q4) ^ ((p4 >> 1) * 8)) * 16 + (p4 & 1) * 8, vmm = my + 8192 + (p4 >> 1) * 256 + ((4 * (g & 1) + q4) ^ ((p4 >> 1) * 8)) * 16 + (p4 & 1) * 8;
    const rsrc_t orr = mkrsrc(p.ws + WH(WS_OA + (size_t)s * 524288 + gap((unsigned)h * 4096u)), 524288u);
    const unsigned oo = frag_off(lane);
    bf16x8 VA[8]; s16x4 VMf[8];
#pragma unroll
    for (int et = 0; et < 8; ++et) { VA[et] = cat8(trd(vx + et * 512), trd(vx + 4096 + et * 512)); VMf[et] = trd(vmm + et * 512); }
    SCHED();
#pragma unroll
    for (int ep = 0; ep < 4; ++ep) {
        f32x4 d[2][2];
#pragma unroll
        for (int e2 = 0; e2 < 2; ++e2)
#pragma unroll
            for (int qt = 0; qt < 2; ++qt) d[qt][e2] = mma16(__builtin_bit_cast(u32x2, VMf[2 * ep + e2]), PM[qt], mma32(VA[2 * ep + e2], asbf(PX[qt]), (f32x4){0.f, 0.f, 0.f, 0.f}));
#pragma unroll
        for (int qt = 0; qt < 2; ++qt) store_frag(orr, oo + ep * 1024, (unsigned)qt * 262144u, d[qt][0], d[qt][1]);
    }
    asm volatile("s_waitcnt lgkmcnt(0)" ::: "memory");
}

DI void attn_b_item(const Params& p, unsigned par, int hq, int qb, ldsp lds) {
    const int tid = fresh_tid(), w = __builtin_amdgcn_readfirstlane(tid >> 6), lane = tid & 63, g = lane >> 4, n = lane & 15;
    const int q0 = qb * 128;
    const rsrc_t br = mkrsrc(p.ws + WH(WS_QKVB + (size_t)hq * 4 * BH), 68u * BH);
    const rsrc_t orr = mkrsrc(p.ws + WH(WS_OB + (size_t)qb * 524288), 524288u);
    constexpr int BUF = 57344;
    unsigned to[3];
#pragma unroll
    for (int i = 0; i < 3; ++i) to[i] = gap((unsigned)max(8 * qb - 16 + w * 3 + i, 0) * 1024u);
    const unsigned l16 = (unsigned)lane * 16u, vsw = (unsigned)(g * 256 + (n ^ ((g & 1) * 8)) * 16);
    auto stage = [&](int i) {
        const unsigned so = (unsigned)i * BH; ldsp d = lds + (i & 1) * BUF;
#pragma unroll
        for (int k = 0; k < 3; ++k) {
            __builtin_amdgcn_raw_ptr_buffer_load_lds(br, (LAS void*)(d + (w * 3 + k) * 1024), 16, l16, so + 32u * BH + to[k], 0, 16);
            __builtin_amdgcn_raw_ptr_buffer_load_lds(br, (LAS void*)(d + 24576 + (w * 3 + k) * 1024), 16, vsw, so + 64u * BH + to[k], 0, 16);
        }
        __builtin_amdgcn_raw_ptr_buffer_load_lds(br, (LAS void*)(d + 49152 + w * 1024), 16, l16, so + (unsigned)(qb * 16384 + w * 1024), 0, 16);
    };
    const float ninf = -__builtin_inff(), csc = 0.17677669529663687f * 1.4426950408889634f;
    const int tmin = (256 - q0 - 16 * w) >> 4;
    const int q4 = n >> 2, p4 = n & 3;
    const int kq = w * 1024 + lane * 16;
    const unsigned vb = (unsigned)(size_t)lds + 24576u + (unsigned)(w * 1024 + (p4 >> 1) * 256 + ((4 * g + q4) ^ ((p4 >> 1) * 8)) * 16 + (p4 & 1) * 8);
    stage(0);
#pragma unroll 1
    for (int i = 0; i < 4; ++i) {
        if (i < 3) { stage(i + 1); WAIT_V(7); } else WAIT_V(0);
        BAR(); asm volatile("" ::: "memory");
        const ldsp bp = lds + (i & 1) * BUF;
        const bf16x8 Q = *(const LAS bf16x8*)(bp + 49152 + kq);
        f32x4 sc[17]; bf16x8 KF[17];
#pragma unroll
        for (int t = 0; t < 17; ++t) KF[t] = *(const LAS bf16x8*)(bp + kq + t * 1024);
        SCHED();
#pragma unroll
        for (int t = 0; t < 17; ++t) sc[t] = mma32(KF[t], Q, (f32x4){0.f, 0.f, 0.f, 0.f});
#pragma unroll
        for (int k = 0; k < 4; ++k) { if (4 * g + k <= n) sc[0][k] = ninf; if (4 * g + k > n) sc[16][k] = ninf; }
        if (tmin > 0) {
#pragma unroll
            for (int t = 0; t < 16; ++t) if (t < tmin) sc[t] = (f32x4){ninf, ninf, ninf, ninf};
        }
        float mq[4];
#pragma unroll
        for (int k = 0; k < 4; ++k) {
            mq[k] = max3f(sc[16][k], sc[k][0], sc[k][1]); mq[k] = max3f(mq[k], sc[k][2], sc[k][3]);
#pragma unroll
            for (int t = 4 + k; t < 16; t += 4) mq[k] = max3f(max3f(mq[k], sc[t][0], sc[t][1]), sc[t][2], sc[t][3]);
        }
        const float m = rows_max(max3f(max3f(mq[0], mq[1], mq[2]), mq[3], mq[3]));
        const float mc = -m * csc;
        f32x4 sum4 = {0.f, 0.f, 0.f, 0.f};
#pragma unroll
        for (int t = 0; t < 17; ++t) {
            sc[t] = sc[t] * csc + mc;
#pragma unroll
            for (int k = 0; k < 4; ++k) sc[t][k] = __builtin_amdgcn_exp2f(sc[t][k]);
            sum4 += sc[t];
        }
        const float inv = __builtin_amdgcn_rcpf(rows_sum((sum4[0] + sum4[1]) + (sum4[2] + sum4[3])));
        const unsigned v0 = vb + (unsigned)((i & 1) * BUF);
        s16x4 V0[17], V1[17];
#define VRD(t) TRD(V0[t], v0, (t) * 1024); TRD(V1[t], v0, (t) * 1024 + 512)
        VRD(0); VRD(1); VRD(2); VRD(3); VRD(4); VRD(5); VRD(6); VRD(7); VRD(8); VRD(9); VRD(10); VRD(11); VRD(12); VRD(13); VRD(14); VRD(15); VRD(16);
#undef VRD
        u32x2 P[17];
#pragma unroll
        for (int t = 0; t < 17; ++t) P[t] = (u32x2){pk_bf16(sc[t][0], sc[t][1]), pk_bf16(sc[t][2], sc[t][3])};
        WAIT_L(0); SCHED();
        f32x4 d0 = {0.f, 0.f, 0.f, 0.f}, d1 = d0;
#pragma unroll
        for (int tp = 0; tp < 8; ++tp) {
            const bf16x8 PP = asbf((u32x4){P[2 * tp].x, P[2 * tp].y, P[2 * tp + 1].x, P[2 * tp + 1].y});
            d0 = mma32(cat8(V0[2 * tp], V0[2 * tp + 1]), PP, d0); d1 = mma32(cat8(V1[2 * tp], V1[2 * tp + 1]), PP, d1);
        }
        d0 = mma16(__builtin_bit_cast(u32x2, V0[16]), P[16], d0); d1 = mma16(__builtin_bit_cast(u32x2, V1[16]), P[16], d1);
        BAR();
        store_frag(orr, frag_off(lane), (unsigned)w * 65536u + gap((unsigned)(hq * 4 + i) * 1024u), d0 * inv, d1 * inv);
    }
    WAIT_V(0);
    signal((unsigned*)(p.ws + WS_CTL), C_OB + qb * 32, 1);
}

DI void out_tail(const Params& p, unsigned par, int c, int q, ldsp lds) {
    const int tid = fresh_tid(), w = __builtin_amdgcn_readfirstlane(tid >> 6), lane = tid & 63, g = lane >> 4, n = lane & 15;
    unsigned* ctl = (unsigned*)(p.ws + WS_CTL);
    bf16x8 WT[16][2];
    {
        ldsp my = lds + w * 16384;
        const int toff = (8 * g + (n >> 2)) * 64 + (n & 3) * 8;
#pragma unroll
        for (int r = 0; r < 2; ++r) {
#pragma unroll
            for (int i = 0; i < 16; ++i)
                __builtin_amdgcn_global_load_lds((const unsigned*)(p.wo + (size_t)(512 * w + 256 * r + 16 * i + (lane >> 2)) * 1024 + 32 * c + (lane & 3) * 8), (LAS unsigned*)(my + i * 1024), 16, 0, 0);
            WAIT_V(0);
#pragma unroll
            for (int k = 0; k < 8; ++k)
#pragma unroll
                for (int ct = 0; ct < 2; ++ct) { WT[8 * r + k][ct] = cat8(trd(my + k * 2048 + ct * 32 + toff), trd(my + k * 2048 + ct * 32 + toff + 256)); }
#pragma unroll
            for (int k = 0; k < 8; ++k) asm volatile("" : "+v"(WT[8 * r + k][0]), "+v"(WT[8 * r + k][1]));
            WAIT_L(0);
        }
    }
    BAR();
#pragma unroll 1
    for (int hs = q; hs < 2 * (NSEG - S_TAIL); hs += 3) {
        const int s = S_TAIL + (hs >> 1), rt = hs & 1;
        if (w == 0) wait_count<3>(ctl, C_OS + (s - S_TAIL) * 128 + (c >> 3) * 32, 4u);
        asm volatile("" ::: "memory"); BAR(); asm volatile("" ::: "memory");
        const rsrc_t ar = mkrsrc(p.ws + WH(WS_OA + (size_t)(2 * s + rt) * 262144 + (size_t)w * 32768), 32768u);
        const unsigned ao = (unsigned)lane * 16u;
        u32x4 A[16];
#pragma unroll
        for (int k = 0; k < 16; ++k) A[k] = ld16_sc1(ar, ao + (k & 3) * 1024, gap((unsigned)(k >> 2) * 4096u));
        SCHED();
#pragma unroll
        for (int ct = 0; ct < 2; ++ct) {
            f32x4 acc = {0.f, 0.f, 0.f, 0.f};
#pragma unroll
            for (int k = 0; k < 16; ++k) acc = mma32(WT[k][ct], asbf(A[k]), acc);
            *(LAS f32x4*)(lds + w * 2048 + ct * 1024 + lane * 16) = acc;
        }
        LDS_BAR();
        if (w < 2) {
            f32x4 r[8];
#pragma unroll
            for (int ww = 0; ww < 8; ++ww) r[ww] = *(const LAS f32x4*)(lds + ww * 2048 + w * 1024 + lane * 16);
            SCHED();
            const f32x4 t = ((r[0] + r[1]) + (r[2] + r[3])) + ((r[4] + r[5]) + (r[6] + r[7]));
            *(u32x2*)(p.out + (size_t)(32 * s + 16 * rt + n) * 2048 + 32 * c + 16 * w + 4 * g) = (u32x2){pk_bf16(t[0], t[1]), pk_bf16(t[2], t[3])};
        }
        WAIT_L(0);
    }
    BAR();
}

DI void big_unit(const Params& p, unsigned par, int id, Unit& u) {
    u.lda = DM * 2; u.nt = 16; u.ak = 128; u.ah = 128 * DM * 2;
    if (id < 384) {
        const int pm = id & 7, pn = id >> 3;
        const u16 *w0 = p.wq, *w1 = p.wk, *w2 = p.wv; const u16* W = pn < 16 ? w0 : (pn < 32 ? w1 : w2);
        u.A = (const char*)(p.ax + (size_t)pm * 256 * DM); u.B = (const char*)(W + (pn & 15) * 256); u.ldb = 4096 * 2;
        u.C = p.ws + WH(WS_QKVX + (size_t)(2 * pn) * XH + (size_t)pm * 131072); u.ldc = 256; u.cw = 1024; u.ch = XH;
        if (pn < 16) { u.sig = C_Q + pm * 32; u.nsig = 1; } else { u.sig = C_KVC(2 * pm, 2 * (pn & 15)); u.nsig = 4; }
    } else if (id < 480) {
        const int pm = (id - 384) & 7, pn = (id - 384) >> 3;
        u.A = (const char*)(p.bx + (size_t)pm * 256 * DM); u.B = (const char*)(p.bwqkv + pn * 256); u.ldb = LDB3 * 2;
        u.C = p.ws + WH(WS_QKVB + (size_t)(8 * pn) * BH + (size_t)pm * 32768); u.ldc = 64; u.cw = BH; u.ch = 4 * BH; u.sig = C_B + pm * 32; u.nsig = 1;
    } else {
        const int pm = (id - 480) >> 2, pn = id & 3;
        u.lda = 0; u.ak = 2048; u.ah = 524288u;
        u.A = p.ws + WH(WS_OB + (size_t)pm * 1048576); u.B = (const char*)(p.bwo + pn * 256); u.ldb = DM * 2;
        u.C = (char*)(p.out + (size_t)pm * 256 * 2048 + 1024 + pn * 256); u.ldc = 4096; u.cw = 64; u.ch = 256; u.sig = 0; u.nsig = 0;
    }
}
struct BigSched { const Params& p; unsigned par; int id0, id1, id2; DI bool next(int i, Unit& u) const { const int id = i == 0 ? id0 : (i == 1 ? id1 : (i == 2 ? id2 : -1)); if (id < 0) return false; big_unit(p, par, id, u); return true; } };

__global__ __launch_bounds__(512, 2) void k_fused(Params p) {
    extern __shared__ __attribute__((aligned(16))) unsigned char shm[];
    ldsp lds = (ldsp)shm;
    unsigned* ctl = (unsigned*)(p.ws + WS_CTL);
    const int b = (blockIdx.x & ~7) | ((blockIdx.x + __builtin_amdgcn_readfirstlane(ctl[C_PHASE])) & 7), id = (b & 3) * 32 + (b >> 3);
#define WSTAMP(code)
    const unsigned par = (unsigned)__builtin_amdgcn_readfirstlane(ctl[C_PAR + 1]) < (unsigned)__builtin_amdgcn_readfirstlane(ctl[C_PAR]) ? 1u : 0u;
    WSTAMP(1);
    if ((b & 7) < 4) {
        chain_role(p, par, id, lds, (unsigned long long*)(p.ws + WS_END));
        BAR();
        WSTAMP(2);
    } else {
#pragma unroll 1
        for (int r = 0; r < 2; ++r) {
            const int rb = 2 * r + (id & 1), ct = id >> 1;
            const u16 *w1 = p.wk, *w2 = p.wv;
            Unit u; u.A = (const char*)(p.ax + (size_t)rb * 128 * DM); u.lda = DM * 2; u.B = (const char*)((ct < 32 ? w1 : w2) + (ct & 31) * 128); u.ldb = 4096 * 2; u.nt = 16;
            u.C = p.ws + WH(WS_QKVX + (size_t)(32 + ct) * XH + (size_t)rb * 65536); u.ldc = 256; u.cw = 1024; u.ch = 0; u.ak = 128; u.ah = 0;
            u.sig = C_KVC(rb, ct & 31); u.nsig = 1;
            gemm_small(lds, u, ctl);
            WSTAMP(3);
        }
        if ((id & 31) >= 24) {
            const int t = fresh_tid(), wv = __builtin_amdgcn_readfirstlane(t >> 6), q = (id >> 5) * 8 + (id & 7);
#pragma unroll 1
            for (int i = 0; i < 64; ++i) {
                const char* src = i < 16 ? (const char*)p.ax + (size_t)q * 131072 + i * 8192 : i < 32 ? (const char*)p.bx + (size_t)q * 131072 + (i - 16) * 8192
                                : i < 56 ? (const char*)p.bwqkv + (size_t)q * 196608 + (i - 32) * 8192 : (const char*)p.bwo + (size_t)q * 65536 + (i - 56) * 8192;
                __builtin_amdgcn_global_load_lds((const unsigned*)(src + t * 16), (LAS unsigned*)(lds + L_SCR + wv * 1024), 16, 0, 0);
            }
        }
    }
    volatile LAS unsigned* qw = (volatile LAS unsigned*)(lds + L_QW);
    const int w = __builtin_amdgcn_readfirstlane(threadIdx.x >> 6);
    const int k = b & 3;
    bool fixed = (b & 7) >= 4;
#pragma unroll 1
    for (;;) {
        int kind, x, it = -1, x2 = -1, x3 = -1;
        if (fixed) { const int i = id & 31; kind = 6; x = i < 24 ? (16 + 8 * k + (i & 7)) * 8 + 2 + (i >> 3) : (4 * k + (i & 3)) * 8 + ((i >> 2) & 1); x2 = x + (i < 24 ? 3 : 2); x3 = i < 24 ? 384 + 24 * k + i : x + 4; fixed = false; }
        else {
        if (threadIdx.x == 0) qw[0] = __hip_atomic_fetch_add(ctl + C_HEAD + k * 32, 1u, RLX_AGENT) + 1u;
        LDS_BAR();
        it = (int)__builtin_amdgcn_readfirstlane(qw[0]);
        LDS_BAR();
        if (it < 6) { kind = 0; x = it * 4 + k; }
        else if (it < 38) { kind = 1; x = ((it - 6) >> 1) * 8 + 2 * k + ((it - 6) & 1); }
        else if (it < 40) { kind = 0; x = (it - 38 + 6) * 4 + k; }
        else if (it < 44) { kind = 4; x = (it - 40) * 4 + k; }
        else if (it < 52) { kind = 6; x = (4 * k + ((it - 44) & 3)) * 8 + 6 + ((it - 44) >> 2); }
        else if (it < 58) { kind = 2; x = it - 52; }
        else if (it < 66) { kind = 6; x = 480 + (it - 58) * 4 + k; }
        else if (it < 70) { kind = 2; x = 6 + it - 66; }
        else if (it < 94) { kind = 5; x = ((it - 70) >> 3) * 32 + 8 * k + ((it - 70) & 7); }
        else break;
        }
        if (kind == 6) {
            if (x >= 480) { const int w0 = C_OB + ((x - 480) >> 2) * 64; wg_wait(ctl, w0, 64u, w0 + 32, 64u); }
            BigSched S{p, par, x, x2, x3};
            gemm_stream(lds, S, ctl);
        } else
        if (kind == 0) {
            const int s0 = (x >> 2) * 4;
            wait_count(ctl, C_Q + (s0 >> 3) * 32, 128u); wait_count(ctl, C_PROG, (unsigned)s0 + 5u);
            asm volatile("" ::: "memory");
#pragma unroll 1
            for (int s = s0; s < s0 + 4; ++s) attn_a_item<false>(p, par, s, (x & 3) * 8 + w, lds + w * 12288, fresh_tid() & 63);
            WAIT_V(0); signal(ctl, C_OA + (x >> 2) * 32, 1);
            BAR();
        } else if (kind == 4) {
#pragma unroll 1
            for (int s = 32 + (x >> 2); s < NSEG; s += 4) {
                wait_count(ctl, C_Q + (s >> 3) * 32, 128u); wait_count(ctl, C_KVC(s >> 2, (x & 3) * 8 + w), 16u);
                asm volatile("" ::: "memory");
                attn_a_item<true>(p, par, s, (x & 3) * 8 + w, lds + w * 12288, fresh_tid() & 63);
                WAIT_V(0);
                if (s < S_TAIL) signal(ctl, C_OA + (s >> 2) * 32, 1);
                else { BAR(); const int l = fresh_tid(); if (l < 4) __hip_atomic_fetch_add(ctl + C_OS + (s - S_TAIL) * 128 + l * 32, 1u, RLX_AGENT); }
            }
            BAR();
        } else if (kind == 1) {
            const int qb = x >> 3;
            wait_count(ctl, C_B + (qb >> 1) * 32, 96u); wait_count(ctl, C_B + max((qb >> 1) - 1, 0) * 32, 96u);
            asm volatile("" ::: "memory");
            attn_b_item(p, par, x & 7, qb, lds);
        } else if (kind == 5) {
            out_tail(p, par, x & 31, x >> 5, lds);
        } else {
            wg_wait(ctl, C_OA + x * 32, x < 8 ? 32u : 128u, C_OA + x * 32, 32u);
            gemm_wide(lds, p.ws + WH(WS_OA + (size_t)x * 2097152), (const char*)(p.wo + k * 256), (char*)(p.out + (size_t)x * 128 * 2048 + k * 256), 64);
        }
        WSTAMP(16 + (kind == 6 && x >= 480 ? (x < 512 ? 7 : 8) : kind) + ((it & 0xffff) << 8));
    }
}

__global__ __launch_bounds__(512) void k_init(char* ws) {
    const rsrc_t xr = mkrsrc(ws + WS_XCH, NWIN_INIT * 16384u);
    unsigned* ctl = (unsigned*)(ws + WS_CTL);
    const u32x4 sv = {SENT, SENT, SENT, SENT};
    const unsigned xcc = (unsigned)__builtin_amdgcn_s_getreg(20 | (3 << 11)) & 7u, half = blockIdx.x >> 3, win = blockIdx.x & 7;
    if (threadIdx.x < 64 && xcc == 1) {
        unsigned b = ~0u;
        for (int i = 0; i < 5; ++i) {
            const unsigned long long t0 = __builtin_amdgcn_s_memtime();
            __builtin_amdgcn_raw_buffer_store_b32(0u, xr, (unsigned)(2 * 16384 + C_PROBE * 4) + half * 8192u + (unsigned)i * 256u, 0, 16);
            asm volatile("s_waitcnt vmcnt(0)" ::: "memory");
            b = min(b, (unsigned)(__builtin_amdgcn_s_memtime() - t0));
        }
        if (threadIdx.x == 0) ctl[C_PAR + half] = b;
    }
    __syncthreads();
    if (win < 7 && win != 5) ST16(sv, xr, win * 16384u + half * 8192u + threadIdx.x * 16u, 0, 16);
    if (blockIdx.x == 0 && threadIdx.x == 0) ctl[C_PHASE] = xcc;
}

extern "C" void kernel_launch(void* const* d_in, const int* in_sizes, int n_in, void* d_out, int out_size, void* d_ws, size_t ws_size, hipStream_t stream) {
    Params p;
    p.ax = (const u16*)d_in[0]; p.mem0 = (const u16*)d_in[1]; p.wq = (const u16*)d_in[2]; p.wk = (const u16*)d_in[3]; p.wv = (const u16*)d_in[4]; p.wo = (const u16*)d_in[5];
    p.bx = (const u16*)d_in[7]; p.bwqkv = (const u16*)d_in[8]; p.bwo = (const u16*)d_in[9];
    p.out = (u16*)d_out; p.ws = (char*)(((uintptr_t)d_ws + WS_SHIFT(ws_size) + 0x1FFFFFu) & ~(uintptr_t)0x1FFFFFu);
    (void)hipFuncSetAttribute((const void*)k_fused, hipFuncAttributeMaxDynamicSharedMemorySize, LDS_BYTES);
    k_init<<<16, 512, 0, stream>>>(p.ws);
    k_fused<<<256, 512, LDS_BYTES, stream>>>(p);
}
```

```cpp
#include <hip/hip_runtime.h>
#include <stdint.h>
#include <stdlib.h>

#define LAS __attribute__((address_space(3)))
#define DI __device__ __forceinline__
typedef unsigned short u16;
typedef __attribute__((ext_vector_type(8))) __bf16 bf16x8;
typedef __attribute__((ext_vector_type(16))) __bf16 bf16x16;
typedef __attribute__((ext_vector_type(2))) __bf16 bf16x2;
typedef __attribute__((ext_vector_type(4))) short s16x4;
typedef __attribute__((ext_vector_type(8))) short s16x8;
typedef __attribute__((ext_vector_type(16))) short s16x16;
typedef __attribute__((ext_vector_type(2))) float f32x2;
typedef __attribute__((ext_vector_type(4))) float f32x4;
typedef __attribute__((ext_vector_type(2))) unsigned u32x2;
typedef __attribute__((ext_vector_type(4))) unsigned u32x4;
typedef LAS unsigned char* ldsp;
typedef __amdgpu_buffer_rsrc_t rsrc_t;

constexpr int S_LEN = 2048, DM = 1024, NH = 32, DH = 128, NSEG = 64;
constexpr int LDB3 = 3072, S_TAIL = 52;
constexpr unsigned XH = S_LEN * 512, BH = S_LEN * 128;
constexpr unsigned SENT = 0xFFFFFFFFu;
constexpr size_t WS_XCH = 2u << 20;
constexpr int NWIN = 640, NWIN_INIT = 7;
constexpr size_t XCH_BYTES = (size_t)(NWIN + 2048 + 512) * 16384;
constexpr size_t WS_CTL = WS_XCH + 2 * 16384;
constexpr int C_TMO = 0, C_PAR = 13312  , C_PHASE = 13314, C_HEAD = 3072, C_PROG = 192, C_PACE = 128, C_Q = 256, C_B = C_Q + 8 * 32, C_OA = C_B + 8 * 32, C_OB = C_OA + 16 * 32, C_OS = 16384, C_KV = 4096, C_PROBE = 12288;
constexpr size_t WS_QKVX = 52u << 20;
constexpr size_t WS_QKVB = WS_QKVX + (size_t)96 * XH;
constexpr size_t WS_OA = WS_QKVB + (size_t)96 * BH;
constexpr size_t WS_OB = WS_OA + (size_t)S_LEN * 4096 * 4;
constexpr size_t WS_END = WS_OB + (size_t)S_LEN * 1024 * 4;
#define C_KVC(rb, h) (C_KV + (h) * 32 + (rb))
#define gap(L) (((L) & 8191u) | (((L) >> 13) << 14))
#define WH(off) ((off) + (size_t)((((unsigned)((off) >> 21) ^ par ^ KWH) & 1u) << 13))
#define KWH 0
#define WS_SHIFT(sz) 0
constexpr int L_QW = 147456, LDS_BYTES = L_QW + 1024 + 8192;

DI unsigned pk_bf16(float a, float b) { f32x2 v = {a, b}; return __builtin_bit_cast(unsigned, __builtin_convertvector(v, bf16x2)); }
DI s16x4 trd(ldsp p) { return __builtin_amdgcn_ds_read_tr16_b64_v4i16((LAS s16x4*)p); }
DI bf16x8 cat8(s16x4 a, s16x4 b) { return __builtin_bit_cast(bf16x8, __builtin_shufflevector(a, b, 0, 1, 2, 3, 4, 5, 6, 7)); }
DI bf16x16 cat16(bf16x8 a, bf16x8 b) { return __builtin_shufflevector(a, b, 0, 1, 2, 3, 4, 5, 6, 7, 8, 9, 10, 11, 12, 13, 14, 15); }
DI bf16x8 lo8(bf16x16 v) { return __builtin_shufflevector(v, v, 0, 1, 2, 3, 4, 5, 6, 7); }
DI bf16x8 hi8(bf16x16 v) { return __builtin_shufflevector(v, v, 8, 9, 10, 11, 12, 13, 14, 15); }
DI bf16x8 asbf(u32x4 v) { return __builtin_bit_cast(bf16x8, v); }
DI f32x4 mma32(bf16x8 a, bf16x8 b, f32x4 c) { return __builtin_amdgcn_mfma_f32_16x16x32_bf16(a, b, c, 0, 0, 0); }
DI f32x4 mma16(u32x2 a, u32x2 b, f32x4 c) { return mma32(asbf((u32x4){a.x, a.y, 0u, 0u}), asbf((u32x4){b.x, b.y, 0u, 0u}), c); }
DI f32x4 smma(u32x4 a, bf16x16 b, f32x4 c, int idx) { return __builtin_amdgcn_smfmac_f32_16x16x64_bf16(asbf(a), b, c, idx, 0, 0); }
DI rsrc_t mkrsrc(const void* p, unsigned bytes) { return __builtin_amdgcn_make_buffer_rsrc((void*)p, 0, bytes, 0x00020000); }
DI u32x4 ld16_sc1(rsrc_t r, unsigned voff, unsigned soff) { return __builtin_amdgcn_raw_buffer_load_b128(r, voff, soff, 16); }
#define ST16(v, r, voff, soff, aux) do { const u32x4 st_v = (v); __builtin_amdgcn_raw_buffer_store_b128(st_v, r, voff, soff, aux); asm volatile("s_nop 1" :: "v"(st_v)); } while (0)
DI void swap16(unsigned& a, unsigned& b) { auto r = __builtin_amdgcn_permlane16_swap(a, b, false, false); a = r[0]; b = r[1]; }
DI void swap32(unsigned& a, unsigned& b) { auto r = __builtin_amdgcn_permlane32_swap(a, b, false, false); a = r[0]; b = r[1]; }
DI void swap16f(float& a, float& b) { unsigned x = __float_as_uint(a), y = __float_as_uint(b); swap16(x, y); a = __uint_as_float(x); b = __uint_as_float(y); }
DI void swap32f(float& a, float& b) { unsigned x = __float_as_uint(a), y = __float_as_uint(b); swap32(x, y); a = __uint_as_float(x); b = __uint_as_float(y); }
DI float rows_max(float v) { float a = v, b = v; swap16f(a, b); v = fmaxf(a, b); a = v; b = v; swap32f(a, b); return fmaxf(a, b); }
DI float rows_sum(float v) { float a = v, b = v; swap16f(a, b); v = a + b; a = v; b = v; swap32f(a, b); return a + b; }
DI unsigned umax4(u32x4 v) { return max(max(v.x, v.y), max(v.z, v.w)); }
#define WAIT_V(n) asm volatile("s_waitcnt vmcnt(" #n ")" ::: "memory")
#define WAIT_L(n) asm volatile("s_waitcnt lgkmcnt(" #n ")" ::: "memory")
#define BAR() __builtin_amdgcn_s_barrier()
#define SCHED() __builtin_amdgcn_sched_barrier(0)
#define LDS_BAR() do { asm volatile("s_waitcnt lgkmcnt(0)" ::: "memory"); __builtin_amdgcn_s_barrier(); asm volatile("" ::: "memory"); } while (0)

constexpr int HTB = 128 * 64 * 2;
DI int lds_byte(int r, int c) { const int st = (r >> 4) * 2 + (c >> 5), rr = r & 15, cc = c & 31, ob = rr * 64 + cc * 2; return st * 1024 + (ob ^ (((ob >> 9) & 1) << 5)); }
DI void stage_rc(int b, int& R, int& C) { const int st = b / 1024, sb = b % 1024, swz = sb ^ (((sb >> 9) & 1) << 5); R = (st >> 1) * 16 + swz / 64; C = (st & 1) * 32 + (swz % 64) / 2; }

struct Params {
    const u16 *ax, *mem0, *wq, *wk, *wv, *wo, *bx, *bwqkv, *bwo;
    u16* out; char* ws;
};
#define RLX_AGENT __ATOMIC_RELAXED, __HIP_MEMORY_SCOPE_AGENT
DI bool spin_out(unsigned& spins, unsigned* ctl) {
    spins = __builtin_amdgcn_readfirstlane(spins) + 1u;
    if ((spins & 63u) == 0u) {
        if (spins > (1u << 18)) { __hip_atomic_store(ctl + C_TMO, 1u, RLX_AGENT); return true; }
        if (__builtin_amdgcn_readfirstlane(__hip_atomic_load(ctl + C_TMO, RLX_AGENT)) != SENT) return true;
    }
    return false;
}
template <int NAP = 8>
DI void wait_count(unsigned* ctl, int word, unsigned need) {
    unsigned spins = 0;
    while (__builtin_amdgcn_readfirstlane(__hip_atomic_load(ctl + word, RLX_AGENT)) + 1u < need) { __builtin_amdgcn_s_sleep(NAP); if (spin_out(spins, ctl)) break; }
}
DI void wg_wait(unsigned* ctl, int word0, unsigned need0, int word1, unsigned need1) {
    if (threadIdx.x < 64) {
        wait_count(ctl, word0, need0); wait_count(ctl, word1, need1);
        __builtin_amdgcn_fence(__ATOMIC_ACQUIRE, "agent");
        asm volatile("s_waitcnt vmcnt(0)" ::: "memory");
    }
    asm volatile("" ::: "memory"); BAR(); asm volatile("" ::: "memory");
}
struct Unit { const char* A; const char* B; char* C; unsigned lda, ldb, ldc, cw, ch, ak, ah; int nt, sig, nsig; };
DI int fresh_tid() { int t = threadIdx.x; asm volatile("" : "+v"(t)); return t; }
DI void signal(unsigned* ctl, int sig, int nsig) { const int lane = fresh_tid() & 63; if (lane < nsig) __hip_atomic_fetch_add(ctl + sig + (lane & 1) * 32 + (lane >> 1), 1u, RLX_AGENT); }
DI u32x4 pack_pair(f32x4 v0, f32x4 v1) {
    unsigned ax = pk_bf16(v0[0], v0[1]), ay = pk_bf16(v0[2], v0[3]), bx = pk_bf16(v1[0], v1[1]), by = pk_bf16(v1[2], v1[3]);
    swap16(ax, bx); swap16(ay, by);
    return (u32x4){ax, ay, bx, by};
}
DI unsigned frag_off(int lane) { return (unsigned)((((lane >> 4) & 1) * 2 + (lane >> 5)) * 256 + (lane & 15) * 16); }
DI void store_frag(rsrc_t cr, unsigned voff, unsigned soff, f32x4 v0, f32x4 v1) { ST16(pack_pair(v0, v1), cr, voff, soff, 16); }
DI u32x4 to_rows(ldsp sc, int lane, f32x4 v0, f32x4 v1) {
    *(volatile LAS u32x4*)(sc + ((lane & 15) * 4 + ((lane >> 4) & 1) * 2 + (lane >> 5)) * 16) = pack_pair(v0, v1);
    return *(volatile LAS u32x4*)(sc + lane * 16);
}
constexpr int L_SCR = L_QW + 1024;
#define STAGE(bufoff, gbase, voff, step2) do { \
        __builtin_amdgcn_global_load_lds((const unsigned*)((gbase) + (voff)), (LAS unsigned*)(lds + (bufoff) + ldsw), 16, 0, 0); \
        __builtin_amdgcn_global_load_lds((const unsigned*)((gbase) + (step2) + (voff)), (LAS unsigned*)(lds + (bufoff) + ldsw + 8192), 16, 0, 0); } while (0)
#define LDA(dst, off) do { _Pragma("unroll") for (int m = 0; m < 4; ++m) _Pragma("unroll") for (int k = 0; k < 2; ++k) dst[m][k] = *(const LAS bf16x8*)(lds + (off) + aoff + m * 2048 + k * 1024); } while (0)
#define TRD(dst, base, off) asm volatile("ds_read_b64_tr_b16 %0, %1 offset:%2" : "=v"(dst) : "v"(base), "i"(off) : "memory")
#define LDB(dst, v0, v1, off) do { s16x4 l0, h0, l1, h1, l2, h2, l3, h3; \
        TRD(l0, v0, (off)); TRD(h0, v0, (off) + 256); TRD(l1, v0, (off) + 8192); TRD(h1, v0, (off) + 8448); \
        TRD(l2, v1, (off)); TRD(h2, v1, (off) + 256); TRD(l3, v1, (off) + 8192); TRD(h3, v1, (off) + 8448); \
        dst[0][0] = cat8(l0, h0); dst[0][1] = cat8(l1, h1); dst[1][0] = cat8(l2, h2); dst[1][1] = cat8(l3, h3); } while (0)
#define MMA(accv, At, Bt) do { SCHED(); __builtin_amdgcn_s_setprio(1); _Pragma("unroll") for (int m = 0; m < 4; ++m) _Pragma("unroll") for (int n = 0; n < 2; ++n) _Pragma("unroll") for (int k = 0; k < 2; ++k) \
        accv[m][n] = mma32(Bt[n][k], At[m][k], accv[m][n]); __builtin_amdgcn_s_setprio(0); } while (0)
DI unsigned va_of(int tid, unsigned lda, int nt) { int R, C; stage_rc(tid * 16, R, C); return lda ? (unsigned)R * lda + (unsigned)C * 2u : (unsigned)((tid >> 7) * nt * 4096 + (tid & 127) * 16); }
DI unsigned vb_of(int tid, int wid, unsigned ldb) { return (unsigned)(8 * (wid >> 1) + ((tid >> 2) & 7)) * ldb + (unsigned)((2 * (wid & 1) + ((tid >> 5) & 1)) * 64 + (((tid & 3) ^ (((wid >> 1) & 1) << 1)) * 16)); }
#define GEMM_LANE_SETUP(lda0) \
    const int tid = fresh_tid(); \
    const int wid = __builtin_amdgcn_readfirstlane(tid >> 6), lane = tid & 63, wr = wid >> 2, wc = wid & 3, fr = lane & 15, fq = lane >> 4; \
    const unsigned ldsw = (unsigned)wid * 1024u; \
    const int aoff = (lda0) ? lds_byte(wr * 64 + fr, fq * 8) : wr * 8192 + lane * 16; \
    const int boff0 = fq * 2048 + wc * 512 + (fr >> 2) * 64 + (fq & 1) * 32 + (fr & 3) * 8, boff1 = boff0 ^ 32;

template <class Sched>
DI void gemm_stream(ldsp lds, const Sched& S, unsigned* ctl) {
    Unit cur, nxt; int ui = 0;
    if (!S.next(0, cur)) return;
    GEMM_LANE_SETUP(cur.lda);
#define SA(b, h) (((b) * 2 + (h)) * HTB)
#define SB(b, h) ((4 + (b) * 2 + (h)) * HTB)
    const unsigned vb0 = (unsigned)(size_t)lds + 65536u + (unsigned)boff0, vb1 = (unsigned)(size_t)lds + 65536u + (unsigned)boff1;
    f32x4 acc[2][2][4][2];
#pragma unroll
    for (int a = 0; a < 2; ++a)
#pragma unroll
        for (int b = 0; b < 2; ++b)
#pragma unroll
            for (int m = 0; m < 4; ++m)
#pragma unroll
                for (int n = 0; n < 2; ++n) acc[a][b][m][n] = (f32x4){0.f, 0.f, 0.f, 0.f};
    bf16x8 At[4][2], B0[2][2], B1[2][2];
    const unsigned vA = va_of(tid, cur.lda, cur.nt); unsigned vB = vb_of(tid, wid, cur.ldb);
    const size_t a2s = cur.lda ? (size_t)64 * cur.lda : (size_t)cur.nt * 16384;
    {
        const size_t ah = cur.ah, b2s = (size_t)32 * cur.ldb, bk = (size_t)64 * cur.ldb;
        STAGE(SB(0, 0), cur.B, vB, b2s); STAGE(SB(0, 1), cur.B + 256, vB, b2s); STAGE(SA(0, 0), cur.A, vA, a2s); STAGE(SA(0, 1), cur.A + ah, vA, a2s);
        if (wr == 1) BAR();
        WAIT_V(2); BAR();
        STAGE(SB(1, 0), cur.B + bk, vB, b2s); STAGE(SA(1, 0), cur.A + cur.ak, vA, a2s); STAGE(SB(1, 1), cur.B + bk + 256, vB, b2s);
        WAIT_V(6); BAR();
    }
    int psig = 0, pn = 0;
    for (;;) {
        const bool has_next = S.next(ui + 1, nxt);
        if (!has_next) nxt = cur;
        int nt = cur.nt; asm volatile("" : "+s"(nt));
        for (int t = 0; t < nt; t += 2) {
            const bool last = (t == nt - 2);
            const char* a1 = cur.A + (cur.lda ? (unsigned)(t + 1) * 128u : gap((unsigned)(t + 1) * 2048u)) + cur.ah;
            if (last) vB = vb_of(fresh_tid(), wid, nxt.ldb);
            const unsigned ldb = last ? nxt.ldb : cur.ldb;
            const char* a2 = last ? nxt.A : cur.A + (cur.lda ? (unsigned)(t + 2) * 128u : gap((unsigned)(t + 2) * 2048u));
            const char* b2 = last ? nxt.B : cur.B + (size_t)(t + 2) * 64 * cur.ldb;
            const size_t ah = cur.ah, b2s = (size_t)32 * ldb;
            const char* a3 = a2 + cur.ak; const char* b3 = b2 + (size_t)64 * ldb;
            LDB(B0, vb0, vb1, 0); LDB(B1, vb0, vb1, HTB); SCHED(); LDA(At, SA(0, 0)); STAGE(SA(1, 1), a1, vA, a2s);
            WAIT_V(8); WAIT_L(0); BAR(); MMA(acc[0][0], At, B0); MMA(acc[0][1], At, B1); BAR(); SCHED();
            LDA(At, SA(0, 1)); STAGE(SB(0, 0), b2, vB, b2s); STAGE(SB(0, 1), b2 + 256, vB, b2s); STAGE(SA(0, 0), a2, vA, a2s);
            WAIT_V(8);
            asm volatile("" : "+s"(pn));
            if (pn) { signal(ctl, psig, pn); pn = 0; }
            WAIT_L(0); BAR(); MMA(acc[1][0], At, B0); MMA(acc[1][1], At, B1); BAR(); SCHED();
            LDB(B0, vb0, vb1, 2 * HTB); LDB(B1, vb0, vb1, 3 * HTB); SCHED(); LDA(At, SA(1, 0)); STAGE(SA(0, 1), a2 + ah, vA, a2s);
            WAIT_V(8); WAIT_L(0); BAR(); MMA(acc[0][0], At, B0); MMA(acc[0][1], At, B1); BAR(); SCHED();
            LDA(At, SA(1, 1)); STAGE(SB(1, 0), b3, vB, b2s); STAGE(SB(1, 1), b3 + 256, vB, b2s); STAGE(SA(1, 0), a3, vA, a2s);
            WAIT_V(8); WAIT_L(0); BAR(); MMA(acc[1][0], At, B0); MMA(acc[1][1], At, B1); BAR(); SCHED();
        }
        if (wr == 0) BAR();
        {
            const rsrc_t cr = mkrsrc(cur.C, 0x1000000u);
            const int tz = fresh_tid() & 63;
            if (cur.nsig) {
                const unsigned co = (unsigned)wc * cur.cw + frag_off(tz);
#pragma unroll
                for (int ai = 0; ai < 2; ++ai)
#pragma unroll
                    for (int m = 0; m < 4; ++m)
#pragma unroll
                        for (int bj = 0; bj < 2; ++bj) store_frag(cr, co, gap((unsigned)(ai * 128 + wr * 64 + m * 16) * cur.ldc) + (unsigned)bj * cur.ch, acc[ai][bj][m][0], acc[ai][bj][m][1]);
            } else {
                const unsigned co = (unsigned)(wr * 64 + (tz >> 2)) * cur.ldc + (unsigned)(wc * 64 + (tz & 3) * 16);
#pragma unroll
                for (int ai = 0; ai < 2; ++ai)
#pragma unroll
                    for (int m = 0; m < 4; ++m)
#pragma unroll
                        for (int bj = 0; bj < 2; ++bj)
                            ST16(to_rows(lds + L_SCR + wid * 1024, tz, acc[ai][bj][m][0], acc[ai][bj][m][1]), cr, co, (unsigned)(ai * 128 + m * 16) * cur.ldc + (unsigned)bj * 256u, 0);
            }
        }
        psig = cur.sig; pn = cur.nsig;
        if (!has_next) break;
#pragma unroll
        for (int a = 0; a < 2; ++a)
#pragma unroll
            for (int b = 0; b < 2; ++b)
#pragma unroll
                for (int m = 0; m < 4; ++m)
#pragma unroll
                    for (int n = 0; n < 2; ++n) acc[a][b][m][n] = (f32x4){0.f, 0.f, 0.f, 0.f};
        cur = nxt; ++ui;
        if (wr == 1) BAR();
    }
    WAIT_V(0);
    signal(ctl, psig, pn);
    BAR();
#undef SA
#undef SB
}

DI void gemm_small(ldsp lds, const Unit u, unsigned* ctl) {
    GEMM_LANE_SETUP(u.lda);
    const unsigned vA = va_of(tid, u.lda, u.nt), vB = vb_of(tid, wid, u.ldb);
    const size_t a2s = u.lda ? (size_t)64 * u.lda : (size_t)u.nt * 16384, b2s = (size_t)32 * u.ldb, bk = (size_t)64 * u.ldb;
    f32x4 acc[4][2];
#pragma unroll
    for (int m = 0; m < 4; ++m)
#pragma unroll
        for (int n = 0; n < 2; ++n) acc[m][n] = (f32x4){0.f, 0.f, 0.f, 0.f};
    int nt = u.nt; asm volatile("" : "+s"(nt));
    const unsigned vb0 = (unsigned)(size_t)lds + (unsigned)boff0, vb1 = (unsigned)(size_t)lds + (unsigned)boff1, vb2 = vb0 + 65536u, vb3 = vb1 + 65536u;
#pragma unroll
    for (int t = 0; t < 4; ++t) { STAGE(t * 32768, u.A + (size_t)t * u.ak, vA, a2s); STAGE(t * 32768 + 16384, u.B + t * bk, vB, b2s); }
    bf16x8 A0[4][2], B0[2][2], A1[4][2], B1[2][2];
    WAIT_V(12); BAR();
    LDB(B0, vb0, vb1, 16384); LDA(A0, 0);
#define RDA(dst, m, off) do { dst[m][0] = *(const LAS bf16x8*)(lds + (off) + aoff + (m) * 2048); dst[m][1] = *(const LAS bf16x8*)(lds + (off) + aoff + (m) * 2048 + 1024); } while (0)
#define RDB(dst, nn, v, off) do { s16x4 l0, h0, l1, h1; TRD(l0, v, (off)); TRD(h0, v, (off) + 256); TRD(l1, v, (off) + 8192); TRD(h1, v, (off) + 8448); dst[nn][0] = cat8(l0, h0); dst[nn][1] = cat8(l1, h1); } while (0)
#define MM4(Ac, Bc, m) do { SCHED(); _Pragma("unroll") for (int n = 0; n < 2; ++n) _Pragma("unroll") for (int k = 0; k < 2; ++k) acc[m][n] = mma32(Bc[n][k], Ac[m][k], acc[m][n]); SCHED(); } while (0)
#define KSTEP(i, Ac, Bc, An, Bn, v0, v1, boffn) do { \
        const int tn = min(t0 + (i) + 4, nt - 1); \
        WAIT_L(0); WAIT_V(8); BAR(); \
        MM4(Ac, Bc, 0); RDB(Bn, 0, v0, boffn); RDA(An, 0, (((i) + 1) & 3) * 32768); \
        MM4(Ac, Bc, 1); RDB(Bn, 1, v1, boffn); RDA(An, 1, (((i) + 1) & 3) * 32768); \
        MM4(Ac, Bc, 2); RDA(An, 2, (((i) + 1) & 3) * 32768); STAGE((i) * 32768, u.A + (u.lda ? (unsigned)tn * 128u : gap((unsigned)tn * 2048u)), vA, a2s); \
        MM4(Ac, Bc, 3); RDA(An, 3, (((i) + 1) & 3) * 32768); STAGE((i) * 32768 + 16384, u.B + (size_t)tn * bk, vB, b2s); } while (0)
    for (int t0 = 0; t0 < nt; t0 += 4) {
        KSTEP(0, A0, B0, A1, B1, vb0, vb1, 49152);
        KSTEP(1, A1, B1, A0, B0, vb2, vb3, 16384);
        KSTEP(2, A0, B0, A1, B1, vb2, vb3, 49152);
        KSTEP(3, A1, B1, A0, B0, vb0, vb1, 16384);
    }
    WAIT_L(0); SCHED();
#undef RDA
#undef RDB
#undef MM4
#undef KSTEP
    const rsrc_t cr = mkrsrc(u.C, 256u * u.ldc);
    if (u.nsig) {
#pragma unroll
        for (int m = 0; m < 4; ++m) store_frag(cr, (unsigned)wc * u.cw + frag_off(lane), gap((unsigned)(wr * 64 + m * 16) * u.ldc), acc[m][0], acc[m][1]);
    } else {
#pragma unroll
        for (int m = 0; m < 4; ++m)
            ST16(to_rows(lds + L_SCR + wid * 1024, lane, acc[m][0], acc[m][1]), cr, (unsigned)(wr * 64 + (lane >> 2)) * u.ldc + (unsigned)(wc * 64 + (lane & 3) * 16), (unsigned)(m * 16) * u.ldc, 0);
    }
    WAIT_V(0);
    signal(ctl, u.sig, u.nsig);
    BAR();
}

DI void gemm_wide(ldsp lds, const char* A, const char* B, char* C, int nt) {
    GEMM_LANE_SETUP(0);
    const unsigned vA = va_of(tid, 0, nt), vB = vb_of(tid, wid, DM * 2);
    const size_t a2s = (size_t)nt * 16384, b2s = 32 * DM * 2, bk = 64 * DM * 2;
    f32x4 acc[2][4][2];
#pragma unroll
    for (int b = 0; b < 2; ++b)
#pragma unroll
        for (int m = 0; m < 4; ++m)
#pragma unroll
            for (int n = 0; n < 2; ++n) acc[b][m][n] = (f32x4){0.f, 0.f, 0.f, 0.f};
    asm volatile("" : "+s"(nt));
    const unsigned vb0 = (unsigned)(size_t)lds + (unsigned)boff0, vb1 = (unsigned)(size_t)lds + (unsigned)boff1;
#define STG3(slot, t) do { STAGE((slot), A + gap((unsigned)(t) * 2048u), vA, a2s); STAGE((slot) + 16384, B + (size_t)(t) * bk, vB, b2s); STAGE((slot) + 32768, B + (size_t)(t) * bk + 256, vB, b2s); } while (0)
    STG3(0, 0); STG3(49152, 1); STG3(98304, 2);
    bf16x8 A0[4][2], P0[2][2], Q0[2][2], A1[4][2], P1[2][2], Q1[2][2];
    WAIT_V(12); BAR();
    LDB(P0, vb0, vb1, 16384); LDB(Q0, vb0, vb1, 32768); LDA(A0, 0);
    int sc = 0, sn = 49152;
#define RDA(dst, m) do { dst[m][0] = *(const LAS bf16x8*)(lds + sn + aoff + (m) * 2048); dst[m][1] = *(const LAS bf16x8*)(lds + sn + aoff + (m) * 2048 + 1024); } while (0)
#define RDB(dst, nn, v, off) do { s16x4 l0, h0, l1, h1; TRD(l0, v, (off)); TRD(h0, v, (off) + 256); TRD(l1, v, (off) + 8192); TRD(h1, v, (off) + 8448); dst[nn][0] = cat8(l0, h0); dst[nn][1] = cat8(l1, h1); } while (0)
#define MM4(b, Ac, Bc, m) do { SCHED(); _Pragma("unroll") for (int n = 0; n < 2; ++n) _Pragma("unroll") for (int k = 0; k < 2; ++k) acc[b][m][n] = mma32(Bc[n][k], Ac[m][k], acc[b][m][n]); SCHED(); } while (0)
#define KSTEP(t, Ac, Pc, Qc, An, Pn, Qn) do { \
        const int tn = min((t) + 3, nt - 1); const unsigned u0 = vb0 + (unsigned)sn, u1 = vb1 + (unsigned)sn; \
        WAIT_L(0); WAIT_V(6); BAR(); \
        MM4(0, Ac, Pc, 0); RDB(Pn, 0, u0, 16384); \
        MM4(1, Ac, Qc, 0); RDB(Pn, 1, u1, 16384); \
        MM4(0, Ac, Pc, 1); RDB(Qn, 0, u0, 32768); \
        MM4(1, Ac, Qc, 1); RDB(Qn, 1, u1, 32768); \
        MM4(0, Ac, Pc, 2); RDA(An, 0); STAGE(sc, A + gap((unsigned)tn * 2048u), vA, a2s); \
        MM4(1, Ac, Qc, 2); RDA(An, 1); STAGE(sc + 16384, B + (size_t)tn * bk, vB, b2s); \
        MM4(0, Ac, Pc, 3); RDA(An, 2); STAGE(sc + 32768, B + (size_t)tn * bk + 256, vB, b2s); \
        MM4(1, Ac, Qc, 3); RDA(An, 3); \
        sc = sn; sn = sn == 98304 ? 0 : sn + 49152; } while (0)
    for (int t0 = 0; t0 < nt; t0 += 2) { KSTEP(t0, A0, P0, Q0, A1, P1, Q1); KSTEP(t0 + 1, A1, P1, Q1, A0, P0, Q0); }
    WAIT_L(0); SCHED();
#undef STG3
#undef RDA
#undef RDB
#undef MM4
#undef KSTEP
    const rsrc_t cr = mkrsrc(C, 128u * 4096u);
#pragma unroll
    for (int m = 0; m < 4; ++m)
#pragma unroll
        for (int b = 0; b < 2; ++b)
            ST16(to_rows(lds + L_SCR + wid * 1024, lane, acc[b][m][0], acc[b][m][1]), cr, (unsigned)(wr * 64 + (lane >> 2)) * 4096u + (unsigned)(wc * 64 + (lane & 3) * 16), (unsigned)(m * 65536 + b * 256), 0);
    WAIT_V(0);
    BAR();
}

#define win_off(idx) ((unsigned)(idx) * 16384u + ((((unsigned)(idx) >> 7) ^ par) & 1u) * 8192u)
#define KKM 8192u
#define km_win(s, h) (win_off(NWIN + (s) * 32 + (h)) ^ KKM)
#define W_MEM(s, x) ((s) == 0 ? 0 : NWIN + 2048 + 8 * (s) + 2 * (x))
#define W_PART(slot) (192 + (slot) * 64)
#define W_X(slot) (384 + (slot) * 128)
DI bool all_set(unsigned mx) { return __builtin_amdgcn_ballot_w64(mx == SENT) == 0ull; }
DI u32x4 rows_gather(unsigned v) { unsigned a = v, b = v; swap16(a, b); unsigned c = a, d = b; swap32(a, c); swap32(b, d); return (u32x4){a, b, c, d}; }
DI float max3f(float a, float b, float c) { return fmaxf(fmaxf(a, b), c); }
#define KD0 2
#define KD1 6
#define KD2 5
#define KRA 3
#define KDM 1
#define KWP 4
#define KNT1 0
#define KNT2 0
#define KPM 4
constexpr int RED_M = 144;
constexpr int L_PX = 32768, L_PM = L_PX + 512, L_ML = L_PM + 512, L_FLAG = L_ML + 256, L_PACE = L_FLAG + 16, L_VM = 34816, L_OWN = L_VM + 2048, L_X = L_OWN + 1536  ;
constexpr unsigned OOB = 0x80000000u;
#define RETRY(i)
#define SEG(i)
#define PST(i)
#define PIN_S(x) asm volatile("" : "+s"(x))
#define PIN_V(x) asm volatile("" : "+v"(x))
DI float ror8(float v) { return __uint_as_float(__builtin_amdgcn_update_dpp(0, __float_as_uint(v), 0x128, 0xf, 0xf, true)); }

template <int ROLE>
DI void chain_loop(const Params& p, const unsigned par, const int rank, const int w, ldsp lds, const bf16x16 (&W)[6][2], const bf16x16 (&WO)[2][2], unsigned long long* stamps) {
    const int lane = fresh_tid() & 63, g = lane >> 4, n = lane & 15;
    const int h = rank >> 2, j = rank & 3;
    unsigned* ctl = (unsigned*)(p.ws + WS_CTL);
    const rsrc_t xr = mkrsrc(p.ws + WS_XCH, (unsigned)XCH_BYTES);
    const rsrc_t qr = mkrsrc(p.ws + WH(WS_QKVX + (size_t)(32 + h) * XH), 33u * XH);
    const int sidx = (n & 2) ? 0xEEEE : 0x4444;
    const unsigned l16 = (unsigned)lane * 16u;
    const int redw = (w * 1024 + (n & 1) * 512 + (n >> 1) * 8 + 2 * (g ^ (n >> 3))) * 4;
    const int redr = (w < 4 ? (n >> 3) * RED_M + (4 * w + g) * 8 + ((n & 7) ^ ((w & 1) * 2)) : 2 * RED_M + (8 * (w & 1) + 2 * g + ((n >> 1) & 1)) * 8 + ((2 * (n >> 2) + (n & 1)) ^ (g & 2))) * 4;
    const bool vown = (g >> 1) == (j & 1);
    const unsigned vg1 = (unsigned)(((j & 2) ^ 2) + (g >> 1)) * 1536u + 1024u + (unsigned)(lane & 31) * 16u;
    const unsigned vg0 = vown ? OOB : (vg1 + (unsigned)(j & 2) * 3072u - 3072u);
    const unsigned pub_off = (g == j ? 0u : (unsigned)(4 * h + g) * 16384u) + (unsigned)j * 1536u + (unsigned)w * 256u + (unsigned)n * 16u;
    const int red = ((j + 1) & 3) * 32 + h;
    const int rho = 4 * (8 * j + w) + 2 * (g >> 1) + ((n >> 2) & 1);
    const unsigned pst_off = n < 8 ? (unsigned)(rho >> 1) * 16384u + (unsigned)(rho & 1) * 4096u + (unsigned)((h ^ rho) & 31) * 128u + (unsigned)((g & 1) * 64 + (n & 3) * 16) : OOB;
    const unsigned mem_off = lane < 32 ? (unsigned)(4 * ((n >> 1) & 1) + 2 * ((n >> 2) & 1) + (n & 1)) * 16u + (unsigned)(lane >> 3) * 32768u : OOB;
    const unsigned xw0 = win_off(W_X(0)), xw1 = win_off(W_X(1));
    unsigned pso = win_off(W_PART(0)), psn = win_off(W_PART(1)), psp = win_off(W_PART(2));
    const unsigned redo = (unsigned)(red >> 1) * 16384u + (unsigned)(red & 1) * 4096u;
    const int vxr = L_X + lane * 16;
    const float csc = 0.08838834764831845f * 1.4426950408889634f;
    float ninf = -__builtin_inff(); PIN_V(ninf);
    u32x4 KX[8], VWX[2];
    unsigned long long kv_ok = 0;
    auto dma_x = [&](int s) {
        if (ROLE == 0 && w < 4) {
            if (!((kv_ok >> (s >> 2)) & 1ull)) {
                kv_ok = __builtin_amdgcn_ballot_w64(__hip_atomic_load(ctl + C_KVC(n, h), RLX_AGENT) + 1u >= 16u);
                if (!((kv_ok >> (s >> 2)) & 1ull)) { wait_count(ctl, C_KVC(s >> 2, h), 16u); kv_ok |= 1ull << (s >> 2); }
                asm volatile("" ::: "memory");
            }
#pragma unroll
            for (int i = 0; i < 2; ++i) {
                const unsigned vo = l16, so = (unsigned)s * 16384u + (unsigned)(w * 2 + i) * 1024u;
                __builtin_amdgcn_raw_ptr_buffer_load_lds(qr, (LAS void*)(lds + L_X + (s & 1) * 16384 + (w * 2 + i) * 1024), 16, vo, so + 32u * XH, 0, 16);
                __builtin_amdgcn_raw_ptr_buffer_load_lds(qr, (LAS void*)(lds + L_X + (s & 1) * 16384 + 8192 + (w * 2 + i) * 1024), 16, vo, so, 0, 16);
            }
        }
    };
    auto load_kx = [&](int s) {
#pragma unroll
        for (int kt = 0; kt < 2; ++kt)
#pragma unroll
            for (int kk = 0; kk < 4; ++kk) KX[kt * 4 + kk] = *(const LAS u32x4*)(lds + vxr + (s & 1) * 16384 + 8192 + kt * 4096 + ((j + kk) & 3) * 1024);
    };
    auto vwx_kt = [&](int s, int kt) {
        bf16x8 VA[4];
#pragma unroll
        for (int kk = 0; kk < 4; ++kk) VA[kk] = *(const LAS bf16x8*)(lds + vxr + (s & 1) * 16384 + kt * 4096 + (kk ^ (j & 2)) * 1024);
#pragma unroll
        for (int ct = 0; ct < 2; ++ct) {
            f32x4 d = {0.f, 0.f, 0.f, 0.f};
#pragma unroll
            for (int kk = 0; kk < 4; ++kk) d = mma32(VA[kk], (kk & 1) ? hi8(WO[ct][kk >> 1]) : lo8(WO[ct][kk >> 1]), d);
            VWX[ct][2 * kt] = pk_bf16(d[0], d[1]); VWX[ct][2 * kt + 1] = pk_bf16(d[2], d[3]);
        }
    };
    auto rmax = [&](float v) { float a = v, b = v; swap16f(a, b); v = max3f(a, b, ninf); a = v; b = v; swap32f(a, b); return max3f(a, b, ninf); };
    auto lo2 = [](f32x4 v) { return (f32x2){v[0], v[1]}; };
    auto hi2 = [](f32x4 v) { return (f32x2){v[2], v[3]}; };
    dma_x(0); dma_x(1);
    asm volatile("s_waitcnt vmcnt(0) lgkmcnt(0)" ::: "memory"); BAR(); asm volatile("" ::: "memory");
    PST(5);
    vwx_kt(0, 0); vwx_kt(0, 1);
    if (ROLE == 2) load_kx(0);
    __builtin_amdgcn_s_setprio(2);

    unsigned iv_t0 = 0, iv_acc = 0;
    unsigned spins = 0, pace_c = 0x7fffffffu, pace_t = 0;
    const u32x4 sv = {SENT, SENT, SENT, SENT};
    for (int s = 0;; ++s) {
        unsigned xw = (s & 1) ? xw1 : xw0;
        unsigned mso = win_off(W_MEM(s, rank >> 5) + (w >> 2)) + (unsigned)(w & 3) * 2048u;
        unsigned pub = pub_off + (g == j ? km_win(s, h) : xw);
        PIN_S(xw); PIN_S(mso); PIN_V(pub);
        {
            if (KD0) __builtin_amdgcn_s_sleep(KD0);
            const u32x2 pv = *(const LAS u32x2*)(lds + L_PACE);
            unsigned target = __builtin_amdgcn_readfirstlane(pv.x); const bool valid = __builtin_amdgcn_readfirstlane(pv.y) + 1u == (unsigned)s && s > 3;
            unsigned now = (unsigned)__builtin_amdgcn_s_memrealtime();
            if (ROLE == 3 && s > 2) pace_c = min(pace_c, now - pace_t);
            if (valid && (int)(target - now) < 100) { while ((int)(target - now) > 0) now = (unsigned)__builtin_amdgcn_s_memrealtime(); }
        }
        SEG(10);
        f32x4 c[6]; u32x4 a1;
        u32x4 a0;
        auto ld_a = [&]() { asm volatile("" ::: "memory"); a0 = ld16_sc1(xr, l16, mso); a1 = ld16_sc1(xr, l16 + 1024, mso); };
        auto ok_a = [&]() { return all_set(max(umax4(a0), umax4(a1))); };
        auto mm_a = [&]() {
#pragma unroll
            for (int i = 0; i < 6; ++i) { c[i] = (f32x4){0.f, 0.f, 0.f, 0.f}; PIN_V(c[i]); }
            SCHED();
            c[0] = smma(a0, W[0][0], c[0], sidx); c[1] = smma(a0, W[1][0], c[1], sidx); c[0] = smma(a1, W[0][1], c[0], sidx); c[1] = smma(a1, W[1][1], c[1], sidx);
            c[2] = smma(a0, W[2][0], c[2], sidx); c[3] = smma(a0, W[3][0], c[3], sidx);
        };
        auto put = [&](int i) { *(LAS f32x2*)(lds + redw + ((i >> 1) * RED_M + (i & 1) * 64) * 4) = lo2(c[i]) + hi2(c[i]); };
        ld_a(); SCHED();
        mm_a();
        if (__builtin_expect(!ok_a(), 0)) { bool out; do { RETRY(0); out = spin_out(spins, ctl); ld_a(); } while (!(ok_a() | out)); mm_a(); }
        SEG(0);
        c[2] = smma(a1, W[2][1], c[2], sidx); c[3] = smma(a1, W[3][1], c[3], sidx);
        put(0); put(1);
        SCHED();
        c[4] = smma(a0, W[4][0], c[4], sidx); c[5] = smma(a0, W[5][0], c[5], sidx); c[4] = smma(a1, W[4][1], c[4], sidx);
        put(2); put(3);
        c[5] = smma(a1, W[5][1], c[5], sidx);
        SCHED();
        put(4); put(5);
        SEG(1);
        LDS_BAR();
        SEG(2);
        if (ROLE <= 1) {
            f32x2 v[8];
#pragma unroll
            for (int ww = 0; ww < 8; ++ww) { const LAS float* q = (const LAS float*)(lds + redr + ww * 4096); v[ww] = (f32x2){q[0], q[512]}; }
            const f32x2 t = ((v[0] + v[1]) + (v[2] + v[3])) + ((v[4] + v[5]) + (v[6] + v[7]));
            const u32x4 o = rows_gather(min(pk_bf16(t.x, t.y), SENT - 1));
            ST16(o, xr, pub, 0, 16);
            *(LAS u32x4*)(lds + L_OWN + w * 256 + n * 16) = o;
        } else {
            if (ROLE == 3 && rank == 0) {
                pace_t = (unsigned)__builtin_amdgcn_s_memrealtime();
                if (lane == 0) { __hip_atomic_store(ctl + C_PROG, (unsigned)s, RLX_AGENT);
                    __hip_atomic_store((unsigned long long*)(ctl + C_PACE + par * 2048), (unsigned long long)(pace_t + pace_c + KPM) | ((unsigned long long)s << 32), RLX_AGENT); }
            }
        }
        SEG(3);
        if (s == NSEG - 1) break;
        LDS_BAR();
        SEG(13);
        unsigned VWM[2];
        auto vwm_pack = [&](const f32x4 (&dv)[2]) {
#pragma unroll
            for (int ct = 0; ct < 2; ++ct) { const f32x2 t = lo2(dv[ct]) + hi2(dv[ct]); VWM[ct] = pk_bf16(t.x, t.y); PIN_V(VWM[ct]); }
        };
        f32x4 dv[2] = {{0.f, 0.f, 0.f, 0.f}, {0.f, 0.f, 0.f, 0.f}}, s0, s1;
        PIN_V(dv[0]); PIN_V(dv[1]);
        u32x4 v0, v1;
        if (ROLE == 0) {
            if (KDM == 1) { WAIT_V(0); dma_x(min(s + 2, NSEG - 2)); }
            else if (KDM == 2) { dma_x(min(s + 2, NSEG - 2)); if (w < 4) WAIT_V(4); else WAIT_V(0); }
            else WAIT_V(0);
        } else {
            unsigned cw = xw + (unsigned)rank * 16384u, c1 = cw + (unsigned)((j + 1) & 3) * 1536u, c2 = cw + (unsigned)((j + 2) & 3) * 1536u, c3 = cw + (unsigned)((j + 3) & 3) * 1536u;
            PIN_S(cw); PIN_S(c1); PIN_S(c2); PIN_S(c3);
            const unsigned xl = ROLE == 2 ? (l16 & ~128u) : l16;
            u32x4 vo;
            f32x4 e0 = {0.f, 0.f, 0.f, 0.f}, e1 = e0;
            if (ROLE == 1) { vo = *(const LAS u32x4*)(lds + L_OWN + 1024 + (lane & 31) * 16); if (!vown) vo = (u32x4){0u, 0u, 0u, 0u}; PIN_V(vo); }
            else {
                const u32x4 X0 = *(const LAS u32x4*)(lds + L_OWN + xl);
                if (ROLE == 2) { e0 = mma32(asbf(KX[0]), asbf(X0), e0); e1 = mma32(asbf(KX[4]), asbf(X0), e1); } else e0 = mma32(asbf(X0), asbf(X0), e0);
                PIN_V(e0); PIN_V(e1);
            }
            if (KD1) __builtin_amdgcn_s_sleep(KD1);
            u32x4 X1, X2, X3;
            auto ld_b = [&]() {
                asm volatile("" ::: "memory");
                if (ROLE >= 2) { X1 = ld16_sc1(xr, xl, c1); X2 = ld16_sc1(xr, xl, c2); X3 = ld16_sc1(xr, xl, c3); }
                else { v0 = ld16_sc1(xr, vg0, cw); v1 = ld16_sc1(xr, vg1, cw); }
            };
            auto ok_b = [&]() { return all_set(ROLE >= 2 ? max(umax4(X1), max(umax4(X2), umax4(X3))) : max(umax4(v0), umax4(v1))); };
            auto mm_b = [&]() {
                if (ROLE == 2) {
                    s0 = mma32(asbf(KX[1]), asbf(X1), e0); s1 = mma32(asbf(KX[5]), asbf(X1), e1);
                    s0 = mma32(asbf(KX[2]), asbf(X2), s0); s1 = mma32(asbf(KX[6]), asbf(X2), s1);
                    s0 = mma32(asbf(KX[3]), asbf(X3), s0); s1 = mma32(asbf(KX[7]), asbf(X3), s1);
                } else if (ROLE == 3) {
                    s0 = mma32(asbf(X1), asbf(X1), e0); s1 = mma32(asbf(X2), asbf(X2), e1); s0 = mma32(asbf(X3), asbf(X3), s0);
                } else {
                    v0 |= vo;
                    *(LAS u32x4*)(lds + L_VM + lane * 16) = v0; *(LAS u32x4*)(lds + L_VM + 1024 + lane * 16) = v1;
                }
            };
            SEG(11);
            ld_b(); SCHED(); mm_b();
            if (__builtin_expect(!ok_b(), 0)) { bool out; do { RETRY(1); out = spin_out(spins, ctl); ld_b(); } while (!(ok_b() | out)); mm_b(); }
            SEG(4);
        }
        float m;
        f32x4 sc;
        if (ROLE >= 2) {
            sc = ROLE == 2 ? (n < 8 ? s0 : s1) : s0 + s1;
            if (ROLE == 3 && g < 2) sc = (f32x4){ninf, ninf, ninf, ninf};
            m = max3f(max3f(sc[0], sc[1], sc[2]), sc[3], ninf);
            if (ROLE == 2) m = max3f(m, ror8(m), ninf);
            m = rmax(m);
            PIN_V(m);
        }
        LDS_BAR();
        if (ROLE == 0) {
            const unsigned ro = w < 3 ? xw + (unsigned)rank * 16384u + (unsigned)w * 2048u : psp + redo + (unsigned)(w - 3) * 2048u;
            if (KDM == 4 && s + 2 < NSEG - 1) dma_x(s + 2);
            ST16(sv, xr, l16, ro, 16); ST16(sv, xr, l16 + 1024, ro, 16);
            if (KDM == 5 && s + 2 < NSEG - 1) dma_x(s + 2);
        }
        if (ROLE != 1) { v0 = *(const LAS u32x4*)(lds + L_VM + lane * 16); v1 = *(const LAS u32x4*)(lds + L_VM + 1024 + lane * 16); }
        if (ROLE >= 2) {
            const float mc = -m * csc;
#pragma unroll
            for (int i = 0; i < 4; ++i) sc[i] = __builtin_amdgcn_exp2f(fmaf(sc[i], csc, mc));
            *(LAS u32x2*)(lds + (ROLE == 2 ? L_PX + (g * 8 + (n & 7)) * 16 + (n >> 3) * 8 : L_PM + (g < 2 ? 256 : 0) + n * 16 + (g & 1) * 8)) = (u32x2){pk_bf16(sc[0], sc[1]), pk_bf16(sc[2], sc[3])};
            SCHED();
            dv[0] = smma(v0, WO[0][0], dv[0], sidx); dv[1] = smma(v0, WO[1][0], dv[1], sidx);
            float l = (sc[0] + sc[1]) + (sc[2] + sc[3]);
            if (ROLE == 2) l += ror8(l);
            dv[0] = smma(v1, WO[0][1], dv[0], sidx); dv[1] = smma(v1, WO[1][1], dv[1], sidx);
            l = rows_sum(l);
            *(LAS f32x2*)(lds + L_ML + n * 16 + (ROLE == 2 ? 0 : 8)) = (f32x2){mc, l};
        } else {
#pragma unroll
            for (int ct = 0; ct < 2; ++ct) { dv[ct] = smma(v0, WO[ct][0], dv[ct], sidx); dv[ct] = smma(v1, WO[ct][1], dv[ct], sidx); }
        }
        vwm_pack(dv);
        SEG(5);
        u32x4 am0 = {VWM[0], 0u, 0u, 0u}, am1 = {VWM[1], 0u, 0u, 0u}, pm = {0u, 0u, 0u, 0u};
        PIN_V(am0); PIN_V(am1); PIN_V(pm);
        LDS_BAR();
        SEG(6);
        {
            const f32x4 ml = *(const LAS f32x4*)(lds + L_ML + n * 16);
            const u32x4 px = *(const LAS u32x4*)(lds + L_PX + (g * 8 + (n & 7)) * 16);
            pm.x = *(const LAS unsigned*)(lds + L_PM + n * 16 + g * 4);
            const f32x4 z = {0.f, 0.f, 0.f, 0.f};
            const f32x4 dx0 = mma32(asbf(VWX[0]), asbf(px), z), dx1 = mma32(asbf(VWX[1]), asbf(px), z), dm0 = mma32(asbf(am0), asbf(pm), z), dm1 = mma32(asbf(am1), asbf(pm), z);
            const float dl = ml[0] - ml[2];
            const float e = __builtin_amdgcn_exp2f(-__builtin_fabsf(dl));
            const float ex = dl > 0.f ? e : 1.f, em = dl > 0.f ? 1.f : e;
            const float inv = __builtin_amdgcn_rcpf(fmaf(ex, ml[1], em * ml[3]));
            const f32x4 d0 = (dx0 * ex + dm0 * em) * inv, d1 = (dx1 * ex + dm1 * em) * inv;
            const u32x4 o = {min(pk_bf16(d0[0], d0[1]), SENT - 1), min(pk_bf16(d0[2], d0[3]), SENT - 1), min(pk_bf16(d1[0], d1[1]), SENT - 1), min(pk_bf16(d1[2], d1[3]), SENT - 1)};
            ST16(o, xr, pst_off, pso, 16);
        }
        SCHED();
        SEG(7);
        __builtin_amdgcn_s_setprio(ROLE == 3 ? 1 : 0);
        if (KDM == 3 && s + 2 < NSEG - 1) dma_x(s + 2);
        unsigned long long pv = 0;
        if (ROLE == 1) { pv = __hip_atomic_load((unsigned long long*)(ctl + C_PACE + par * 2048), RLX_AGENT); SCHED(); }
        vwx_kt(s + 1, 0); vwx_kt(s + 1, 1);
        SCHED();
        if (ROLE == 3) __builtin_amdgcn_s_setprio(2);
        if (ROLE == 3) {
            u32x4 SEL;
            { const unsigned one = (n & 1) ? 0x3f800000u : 0x00003f80u; const int dw = (n & 7) >> 1; SEL = (u32x4){dw == 0 ? one : 0u, dw == 1 ? one : 0u, dw == 2 ? one : 0u, dw == 3 ? one : 0u}; }
            unsigned so = pso + redo, mo = win_off(W_MEM(s + 1, 0) + (red >> 6)) + (unsigned)(red & 63) * 128u;
            PIN_S(so); PIN_S(mo); PIN_V(SEL);
            if (KD2) __builtin_amdgcn_s_sleep(KD2);
            f32x4 ta, tb;
            u32x4 q[4];
            auto ld_c = [&]() {
                asm volatile("" ::: "memory");
#pragma unroll
                for (int i = 0; i < 4; ++i) q[i] = ld16_sc1(xr, l16 + i * 1024, so);
            };
            auto ok_c = [&]() { return all_set(max(max(umax4(q[0]), umax4(q[1])), max(umax4(q[2]), umax4(q[3])))); };
            auto mm_c = [&]() {
                ta = mma32(asbf(SEL), asbf(q[0]), (f32x4){0.f, 0.f, 0.f, 0.f}); tb = mma32(asbf(SEL), asbf(q[1]), (f32x4){0.f, 0.f, 0.f, 0.f});
                ta = mma32(asbf(SEL), asbf(q[2]), ta); tb = mma32(asbf(SEL), asbf(q[3]), tb);
            };
            SEG(12);
            ld_c(); mm_c();
            if (__builtin_expect(!ok_c(), 0)) { bool out; do { RETRY(2); out = spin_out(spins, ctl); ld_c(); } while (!(ok_c() | out)); mm_c(); }
            SEG(8);
            f32x4 t = ta + tb;
#pragma unroll
            for (int i = 0; i < 4; ++i) t[i] += ror8(t[i]);
            unsigned a0 = min(pk_bf16(t[0], t[1]), SENT - 1), a1 = min(pk_bf16(t[2], t[3]), SENT - 1);
            unsigned b0 = a0, b1 = a1; swap16(a0, b0); swap16(a1, b1);
            const u32x4 o = {a0, a1, b0, b1};
            ST16(o, xr, mem_off, mo, 16);
        } else {
            if (KDM == 0 && s + 2 < NSEG - 1) dma_x(s + 2);
            if (ROLE == 2) load_kx(s + 1);
            if (ROLE == 1) *(LAS u32x2*)(lds + L_PACE) = (u32x2){(unsigned)pv, (unsigned)(pv >> 32)};
        }
        { const unsigned t = psp; psp = pso; pso = psn; psn = t; }
        SEG(9);
        if (ROLE != 3) __builtin_amdgcn_s_setprio(2);
        LDS_BAR();
        SEG(14);
    }
    WAIT_V(0);
    __builtin_amdgcn_s_setprio(0);
}

DI void chain_role(const Params& p, unsigned par, int rank, ldsp lds, unsigned long long* stamps) {
    const int tid = fresh_tid(), w = __builtin_amdgcn_readfirstlane(tid >> 6), lane = tid & 63, g = lane >> 4, n = lane & 15;
    const int h = rank >> 2, j = rank & 3;
    const rsrc_t xr = mkrsrc(p.ws + WS_XCH, (unsigned)XCH_BYTES);
    unsigned* ctl = (unsigned*)(p.ws + WS_CTL);
    PST(0);
    const int red = ((j + 1) & 3) * 32 + h;
    bf16x16 W[6][2], WO[2][2];
    {
        ldsp my = lds + w * 16384;
        const int rrow = lane >> 2, rc = (lane & 3) * 8, q4 = n >> 2;
        const int toffo = (8 * g + q4) * 64 + (n & 3) * 8;
        const int toffq = (16 * (g & 1) + 8 * (g >> 1) + 4 * (q4 >> 1) + (q4 & 1)) * 64 + (n & 3) * 8;
#define FRAG(base, st) cat16(cat8(trd((base)), trd((base) + (st))), cat8(trd((base) + 2048), trd((base) + 2048 + (st))))
#pragma unroll
        for (int i = 0; i < 8; ++i) {
            __builtin_amdgcn_global_load_lds((const unsigned*)(p.wq + (size_t)(128 * w + 16 * i + rrow) * 4096 + h * 128 + 32 * j + rc), (LAS unsigned*)(my + i * 1024), 16, 0, KNT1);
            __builtin_amdgcn_global_load_lds((const unsigned*)(p.wo + (size_t)(h * 128 + 16 * i + rrow) * 1024 + 256 * j + 32 * w + rc), (LAS unsigned*)(my + 8192 + i * 1024), 16, 0, KNT1);
        }
        {
            const u32x4 sv = {SENT, SENT, SENT, SENT};
            if (w < 6) {
                ST16(sv, xr, (unsigned)lane * 16u, win_off(W_X(0)) + (unsigned)rank * 16384u + (unsigned)w * 1024u, 16);
                ST16(sv, xr, (unsigned)lane * 16u, win_off(W_X(1)) + (unsigned)rank * 16384u + (unsigned)w * 1024u, 16);
            }
            ST16(sv, xr, (unsigned)lane * 16u, win_off(W_PART(w >> 2)) + (unsigned)(red >> 1) * 16384u + (unsigned)(red & 1) * 4096u + (unsigned)(w & 3) * 1024u, 16);
            ST16(sv, xr, (unsigned)lane * 16u, km_win(56 + w, h) + (unsigned)j * 1536u, 16);
            ST16(sv, xr, lane < 32 ? (unsigned)lane * 16u + 1024u : OOB, km_win(56 + w, h) + (unsigned)j * 1536u, 16);
            const int s = 8 * w + (lane >> 3);
#pragma unroll
            for (int x = 0; x < 4; ++x) if (s > 0) ST16(sv, xr, win_off(W_MEM(s, x) + (red >> 6)) + (unsigned)(red & 63) * 128u + (unsigned)(lane & 7) * 16u, 0, 16);
        }
        PST(3);
        WAIT_V(0);
#pragma unroll
        for (int T = 0; T < 2; ++T)
#pragma unroll
            for (int t = 0; t < 2; ++t) { W[T][t] = FRAG(my + t * 4096 + T * 32 + toffq, 128); WO[T][t] = FRAG(my + 8192 + (t ^ (j >> 1)) * 4096 + T * 32 + toffo, 256); }
#pragma unroll
        for (int T = 0; T < 2; ++T) asm volatile("" : "+v"(W[T][0]), "+v"(W[T][1]), "+v"(WO[T][0]), "+v"(WO[T][1]));
        WAIT_L(0);
        PST(1);
#pragma unroll
        for (int i = 0; i < 8; ++i) {
            __builtin_amdgcn_global_load_lds((const unsigned*)(p.wk + (size_t)(128 * w + 16 * i + rrow) * 4096 + h * 128 + 32 * j + rc), (LAS unsigned*)(my + i * 1024), 16, 0, KNT2);
            __builtin_amdgcn_global_load_lds((const unsigned*)(p.wv + (size_t)(128 * w + 16 * i + rrow) * 4096 + h * 128 + 32 * j + rc), (LAS unsigned*)(my + 8192 + i * 1024), 16, 0, KNT2);
        }
        WAIT_V(0);
#pragma unroll
        for (int T = 0; T < 4; ++T)
#pragma unroll
            for (int t = 0; t < 2; ++t) W[2 + T][t] = FRAG(my + (T >> 1) * 8192 + t * 4096 + (T & 1) * 32 + toffq, 128);
#pragma unroll
        for (int T = 0; T < 4; ++T) asm volatile("" : "+v"(W[2 + T][0]), "+v"(W[2 + T][1]));
        WAIT_L(0);
#undef FRAG
    }
    PST(2);
    BAR();
    PST(4);
    if (w == 7 && lane < 8) {
        const unsigned* src = (const unsigned*)(p.mem0 + (4 * (red & 1) + 2 * (lane >> 2) + (lane & 1)) * DM + 32 * (red >> 2) + 8 * ((red >> 1) & 1) + 4 * ((lane >> 1) & 1));
        const u32x4 v = {min(src[0], SENT - 1), min(src[1], SENT - 1), min(src[8], SENT - 1), min(src[9], SENT - 1)};
        ST16(v, xr, (unsigned)lane * 16u + (unsigned)(red & 63) * 128u, win_off(W_MEM(0, 0) + (red >> 6)), 16);
    }
    if (w < 5) chain_loop<0>(p, par, rank, w, lds, W, WO, stamps);
    else if (w == 5) chain_loop<1>(p, par, rank, 5, lds, W, WO, stamps);
    else if (w == 6) chain_loop<2>(p, par, rank, 6, lds, W, WO, stamps);
    else chain_loop<3>(p, par, rank, 7, lds, W, WO, stamps);
}

template <bool POLL>
DI void attn_a_item(const Params& p, unsigned par, int s, int h, ldsp my, int lane) {
    const int g = lane >> 4, n = lane & 15;
    const rsrc_t xr = mkrsrc(p.ws + WS_XCH, (unsigned)XCH_BYTES);
    const rsrc_t qr = mkrsrc(p.ws + WH(WS_QKVX + (size_t)h * XH + (size_t)s * 16384), 65u * XH);
#pragma unroll
    for (int i = 0; i < 8; ++i)
        __builtin_amdgcn_raw_ptr_buffer_load_lds(qr, (LAS void*)(my + i * 1024), 16, (unsigned)(g * 256 + (n ^ ((g & 1) * 8)) * 16), 64u * XH + (unsigned)i * 1024u, 0, 16);
    u32x4 Q[2][4], K[2][4], XM[4];
    const unsigned ro = (unsigned)lane * 16u;
#pragma unroll
    for (int t = 0; t < 2; ++t)
#pragma unroll
        for (int k = 0; k < 4; ++k) { Q[t][k] = ld16_sc1(qr, ro + k * 1024, (unsigned)t * 4096u); K[t][k] = ld16_sc1(qr, ro + k * 1024, (unsigned)t * 4096u + 32u * XH); }
    const unsigned qwin = km_win(s, h);
    u32x4 VM[2];
    if (POLL) {
        wait_count<4>((unsigned*)(p.ws + WS_CTL), C_PROG, (unsigned)s + (s < 56 ? 2u : 1u));
        if (s >= 56) __builtin_amdgcn_s_sleep(16);
    }
    for (unsigned spins = 0;;) {
        asm volatile("" ::: "memory");
#pragma unroll
        for (int k = 0; k < 4; ++k) XM[k] = ld16_sc1(xr, (unsigned)lane * 16u + (k & 1) * 1536, qwin + (k >> 1) * 3072);
#pragma unroll
        for (int ts = 0; ts < 2; ++ts) VM[ts] = ld16_sc1(xr, (unsigned)(g >> 1) * 1536u + 1024u + (unsigned)(lane & 31) * 16u, qwin + ts * 3072);
        if (!POLL || s < 56) break;
        if (all_set(max(max(max(umax4(XM[0]), umax4(XM[1])), max(umax4(XM[2]), umax4(XM[3]))), max(umax4(VM[0]), umax4(VM[1]))))) break;
        __builtin_amdgcn_s_sleep(4);
        if (spin_out(spins, (unsigned*)(p.ws + WS_CTL))) break;
    }
    SCHED();
#pragma unroll
    for (int ts = 0; ts < 2; ++ts) {
        const u32x4 v = VM[ts];
        const int m = 2 * (n >> 2) + (n & 1), hf = (n >> 1) & 1;
#pragma unroll
        for (int i = 0; i < 4; ++i) { const int e = 64 * ts + 16 * g + 4 * i + 2 * hf;
            *(LAS unsigned*)(my + 8192 + (e >> 3) * 256 + (m ^ (e & 8)) * 16 + (e & 7) * 2) = v[i]; }
    }
    const float csc = 0.08838834764831845f * 1.4426950408889634f, ninf = -__builtin_inff();
    u32x4 PX[2]; u32x2 PM[2];
#pragma unroll
    for (int qt = 0; qt < 2; ++qt) {
        f32x4 sm = {0.f, 0.f, 0.f, 0.f}, s0 = sm, s1 = sm;
#pragma unroll
        for (int k = 0; k < 4; ++k) { sm = mma32(asbf(XM[k]), asbf(Q[qt][k]), sm); s0 = mma32(asbf(K[0][k]), asbf(Q[qt][k]), s0); if (qt == 1) s1 = mma32(asbf(K[1][k]), asbf(Q[qt][k]), s1); }
#pragma unroll
        for (int i = 0; i < 4; ++i) {
            if (g < 2) sm[i] = ninf;
            if (qt == 0) { if (4 * g + i > n) s0[i] = ninf; s1[i] = ninf; }
            else if (4 * g + i > n) s1[i] = ninf;
        }
        float m = fmaxf(fmaxf(fmaxf(s0[0], s0[1]), fmaxf(s0[2], s0[3])), fmaxf(fmaxf(s1[0], s1[1]), fmaxf(s1[2], s1[3])));
        m = fmaxf(m, fmaxf(fmaxf(sm[0], sm[1]), fmaxf(sm[2], sm[3])));
        m = rows_max(m);
        const float mc = -m * csc; float sum = 0.f;
#pragma unroll
        for (int i = 0; i < 4; ++i) { s0[i] = __builtin_amdgcn_exp2f(fmaf(s0[i], csc, mc)); s1[i] = __builtin_amdgcn_exp2f(fmaf(s1[i], csc, mc)); sm[i] = __builtin_amdgcn_exp2f(fmaf(sm[i], csc, mc));
            sum += (s0[i] + s1[i]) + sm[i]; }
        sum = rows_sum(sum);
        const float inv = __builtin_amdgcn_rcpf(sum);
        PX[qt] = (u32x4){pk_bf16(s0[0] * inv, s0[1] * inv), pk_bf16(s0[2] * inv, s0[3] * inv), pk_bf16(s1[0] * inv, s1[1] * inv), pk_bf16(s1[2] * inv, s1[3] * inv)};
        PM[qt] = (u32x2){pk_bf16(sm[0] * inv, sm[1] * inv), pk_bf16(sm[2] * inv, sm[3] * inv)};
    }
    asm volatile("s_waitcnt vmcnt(0) lgkmcnt(0)" ::: "memory");
    const int q4 = n >> 2, p4 = n & 3;
    const ldsp vx = my + (p4 >> 1) * 256 + ((4 * g + q4) ^ ((p4 >> 1) * 8)) * 16 + (p4 & 1) * 8, vmm = my + 8192 + (p4 >> 1) * 256 + ((4 * (g & 1) + q4) ^ ((p4 >> 1) * 8)) * 16 + (p4 & 1) * 8;
    const rsrc_t orr = mkrsrc(p.ws + WH(WS_OA + (size_t)s * 524288 + gap((unsigned)h * 4096u)), 524288u);
    const unsigned oo = frag_off(lane);
    bf16x8 VA[8]; s16x4 VMf[8];
#pragma unroll
    for (int et = 0; et < 8; ++et) { VA[et] = cat8(trd(vx + et * 512), trd(vx + 4096 + et * 512)); VMf[et] = trd(vmm + et * 512); }
    SCHED();
#pragma unroll
    for (int ep = 0; ep < 4; ++ep) {
        f32x4 d[2][2];
#pragma unroll
        for (int e2 = 0; e2 < 2; ++e2)
#pragma unroll
            for (int qt = 0; qt < 2; ++qt) d[qt][e2] = mma16(__builtin_bit_cast(u32x2, VMf[2 * ep + e2]), PM[qt], mma32(VA[2 * ep + e2], asbf(PX[qt]), (f32x4){0.f, 0.f, 0.f, 0.f}));
#pragma unroll
        for (int qt = 0; qt < 2; ++qt) store_frag(orr, oo + ep * 1024, (unsigned)qt * 262144u, d[qt][0], d[qt][1]);
    }
    asm volatile("s_waitcnt lgkmcnt(0)" ::: "memory");
}

DI void attn_b_item(const Params& p, unsigned par, int hq, int qb, ldsp lds) {
    const int tid = fresh_tid(), w = __builtin_amdgcn_readfirstlane(tid >> 6), lane = tid & 63, g = lane >> 4, n = lane & 15;
    const int q0 = qb * 128;
    const rsrc_t br = mkrsrc(p.ws + WH(WS_QKVB + (size_t)hq * 4 * BH), 68u * BH);
    const rsrc_t orr = mkrsrc(p.ws + WH(WS_OB + (size_t)qb * 524288), 524288u);
    constexpr int BUF = 57344;
    unsigned to[3];
#pragma unroll
    for (int i = 0; i < 3; ++i) to[i] = gap((unsigned)max(8 * qb - 16 + w * 3 + i, 0) * 1024u);
    const unsigned l16 = (unsigned)lane * 16u, vsw = (unsigned)(g * 256 + (n ^ ((g & 1) * 8)) * 16);
    auto stage = [&](int i) {
        const unsigned so = (unsigned)i * BH; ldsp d = lds + (i & 1) * BUF;
#pragma unroll
        for (int k = 0; k < 3; ++k) {
            __builtin_amdgcn_raw_ptr_buffer_load_lds(br, (LAS void*)(d + (w * 3 + k) * 1024), 16, l16, so + 32u * BH + to[k], 0, 16);
            __builtin_amdgcn_raw_ptr_buffer_load_lds(br, (LAS void*)(d + 24576 + (w * 3 + k) * 1024), 16, vsw, so + 64u * BH + to[k], 0, 16);
        }
        __builtin_amdgcn_raw_ptr_buffer_load_lds(br, (LAS void*)(d + 49152 + w * 1024), 16, l16, so + (unsigned)(qb * 16384 + w * 1024), 0, 16);
    };
    const float ninf = -__builtin_inff(), csc = 0.17677669529663687f * 1.4426950408889634f;
    const int tmin = (256 - q0 - 16 * w) >> 4;
    const int q4 = n >> 2, p4 = n & 3;
    const int kq = w * 1024 + lane * 16;
    const unsigned vb = (unsigned)(size_t)lds + 24576u + (unsigned)(w * 1024 + (p4 >> 1) * 256 + ((4 * g + q4) ^ ((p4 >> 1) * 8)) * 16 + (p4 & 1) * 8);
    stage(0);
#pragma unroll 1
    for (int i = 0; i < 4; ++i) {
        if (i < 3) { stage(i + 1); WAIT_V(7); } else WAIT_V(0);
        BAR(); asm volatile("" ::: "memory");
        const ldsp bp = lds + (i & 1) * BUF;
        const bf16x8 Q = *(const LAS bf16x8*)(bp + 49152 + kq);
        f32x4 sc[17]; bf16x8 KF[17];
#pragma unroll
        for (int t = 0; t < 17; ++t) KF[t] = *(const LAS bf16x8*)(bp + kq + t * 1024);
        SCHED();
#pragma unroll
        for (int t = 0; t < 17; ++t) sc[t] = mma32(KF[t], Q, (f32x4){0.f, 0.f, 0.f, 0.f});
#pragma unroll
        for (int k = 0; k < 4; ++k) { if (4 * g + k <= n) sc[0][k] = ninf; if (4 * g + k > n) sc[16][k] = ninf; }
        if (tmin > 0) {
#pragma unroll
            for (int t = 0; t < 16; ++t) if (t < tmin) sc[t] = (f32x4){ninf, ninf, ninf, ninf};
        }
        float mq[4];
#pragma unroll
        for (int k = 0; k < 4; ++k) {
            mq[k] = max3f(sc[16][k], sc[k][0], sc[k][1]); mq[k] = max3f(mq[k], sc[k][2], sc[k][3]);
#pragma unroll
            for (int t = 4 + k; t < 16; t += 4) mq[k] = max3f(max3f(mq[k], sc[t][0], sc[t][1]), sc[t][2], sc[t][3]);
        }
        const float m = rows_max(max3f(max3f(mq[0], mq[1], mq[2]), mq[3], mq[3]));
        const float mc = -m * csc;
        f32x4 sum4 = {0.f, 0.f, 0.f, 0.f};
#pragma unroll
        for (int t = 0; t < 17; ++t) {
            sc[t] = sc[t] * csc + mc;
#pragma unroll
            for (int k = 0; k < 4; ++k) sc[t][k] = __builtin_amdgcn_exp2f(sc[t][k]);
            sum4 += sc[t];
        }
        const float inv = __builtin_amdgcn_rcpf(rows_sum((sum4[0] + sum4[1]) + (sum4[2] + sum4[3])));
        const unsigned v0 = vb + (unsigned)((i & 1) * BUF);
        s16x4 V0[17], V1[17];
#define VRD(t) TRD(V0[t], v0, (t) * 1024); TRD(V1[t], v0, (t) * 1024 + 512)
        VRD(0); VRD(1); VRD(2); VRD(3); VRD(4); VRD(5); VRD(6); VRD(7); VRD(8); VRD(9); VRD(10); VRD(11); VRD(12); VRD(13); VRD(14); VRD(15); VRD(16);
#undef VRD
        u32x2 P[17];
#pragma unroll
        for (int t = 0; t < 17; ++t) P[t] = (u32x2){pk_bf16(sc[t][0], sc[t][1]), pk_bf16(sc[t][2], sc[t][3])};
        WAIT_L(0); SCHED();
        f32x4 d0 = {0.f, 0.f, 0.f, 0.f}, d1 = d0;
#pragma unroll
        for (int tp = 0; tp < 8; ++tp) {
            const bf16x8 PP = asbf((u32x4){P[2 * tp].x, P[2 * tp].y, P[2 * tp + 1].x, P[2 * tp + 1].y});
            d0 = mma32(cat8(V0[2 * tp], V0[2 * tp + 1]), PP, d0); d1 = mma32(cat8(V1[2 * tp], V1[2 * tp + 1]), PP, d1);
        }
        d0 = mma16(__builtin_bit_cast(u32x2, V0[16]), P[16], d0); d1 = mma16(__builtin_bit_cast(u32x2, V1[16]), P[16], d1);
        BAR();
        store_frag(orr, frag_off(lane), (unsigned)w * 65536u + gap((unsigned)(hq * 4 + i) * 1024u), d0 * inv, d1 * inv);
    }
    WAIT_V(0);
    signal((unsigned*)(p.ws + WS_CTL), C_OB + qb * 32, 1);
}

DI void out_tail(const Params& p, unsigned par, int c, int q, ldsp lds) {
    const int tid = fresh_tid(), w = __builtin_amdgcn_readfirstlane(tid >> 6), lane = tid & 63, g = lane >> 4, n = lane & 15;
    unsigned* ctl = (unsigned*)(p.ws + WS_CTL);
    bf16x8 WT[16][2];
    {
        ldsp my = lds + w * 16384;
        const int toff = (8 * g + (n >> 2)) * 64 + (n & 3) * 8;
#pragma unroll
        for (int r = 0; r < 2; ++r) {
#pragma unroll
            for (int i = 0; i < 16; ++i)
                __builtin_amdgcn_global_load_lds((const unsigned*)(p.wo + (size_t)(512 * w + 256 * r + 16 * i + (lane >> 2)) * 1024 + 32 * c + (lane & 3) * 8), (LAS unsigned*)(my + i * 1024), 16, 0, 0);
            WAIT_V(0);
#pragma unroll
            for (int k = 0; k < 8; ++k)
#pragma unroll
                for (int ct = 0; ct < 2; ++ct) { WT[8 * r + k][ct] = cat8(trd(my + k * 2048 + ct * 32 + toff), trd(my + k * 2048 + ct * 32 + toff + 256)); }
#pragma unroll
            for (int k = 0; k < 8; ++k) asm volatile("" : "+v"(WT[8 * r + k][0]), "+v"(WT[8 * r + k][1]));
            WAIT_L(0);
        }
    }
    BAR();
#pragma unroll 1
    for (int hs = q; hs < 2 * (NSEG - S_TAIL); hs += 2) {
        const int s = S_TAIL + (hs >> 1), rt = hs & 1;
        if (w == 0) wait_count<3>(ctl, C_OS + (s - S_TAIL) * 128 + (c >> 3) * 32, 4u);
        asm volatile("" ::: "memory"); BAR(); asm volatile("" ::: "memory");
        const rsrc_t ar = mkrsrc(p.ws + WH(WS_OA + (size_t)(2 * s + rt) * 262144 + (size_t)w * 32768), 32768u);
        const unsigned ao = (unsigned)lane * 16u;
        u32x4 A[16];
#pragma unroll
        for (int k = 0; k < 16; ++k) A[k] = ld16_sc1(ar, ao + (k & 3) * 1024, gap((unsigned)(k >> 2) * 4096u));
        SCHED();
#pragma unroll
        for (int ct = 0; ct < 2; ++ct) {
            f32x4 acc = {0.f, 0.f, 0.f, 0.f};
#pragma unroll
            for (int k = 0; k < 16; ++k) acc = mma32(WT[k][ct], asbf(A[k]), acc);
            *(LAS f32x4*)(lds + w * 2048 + ct * 1024 + lane * 16) = acc;
        }
        LDS_BAR();
        if (w < 2) {
            f32x4 r[8];
#pragma unroll
            for (int ww = 0; ww < 8; ++ww) r[ww] = *(const LAS f32x4*)(lds + ww * 2048 + w * 1024 + lane * 16);
            SCHED();
            const f32x4 t = ((r[0] + r[1]) + (r[2] + r[3])) + ((r[4] + r[5]) + (r[6] + r[7]));
            *(u32x2*)(p.out + (size_t)(32 * s + 16 * rt + n) * 2048 + 32 * c + 16 * w + 4 * g) = (u32x2){pk_bf16(t[0], t[1]), pk_bf16(t[2], t[3])};
        }
        WAIT_L(0);
    }
    BAR();
}

DI void big_unit(const Params& p, unsigned par, int id, Unit& u) {
    u.lda = DM * 2; u.nt = 16; u.ak = 128; u.ah = 128 * DM * 2;
    if (id < 384) {
        const int pm = id & 7, pn = id >> 3;
        const u16 *w0 = p.wq, *w1 = p.wk, *w2 = p.wv; const u16* W = pn < 16 ? w0 : (pn < 32 ? w1 : w2);
        u.A = (const char*)(p.ax + (size_t)pm * 256 * DM); u.B = (const char*)(W + (pn & 15) * 256); u.ldb = 4096 * 2;
        u.C = p.ws + WH(WS_QKVX + (size_t)(2 * pn) * XH + (size_t)pm * 131072); u.ldc = 256; u.cw = 1024; u.ch = XH;
        if (pn < 16) { u.sig = C_Q + pm * 32; u.nsig = 1; } else { u.sig = C_KVC(2 * pm, 2 * (pn & 15)); u.nsig = 4; }
    } else if (id < 480) {
        const int pm = (id - 384) & 7, pn = (id - 384) >> 3;
        u.A = (const char*)(p.bx + (size_t)pm * 256 * DM); u.B = (const char*)(p.bwqkv + pn * 256); u.ldb = LDB3 * 2;
        u.C = p.ws + WH(WS_QKVB + (size_t)(8 * pn) * BH + (size_t)pm * 32768); u.ldc = 64; u.cw = BH; u.ch = 4 * BH; u.sig = C_B + pm * 32; u.nsig = 1;
    } else {
        const int pm = (id - 480) >> 2, pn = id & 3;
        u.lda = 0; u.ak = 2048; u.ah = 524288u;
        u.A = p.ws + WH(WS_OB + (size_t)pm * 1048576); u.B = (const char*)(p.bwo + pn * 256); u.ldb = DM * 2;
        u.C = (char*)(p.out + (size_t)pm * 256 * 2048 + 1024 + pn * 256); u.ldc = 4096; u.cw = 64; u.ch = 256; u.sig = 0; u.nsig = 0;
    }
}
struct BigSched { const Params& p; unsigned par; int id0, id1, id2; DI bool next(int i, Unit& u) const { const int id = i == 0 ? id0 : (i == 1 ? id1 : (i == 2 ? id2 : -1)); if (id < 0) return false; big_unit(p, par, id, u); return true; } };

__global__ __launch_bounds__(512, 2) void k_fused(Params p) {
    extern __shared__ __attribute__((aligned(16))) unsigned char shm[];
    ldsp lds = (ldsp)shm;
    unsigned* ctl = (unsigned*)(p.ws + WS_CTL);
    const int b = (blockIdx.x & ~7) | ((blockIdx.x + __builtin_amdgcn_readfirstlane(ctl[C_PHASE])) & 7), id = (b & 3) * 32 + (b >> 3);
#define WSTAMP(code)
    const unsigned par = (unsigned)__builtin_amdgcn_readfirstlane(ctl[C_PAR + 1]) < (unsigned)__builtin_amdgcn_readfirstlane(ctl[C_PAR]) ? 1u : 0u;
    WSTAMP(1);
    if ((b & 7) < 4) {
        chain_role(p, par, id, lds, (unsigned long long*)(p.ws + WS_END));
        BAR();
        WSTAMP(2);
    } else {
#pragma unroll 1
        for (int r = 0; r < 2; ++r) {
            const int rb = 2 * r + (id & 1), ct = id >> 1;
            const u16 *w1 = p.wk, *w2 = p.wv;
            Unit u; u.A = (const char*)(p.ax + (size_t)rb * 128 * DM); u.lda = DM * 2; u.B = (const char*)((ct < 32 ? w1 : w2) + (ct & 31) * 128); u.ldb = 4096 * 2; u.nt = 16;
            u.C = p.ws + WH(WS_QKVX + (size_t)(32 + ct) * XH + (size_t)rb * 65536); u.ldc = 256; u.cw = 1024; u.ch = 0; u.ak = 128; u.ah = 0;
            u.sig = C_KVC(rb, ct & 31); u.nsig = 1;
            gemm_small(lds, u, ctl);
            WSTAMP(3);
        }
        if ((id & 31) >= 24) {
            const int t = fresh_tid(), wv = __builtin_amdgcn_readfirstlane(t >> 6), q = (id >> 5) * 8 + (id & 7);
#pragma unroll 1
            for (int i = 0; i < 64; ++i) {
                const char* src = i < 16 ? (const char*)p.ax + (size_t)q * 131072 + i * 8192 : i < 32 ? (const char*)p.bx + (size_t)q * 131072 + (i - 16) * 8192
                                : i < 56 ? (const char*)p.bwqkv + (size_t)q * 196608 + (i - 32) * 8192 : (const char*)p.bwo + (size_t)q * 65536 + (i - 56) * 8192;
                __builtin_amdgcn_global_load_lds((const unsigned*)(src + t * 16), (LAS unsigned*)(lds + L_SCR + wv * 1024), 16, 0, 0);
            }
        }
    }
    volatile LAS unsigned* qw = (volatile LAS unsigned*)(lds + L_QW);
    const int w = __builtin_amdgcn_readfirstlane(threadIdx.x >> 6);
    const int k = b & 3;
    bool fixed = (b & 7) >= 4;
#pragma unroll 1
    for (;;) {
        int kind, x, it = -1, x2 = -1, x3 = -1;
        if (fixed) { const int i = id & 31; kind = 6; x = i < 24 ? (16 + 8 * k + (i & 7)) * 8 + 2 + (i >> 3) : (4 * k + (i & 3)) * 8 + ((i >> 2) & 1); x2 = x + (i < 24 ? 3 : 2); x3 = i < 24 ? 384 + 24 * k + i : x + 4; fixed = false; }
        else {
        if (threadIdx.x == 0) qw[0] = __hip_atomic_fetch_add(ctl + C_HEAD + k * 32, 1u, RLX_AGENT) + 1u;
        LDS_BAR();
        it = (int)__builtin_amdgcn_readfirstlane(qw[0]);
        LDS_BAR();
        if (it < 6) { kind = 0; x = it * 4 + k; }
        else if (it < 38) { kind = 1; x = ((it - 6) >> 1) * 8 + 2 * k + ((it - 6) & 1); }
        else if (it < 40) { kind = 0; x = (it - 38 + 6) * 4 + k; }
        else if (it < 44) { kind = 4; x = (it - 40) * 4 + k; }
        else if (it < 52) { kind = 6; x = (4 * k + ((it - 44) & 3)) * 8 + 6 + ((it - 44) >> 2); }
        else if (it < 58) { kind = 2; x = it - 52; }
        else if (it < 66) { kind = 6; x = 480 + (it - 58) * 4 + k; }
        else if (it < 71) { kind = 2; x = 6 + it - 66; }
        else if (it < 87) { kind = 5; x = ((it - 71) >> 3) * 32 + 8 * k + ((it - 71) & 7); }
        else if (it < 91) { kind = 3; x = (11 + ((it - 87) >> 1)) * 8 + 2 * k + ((it - 87) & 1); }
        else break;
        }
        if (kind == 6) {
            if (x >= 480) { const int w0 = C_OB + ((x - 480) >> 2) * 64; wg_wait(ctl, w0, 64u, w0 + 32, 64u); }
            BigSched S{p, par, x, x2, x3};
            gemm_stream(lds, S, ctl);
        } else
        if (kind == 0) {
            const int s0 = (x >> 2) * 4;
            wait_count(ctl, C_Q + (s0 >> 3) * 32, 128u); wait_count(ctl, C_PROG, (unsigned)s0 + 5u);
            asm volatile("" ::: "memory");
#pragma unroll 1
            for (int s = s0; s < s0 + 4; ++s) attn_a_item<false>(p, par, s, (x & 3) * 8 + w, lds + w * 12288, fresh_tid() & 63);
            WAIT_V(0); signal(ctl, C_OA + (x >> 2) * 32, 1);
            BAR();
        } else if (kind == 4) {
#pragma unroll 1
            for (int s = 32 + (x >> 2); s < NSEG; s += 4) {
                wait_count(ctl, C_Q + (s >> 3) * 32, 128u); wait_count(ctl, C_KVC(s >> 2, (x & 3) * 8 + w), 16u);
                asm volatile("" ::: "memory");
                attn_a_item<true>(p, par, s, (x & 3) * 8 + w, lds + w * 12288, fresh_tid() & 63);
                WAIT_V(0);
                if (s < S_TAIL) signal(ctl, C_OA + (s >> 2) * 32, 1);
                else { BAR(); const int l = fresh_tid(); if (l < 4) __hip_atomic_fetch_add(ctl + C_OS + (s - S_TAIL) * 128 + l * 32, 1u, RLX_AGENT); }
            }
            BAR();
        } else if (kind == 1) {
            const int qb = x >> 3;
            wait_count(ctl, C_B + (qb >> 1) * 32, 96u); wait_count(ctl, C_B + max((qb >> 1) - 1, 0) * 32, 96u);
            asm volatile("" ::: "memory");
            attn_b_item(p, par, x & 7, qb, lds);
        } else if (kind == 5) {
            out_tail(p, par, x & 31, x >> 5, lds);
        } else if (kind == 3) {
            const int rb = x >> 3, ct = x & 7;
            Unit u; u.ldb = DM * 2; u.ldc = 2048 * 2; u.sig = 0; u.nsig = 0; u.lda = 0; u.ak = 2048; u.ah = 0; u.cw = 64; u.ch = 0;
            u.A = p.ws + WH(WS_OA + (size_t)rb * 2097152); u.B = (const char*)(p.wo + ct * 128); u.nt = 64; u.C = (char*)(p.out + (size_t)rb * 128 * 2048 + ct * 128);
            wg_wait(ctl, C_OA + rb * 32, 128u, C_OA + rb * 32, 128u);
            gemm_small(lds, u, ctl);
        } else {
            wg_wait(ctl, C_OA + x * 32, x < 8 ? 32u : 128u, C_OA + x * 32, 32u);
            gemm_wide(lds, p.ws + WH(WS_OA + (size_t)x * 2097152), (const char*)(p.wo + k * 256), (char*)(p.out + (size_t)x * 128 * 2048 + k * 256), 64);
        }
        WSTAMP(16 + (kind == 6 && x >= 480 ? (x < 512 ? 7 : 8) : kind) + ((it & 0xffff) << 8));
    }
}

__global__ __launch_bounds__(512) void k_init(char* ws) {
    const rsrc_t xr = mkrsrc(ws + WS_XCH, NWIN_INIT * 16384u);
    unsigned* ctl = (unsigned*)(ws + WS_CTL);
    const u32x4 sv = {SENT, SENT, SENT, SENT};
    const unsigned xcc = (unsigned)__builtin_amdgcn_s_getreg(20 | (3 << 11)) & 7u, half = blockIdx.x >> 3, win = blockIdx.x & 7;
    if (threadIdx.x < 64 && xcc == 1) {
        unsigned b = ~0u;
        for (int i = 0; i < 5; ++i) {
            const unsigned long long t0 = __builtin_amdgcn_s_memtime();
            __builtin_amdgcn_raw_buffer_store_b32(0u, xr, (unsigned)(2 * 16384 + C_PROBE * 4) + half * 8192u + (unsigned)i * 256u, 0, 16);
            asm volatile("s_waitcnt vmcnt(0)" ::: "memory");
            b = min(b, (unsigned)(__builtin_amdgcn_s_memtime() - t0));
        }
        if (threadIdx.x == 0) ctl[C_PAR + half] = b;
    }
    __syncthreads();
    if (win < 7 && win != 5) ST16(sv, xr, win * 16384u + half * 8192u + threadIdx.x * 16u, 0, 16);
    if (blockIdx.x == 0 && threadIdx.x == 0) ctl[C_PHASE] = xcc;
}

extern "C" void kernel_launch(void* const* d_in, const int* in_sizes, int n_in, void* d_out, int out_size, void* d_ws, size_t ws_size, hipStream_t stream) {
    Params p;
    p.ax = (const u16*)d_in[0]; p.mem0 = (const u16*)d_in[1]; p.wq = (const u16*)d_in[2]; p.wk = (const u16*)d_in[3]; p.wv = (const u16*)d_in[4]; p.wo = (const u16*)d_in[5];
    p.bx = (const u16*)d_in[7]; p.bwqkv = (const u16*)d_in[8]; p.bwo = (const u16*)d_in[9];
    p.out = (u16*)d_out; p.ws = (char*)(((uintptr_t)d_ws + WS_SHIFT(ws_size) + 0x1FFFFFu) & ~(uintptr_t)0x1FFFFFu);
    (void)hipFuncSetAttribute((const void*)k_fused, hipFuncAttributeMaxDynamicSharedMemorySize, LDS_BYTES);
    k_init<<<16, 512, 0, stream>>>(p.ws);
    k_fused<<<256, 512, LDS_BYTES, stream>>>(p);
}
```

```cpp
#include <hip/hip_runtime.h>
#include <stdint.h>

#define LAS __attribute__((address_space(3)))
#define DI __device__ __forceinline__
typedef unsigned short u16;
typedef __attribute__((ext_vector_type(8))) __bf16 bf16x8;
typedef __attribute__((ext_vector_type(16))) __bf16 bf16x16;
typedef __attribute__((ext_vector_type(2))) __bf16 bf16x2;
typedef __attribute__((ext_vector_type(4))) short s16x4;
typedef __attribute__((ext_vector_type(8))) short s16x8;
typedef __attribute__((ext_vector_type(16))) short s16x16;
typedef __attribute__((ext_vector_type(2))) float f32x2;
typedef __attribute__((ext_vector_type(4))) float f32x4;
typedef __attribute__((ext_vector_type(2))) unsigned u32x2;
typedef __attribute__((ext_vector_type(4))) unsigned u32x4;
typedef LAS unsigned char* ldsp;
typedef __amdgpu_buffer_rsrc_t rsrc_t;

constexpr int S_LEN = 2048, DM = 1024, NH = 32, DH = 128, NSEG = 64;
constexpr int LDB3 = 3072, S_TAIL = 52;
constexpr unsigned XH = S_LEN * 512, BH = S_LEN * 128;
constexpr unsigned SENT = 0xFFFFFFFFu;
constexpr size_t WS_XCH = 2u << 20;
constexpr int NWIN = 640, NWIN_INIT = 7;
constexpr size_t XCH_BYTES = (size_t)(NWIN + 2048 + 512) * 16384;
constexpr size_t WS_CTL = WS_XCH + 2 * 16384;
constexpr int C_TMO = 0, C_PAR = 13312  , C_PHASE = 13314, C_HEAD = 3072, C_PROG = 192, C_PACE = 128, C_Q = 256, C_B = C_Q + 8 * 32, C_OA = C_B + 8 * 32, C_OB = C_OA + 16 * 32, C_OS = 16384, C_KV = 4096, C_PROBE = 12288;
constexpr size_t WS_QKVX = 52u << 20;
constexpr size_t WS_QKVB = WS_QKVX + (size_t)96 * XH;
constexpr size_t WS_OA = WS_QKVB + (size_t)96 * BH;
constexpr size_t WS_OB = WS_OA + (size_t)S_LEN * 4096 * 4;
constexpr size_t WS_END = WS_OB + (size_t)S_LEN * 1024 * 4;
#define C_KVC(rb, h) (C_KV + (h) * 32 + (rb))
#define gap(L) (((L) & 8191u) | (((L) >> 13) << 14))
#define WH(off) ((off) + (size_t)((((unsigned)((off) >> 21) ^ par) & 1u) << 13))
constexpr int L_QW = 147456, LDS_BYTES = L_QW + 1024 + 8192;

DI unsigned pk_bf16(float a, float b) { f32x2 v = {a, b}; return __builtin_bit_cast(unsigned, __builtin_convertvector(v, bf16x2)); }
DI s16x4 trd(ldsp p) { return __builtin_amdgcn_ds_read_tr16_b64_v4i16((LAS s16x4*)p); }
DI bf16x8 cat8(s16x4 a, s16x4 b) { return __builtin_bit_cast(bf16x8, __builtin_shufflevector(a, b, 0, 1, 2, 3, 4, 5, 6, 7)); }
DI bf16x16 cat16(bf16x8 a, bf16x8 b) { return __builtin_shufflevector(a, b, 0, 1, 2, 3, 4, 5, 6, 7, 8, 9, 10, 11, 12, 13, 14, 15); }
DI bf16x8 lo8(bf16x16 v) { return __builtin_shufflevector(v, v, 0, 1, 2, 3, 4, 5, 6, 7); }
DI bf16x8 hi8(bf16x16 v) { return __builtin_shufflevector(v, v, 8, 9, 10, 11, 12, 13, 14, 15); }
DI bf16x8 asbf(u32x4 v) { return __builtin_bit_cast(bf16x8, v); }
DI f32x4 mma32(bf16x8 a, bf16x8 b, f32x4 c) { return __builtin_amdgcn_mfma_f32_16x16x32_bf16(a, b, c, 0, 0, 0); }
DI f32x4 mma16(u32x2 a, u32x2 b, f32x4 c) { return mma32(asbf((u32x4){a.x, a.y, 0u, 0u}), asbf((u32x4){b.x, b.y, 0u, 0u}), c); }
DI f32x4 smma(u32x4 a, bf16x16 b, f32x4 c, int idx) { return __builtin_amdgcn_smfmac_f32_16x16x64_bf16(asbf(a), b, c, idx, 0, 0); }
DI rsrc_t mkrsrc(const void* p, unsigned bytes) { return __builtin_amdgcn_make_buffer_rsrc((void*)p, 0, bytes, 0x00020000); }
DI u32x4 ld16_sc1(rsrc_t r, unsigned voff, unsigned soff) { return __builtin_amdgcn_raw_buffer_load_b128(r, voff, soff, 16); }
#define ST16(v, r, voff, soff, aux) do { const u32x4 st_v = (v); __builtin_amdgcn_raw_buffer_store_b128(st_v, r, voff, soff, aux); asm volatile("s_nop 1" :: "v"(st_v)); } while (0)
DI void swap16(unsigned& a, unsigned& b) { auto r = __builtin_amdgcn_permlane16_swap(a, b, false, false); a = r[0]; b = r[1]; }
DI void swap32(unsigned& a, unsigned& b) { auto r = __builtin_amdgcn_permlane32_swap(a, b, false, false); a = r[0]; b = r[1]; }
DI void swap16f(float& a, float& b) { unsigned x = __float_as_uint(a), y = __float_as_uint(b); swap16(x, y); a = __uint_as_float(x); b = __uint_as_float(y); }
DI void swap32f(float& a, float& b) { unsigned x = __float_as_uint(a), y = __float_as_uint(b); swap32(x, y); a = __uint_as_float(x); b = __uint_as_float(y); }
DI float rows_max(float v) { float a = v, b = v; swap16f(a, b); v = fmaxf(a, b); a = v; b = v; swap32f(a, b); return fmaxf(a, b); }
DI float rows_sum(float v) { float a = v, b = v; swap16f(a, b); v = a + b; a = v; b = v; swap32f(a, b); return a + b; }
DI unsigned umax4(u32x4 v) { return max(max(v.x, v.y), max(v.z, v.w)); }
#define WAIT_V(n) asm volatile("s_waitcnt vmcnt(" #n ")" ::: "memory")
#define WAIT_L(n) asm volatile("s_waitcnt lgkmcnt(" #n ")" ::: "memory")
#define BAR() __builtin_amdgcn_s_barrier()
#define SCHED() __builtin_amdgcn_sched_barrier(0)
#define LDS_BAR() do { asm volatile("s_waitcnt lgkmcnt(0)" ::: "memory"); __builtin_amdgcn_s_barrier(); asm volatile("" ::: "memory"); } while (0)

constexpr int HTB = 128 * 64 * 2;
DI int lds_byte(int r, int c) { const int st = (r >> 4) * 2 + (c >> 5), rr = r & 15, cc = c & 31, ob = rr * 64 + cc * 2; return st * 1024 + (ob ^ (((ob >> 9) & 1) << 5)); }
DI void stage_rc(int b, int& R, int& C) { const int st = b / 1024, sb = b % 1024, swz = sb ^ (((sb >> 9) & 1) << 5); R = (st >> 1) * 16 + swz / 64; C = (st & 1) * 32 + (swz % 64) / 2; }

struct Params {
    const u16 *ax, *mem0, *wq, *wk, *wv, *wo, *bx, *bwqkv, *bwo;
    u16* out; char* ws;
};
#define RLX_AGENT __ATOMIC_RELAXED, __HIP_MEMORY_SCOPE_AGENT
DI bool spin_out(unsigned& spins, unsigned* ctl) {
    spins = __builtin_amdgcn_readfirstlane(spins) + 1u;
    if ((spins & 63u) == 0u) {
        if (spins > (1u << 18)) { __hip_atomic_store(ctl + C_TMO, 1u, RLX_AGENT); return true; }
        if (__builtin_amdgcn_readfirstlane(__hip_atomic_load(ctl + C_TMO, RLX_AGENT)) != SENT) return true;
    }
    return false;
}
template <int NAP = 8>
DI void wait_count(unsigned* ctl, int word, unsigned need) {
    unsigned spins = 0;
    while (__builtin_amdgcn_readfirstlane(__hip_atomic_load(ctl + word, RLX_AGENT)) + 1u < need) { __builtin_amdgcn_s_sleep(NAP); if (spin_out(spins, ctl)) break; }
}
DI void wg_wait(unsigned* ctl, int word0, unsigned need0, int word1, unsigned need1) {
    if (threadIdx.x < 64) {
        wait_count(ctl, word0, need0); wait_count(ctl, word1, need1);
        __builtin_amdgcn_fence(__ATOMIC_ACQUIRE, "agent");
        asm volatile("s_waitcnt vmcnt(0)" ::: "memory");
    }
    asm volatile("" ::: "memory"); BAR(); asm volatile("" ::: "memory");
}
struct Unit { const char* A; const char* B; char* C; unsigned lda, ldb, ldc, cw, ch, ak, ah; int nt, sig, nsig; };
DI int fresh_tid() { int t = threadIdx.x; asm volatile("" : "+v"(t)); return t; }
DI void signal(unsigned* ctl, int sig, int nsig) { const int lane = fresh_tid() & 63; if (lane < nsig) __hip_atomic_fetch_add(ctl + sig + (lane & 1) * 32 + (lane >> 1), 1u, RLX_AGENT); }
DI u32x4 pack_pair(f32x4 v0, f32x4 v1) {
    unsigned ax = pk_bf16(v0[0], v0[1]), ay = pk_bf16(v0[2], v0[3]), bx = pk_bf16(v1[0], v1[1]), by = pk_bf16(v1[2], v1[3]);
    swap16(ax, bx); swap16(ay, by);
    return (u32x4){ax, ay, bx, by};
}
DI unsigned frag_off(int lane) { return (unsigned)((((lane >> 4) & 1) * 2 + (lane >> 5)) * 256 + (lane & 15) * 16); }
DI void store_frag(rsrc_t cr, unsigned voff, unsigned soff, f32x4 v0, f32x4 v1) { ST16(pack_pair(v0, v1), cr, voff, soff, 16); }
DI u32x4 to_rows(ldsp sc, int lane, f32x4 v0, f32x4 v1) {
    *(volatile LAS u32x4*)(sc + ((lane & 15) * 4 + ((lane >> 4) & 1) * 2 + (lane >> 5)) * 16) = pack_pair(v0, v1);
    return *(volatile LAS u32x4*)(sc + lane * 16);
}
constexpr int L_SCR = L_QW + 1024;
#define STAGE(bufoff, gbase, voff, step2) do { \
        __builtin_amdgcn_global_load_lds((const unsigned*)((gbase) + (voff)), (LAS unsigned*)(lds + (bufoff) + ldsw), 16, 0, 0); \
        __builtin_amdgcn_global_load_lds((const unsigned*)((gbase) + (step2) + (voff)), (LAS unsigned*)(lds + (bufoff) + ldsw + 8192), 16, 0, 0); } while (0)
#define LDA(dst, off) do { _Pragma("unroll") for (int m = 0; m < 4; ++m) _Pragma("unroll") for (int k = 0; k < 2; ++k) dst[m][k] = *(const LAS bf16x8*)(lds + (off) + aoff + m * 2048 + k * 1024); } while (0)
#define TRD(dst, base, off) asm volatile("ds_read_b64_tr_b16 %0, %1 offset:%2" : "=v"(dst) : "v"(base), "i"(off) : "memory")
#define LDB(dst, v0, v1, off) do { s16x4 l0, h0, l1, h1, l2, h2, l3, h3; \
        TRD(l0, v0, (off)); TRD(h0, v0, (off) + 256); TRD(l1, v0, (off) + 8192); TRD(h1, v0, (off) + 8448); \
        TRD(l2, v1, (off)); TRD(h2, v1, (off) + 256); TRD(l3, v1, (off) + 8192); TRD(h3, v1, (off) + 8448); \
        dst[0][0] = cat8(l0, h0); dst[0][1] = cat8(l1, h1); dst[1][0] = cat8(l2, h2); dst[1][1] = cat8(l3, h3); } while (0)
#define MMA(accv, At, Bt) do { SCHED(); __builtin_amdgcn_s_setprio(1); _Pragma("unroll") for (int m = 0; m < 4; ++m) _Pragma("unroll") for (int n = 0; n < 2; ++n) _Pragma("unroll") for (int k = 0; k < 2; ++k) \
        accv[m][n] = mma32(Bt[n][k], At[m][k], accv[m][n]); __builtin_amdgcn_s_setprio(0); } while (0)
DI unsigned va_of(int tid, unsigned lda, int nt) { int R, C; stage_rc(tid * 16, R, C); return lda ? (unsigned)R * lda + (unsigned)C * 2u : (unsigned)((tid >> 7) * nt * 4096 + (tid & 127) * 16); }
DI unsigned vb_of(int tid, int wid, unsigned ldb) { return (unsigned)(8 * (wid >> 1) + ((tid >> 2) & 7)) * ldb + (unsigned)((2 * (wid & 1) + ((tid >> 5) & 1)) * 64 + (((tid & 3) ^ (((wid >> 1) & 1) << 1)) * 16)); }
#define GEMM_LANE_SETUP(lda0) \
    const int tid = fresh_tid(); \
    const int wid = __builtin_amdgcn_readfirstlane(tid >> 6), lane = tid & 63, wr = wid >> 2, wc = wid & 3, fr = lane & 15, fq = lane >> 4; \
    const unsigned ldsw = (unsigned)wid * 1024u; \
    const int aoff = (lda0) ? lds_byte(wr * 64 + fr, fq * 8) : wr * 8192 + lane * 16; \
    const int boff0 = fq * 2048 + wc * 512 + (fr >> 2) * 64 + (fq & 1) * 32 + (fr & 3) * 8, boff1 = boff0 ^ 32;

template <class Sched>
DI void gemm_stream(ldsp lds, const Sched& S, unsigned* ctl) {
    Unit cur, nxt; int ui = 0;
    if (!S.next(0, cur)) return;
    GEMM_LANE_SETUP(cur.lda);
#define SA(b, h) (((b) * 2 + (h)) * HTB)
#define SB(b, h) ((4 + (b) * 2 + (h)) * HTB)
    const unsigned vb0 = (unsigned)(size_t)lds + 65536u + (unsigned)boff0, vb1 = (unsigned)(size_t)lds + 65536u + (unsigned)boff1;
    f32x4 acc[2][2][4][2];
#pragma unroll
    for (int a = 0; a < 2; ++a)
#pragma unroll
        for (int b = 0; b < 2; ++b)
#pragma unroll
            for (int m = 0; m < 4; ++m)
#pragma unroll
                for (int n = 0; n < 2; ++n) acc[a][b][m][n] = (f32x4){0.f, 0.f, 0.f, 0.f};
    bf16x8 At[4][2], B0[2][2], B1[2][2];
    const unsigned vA = va_of(tid, cur.lda, cur.nt); unsigned vB = vb_of(tid, wid, cur.ldb);
    const size_t a2s = cur.lda ? (size_t)64 * cur.lda : (size_t)cur.nt * 16384;
    {
        const size_t ah = cur.ah, b2s = (size_t)32 * cur.ldb, bk = (size_t)64 * cur.ldb;
        STAGE(SB(0, 0), cur.B, vB, b2s); STAGE(SB(0, 1), cur.B + 256, vB, b2s); STAGE(SA(0, 0), cur.A, vA, a2s); STAGE(SA(0, 1), cur.A + ah, vA, a2s);
        if (wr == 1) BAR();
        WAIT_V(2); BAR();
        STAGE(SB(1, 0), cur.B + bk, vB, b2s); STAGE(SA(1, 0), cur.A + cur.ak, vA, a2s); STAGE(SB(1, 1), cur.B + bk + 256, vB, b2s);
        WAIT_V(6); BAR();
    }
    int psig = 0, pn = 0;
    for (;;) {
        const bool has_next = S.next(ui + 1, nxt);
        if (!has_next) nxt = cur;
        int nt = cur.nt; asm volatile("" : "+s"(nt));
        for (int t = 0; t < nt; t += 2) {
            const bool last = (t == nt - 2);
            const char* a1 = cur.A + (cur.lda ? (unsigned)(t + 1) * 128u : gap((unsigned)(t + 1) * 2048u)) + cur.ah;
            if (last) vB = vb_of(fresh_tid(), wid, nxt.ldb);
            const unsigned ldb = last ? nxt.ldb : cur.ldb;
            const char* a2 = last ? nxt.A : cur.A + (cur.lda ? (unsigned)(t + 2) * 128u : gap((unsigned)(t + 2) * 2048u));
            const char* b2 = last ? nxt.B : cur.B + (size_t)(t + 2) * 64 * cur.ldb;
            const size_t ah = cur.ah, b2s = (size_t)32 * ldb;
            const char* a3 = a2 + cur.ak; const char* b3 = b2 + (size_t)64 * ldb;
            LDB(B0, vb0, vb1, 0); LDB(B1, vb0, vb1, HTB); SCHED(); LDA(At, SA(0, 0)); STAGE(SA(1, 1), a1, vA, a2s);
            WAIT_V(8); WAIT_L(0); BAR(); MMA(acc[0][0], At, B0); MMA(acc[0][1], At, B1); BAR(); SCHED();
            LDA(At, SA(0, 1)); STAGE(SB(0, 0), b2, vB, b2s); STAGE(SB(0, 1), b2 + 256, vB, b2s); STAGE(SA(0, 0), a2, vA, a2s);
            WAIT_V(8);
            asm volatile("" : "+s"(pn));
            if (pn) { signal(ctl, psig, pn); pn = 0; }
            WAIT_L(0); BAR(); MMA(acc[1][0], At, B0); MMA(acc[1][1], At, B1); BAR(); SCHED();
            LDB(B0, vb0, vb1, 2 * HTB); LDB(B1, vb0, vb1, 3 * HTB); SCHED(); LDA(At, SA(1, 0)); STAGE(SA(0, 1), a2 + ah, vA, a2s);
            WAIT_V(8); WAIT_L(0); BAR(); MMA(acc[0][0], At, B0); MMA(acc[0][1], At, B1); BAR(); SCHED();
            LDA(At, SA(1, 1)); STAGE(SB(1, 0), b3, vB, b2s); STAGE(SB(1, 1), b3 + 256, vB, b2s); STAGE(SA(1, 0), a3, vA, a2s);
            WAIT_V(8); WAIT_L(0); BAR(); MMA(acc[1][0], At, B0); MMA(acc[1][1], At, B1); BAR(); SCHED();
        }
        if (wr == 0) BAR();
        {
            const rsrc_t cr = mkrsrc(cur.C, 0x1000000u);
            const int tz = fresh_tid() & 63;
            if (cur.nsig) {
                const unsigned co = (unsigned)wc * cur.cw + frag_off(tz);
#pragma unroll
                for (int ai = 0; ai < 2; ++ai)
#pragma unroll
                    for (int m = 0; m < 4; ++m)
#pragma unroll
                        for (int bj = 0; bj < 2; ++bj) store_frag(cr, co, gap((unsigned)(ai * 128 + wr * 64 + m * 16) * cur.ldc) + (unsigned)bj * cur.ch, acc[ai][bj][m][0], acc[ai][bj][m][1]);
            } else {
                const unsigned co = (unsigned)(wr * 64 + (tz >> 2)) * cur.ldc + (unsigned)(wc * 64 + (tz & 3) * 16);
#pragma unroll
                for (int ai = 0; ai < 2; ++ai)
#pragma unroll
                    for (int m = 0; m < 4; ++m)
#pragma unroll
                        for (int bj = 0; bj < 2; ++bj)
                            ST16(to_rows(lds + L_SCR + wid * 1024, tz, acc[ai][bj][m][0], acc[ai][bj][m][1]), cr, co, (unsigned)(ai * 128 + m * 16) * cur.ldc + (unsigned)bj * 256u, 0);
            }
        }
        psig = cur.sig; pn = cur.nsig;
        if (!has_next) break;
#pragma unroll
        for (int a = 0; a < 2; ++a)
#pragma unroll
            for (int b = 0; b < 2; ++b)
#pragma unroll
                for (int m = 0; m < 4; ++m)
#pragma unroll
                    for (int n = 0; n < 2; ++n) acc[a][b][m][n] = (f32x4){0.f, 0.f, 0.f, 0.f};
        cur = nxt; ++ui;
        if (wr == 1) BAR();
    }
    WAIT_V(0);
    signal(ctl, psig, pn);
    BAR();
#undef SA
#undef SB
}

DI void gemm_small(ldsp lds, const Unit u, unsigned* ctl) {
    GEMM_LANE_SETUP(u.lda);
    const unsigned vA = va_of(tid, u.lda, u.nt), vB = vb_of(tid, wid, u.ldb);
    const size_t a2s = u.lda ? (size_t)64 * u.lda : (size_t)u.nt * 16384, b2s = (size_t)32 * u.ldb, bk = (size_t)64 * u.ldb;
    f32x4 acc[4][2];
#pragma unroll
    for (int m = 0; m < 4; ++m)
#pragma unroll
        for (int n = 0; n < 2; ++n) acc[m][n] = (f32x4){0.f, 0.f, 0.f, 0.f};
    int nt = u.nt; asm volatile("" : "+s"(nt));
    const unsigned vb0 = (unsigned)(size_t)lds + (unsigned)boff0, vb1 = (unsigned)(size_t)lds + (unsigned)boff1, vb2 = vb0 + 65536u, vb3 = vb1 + 65536u;
#pragma unroll
    for (int t = 0; t < 4; ++t) { STAGE(t * 32768, u.A + (size_t)t * u.ak, vA, a2s); STAGE(t * 32768 + 16384, u.B + t * bk, vB, b2s); }
    bf16x8 A0[4][2], B0[2][2], A1[4][2], B1[2][2];
    WAIT_V(12); BAR();
    LDB(B0, vb0, vb1, 16384); LDA(A0, 0);
#define RDA(dst, m, off) do { dst[m][0] = *(const LAS bf16x8*)(lds + (off) + aoff + (m) * 2048); dst[m][1] = *(const LAS bf16x8*)(lds + (off) + aoff + (m) * 2048 + 1024); } while (0)
#define RDB(dst, nn, v, off) do { s16x4 l0, h0, l1, h1; TRD(l0, v, (off)); TRD(h0, v, (off) + 256); TRD(l1, v, (off) + 8192); TRD(h1, v, (off) + 8448); dst[nn][0] = cat8(l0, h0); dst[nn][1] = cat8(l1, h1); } while (0)
#define MM4(Ac, Bc, m) do { SCHED(); _Pragma("unroll") for (int n = 0; n < 2; ++n) _Pragma("unroll") for (int k = 0; k < 2; ++k) acc[m][n] = mma32(Bc[n][k], Ac[m][k], acc[m][n]); SCHED(); } while (0)
#define KSTEP(i, Ac, Bc, An, Bn, v0, v1, boffn) do { \
        const int tn = min(t0 + (i) + 4, nt - 1); \
        WAIT_L(0); WAIT_V(8); BAR(); \
        MM4(Ac, Bc, 0); RDB(Bn, 0, v0, boffn); RDA(An, 0, (((i) + 1) & 3) * 32768); \
        MM4(Ac, Bc, 1); RDB(Bn, 1, v1, boffn); RDA(An, 1, (((i) + 1) & 3) * 32768); \
        MM4(Ac, Bc, 2); RDA(An, 2, (((i) + 1) & 3) * 32768); STAGE((i) * 32768, u.A + (u.lda ? (unsigned)tn * 128u : gap((unsigned)tn * 2048u)), vA, a2s); \
        MM4(Ac, Bc, 3); RDA(An, 3, (((i) + 1) & 3) * 32768); STAGE((i) * 32768 + 16384, u.B + (size_t)tn * bk, vB, b2s); } while (0)
    for (int t0 = 0; t0 < nt; t0 += 4) {
        KSTEP(0, A0, B0, A1, B1, vb0, vb1, 49152);
        KSTEP(1, A1, B1, A0, B0, vb2, vb3, 16384);
        KSTEP(2, A0, B0, A1, B1, vb2, vb3, 49152);
        KSTEP(3, A1, B1, A0, B0, vb0, vb1, 16384);
    }
    WAIT_L(0); SCHED();
#undef RDA
#undef RDB
#undef MM4
#undef KSTEP
    const rsrc_t cr = mkrsrc(u.C, 256u * u.ldc);
    if (u.nsig) {
#pragma unroll
        for (int m = 0; m < 4; ++m) store_frag(cr, (unsigned)wc * u.cw + frag_off(lane), gap((unsigned)(wr * 64 + m * 16) * u.ldc), acc[m][0], acc[m][1]);
    } else {
#pragma unroll
        for (int m = 0; m < 4; ++m)
            ST16(to_rows(lds + L_SCR + wid * 1024, lane, acc[m][0], acc[m][1]), cr, (unsigned)(wr * 64 + (lane >> 2)) * u.ldc + (unsigned)(wc * 64 + (lane & 3) * 16), (unsigned)(m * 16) * u.ldc, 0);
    }
    WAIT_V(0);
    signal(ctl, u.sig, u.nsig);
    BAR();
}

DI void gemm_wide(ldsp lds, const Unit u, unsigned* ctl) {
    GEMM_LANE_SETUP(u.lda);
    const char *A = u.A, *B = u.B; int nt = u.nt;
    const unsigned vA = va_of(tid, u.lda, nt), vB = vb_of(tid, wid, u.ldb);
    const size_t a2s = u.lda ? (size_t)64 * u.lda : (size_t)nt * 16384, b2s = (size_t)32 * u.ldb, bk = (size_t)64 * u.ldb;
    f32x4 acc[2][4][2];
#pragma unroll
    for (int b = 0; b < 2; ++b)
#pragma unroll
        for (int m = 0; m < 4; ++m)
#pragma unroll
            for (int n = 0; n < 2; ++n) acc[b][m][n] = (f32x4){0.f, 0.f, 0.f, 0.f};
    asm volatile("" : "+s"(nt));
    const unsigned vb0 = (unsigned)(size_t)lds + (unsigned)boff0, vb1 = (unsigned)(size_t)lds + (unsigned)boff1;
#define STG3(slot, t) do { STAGE((slot), A + (t) * u.ak, vA, a2s); STAGE((slot) + 16384, B + (size_t)(t) * bk, vB, b2s); STAGE((slot) + 32768, B + (size_t)(t) * bk + 256, vB, b2s); } while (0)
    STG3(0, 0); STG3(49152, 1); STG3(98304, 2);
    bf16x8 A0[4][2], P0[2][2], Q0[2][2], A1[4][2], P1[2][2], Q1[2][2];
    WAIT_V(12); BAR();
    LDB(P0, vb0, vb1, 16384); LDB(Q0, vb0, vb1, 32768); LDA(A0, 0);
    int sc = 0, sn = 49152;
#define RDA(dst, m) do { dst[m][0] = *(const LAS bf16x8*)(lds + sn + aoff + (m) * 2048); dst[m][1] = *(const LAS bf16x8*)(lds + sn + aoff + (m) * 2048 + 1024); } while (0)
#define RDB(dst, nn, v, off) do { s16x4 l0, h0, l1, h1; TRD(l0, v, (off)); TRD(h0, v, (off) + 256); TRD(l1, v, (off) + 8192); TRD(h1, v, (off) + 8448); dst[nn][0] = cat8(l0, h0); dst[nn][1] = cat8(l1, h1); } while (0)
#define MM4(b, Ac, Bc, m) do { SCHED(); _Pragma("unroll") for (int n = 0; n < 2; ++n) _Pragma("unroll") for (int k = 0; k < 2; ++k) acc[b][m][n] = mma32(Bc[n][k], Ac[m][k], acc[b][m][n]); SCHED(); } while (0)
#define KSTEP(t, Ac, Pc, Qc, An, Pn, Qn) do { \
        const int tn = min((t) + 3, nt - 1); const unsigned u0 = vb0 + (unsigned)sn, u1 = vb1 + (unsigned)sn; \
        WAIT_L(0); WAIT_V(6); BAR(); \
        MM4(0, Ac, Pc, 0); RDB(Pn, 0, u0, 16384); \
        MM4(1, Ac, Qc, 0); RDB(Pn, 1, u1, 16384); \
        MM4(0, Ac, Pc, 1); RDB(Qn, 0, u0, 32768); \
        MM4(1, Ac, Qc, 1); RDB(Qn, 1, u1, 32768); \
        MM4(0, Ac, Pc, 2); RDA(An, 0); STAGE(sc, A + (u.lda ? (unsigned)tn * 128u : gap((unsigned)tn * 2048u)), vA, a2s); \
        MM4(1, Ac, Qc, 2); RDA(An, 1); STAGE(sc + 16384, B + (size_t)tn * bk, vB, b2s); \
        MM4(0, Ac, Pc, 3); RDA(An, 2); STAGE(sc + 32768, B + (size_t)tn * bk + 256, vB, b2s); \
        MM4(1, Ac, Qc, 3); RDA(An, 3); \
        sc = sn; sn = sn == 98304 ? 0 : sn + 49152; } while (0)
    for (int t0 = 0; t0 < nt; t0 += 2) { KSTEP(t0, A0, P0, Q0, A1, P1, Q1); KSTEP(t0 + 1, A1, P1, Q1, A0, P0, Q0); }
    WAIT_L(0); SCHED();
#undef STG3
#undef RDA
#undef RDB
#undef MM4
#undef KSTEP
    const rsrc_t cr = mkrsrc(u.C, 0x1000000u);
    if (u.nsig) {
#pragma unroll
        for (int m = 0; m < 4; ++m)
#pragma unroll
            for (int b = 0; b < 2; ++b) store_frag(cr, (unsigned)wc * u.cw + frag_off(lane), gap((unsigned)(wr * 64 + m * 16) * u.ldc) + (unsigned)b * u.ch, acc[b][m][0], acc[b][m][1]);
    } else {
#pragma unroll
        for (int m = 0; m < 4; ++m)
#pragma unroll
            for (int b = 0; b < 2; ++b)
                ST16(to_rows(lds + L_SCR + wid * 1024, lane, acc[b][m][0], acc[b][m][1]), cr, (unsigned)(wr * 64 + (lane >> 2)) * 4096u + (unsigned)(wc * 64 + (lane & 3) * 16), (unsigned)(m * 65536 + b * 256), 0);
    }
    WAIT_V(0);
    signal(ctl, u.sig, u.nsig);
    BAR();
}

#define win_off(idx) ((unsigned)(idx) * 16384u + ((((unsigned)(idx) >> 7) ^ par) & 1u) * 8192u)
#define km_win(s, h) (win_off(NWIN + (s) * 32 + (h)) ^ 8192u)
#define W_MEM(s, x) ((s) == 0 ? 0 : NWIN + 2048 + 8 * (s) + 2 * (x))
#define W_PART(slot) (192 + (slot) * 64)
#define W_X(slot) (384 + (slot) * 128)
DI bool all_set(unsigned mx) { return __builtin_amdgcn_ballot_w64(mx == SENT) == 0ull; }
DI u32x4 rows_gather(unsigned v) { unsigned a = v, b = v; swap16(a, b); unsigned c = a, d = b; swap32(a, c); swap32(b, d); return (u32x4){a, b, c, d}; }
DI float max3f(float a, float b, float c) { return fmaxf(fmaxf(a, b), c); }
#define KD0 2
#define KD1 6
#define KD2 5
#define NSTR 3
#define KPM 4
constexpr int RED_M = 144;
constexpr int L_PX = 32768, L_PM = L_PX + 512, L_ML = L_PM + 512, L_FLAG = L_ML + 256, L_PACE = L_FLAG + 16, L_VM = 34816, L_OWN = L_VM + 2048, L_X = L_OWN + 1536  ;
constexpr unsigned OOB = 0x80000000u;
#define RETRY(i)
#define SEG(i)
#define PST(i)
#define PIN_S(x) asm volatile("" : "+s"(x))
#define PIN_V(x) asm volatile("" : "+v"(x))
DI float ror8(float v) { return __uint_as_float(__builtin_amdgcn_update_dpp(0, __float_as_uint(v), 0x128, 0xf, 0xf, true)); }

template <int ROLE>
DI void chain_loop(const Params& p, const unsigned par, const int rank, const int w, ldsp lds, const bf16x16 (&W)[6][2], const bf16x16 (&WO)[2][2], unsigned long long* stamps) {
    const int lane = fresh_tid() & 63, g = lane >> 4, n = lane & 15;
    const int h = rank >> 2, j = rank & 3;
    unsigned* ctl = (unsigned*)(p.ws + WS_CTL);
    const rsrc_t xr = mkrsrc(p.ws + WS_XCH, (unsigned)XCH_BYTES);
    const rsrc_t qr = mkrsrc(p.ws + WH(WS_QKVX + (size_t)(32 + h) * XH), 33u * XH);
    const int sidx = (n & 2) ? 0xEEEE : 0x4444;
    const unsigned l16 = (unsigned)lane * 16u;
    const int redw = (w * 1024 + (n & 1) * 512 + (n >> 1) * 8 + 2 * (g ^ (n >> 3))) * 4;
    const int redr = (w < 4 ? (n >> 3) * RED_M + (4 * w + g) * 8 + ((n & 7) ^ ((w & 1) * 2)) : 2 * RED_M + (8 * (w & 1) + 2 * g + ((n >> 1) & 1)) * 8 + ((2 * (n >> 2) + (n & 1)) ^ (g & 2))) * 4;
    const bool vown = (g >> 1) == (j & 1);
    const unsigned vg1 = (unsigned)(((j & 2) ^ 2) + (g >> 1)) * 1536u + 1024u + (unsigned)(lane & 31) * 16u;
    const unsigned vg0 = vown ? OOB : (vg1 + (unsigned)(j & 2) * 3072u - 3072u);
    const unsigned pub_off = (g == j ? 0u : (unsigned)(4 * h + g) * 16384u) + (unsigned)j * 1536u + (unsigned)w * 256u + (unsigned)n * 16u;
    const int red = ((j + 1) & 3) * 32 + h;
    const int rho = 4 * (8 * j + w) + 2 * (g >> 1) + ((n >> 2) & 1);
    const unsigned pst_off = n < 8 ? (unsigned)(rho >> 1) * 16384u + (unsigned)(rho & 1) * 4096u + (unsigned)((h ^ rho) & 31) * 128u + (unsigned)((g & 1) * 64 + (n & 3) * 16) : OOB;
    const unsigned mem_off = lane < 32 ? (unsigned)(4 * ((n >> 1) & 1) + 2 * ((n >> 2) & 1) + (n & 1)) * 16u + (unsigned)(lane >> 3) * 32768u : OOB;
    const unsigned xw0 = win_off(W_X(0)), xw1 = win_off(W_X(1));
    unsigned pso = win_off(W_PART(0)), psn = win_off(W_PART(1)), psp = win_off(W_PART(2));
    const unsigned redo = (unsigned)(red >> 1) * 16384u + (unsigned)(red & 1) * 4096u;
    const int vxr = L_X + lane * 16;
    const float csc = 0.08838834764831845f * 1.4426950408889634f;
    float ninf = -__builtin_inff(); PIN_V(ninf);
    u32x4 KX[8], VWX[2];
    unsigned long long kv_ok = 0;
    auto dma_x = [&](int s) {
        if (ROLE == 0 && w < 4) {
            if (!((kv_ok >> (s >> 2)) & 1ull)) {
                kv_ok = __builtin_amdgcn_ballot_w64(__hip_atomic_load(ctl + C_KVC(n, h), RLX_AGENT) + 1u >= 16u);
                if (!((kv_ok >> (s >> 2)) & 1ull)) { wait_count(ctl, C_KVC(s >> 2, h), 16u); kv_ok |= 1ull << (s >> 2); }
                asm volatile("" ::: "memory");
            }
#pragma unroll
            for (int i = 0; i < 2; ++i) {
                const unsigned vo = l16, so = (unsigned)s * 16384u + (unsigned)(w * 2 + i) * 1024u;
                __builtin_amdgcn_raw_ptr_buffer_load_lds(qr, (LAS void*)(lds + L_X + (s & 1) * 16384 + (w * 2 + i) * 1024), 16, vo, so + 32u * XH, 0, 16);
                __builtin_amdgcn_raw_ptr_buffer_load_lds(qr, (LAS void*)(lds + L_X + (s & 1) * 16384 + 8192 + (w * 2 + i) * 1024), 16, vo, so, 0, 16);
            }
        }
    };
    auto load_kx = [&](int s) {
#pragma unroll
        for (int kt = 0; kt < 2; ++kt)
#pragma unroll
            for (int kk = 0; kk < 4; ++kk) KX[kt * 4 + kk] = *(const LAS u32x4*)(lds + vxr + (s & 1) * 16384 + 8192 + kt * 4096 + ((j + kk) & 3) * 1024);
    };
    auto vwx_kt = [&](int s, int kt) {
        bf16x8 VA[4];
#pragma unroll
        for (int kk = 0; kk < 4; ++kk) VA[kk] = *(const LAS bf16x8*)(lds + vxr + (s & 1) * 16384 + kt * 4096 + (kk ^ (j & 2)) * 1024);
#pragma unroll
        for (int ct = 0; ct < 2; ++ct) {
            f32x4 d = {0.f, 0.f, 0.f, 0.f};
#pragma unroll
            for (int kk = 0; kk < 4; ++kk) d = mma32(VA[kk], (kk & 1) ? hi8(WO[ct][kk >> 1]) : lo8(WO[ct][kk >> 1]), d);
            VWX[ct][2 * kt] = pk_bf16(d[0], d[1]); VWX[ct][2 * kt + 1] = pk_bf16(d[2], d[3]);
        }
    };
    auto rmax = [&](float v) { float a = v, b = v; swap16f(a, b); v = max3f(a, b, ninf); a = v; b = v; swap32f(a, b); return max3f(a, b, ninf); };
    auto lo2 = [](f32x4 v) { return (f32x2){v[0], v[1]}; };
    auto hi2 = [](f32x4 v) { return (f32x2){v[2], v[3]}; };
    dma_x(0); dma_x(1);
    asm volatile("s_waitcnt vmcnt(0) lgkmcnt(0)" ::: "memory"); BAR(); asm volatile("" ::: "memory");
    PST(5);
    vwx_kt(0, 0); vwx_kt(0, 1);
    if (ROLE == 2) load_kx(0);
    __builtin_amdgcn_s_setprio(2);

    unsigned iv_t0 = 0, iv_acc = 0;
    unsigned spins = 0, pace_c = 0x7fffffffu, pace_t = 0;
    const u32x4 sv = {SENT, SENT, SENT, SENT};
    for (int s = 0;; ++s) {
        unsigned xw = (s & 1) ? xw1 : xw0;
        unsigned mso = win_off(W_MEM(s, rank >> 5) + (w >> 2)) + (unsigned)(w & 3) * 2048u;
        unsigned pub = pub_off + (g == j ? km_win(s, h) : xw);
        PIN_S(xw); PIN_S(mso); PIN_V(pub);
        {
            if (KD0) __builtin_amdgcn_s_sleep(KD0);
            const u32x2 pv = *(const LAS u32x2*)(lds + L_PACE);
            unsigned target = __builtin_amdgcn_readfirstlane(pv.x); const bool valid = __builtin_amdgcn_readfirstlane(pv.y) + 1u == (unsigned)s && s > 3;
            unsigned now = (unsigned)__builtin_amdgcn_s_memrealtime();
            if (ROLE == 3 && s > 2) pace_c = min(pace_c, now - pace_t);
            if (valid && (int)(target - now) < 100) { while ((int)(target - now) > 0) now = (unsigned)__builtin_amdgcn_s_memrealtime(); }
        }
        SEG(10);
        f32x4 c[6]; u32x4 a1;
        u32x4 a0;
        auto ld_a = [&]() { asm volatile("" ::: "memory"); a0 = ld16_sc1(xr, l16, mso); a1 = ld16_sc1(xr, l16 + 1024, mso); };
        auto ok_a = [&]() { return all_set(max(umax4(a0), umax4(a1))); };
        auto mm_a = [&]() {
#pragma unroll
            for (int i = 0; i < 6; ++i) { c[i] = (f32x4){0.f, 0.f, 0.f, 0.f}; PIN_V(c[i]); }
            SCHED();
            c[0] = smma(a0, W[0][0], c[0], sidx); c[1] = smma(a0, W[1][0], c[1], sidx); c[0] = smma(a1, W[0][1], c[0], sidx); c[1] = smma(a1, W[1][1], c[1], sidx);
            c[2] = smma(a0, W[2][0], c[2], sidx); c[3] = smma(a0, W[3][0], c[3], sidx);
        };
        auto put = [&](int i) { *(LAS f32x2*)(lds + redw + ((i >> 1) * RED_M + (i & 1) * 64) * 4) = lo2(c[i]) + hi2(c[i]); };
        ld_a(); SCHED();
        mm_a();
        if (__builtin_expect(!ok_a(), 0)) { bool out; do { RETRY(0); out = spin_out(spins, ctl); ld_a(); } while (!(ok_a() | out)); mm_a(); }
        SEG(0);
        c[2] = smma(a1, W[2][1], c[2], sidx); c[3] = smma(a1, W[3][1], c[3], sidx);
        put(0); put(1);
        SCHED();
        c[4] = smma(a0, W[4][0], c[4], sidx); c[5] = smma(a0, W[5][0], c[5], sidx); c[4] = smma(a1, W[4][1], c[4], sidx);
        put(2); put(3);
        c[5] = smma(a1, W[5][1], c[5], sidx);
        SCHED();
        put(4); put(5);
        SEG(1);
        LDS_BAR();
        SEG(2);
        if (ROLE <= 1) {
            f32x2 v[8];
#pragma unroll
            for (int ww = 0; ww < 8; ++ww) { const LAS float* q = (const LAS float*)(lds + redr + ww * 4096); v[ww] = (f32x2){q[0], q[512]}; }
            const f32x2 t = ((v[0] + v[1]) + (v[2] + v[3])) + ((v[4] + v[5]) + (v[6] + v[7]));
            const u32x4 o = rows_gather(min(pk_bf16(t.x, t.y), SENT - 1));
            ST16(o, xr, pub, 0, 16);
            *(LAS u32x4*)(lds + L_OWN + w * 256 + n * 16) = o;
        } else {
            if (ROLE == 3 && rank == 0) {
                pace_t = (unsigned)__builtin_amdgcn_s_memrealtime();
                if (lane == 0) { __hip_atomic_store(ctl + C_PROG, (unsigned)s, RLX_AGENT);
                    __hip_atomic_store((unsigned long long*)(ctl + C_PACE + par * 2048), (unsigned long long)(pace_t + pace_c + KPM) | ((unsigned long long)s << 32), RLX_AGENT); }
            }
        }
        SEG(3);
        if (s == NSEG - 1) break;
        LDS_BAR();
        SEG(13);
        unsigned VWM[2];
        auto vwm_pack = [&](const f32x4 (&dv)[2]) {
#pragma unroll
            for (int ct = 0; ct < 2; ++ct) { const f32x2 t = lo2(dv[ct]) + hi2(dv[ct]); VWM[ct] = pk_bf16(t.x, t.y); PIN_V(VWM[ct]); }
        };
        f32x4 dv[2] = {{0.f, 0.f, 0.f, 0.f}, {0.f, 0.f, 0.f, 0.f}}, s0, s1;
        PIN_V(dv[0]); PIN_V(dv[1]);
        u32x4 v0, v1;
        if (ROLE == 0) {
            WAIT_V(0);
            if (s + 2 < NSEG - 1) dma_x(s + 2);
        } else {
            unsigned cw = xw + (unsigned)rank * 16384u, c1 = cw + (unsigned)((j + 1) & 3) * 1536u, c2 = cw + (unsigned)((j + 2) & 3) * 1536u, c3 = cw + (unsigned)((j + 3) & 3) * 1536u;
            PIN_S(cw); PIN_S(c1); PIN_S(c2); PIN_S(c3);
            const unsigned xl = ROLE == 2 ? (l16 & ~128u) : l16;
            u32x4 vo;
            f32x4 e0 = {0.f, 0.f, 0.f, 0.f}, e1 = e0;
            if (ROLE == 1) { vo = *(const LAS u32x4*)(lds + L_OWN + 1024 + (lane & 31) * 16); if (!vown) vo = (u32x4){0u, 0u, 0u, 0u}; PIN_V(vo); }
            else {
                const u32x4 X0 = *(const LAS u32x4*)(lds + L_OWN + xl);
                if (ROLE == 2) { e0 = mma32(asbf(KX[0]), asbf(X0), e0); e1 = mma32(asbf(KX[4]), asbf(X0), e1); } else e0 = mma32(asbf(X0), asbf(X0), e0);
                PIN_V(e0); PIN_V(e1);
            }
            if (KD1) __builtin_amdgcn_s_sleep(KD1);
            u32x4 X1, X2, X3;
            auto ld_b = [&]() {
                asm volatile("" ::: "memory");
                if (ROLE >= 2) { X1 = ld16_sc1(xr, xl, c1); X2 = ld16_sc1(xr, xl, c2); X3 = ld16_sc1(xr, xl, c3); }
                else { v0 = ld16_sc1(xr, vg0, cw); v1 = ld16_sc1(xr, vg1, cw); }
            };
            auto ok_b = [&]() { return all_set(ROLE >= 2 ? max(umax4(X1), max(umax4(X2), umax4(X3))) : max(umax4(v0), umax4(v1))); };
            auto mm_b = [&]() {
                if (ROLE == 2) {
                    s0 = mma32(asbf(KX[1]), asbf(X1), e0); s1 = mma32(asbf(KX[5]), asbf(X1), e1);
                    s0 = mma32(asbf(KX[2]), asbf(X2), s0); s1 = mma32(asbf(KX[6]), asbf(X2), s1);
                    s0 = mma32(asbf(KX[3]), asbf(X3), s0); s1 = mma32(asbf(KX[7]), asbf(X3), s1);
                } else if (ROLE == 3) {
                    s0 = mma32(asbf(X1), asbf(X1), e0); s1 = mma32(asbf(X2), asbf(X2), e1); s0 = mma32(asbf(X3), asbf(X3), s0);
                } else {
                    v0 |= vo;
                    *(LAS u32x4*)(lds + L_VM + lane * 16) = v0; *(LAS u32x4*)(lds + L_VM + 1024 + lane * 16) = v1;
                }
            };
            SEG(11);
            ld_b(); SCHED(); mm_b();
            if (__builtin_expect(!ok_b(), 0)) { bool out; do { RETRY(1); out = spin_out(spins, ctl); ld_b(); } while (!(ok_b() | out)); mm_b(); }
            SEG(4);
        }
        float m;
        f32x4 sc;
        if (ROLE >= 2) {
            sc = ROLE == 2 ? (n < 8 ? s0 : s1) : s0 + s1;
            if (ROLE == 3 && g < 2) sc = (f32x4){ninf, ninf, ninf, ninf};
            m = max3f(max3f(sc[0], sc[1], sc[2]), sc[3], ninf);
            if (ROLE == 2) m = max3f(m, ror8(m), ninf);
            m = rmax(m);
            PIN_V(m);
        }
        LDS_BAR();
        if (ROLE == 0) {
            const unsigned ro = w < 3 ? xw + (unsigned)rank * 16384u + (unsigned)w * 2048u : psp + redo + (unsigned)(w - 3) * 2048u;
            ST16(sv, xr, l16, ro, 16); ST16(sv, xr, l16 + 1024, ro, 16);
        }
        if (ROLE != 1) { v0 = *(const LAS u32x4*)(lds + L_VM + lane * 16); v1 = *(const LAS u32x4*)(lds + L_VM + 1024 + lane * 16); }
        if (ROLE >= 2) {
            sc = (sc - m) * csc;
#pragma unroll
            for (int i = 0; i < 4; ++i) sc[i] = __builtin_amdgcn_exp2f(sc[i]);
            *(LAS u32x2*)(lds + (ROLE == 2 ? L_PX + (g * 8 + (n & 7)) * 16 + (n >> 3) * 8 : L_PM + (g < 2 ? 256 : 0) + n * 16 + (g & 1) * 8)) = (u32x2){pk_bf16(sc[0], sc[1]), pk_bf16(sc[2], sc[3])};
            SCHED();
            dv[0] = smma(v0, WO[0][0], dv[0], sidx); dv[1] = smma(v0, WO[1][0], dv[1], sidx);
            float l = (sc[0] + sc[1]) + (sc[2] + sc[3]);
            if (ROLE == 2) l += ror8(l);
            dv[0] = smma(v1, WO[0][1], dv[0], sidx); dv[1] = smma(v1, WO[1][1], dv[1], sidx);
            l = rows_sum(l);
            *(LAS f32x2*)(lds + L_ML + n * 16 + (ROLE == 2 ? 0 : 8)) = (f32x2){m, l};
        } else {
#pragma unroll
            for (int ct = 0; ct < 2; ++ct) { dv[ct] = smma(v0, WO[ct][0], dv[ct], sidx); dv[ct] = smma(v1, WO[ct][1], dv[ct], sidx); }
        }
        vwm_pack(dv);
        SEG(5);
        u32x4 am0 = {VWM[0], 0u, 0u, 0u}, am1 = {VWM[1], 0u, 0u, 0u}, pm = {0u, 0u, 0u, 0u};
        PIN_V(am0); PIN_V(am1); PIN_V(pm);
        LDS_BAR();
        SEG(6);
        {
            const f32x4 ml = *(const LAS f32x4*)(lds + L_ML + n * 16);
            const u32x4 px = *(const LAS u32x4*)(lds + L_PX + (g * 8 + (n & 7)) * 16);
            pm.x = *(const LAS unsigned*)(lds + L_PM + n * 16 + g * 4);
            const f32x4 z = {0.f, 0.f, 0.f, 0.f};
            const f32x4 dx0 = mma32(asbf(VWX[0]), asbf(px), z), dx1 = mma32(asbf(VWX[1]), asbf(px), z), dm0 = mma32(asbf(am0), asbf(pm), z), dm1 = mma32(asbf(am1), asbf(pm), z);
            const float dl = (ml[2] - ml[0]) * csc;
            const float e = __builtin_amdgcn_exp2f(-__builtin_fabsf(dl));
            const float ex = dl > 0.f ? e : 1.f, em = dl > 0.f ? 1.f : e;
            const float inv = __builtin_amdgcn_rcpf(fmaf(ex, ml[1], em * ml[3]));
            const f32x4 d0 = (dx0 * ex + dm0 * em) * inv, d1 = (dx1 * ex + dm1 * em) * inv;
            const u32x4 o = {min(pk_bf16(d0[0], d0[1]), SENT - 1), min(pk_bf16(d0[2], d0[3]), SENT - 1), min(pk_bf16(d1[0], d1[1]), SENT - 1), min(pk_bf16(d1[2], d1[3]), SENT - 1)};
            ST16(o, xr, pst_off, pso, 16);
        }
        SCHED();
        SEG(7);
        __builtin_amdgcn_s_setprio(ROLE == 3 ? 1 : 0);
        unsigned long long pv = 0;
        if (ROLE == 1) { pv = __hip_atomic_load((unsigned long long*)(ctl + C_PACE + par * 2048), RLX_AGENT); SCHED(); }
        vwx_kt(s + 1, 0); vwx_kt(s + 1, 1);
        SCHED();
        if (ROLE == 3) __builtin_amdgcn_s_setprio(2);
        if (ROLE == 3) {
            u32x4 SEL;
            { const unsigned one = (n & 1) ? 0x3f800000u : 0x00003f80u; const int dw = (n & 7) >> 1; SEL = (u32x4){dw == 0 ? one : 0u, dw == 1 ? one : 0u, dw == 2 ? one : 0u, dw == 3 ? one : 0u}; }
            unsigned so = pso + redo, mo = win_off(W_MEM(s + 1, 0) + (red >> 6)) + (unsigned)(red & 63) * 128u;
            PIN_S(so); PIN_S(mo); PIN_V(SEL);
            if (KD2) __builtin_amdgcn_s_sleep(KD2);
            f32x4 ta, tb;
            u32x4 q[4];
            auto ld_c = [&]() {
                asm volatile("" ::: "memory");
#pragma unroll
                for (int i = 0; i < 4; ++i) q[i] = ld16_sc1(xr, l16 + i * 1024, so);
            };
            auto ok_c = [&]() { return all_set(max(max(umax4(q[0]), umax4(q[1])), max(umax4(q[2]), umax4(q[3])))); };
            auto mm_c = [&]() {
                ta = mma32(asbf(SEL), asbf(q[0]), (f32x4){0.f, 0.f, 0.f, 0.f}); tb = mma32(asbf(SEL), asbf(q[1]), (f32x4){0.f, 0.f, 0.f, 0.f});
                ta = mma32(asbf(SEL), asbf(q[2]), ta); tb = mma32(asbf(SEL), asbf(q[3]), tb);
            };
            SEG(12);
            ld_c(); mm_c();
            if (__builtin_expect(!ok_c(), 0)) { bool out; do { RETRY(2); out = spin_out(spins, ctl); ld_c(); } while (!(ok_c() | out)); mm_c(); }
            SEG(8);
            f32x4 t = ta + tb;
#pragma unroll
            for (int i = 0; i < 4; ++i) t[i] += ror8(t[i]);
            unsigned a0 = min(pk_bf16(t[0], t[1]), SENT - 1), a1 = min(pk_bf16(t[2], t[3]), SENT - 1);
            unsigned b0 = a0, b1 = a1; swap16(a0, b0); swap16(a1, b1);
            const u32x4 o = {a0, a1, b0, b1};
            ST16(o, xr, mem_off, mo, 16);
        } else {
            if (ROLE == 2) load_kx(s + 1);
            if (ROLE == 1) *(LAS u32x2*)(lds + L_PACE) = (u32x2){(unsigned)pv, (unsigned)(pv >> 32)};
        }
        { const unsigned t = psp; psp = pso; pso = psn; psn = t; }
        SEG(9);
        if (ROLE != 3) __builtin_amdgcn_s_setprio(2);
        LDS_BAR();
        SEG(14);
    }
    WAIT_V(0);
    __builtin_amdgcn_s_setprio(0);
}

DI void chain_role(const Params& p, unsigned par, int rank, ldsp lds, unsigned long long* stamps) {
    const int tid = fresh_tid(), w = __builtin_amdgcn_readfirstlane(tid >> 6), lane = tid & 63, g = lane >> 4, n = lane & 15;
    const int h = rank >> 2, j = rank & 3;
    const rsrc_t xr = mkrsrc(p.ws + WS_XCH, (unsigned)XCH_BYTES);
    unsigned* ctl = (unsigned*)(p.ws + WS_CTL);
    PST(0);
    const int red = ((j + 1) & 3) * 32 + h;
    bf16x16 W[6][2], WO[2][2];
    {
        ldsp my = lds + w * 16384;
        const int rrow = lane >> 2, rc = (lane & 3) * 8, q4 = n >> 2;
        const int toffo = (8 * g + q4) * 64 + (n & 3) * 8;
        const int toffq = (16 * (g & 1) + 8 * (g >> 1) + 4 * (q4 >> 1) + (q4 & 1)) * 64 + (n & 3) * 8;
#define FRAG(base, st) cat16(cat8(trd((base)), trd((base) + (st))), cat8(trd((base) + 2048), trd((base) + 2048 + (st))))
#pragma unroll
        for (int i = 0; i < 8; ++i) {
            __builtin_amdgcn_global_load_lds((const unsigned*)(p.wq + (size_t)(128 * w + 16 * i + rrow) * 4096 + h * 128 + 32 * j + rc), (LAS unsigned*)(my + i * 1024), 16, 0, 0);
            __builtin_amdgcn_global_load_lds((const unsigned*)(p.wo + (size_t)(h * 128 + 16 * i + rrow) * 1024 + 256 * j + 32 * w + rc), (LAS unsigned*)(my + 8192 + i * 1024), 16, 0, 0);
        }
        {
            const u32x4 sv = {SENT, SENT, SENT, SENT};
            if (w < 6) {
                ST16(sv, xr, (unsigned)lane * 16u, win_off(W_X(0)) + (unsigned)rank * 16384u + (unsigned)w * 1024u, 16);
                ST16(sv, xr, (unsigned)lane * 16u, win_off(W_X(1)) + (unsigned)rank * 16384u + (unsigned)w * 1024u, 16);
            }
            ST16(sv, xr, (unsigned)lane * 16u, win_off(W_PART(w >> 2)) + (unsigned)(red >> 1) * 16384u + (unsigned)(red & 1) * 4096u + (unsigned)(w & 3) * 1024u, 16);
            ST16(sv, xr, (unsigned)lane * 16u, km_win(56 + w, h) + (unsigned)j * 1536u, 16);
            ST16(sv, xr, lane < 32 ? (unsigned)lane * 16u + 1024u : OOB, km_win(56 + w, h) + (unsigned)j * 1536u, 16);
            const int s = 8 * w + (lane >> 3);
#pragma unroll
            for (int x = 0; x < 4; ++x) if (s > 0) ST16(sv, xr, win_off(W_MEM(s, x) + (red >> 6)) + (unsigned)(red & 63) * 128u + (unsigned)(lane & 7) * 16u, 0, 16);
        }
        PST(3);
        WAIT_V(0);
#pragma unroll
        for (int T = 0; T < 2; ++T)
#pragma unroll
            for (int t = 0; t < 2; ++t) { W[T][t] = FRAG(my + t * 4096 + T * 32 + toffq, 128); WO[T][t] = FRAG(my + 8192 + (t ^ (j >> 1)) * 4096 + T * 32 + toffo, 256); }
#pragma unroll
        for (int T = 0; T < 2; ++T) asm volatile("" : "+v"(W[T][0]), "+v"(W[T][1]), "+v"(WO[T][0]), "+v"(WO[T][1]));
        WAIT_L(0);
        PST(1);
#pragma unroll
        for (int i = 0; i < 8; ++i) {
            __builtin_amdgcn_global_load_lds((const unsigned*)(p.wk + (size_t)(128 * w + 16 * i + rrow) * 4096 + h * 128 + 32 * j + rc), (LAS unsigned*)(my + i * 1024), 16, 0, 0);
            __builtin_amdgcn_global_load_lds((const unsigned*)(p.wv + (size_t)(128 * w + 16 * i + rrow) * 4096 + h * 128 + 32 * j + rc), (LAS unsigned*)(my + 8192 + i * 1024), 16, 0, 0);
        }
        WAIT_V(0);
#pragma unroll
        for (int T = 0; T < 4; ++T)
#pragma unroll
            for (int t = 0; t < 2; ++t) W[2 + T][t] = FRAG(my + (T >> 1) * 8192 + t * 4096 + (T & 1) * 32 + toffq, 128);
#pragma unroll
        for (int T = 0; T < 4; ++T) asm volatile("" : "+v"(W[2 + T][0]), "+v"(W[2 + T][1]));
        WAIT_L(0);
#undef FRAG
    }
    PST(2);
    BAR();
    PST(4);
    if (w == 7 && lane < 8) {
        const unsigned* src = (const unsigned*)(p.mem0 + (4 * (red & 1) + 2 * (lane >> 2) + (lane & 1)) * DM + 32 * (red >> 2) + 8 * ((red >> 1) & 1) + 4 * ((lane >> 1) & 1));
        const u32x4 v = {min(src[0], SENT - 1), min(src[1], SENT - 1), min(src[8], SENT - 1), min(src[9], SENT - 1)};
        ST16(v, xr, (unsigned)lane * 16u + (unsigned)(red & 63) * 128u, win_off(W_MEM(0, 0) + (red >> 6)), 16);
    }
    if (w < 5) chain_loop<0>(p, par, rank, w, lds, W, WO, stamps);
    else if (w == 5) chain_loop<1>(p, par, rank, 5, lds, W, WO, stamps);
    else if (w == 6) chain_loop<2>(p, par, rank, 6, lds, W, WO, stamps);
    else chain_loop<3>(p, par, rank, 7, lds, W, WO, stamps);
}

template <bool POLL>
DI void attn_a_item(const Params& p, unsigned par, int s, int h, ldsp my, int lane) {
    const int g = lane >> 4, n = lane & 15;
    const rsrc_t xr = mkrsrc(p.ws + WS_XCH, (unsigned)XCH_BYTES);
    const rsrc_t qr = mkrsrc(p.ws + WH(WS_QKVX + (size_t)h * XH + (size_t)s * 16384), 65u * XH);
#pragma unroll
    for (int i = 0; i < 8; ++i)
        __builtin_amdgcn_raw_ptr_buffer_load_lds(qr, (LAS void*)(my + i * 1024), 16, (unsigned)(g * 256 + (n ^ ((g & 1) * 8)) * 16), 64u * XH + (unsigned)i * 1024u, 0, 16);
    u32x4 Q[2][4], K[2][4], XM[4];
    const unsigned ro = (unsigned)lane * 16u;
#pragma unroll
    for (int t = 0; t < 2; ++t)
#pragma unroll
        for (int k = 0; k < 4; ++k) { Q[t][k] = ld16_sc1(qr, ro + k * 1024, (unsigned)t * 4096u); K[t][k] = ld16_sc1(qr, ro + k * 1024, (unsigned)t * 4096u + 32u * XH); }
    const unsigned qwin = km_win(s, h);
    u32x4 VM[2];
    if (POLL) {
        if (s < 56) wait_count<24>((unsigned*)(p.ws + WS_CTL), C_PROG, (unsigned)s + 2u);
        else { wait_count<4>((unsigned*)(p.ws + WS_CTL), C_PROG, (unsigned)s + 1u); __builtin_amdgcn_s_sleep(16); }
    }
    for (unsigned spins = 0;;) {
        asm volatile("" ::: "memory");
#pragma unroll
        for (int k = 0; k < 4; ++k) XM[k] = ld16_sc1(xr, (unsigned)lane * 16u + (k & 1) * 1536, qwin + (k >> 1) * 3072);
#pragma unroll
        for (int ts = 0; ts < 2; ++ts) VM[ts] = ld16_sc1(xr, (unsigned)(g >> 1) * 1536u + 1024u + (unsigned)(lane & 31) * 16u, qwin + ts * 3072);
        if (!POLL || s < 56) break;
        if (all_set(max(max(max(umax4(XM[0]), umax4(XM[1])), max(umax4(XM[2]), umax4(XM[3]))), max(umax4(VM[0]), umax4(VM[1]))))) break;
        __builtin_amdgcn_s_sleep(4);
        if (spin_out(spins, (unsigned*)(p.ws + WS_CTL))) break;
    }
    SCHED();
#pragma unroll
    for (int ts = 0; ts < 2; ++ts) {
        const u32x4 v = VM[ts];
        const int m = 2 * (n >> 2) + (n & 1), hf = (n >> 1) & 1;
#pragma unroll
        for (int i = 0; i < 4; ++i) { const int e = 64 * ts + 16 * g + 4 * i + 2 * hf;
            *(LAS unsigned*)(my + 8192 + (e >> 3) * 256 + (m ^ (e & 8)) * 16 + (e & 7) * 2) = v[i]; }
    }
    const float csc = 0.08838834764831845f * 1.4426950408889634f, ninf = -__builtin_inff();
    u32x4 PX[2]; u32x2 PM[2];
#pragma unroll
    for (int qt = 0; qt < 2; ++qt) {
        f32x4 sm = {0.f, 0.f, 0.f, 0.f}, s0 = sm, s1 = sm;
#pragma unroll
        for (int k = 0; k < 4; ++k) { sm = mma32(asbf(XM[k]), asbf(Q[qt][k]), sm); s0 = mma32(asbf(K[0][k]), asbf(Q[qt][k]), s0); if (qt == 1) s1 = mma32(asbf(K[1][k]), asbf(Q[qt][k]), s1); }
#pragma unroll
        for (int i = 0; i < 4; ++i) {
            if (g < 2) sm[i] = ninf;
            if (qt == 0) { if (4 * g + i > n) s0[i] = ninf; s1[i] = ninf; }
            else if (4 * g + i > n) s1[i] = ninf;
        }
        float m = fmaxf(fmaxf(fmaxf(s0[0], s0[1]), fmaxf(s0[2], s0[3])), fmaxf(fmaxf(s1[0], s1[1]), fmaxf(s1[2], s1[3])));
        m = fmaxf(m, fmaxf(fmaxf(sm[0], sm[1]), fmaxf(sm[2], sm[3])));
        m = rows_max(m);
        float sum = 0.f;
        s0 = (s0 - m) * csc; s1 = (s1 - m) * csc; sm = (sm - m) * csc;
#pragma unroll
        for (int i = 0; i < 4; ++i) { s0[i] = __builtin_amdgcn_exp2f(s0[i]); s1[i] = __builtin_amdgcn_exp2f(s1[i]); sm[i] = __builtin_amdgcn_exp2f(sm[i]);
            sum += (s0[i] + s1[i]) + sm[i]; }
        sum = rows_sum(sum);
        const float inv = __builtin_amdgcn_rcpf(sum);
        PX[qt] = (u32x4){pk_bf16(s0[0] * inv, s0[1] * inv), pk_bf16(s0[2] * inv, s0[3] * inv), pk_bf16(s1[0] * inv, s1[1] * inv), pk_bf16(s1[2] * inv, s1[3] * inv)};
        PM[qt] = (u32x2){pk_bf16(sm[0] * inv, sm[1] * inv), pk_bf16(sm[2] * inv, sm[3] * inv)};
    }
    asm volatile("s_waitcnt vmcnt(0) lgkmcnt(0)" ::: "memory");
    const int q4 = n >> 2, p4 = n & 3;
    const ldsp vx = my + (p4 >> 1) * 256 + ((4 * g + q4) ^ ((p4 >> 1) * 8)) * 16 + (p4 & 1) * 8, vmm = my + 8192 + (p4 >> 1) * 256 + ((4 * (g & 1) + q4) ^ ((p4 >> 1) * 8)) * 16 + (p4 & 1) * 8;
    const rsrc_t orr = mkrsrc(p.ws + WH(WS_OA + (size_t)s * 524288 + gap((unsigned)h * 4096u)), 524288u);
    const unsigned oo = frag_off(lane);
    bf16x8 VA[8]; s16x4 VMf[8];
#pragma unroll
    for (int et = 0; et < 8; ++et) { VA[et] = cat8(trd(vx + et * 512), trd(vx + 4096 + et * 512)); VMf[et] = trd(vmm + et * 512); }
    SCHED();
#pragma unroll
    for (int ep = 0; ep < 4; ++ep) {
        f32x4 d[2][2];
#pragma unroll
        for (int e2 = 0; e2 < 2; ++e2)
#pragma unroll
            for (int qt = 0; qt < 2; ++qt) d[qt][e2] = mma16(__builtin_bit_cast(u32x2, VMf[2 * ep + e2]), PM[qt], mma32(VA[2 * ep + e2], asbf(PX[qt]), (f32x4){0.f, 0.f, 0.f, 0.f}));
#pragma unroll
        for (int qt = 0; qt < 2; ++qt) store_frag(orr, oo + ep * 1024, (unsigned)qt * 262144u, d[qt][0], d[qt][1]);
    }
    asm volatile("s_waitcnt lgkmcnt(0)" ::: "memory");
}

DI void attn_b_item(const Params& p, unsigned par, int hq, int qb, ldsp lds) {
    const int tid = fresh_tid(), w = __builtin_amdgcn_readfirstlane(tid >> 6), lane = tid & 63, g = lane >> 4, n = lane & 15;
    const int q0 = qb * 128;
    const rsrc_t br = mkrsrc(p.ws + WH(WS_QKVB + (size_t)hq * 4 * BH), 68u * BH);
    const rsrc_t orr = mkrsrc(p.ws + WH(WS_OB + (size_t)qb * 524288), 524288u);
    constexpr int BUF = 57344;
    unsigned to[3];
#pragma unroll
    for (int i = 0; i < 3; ++i) to[i] = gap((unsigned)max(8 * qb - 16 + w * 3 + i, 0) * 1024u);
    const unsigned l16 = (unsigned)lane * 16u, vsw = (unsigned)(g * 256 + (n ^ ((g & 1) * 8)) * 16);
    auto stage = [&](int i) {
        const unsigned so = (unsigned)i * BH; ldsp d = lds + (i & 1) * BUF;
#pragma unroll
        for (int k = 0; k < 3; ++k) {
            __builtin_amdgcn_raw_ptr_buffer_load_lds(br, (LAS void*)(d + (w * 3 + k) * 1024), 16, l16, so + 32u * BH + to[k], 0, 16);
            __builtin_amdgcn_raw_ptr_buffer_load_lds(br, (LAS void*)(d + 24576 + (w * 3 + k) * 1024), 16, vsw, so + 64u * BH + to[k], 0, 16);
        }
        __builtin_amdgcn_raw_ptr_buffer_load_lds(br, (LAS void*)(d + 49152 + w * 1024), 16, l16, so + (unsigned)(qb * 16384 + w * 1024), 0, 16);
    };
    const float ninf = -__builtin_inff(), csc = 0.17677669529663687f * 1.4426950408889634f;
    const int tmin = (256 - q0 - 16 * w) >> 4;
    const int q4 = n >> 2, p4 = n & 3;
    const int kq = w * 1024 + lane * 16;
    const unsigned vb = (unsigned)(size_t)lds + 24576u + (unsigned)(w * 1024 + (p4 >> 1) * 256 + ((4 * g + q4) ^ ((p4 >> 1) * 8)) * 16 + (p4 & 1) * 8);
    stage(0);
#pragma unroll 1
    for (int i = 0; i < 4; ++i) {
        if (i < 3) { stage(i + 1); WAIT_V(7); } else WAIT_V(0);
        BAR(); asm volatile("" ::: "memory");
        const ldsp bp = lds + (i & 1) * BUF;
        const bf16x8 Q = *(const LAS bf16x8*)(bp + 49152 + kq);
        f32x4 sc[17]; bf16x8 KF[17];
#pragma unroll
        for (int t = 0; t < 17; ++t) KF[t] = *(const LAS bf16x8*)(bp + kq + t * 1024);
        SCHED();
#pragma unroll
        for (int t = 0; t < 17; ++t) sc[t] = mma32(KF[t], Q, (f32x4){0.f, 0.f, 0.f, 0.f});
#pragma unroll
        for (int k = 0; k < 4; ++k) { if (4 * g + k <= n) sc[0][k] = ninf; if (4 * g + k > n) sc[16][k] = ninf; }
        if (tmin > 0) {
#pragma unroll
            for (int t = 0; t < 16; ++t) if (t < tmin) sc[t] = (f32x4){ninf, ninf, ninf, ninf};
        }
        float mq[4];
#pragma unroll
        for (int k = 0; k < 4; ++k) {
            mq[k] = max3f(sc[16][k], sc[k][0], sc[k][1]); mq[k] = max3f(mq[k], sc[k][2], sc[k][3]);
#pragma unroll
            for (int t = 4 + k; t < 16; t += 4) mq[k] = max3f(max3f(mq[k], sc[t][0], sc[t][1]), sc[t][2], sc[t][3]);
        }
        const float m = rows_max(max3f(max3f(mq[0], mq[1], mq[2]), mq[3], mq[3]));
        f32x4 sum4 = {0.f, 0.f, 0.f, 0.f};
#pragma unroll
        for (int t = 0; t < 17; ++t) {
            sc[t] = (sc[t] - m) * csc;
#pragma unroll
            for (int k = 0; k < 4; ++k) sc[t][k] = __builtin_amdgcn_exp2f(sc[t][k]);
            sum4 += sc[t];
        }
        const float inv = __builtin_amdgcn_rcpf(rows_sum((sum4[0] + sum4[1]) + (sum4[2] + sum4[3])));
        const unsigned v0 = vb + (unsigned)((i & 1) * BUF);
        s16x4 V0[17], V1[17];
#define VRD(t) TRD(V0[t], v0, (t) * 1024); TRD(V1[t], v0, (t) * 1024 + 512)
        VRD(0); VRD(1); VRD(2); VRD(3); VRD(4); VRD(5); VRD(6); VRD(7); VRD(8); VRD(9); VRD(10); VRD(11); VRD(12); VRD(13); VRD(14); VRD(15); VRD(16);
#undef VRD
        u32x2 P[17];
#pragma unroll
        for (int t = 0; t < 17; ++t) P[t] = (u32x2){pk_bf16(sc[t][0], sc[t][1]), pk_bf16(sc[t][2], sc[t][3])};
        WAIT_L(0); SCHED();
        f32x4 d0 = {0.f, 0.f, 0.f, 0.f}, d1 = d0;
#pragma unroll
        for (int tp = 0; tp < 8; ++tp) {
            const bf16x8 PP = asbf((u32x4){P[2 * tp].x, P[2 * tp].y, P[2 * tp + 1].x, P[2 * tp + 1].y});
            d0 = mma32(cat8(V0[2 * tp], V0[2 * tp + 1]), PP, d0); d1 = mma32(cat8(V1[2 * tp], V1[2 * tp + 1]), PP, d1);
        }
        d0 = mma16(__builtin_bit_cast(u32x2, V0[16]), P[16], d0); d1 = mma16(__builtin_bit_cast(u32x2, V1[16]), P[16], d1);
        BAR();
        store_frag(orr, frag_off(lane), (unsigned)w * 65536u + gap((unsigned)(hq * 4 + i) * 1024u), d0 * inv, d1 * inv);
    }
    WAIT_V(0);
    signal((unsigned*)(p.ws + WS_CTL), C_OB + qb * 32, 1);
}

DI void out_tail(const Params& p, unsigned par, int c, int q, ldsp lds) {
    const int tid = fresh_tid(), w = __builtin_amdgcn_readfirstlane(tid >> 6), lane = tid & 63, g = lane >> 4, n = lane & 15;
    unsigned* ctl = (unsigned*)(p.ws + WS_CTL);
    bf16x8 WT[16][2];
    {
        ldsp my = lds + w * 16384;
        const int toff = (8 * g + (n >> 2)) * 64 + (n & 3) * 8;
#pragma unroll
        for (int r = 0; r < 2; ++r) {
#pragma unroll
            for (int i = 0; i < 16; ++i)
                __builtin_amdgcn_global_load_lds((const unsigned*)(p.wo + (size_t)(512 * w + 256 * r + 16 * i + (lane >> 2)) * 1024 + 32 * c + (lane & 3) * 8), (LAS unsigned*)(my + i * 1024), 16, 0, 0);
            WAIT_V(0);
#pragma unroll
            for (int k = 0; k < 8; ++k)
#pragma unroll
                for (int ct = 0; ct < 2; ++ct) { WT[8 * r + k][ct] = cat8(trd(my + k * 2048 + ct * 32 + toff), trd(my + k * 2048 + ct * 32 + toff + 256)); }
#pragma unroll
            for (int k = 0; k < 8; ++k) asm volatile("" : "+v"(WT[8 * r + k][0]), "+v"(WT[8 * r + k][1]));
            WAIT_L(0);
        }
    }
    BAR();
#pragma unroll 1
    for (int hs = q; hs < 2 * (NSEG - S_TAIL); hs += 2) {
        const int s = S_TAIL + (hs >> 1), rt = hs & 1;
        if (w == 0) wait_count<3>(ctl, C_OS + (s - S_TAIL) * 128 + (c >> 3) * 32, 4u);
        asm volatile("" ::: "memory"); BAR(); asm volatile("" ::: "memory");
        const rsrc_t ar = mkrsrc(p.ws + WH(WS_OA + (size_t)(2 * s + rt) * 262144 + (size_t)w * 32768), 32768u);
        const unsigned ao = (unsigned)lane * 16u;
        u32x4 A[16];
#pragma unroll
        for (int k = 0; k < 16; ++k) A[k] = ld16_sc1(ar, ao + (k & 3) * 1024, gap((unsigned)(k >> 2) * 4096u));
        SCHED();
#pragma unroll
        for (int ct = 0; ct < 2; ++ct) {
            f32x4 acc = {0.f, 0.f, 0.f, 0.f};
#pragma unroll
            for (int k = 0; k < 16; ++k) acc = mma32(WT[k][ct], asbf(A[k]), acc);
            *(LAS f32x4*)(lds + w * 2048 + ct * 1024 + lane * 16) = acc;
        }
        LDS_BAR();
        if (w < 2) {
            f32x4 r[8];
#pragma unroll
            for (int ww = 0; ww < 8; ++ww) r[ww] = *(const LAS f32x4*)(lds + ww * 2048 + w * 1024 + lane * 16);
            SCHED();
            const f32x4 t = ((r[0] + r[1]) + (r[2] + r[3])) + ((r[4] + r[5]) + (r[6] + r[7]));
            *(u32x2*)(p.out + (size_t)(32 * s + 16 * rt + n) * 2048 + 32 * c + 16 * w + 4 * g) = (u32x2){pk_bf16(t[0], t[1]), pk_bf16(t[2], t[3])};
        }
        WAIT_L(0);
    }
    BAR();
}

DI void big_unit(const Params& p, unsigned par, int id, Unit& u) {
    u.lda = DM * 2; u.nt = 16; u.ak = 128; u.ah = 128 * DM * 2;
    if (id < 384) {
        const int pm = id & 7, pn = id >> 3;
        const u16 *w0 = p.wq, *w1 = p.wk, *w2 = p.wv; const u16* W = pn < 16 ? w0 : (pn < 32 ? w1 : w2);
        u.A = (const char*)(p.ax + (size_t)pm * 256 * DM); u.B = (const char*)(W + (pn & 15) * 256); u.ldb = 4096 * 2;
        u.C = p.ws + WH(WS_QKVX + (size_t)(2 * pn) * XH + (size_t)pm * 131072); u.ldc = 256; u.cw = 1024; u.ch = XH;
        if (pn < 16) { u.sig = C_Q + pm * 32; u.nsig = 1; } else { u.sig = C_KVC(2 * pm, 2 * (pn & 15)); u.nsig = 4; }
    } else if (id < 480) {
        const int pm = (id - 384) & 7, pn = (id - 384) >> 3;
        u.A = (const char*)(p.bx + (size_t)pm * 256 * DM); u.B = (const char*)(p.bwqkv + pn * 256); u.ldb = LDB3 * 2;
        u.C = p.ws + WH(WS_QKVB + (size_t)(8 * pn) * BH + (size_t)pm * 32768); u.ldc = 64; u.cw = BH; u.ch = 4 * BH; u.sig = C_B + pm * 32; u.nsig = 1;
    } else {
        const int pm = (id - 480) >> 2, pn = id & 3;
        u.lda = 0; u.ak = 2048; u.ah = 524288u;
        u.A = p.ws + WH(WS_OB + (size_t)pm * 1048576); u.B = (const char*)(p.bwo + pn * 256); u.ldb = DM * 2;
        u.C = (char*)(p.out + (size_t)pm * 256 * 2048 + 1024 + pn * 256); u.ldc = 4096; u.cw = 64; u.ch = 256; u.sig = 0; u.nsig = 0;
    }
}
struct BigSched { const Params& p; unsigned par; int id0, id1, id2; DI bool next(int i, Unit& u) const { const int id = i == 0 ? id0 : (i == 1 ? id1 : (i == 2 ? id2 : -1)); if (id < 0) return false; big_unit(p, par, id, u); return true; } };

__global__ __launch_bounds__(512, 2) void k_fused(Params p) {
    extern __shared__ __attribute__((aligned(16))) unsigned char shm[];
    ldsp lds = (ldsp)shm;
    unsigned* ctl = (unsigned*)(p.ws + WS_CTL);
    const int b = (blockIdx.x & ~7) | ((blockIdx.x + __builtin_amdgcn_readfirstlane(ctl[C_PHASE])) & 7), id = (b & 3) * 32 + (b >> 3);
#define WSTAMP(code)
    const unsigned par = (unsigned)__builtin_amdgcn_readfirstlane(ctl[C_PAR + 1]) < (unsigned)__builtin_amdgcn_readfirstlane(ctl[C_PAR]) ? 1u : 0u;
    WSTAMP(1);
    if ((b & 7) < 4) {
        chain_role(p, par, id, lds, (unsigned long long*)(p.ws + WS_END));
        BAR();
        WSTAMP(2);
    } else {
        {
            const int i = id & 31, kx = id >> 5;
            const u16 *w1 = p.wk, *w2 = p.wv; const u16* Wm = kx < 2 ? w1 : w2;
            Unit u; u.lda = DM * 2; u.ldb = 4096 * 2; u.nt = 16; u.ldc = 256; u.cw = 1024; u.ch = XH; u.ak = 128; u.ah = 0;
            if (i >= 16) {
                const int h = (kx & 1) * 16 + i - 16;
                u.A = (const char*)p.ax; u.B = (const char*)(Wm + h * 128); u.C = p.ws + WH(WS_QKVX + (size_t)(32 + (kx >> 1) * 32 + h) * XH); u.sig = C_KVC(0, h); u.nsig = 1;
                gemm_small(lds, u, ctl);
                WSTAMP(3);
            }
            if (i < 24) {
                const int rb = i < 16 ? 1 + (i & 1) : 3, h = (kx & 1) * 16 + (i < 16 ? i & ~1 : 2 * (i - 16));
                u.A = (const char*)(p.ax + (size_t)rb * 128 * DM); u.B = (const char*)(Wm + h * 128);
                u.C = p.ws + WH(WS_QKVX + (size_t)(32 + (kx >> 1) * 32 + h) * XH + (size_t)rb * 65536); u.sig = C_KVC(rb, h); u.nsig = 2;
                gemm_wide(lds, u, ctl);
                WSTAMP(4);
            }
        }
        if ((id & 31) >= 24) {
            const int t = fresh_tid(), wv = __builtin_amdgcn_readfirstlane(t >> 6), q = (id >> 5) * 8 + (id & 7);
#pragma unroll 1
            for (int i = 0; i < 64; ++i) {
                const char* src = i < 16 ? (const char*)p.ax + (size_t)q * 131072 + i * 8192 : i < 32 ? (const char*)p.bx + (size_t)q * 131072 + (i - 16) * 8192
                                : i < 56 ? (const char*)p.bwqkv + (size_t)q * 196608 + (i - 32) * 8192 : (const char*)p.bwo + (size_t)q * 65536 + (i - 56) * 8192;
                __builtin_amdgcn_global_load_lds((const unsigned*)(src + t * 16), (LAS unsigned*)(lds + L_SCR + wv * 1024), 16, 0, 0);
            }
        }
    }
    volatile LAS unsigned* qw = (volatile LAS unsigned*)(lds + L_QW);
    const int w = __builtin_amdgcn_readfirstlane(threadIdx.x >> 6);
    const int k = b & 3;
    bool fixed = (b & 7) >= 4;
#pragma unroll 1
    for (;;) {
        int kind, x, it = -1, x2 = -1, x3 = -1;
        if (fixed) { const int i = id & 31; kind = 6; x = i < 24 ? (16 + 8 * k + (i & 7)) * 8 + 2 + (i >> 3) : (4 * k + (i & 3)) * 8 + ((i >> 2) & 1); x2 = x + (i < 24 ? 3 : 2); x3 = i < 24 ? 384 + 24 * k + i : -1; fixed = false; }
        else {
        if (threadIdx.x == 0) qw[0] = __hip_atomic_fetch_add(ctl + C_HEAD + k * 32, 1u, RLX_AGENT) + 1u;
        LDS_BAR();
        it = (int)__builtin_amdgcn_readfirstlane(qw[0]);
        LDS_BAR();
        if (it < 4) { kind = 0; x = it * 4 + k; }
        else if (it < 8) { kind = 6; x = (4 * k + it - 4) * 8 + 4; }
        else if (it < 12) { kind = 2; x = it - 8; }
        else if (it < 16) { kind = 6; x = (4 * k + it - 12) * 8 + 5; }
        else if (it < 20) { kind = 0; x = (it - 12) * 4 + k; }
        else if (it < 52) { kind = 1; x = ((it - 20) >> 1) * 8 + 2 * k + ((it - 20) & 1); }
        else if (it < 52 + NSTR) { kind = 4; x = (it - 52) * 4 + k; }
        else if (it < 60 + NSTR) { const int i = it - 52 - NSTR; kind = 6; x = (4 * k + (i & 3)) * 8 + 6 + (i >> 2); }
        else if (it < 64 + NSTR) { kind = 2; x = 4 + it - 60 - NSTR; }
        else if (it < 72 + NSTR) { kind = 6; x = 480 + (it - 64 - NSTR) * 4 + k; }
        else if (it < 74 + NSTR) { kind = 2; x = 8 + it - 72 - NSTR; }
        else if (it < 76 + NSTR) { kind = 3; x = 80 + 2 * k + it - 74 - NSTR; }
        else if (it < 92 + NSTR) { const int i = it - 76 - NSTR; kind = 5; x = (i >> 3) * 32 + 8 * k + (i & 7); }
        else if (it < 96 + NSTR) { const int i = it - 92 - NSTR; kind = 3; x = (11 + (i >> 1)) * 8 + 2 * k + (i & 1); }
        else break;
        }
        if (kind == 6) {
            if (x >= 480) { const int w0 = C_OB + ((x - 480) >> 2) * 64; wg_wait(ctl, w0, 64u, w0 + 32, 64u); }
            BigSched S{p, par, x, x2, x3};
            gemm_stream(lds, S, ctl);
        } else
        if (kind == 0) {
            const int s0 = (x >> 2) * 4;
            wait_count(ctl, C_Q + (s0 >> 3) * 32, 128u); wait_count<24>(ctl, C_PROG, (unsigned)s0 + 5u);
            asm volatile("" ::: "memory");
#pragma unroll 1
            for (int s = s0; s < s0 + 4; ++s) attn_a_item<false>(p, par, s, (x & 3) * 8 + w, lds + w * 12288, fresh_tid() & 63);
            WAIT_V(0); signal(ctl, C_OA + (x >> 2) * 32, 1);
            BAR();
        } else if (kind == 4) {
#pragma unroll 1
            for (int s = 32 + (x >> 2); s < NSEG; s += NSTR) {
                wait_count(ctl, C_Q + (s >> 3) * 32, 128u); wait_count(ctl, C_KVC(s >> 2, (x & 3) * 8 + w), 16u);
                asm volatile("" ::: "memory");
                attn_a_item<true>(p, par, s, (x & 3) * 8 + w, lds + w * 12288, fresh_tid() & 63);
                WAIT_V(0);
                if (s < S_TAIL) signal(ctl, C_OA + (s >> 2) * 32, 1);
                else { BAR(); const int l = fresh_tid(); if (l < 4) __hip_atomic_fetch_add(ctl + C_OS + (s - S_TAIL) * 128 + l * 32, 1u, RLX_AGENT); }
            }
            BAR();
        } else if (kind == 1) {
            const int qb = x >> 3;
            wait_count(ctl, C_B + (qb >> 1) * 32, 96u); wait_count(ctl, C_B + max((qb >> 1) - 1, 0) * 32, 96u);
            asm volatile("" ::: "memory");
            attn_b_item(p, par, x & 7, qb, lds);
        } else if (kind == 5) {
            out_tail(p, par, x & 31, x >> 5, lds);
        } else {
            const int rb = kind == 2 ? x : x >> 3, col = kind == 2 ? k * 256 : (x & 7) * 128;
            Unit u; u.ldb = DM * 2; u.ldc = 2048 * 2; u.sig = 0; u.nsig = 0; u.lda = 0; u.ak = 2048; u.ah = 0; u.cw = 64; u.ch = 0;
            u.A = p.ws + WH(WS_OA + (size_t)rb * 2097152); u.B = (const char*)(p.wo + col); u.nt = 64; u.C = (char*)(p.out + (size_t)rb * 128 * 2048 + col);
            wg_wait(ctl, C_OA + rb * 32, rb < 8 ? 32u : 128u, C_OA + rb * 32, 32u);
            if (kind == 2) gemm_wide(lds, u, ctl); else gemm_small(lds, u, ctl);
        }
        WSTAMP(16 + (kind == 6 && x >= 480 ? (x < 512 ? 7 : 8) : kind) + ((it & 0xffff) << 8));
    }
}

__global__ __launch_bounds__(512) void k_init(char* ws) {
    const rsrc_t xr = mkrsrc(ws + WS_XCH, NWIN_INIT * 16384u);
    unsigned* ctl = (unsigned*)(ws + WS_CTL);
    const u32x4 sv = {SENT, SENT, SENT, SENT};
    const unsigned xcc = (unsigned)__builtin_amdgcn_s_getreg(20 | (3 << 11)) & 7u, half = blockIdx.x >> 3, win = blockIdx.x & 7;
    if (threadIdx.x < 64 && xcc == 1) {
        unsigned b = ~0u;
        for (int i = 0; i < 5; ++i) {
            const unsigned long long t0 = __builtin_amdgcn_s_memtime();
            __builtin_amdgcn_raw_buffer_store_b32(0u, xr, (unsigned)(2 * 16384 + C_PROBE * 4) + half * 8192u + (unsigned)i * 256u, 0, 16);
            asm volatile("s_waitcnt vmcnt(0)" ::: "memory");
            b = min(b, (unsigned)(__builtin_amdgcn_s_memtime() - t0));
        }
        if (threadIdx.x == 0) ctl[C_PAR + half] = b;
    }
    __syncthreads();
    if (win < 7 && win != 5) ST16(sv, xr, win * 16384u + half * 8192u + threadIdx.x * 16u, 0, 16);
    if (blockIdx.x == 0 && threadIdx.x == 0) ctl[C_PHASE] = xcc;
}

extern "C" void kernel_launch(void* const* d_in, const int* in_sizes, int n_in, void* d_out, int out_size, void* d_ws, size_t ws_size, hipStream_t stream) {
    Params p;
    p.ax = (const u16*)d_in[0]; p.mem0 = (const u16*)d_in[1]; p.wq = (const u16*)d_in[2]; p.wk = (const u16*)d_in[3]; p.wv = (const u16*)d_in[4]; p.wo = (const u16*)d_in[5];
    p.bx = (const u16*)d_in[7]; p.bwqkv = (const u16*)d_in[8]; p.bwo = (const u16*)d_in[9];
    p.out = (u16*)d_out; p.ws = (char*)(((uintptr_t)d_ws + 0x1FFFFFu) & ~(uintptr_t)0x1FFFFFu);
    (void)hipFuncSetAttribute((const void*)k_fused, hipFuncAttributeMaxDynamicSharedMemorySize, LDS_BYTES);
    k_init<<<16, 512, 0, stream>>>(p.ws);
    k_fused<<<256, 512, LDS_BYTES, stream>>>(p);
}
```

```cpp
#include <hip/hip_runtime.h>
#include <stdint.h>

#define LAS __attribute__((address_space(3)))
#define DI __device__ __forceinline__
typedef unsigned short u16;
typedef __attribute__((ext_vector_type(8))) __bf16 bf16x8;
typedef __attribute__((ext_vector_type(16))) __bf16 bf16x16;
typedef __attribute__((ext_vector_type(2))) __bf16 bf16x2;
typedef __attribute__((ext_vector_type(4))) short s16x4;
typedef __attribute__((ext_vector_type(8))) short s16x8;
typedef __attribute__((ext_vector_type(16))) short s16x16;
typedef __attribute__((ext_vector_type(2))) float f32x2;
typedef __attribute__((ext_vector_type(4))) float f32x4;
typedef __attribute__((ext_vector_type(2))) unsigned u32x2;
typedef __attribute__((ext_vector_type(4))) unsigned u32x4;
typedef LAS unsigned char* ldsp;
typedef __amdgpu_buffer_rsrc_t rsrc_t;

constexpr int S_LEN = 2048, DM = 1024, NH = 32, DH = 128, NSEG = 64;
constexpr int LDB3 = 3072, S_TAIL = 52;
constexpr unsigned XH = S_LEN * 512, BH = S_LEN * 128;
constexpr unsigned SENT = 0xFFFFFFFFu;
constexpr size_t WS_XCH = 2u << 20;
constexpr int NWIN = 640, NWIN_INIT = 7;
constexpr size_t XCH_BYTES = (size_t)(NWIN + 2048 + 512) * 16384;
constexpr size_t WS_CTL = WS_XCH + 2 * 16384;
constexpr int C_TMO = 0, C_PAR = 13312  , C_PHASE = 13314, C_HEAD = 3072, C_PROG = 192, C_PACE = 128, C_Q = 256, C_B = C_Q + 8 * 32, C_OA = C_B + 8 * 32, C_OB = C_OA + 16 * 32, C_OS = 16384, C_KV = 4096, C_PROBE = 12288;
constexpr size_t WS_QKVX = 52u << 20;
constexpr size_t WS_QKVB = WS_QKVX + (size_t)96 * XH;
constexpr size_t WS_OA = WS_QKVB + (size_t)96 * BH;
constexpr size_t WS_OB = WS_OA + (size_t)S_LEN * 4096 * 4;
constexpr size_t WS_END = WS_OB + (size_t)S_LEN * 1024 * 4;
#define C_KVC(rb, h) (C_KV + (h) * 32 + (rb))
#define gap(L) (((L) & 8191u) | (((L) >> 13) << 14))
#define WH(off) ((off) + (size_t)((((unsigned)((off) >> 21) ^ par) & 1u) << 13))
constexpr int L_QW = 147456, LDS_BYTES = L_QW + 1024 + 8192;

DI unsigned pk_bf16(float a, float b) { f32x2 v = {a, b}; return __builtin_bit_cast(unsigned, __builtin_convertvector(v, bf16x2)); }
DI s16x4 trd(ldsp p) { return __builtin_amdgcn_ds_read_tr16_b64_v4i16((LAS s16x4*)p); }
DI bf16x8 cat8(s16x4 a, s16x4 b) { return __builtin_bit_cast(bf16x8, __builtin_shufflevector(a, b, 0, 1, 2, 3, 4, 5, 6, 7)); }
DI bf16x16 cat16(bf16x8 a, bf16x8 b) { return __builtin_shufflevector(a, b, 0, 1, 2, 3, 4, 5, 6, 7, 8, 9, 10, 11, 12, 13, 14, 15); }
DI bf16x8 lo8(bf16x16 v) { return __builtin_shufflevector(v, v, 0, 1, 2, 3, 4, 5, 6, 7); }
DI bf16x8 hi8(bf16x16 v) { return __builtin_shufflevector(v, v, 8, 9, 10, 11, 12, 13, 14, 15); }
DI bf16x8 asbf(u32x4 v) { return __builtin_bit_cast(bf16x8, v); }
DI f32x4 mma32(bf16x8 a, bf16x8 b, f32x4 c) { return __builtin_amdgcn_mfma_f32_16x16x32_bf16(a, b, c, 0, 0, 0); }
DI f32x4 mma16(u32x2 a, u32x2 b, f32x4 c) { return mma32(asbf((u32x4){a.x, a.y, 0u, 0u}), asbf((u32x4){b.x, b.y, 0u, 0u}), c); }
DI f32x4 smma(u32x4 a, bf16x16 b, f32x4 c, int idx) { return __builtin_amdgcn_smfmac_f32_16x16x64_bf16(asbf(a), b, c, idx, 0, 0); }
DI rsrc_t mkrsrc(const void* p, unsigned bytes) { return __builtin_amdgcn_make_buffer_rsrc((void*)p, 0, bytes, 0x00020000); }
DI u32x4 ld16_sc1(rsrc_t r, unsigned voff, unsigned soff) { return __builtin_amdgcn_raw_buffer_load_b128(r, voff, soff, 16); }
#define ST16(v, r, voff, soff, aux) do { const u32x4 st_v = (v); __builtin_amdgcn_raw_buffer_store_b128(st_v, r, voff, soff, aux); asm volatile("s_nop 1" :: "v"(st_v)); } while (0)
DI void swap16(unsigned& a, unsigned& b) { auto r = __builtin_amdgcn_permlane16_swap(a, b, false, false); a = r[0]; b = r[1]; }
DI void swap32(unsigned& a, unsigned& b) { auto r = __builtin_amdgcn_permlane32_swap(a, b, false, false); a = r[0]; b = r[1]; }
DI void swap16f(float& a, float& b) { unsigned x = __float_as_uint(a), y = __float_as_uint(b); swap16(x, y); a = __uint_as_float(x); b = __uint_as_float(y); }
DI void swap32f(float& a, float& b) { unsigned x = __float_as_uint(a), y = __float_as_uint(b); swap32(x, y); a = __uint_as_float(x); b = __uint_as_float(y); }
DI float rows_max(float v) { float a = v, b = v; swap16f(a, b); v = fmaxf(a, b); a = v; b = v; swap32f(a, b); return fmaxf(a, b); }
DI float rows_sum(float v) { float a = v, b = v; swap16f(a, b); v = a + b; a = v; b = v; swap32f(a, b); return a + b; }
DI unsigned umax4(u32x4 v) { return max(max(v.x, v.y), max(v.z, v.w)); }
#define WAIT_V(n) asm volatile("s_waitcnt vmcnt(" #n ")" ::: "memory")
#define WAIT_L(n) asm volatile("s_waitcnt lgkmcnt(" #n ")" ::: "memory")
#define BAR() __builtin_amdgcn_s_barrier()
#define SCHED() __builtin_amdgcn_sched_barrier(0)
#define LDS_BAR() do { asm volatile("s_waitcnt lgkmcnt(0)" ::: "memory"); __builtin_amdgcn_s_barrier(); asm volatile("" ::: "memory"); } while (0)

constexpr int HTB = 128 * 64 * 2;
DI int lds_byte(int r, int c) { const int st = (r >> 4) * 2 + (c >> 5), rr = r & 15, cc = c & 31, ob = rr * 64 + cc * 2; return st * 1024 + (ob ^ (((ob >> 9) & 1) << 5)); }
DI void stage_rc(int b, int& R, int& C) { const int st = b / 1024, sb = b % 1024, swz = sb ^ (((sb >> 9) & 1) << 5); R = (st >> 1) * 16 + swz / 64; C = (st & 1) * 32 + (swz % 64) / 2; }

struct Params {
    const u16 *ax, *mem0, *wq, *wk, *wv, *wo, *bx, *bwqkv, *bwo;
    u16* out; char* ws;
};
#define RLX_AGENT __ATOMIC_RELAXED, __HIP_MEMORY_SCOPE_AGENT
DI bool spin_out(unsigned& spins, unsigned* ctl) {
    spins = __builtin_amdgcn_readfirstlane(spins) + 1u;
    if ((spins & 63u) == 0u) {
        if (spins > (1u << 18)) { __hip_atomic_store(ctl + C_TMO, 1u, RLX_AGENT); return true; }
        if (__builtin_amdgcn_readfirstlane(__hip_atomic_load(ctl + C_TMO, RLX_AGENT)) != SENT) return true;
    }
    return false;
}
template <int NAP = 8>
DI void wait_count(unsigned* ctl, int word, unsigned need) {
    unsigned spins = 0;
    while (__builtin_amdgcn_readfirstlane(__hip_atomic_load(ctl + word, RLX_AGENT)) + 1u < need) { __builtin_amdgcn_s_sleep(NAP); if (spin_out(spins, ctl)) break; }
}
DI void wg_wait(unsigned* ctl, int word0, unsigned need0, int word1, unsigned need1) {
    if (threadIdx.x < 64) {
        wait_count(ctl, word0, need0); wait_count(ctl, word1, need1);
        __builtin_amdgcn_fence(__ATOMIC_ACQUIRE, "agent");
        asm volatile("s_waitcnt vmcnt(0)" ::: "memory");
    }
    asm volatile("" ::: "memory"); BAR(); asm volatile("" ::: "memory");
}
struct Unit { const char* A; const char* B; char* C; unsigned lda, ldb, ldc, cw, ch, ak, ah; int nt, sig, nsig; };
DI int fresh_tid() { int t = threadIdx.x; asm volatile("" : "+v"(t)); return t; }
DI void signal(unsigned* ctl, int sig, int nsig) { const int lane = fresh_tid() & 63; if (lane < nsig) __hip_atomic_fetch_add(ctl + sig + (lane & 1) * 32 + (lane >> 1), 1u, RLX_AGENT); }
DI u32x4 pack_pair(f32x4 v0, f32x4 v1) {
    unsigned ax = pk_bf16(v0[0], v0[1]), ay = pk_bf16(v0[2], v0[3]), bx = pk_bf16(v1[0], v1[1]), by = pk_bf16(v1[2], v1[3]);
    swap16(ax, bx); swap16(ay, by);
    return (u32x4){ax, ay, bx, by};
}
DI unsigned frag_off(int lane) { return (unsigned)((((lane >> 4) & 1) * 2 + (lane >> 5)) * 256 + (lane & 15) * 16); }
DI void store_frag(rsrc_t cr, unsigned voff, unsigned soff, f32x4 v0, f32x4 v1) { ST16(pack_pair(v0, v1), cr, voff, soff, 16); }
DI u32x4 to_rows(ldsp sc, int lane, f32x4 v0, f32x4 v1) {
    *(volatile LAS u32x4*)(sc + ((lane & 15) * 4 + ((lane >> 4) & 1) * 2 + (lane >> 5)) * 16) = pack_pair(v0, v1);
    return *(volatile LAS u32x4*)(sc + lane * 16);
}
constexpr int L_SCR = L_QW + 1024;
#define STAGE(bufoff, gbase, voff, step2) do { \
        __builtin_amdgcn_global_load_lds((const unsigned*)((gbase) + (voff)), (LAS unsigned*)(lds + (bufoff) + ldsw), 16, 0, 0); \
        __builtin_amdgcn_global_load_lds((const unsigned*)((gbase) + (step2) + (voff)), (LAS unsigned*)(lds + (bufoff) + ldsw + 8192), 16, 0, 0); } while (0)
#define LDA(dst, off) do { _Pragma("unroll") for (int m = 0; m < 4; ++m) _Pragma("unroll") for (int k = 0; k < 2; ++k) dst[m][k] = *(const LAS bf16x8*)(lds + (off) + aoff + m * 2048 + k * 1024); } while (0)
#define TRD(dst, base, off) asm volatile("ds_read_b64_tr_b16 %0, %1 offset:%2" : "=v"(dst) : "v"(base), "i"(off) : "memory")
#define LDB(dst, v0, v1, off) do { s16x4 l0, h0, l1, h1, l2, h2, l3, h3; \
        TRD(l0, v0, (off)); TRD(h0, v0, (off) + 256); TRD(l1, v0, (off) + 8192); TRD(h1, v0, (off) + 8448); \
        TRD(l2, v1, (off)); TRD(h2, v1, (off) + 256); TRD(l3, v1, (off) + 8192); TRD(h3, v1, (off) + 8448); \
        dst[0][0] = cat8(l0, h0); dst[0][1] = cat8(l1, h1); dst[1][0] = cat8(l2, h2); dst[1][1] = cat8(l3, h3); } while (0)
#define MMA(accv, At, Bt) do { SCHED(); __builtin_amdgcn_s_setprio(1); _Pragma("unroll") for (int m = 0; m < 4; ++m) _Pragma("unroll") for (int n = 0; n < 2; ++n) _Pragma("unroll") for (int k = 0; k < 2; ++k) \
        accv[m][n] = mma32(Bt[n][k], At[m][k], accv[m][n]); __builtin_amdgcn_s_setprio(0); } while (0)
DI unsigned va_of(int tid, unsigned lda, int nt) { int R, C; stage_rc(tid * 16, R, C); return lda ? (unsigned)R * lda + (unsigned)C * 2u : (unsigned)((tid >> 7) * nt * 4096 + (tid & 127) * 16); }
DI unsigned vb_of(int tid, int wid, unsigned ldb) { return (unsigned)(8 * (wid >> 1) + ((tid >> 2) & 7)) * ldb + (unsigned)((2 * (wid & 1) + ((tid >> 5) & 1)) * 64 + (((tid & 3) ^ (((wid >> 1) & 1) << 1)) * 16)); }
#define GEMM_LANE_SETUP(lda0) \
    const int tid = fresh_tid(); \
    const int wid = __builtin_amdgcn_readfirstlane(tid >> 6), lane = tid & 63, wr = wid >> 2, wc = wid & 3, fr = lane & 15, fq = lane >> 4; \
    const unsigned ldsw = (unsigned)wid * 1024u; \
    const int aoff = (lda0) ? lds_byte(wr * 64 + fr, fq * 8) : wr * 8192 + lane * 16; \
    const int boff0 = fq * 2048 + wc * 512 + (fr >> 2) * 64 + (fq & 1) * 32 + (fr & 3) * 8, boff1 = boff0 ^ 32;

template <class Sched>
DI void gemm_stream(ldsp lds, const Sched& S, unsigned* ctl) {
    Unit cur, nxt; int ui = 0;
    if (!S.next(0, cur)) return;
    GEMM_LANE_SETUP(cur.lda);
#define SA(b, h) (((b) * 2 + (h)) * HTB)
#define SB(b, h) ((4 + (b) * 2 + (h)) * HTB)
    const unsigned vb0 = (unsigned)(size_t)lds + 65536u + (unsigned)boff0, vb1 = (unsigned)(size_t)lds + 65536u + (unsigned)boff1;
    f32x4 acc[2][2][4][2];
#pragma unroll
    for (int a = 0; a < 2; ++a)
#pragma unroll
        for (int b = 0; b < 2; ++b)
#pragma unroll
            for (int m = 0; m < 4; ++m)
#pragma unroll
                for (int n = 0; n < 2; ++n) acc[a][b][m][n] = (f32x4){0.f, 0.f, 0.f, 0.f};
    bf16x8 At[4][2], B0[2][2], B1[2][2];
    const unsigned vA = va_of(tid, cur.lda, cur.nt); unsigned vB = vb_of(tid, wid, cur.ldb);
    const size_t a2s = cur.lda ? (size_t)64 * cur.lda : (size_t)cur.nt * 16384;
    {
        const size_t ah = cur.ah, b2s = (size_t)32 * cur.ldb, bk = (size_t)64 * cur.ldb;
        STAGE(SB(0, 0), cur.B, vB, b2s); STAGE(SB(0, 1), cur.B + 256, vB, b2s); STAGE(SA(0, 0), cur.A, vA, a2s); STAGE(SA(0, 1), cur.A + ah, vA, a2s);
        if (wr == 1) BAR();
        WAIT_V(2); BAR();
        STAGE(SB(1, 0), cur.B + bk, vB, b2s); STAGE(SA(1, 0), cur.A + cur.ak, vA, a2s); STAGE(SB(1, 1), cur.B + bk + 256, vB, b2s);
        WAIT_V(6); BAR();
    }
    int psig = 0, pn = 0;
    for (;;) {
        const bool has_next = S.next(ui + 1, nxt);
        if (!has_next) nxt = cur;
        int nt = cur.nt; asm volatile("" : "+s"(nt));
        for (int t = 0; t < nt; t += 2) {
            const bool last = (t == nt - 2);
            const char* a1 = cur.A + (cur.lda ? (unsigned)(t + 1) * 128u : gap((unsigned)(t + 1) * 2048u)) + cur.ah;
            if (last) vB = vb_of(fresh_tid(), wid, nxt.ldb);
            const unsigned ldb = last ? nxt.ldb : cur.ldb;
            const char* a2 = last ? nxt.A : cur.A + (cur.lda ? (unsigned)(t + 2) * 128u : gap((unsigned)(t + 2) * 2048u));
            const char* b2 = last ? nxt.B : cur.B + (size_t)(t + 2) * 64 * cur.ldb;
            const size_t ah = cur.ah, b2s = (size_t)32 * ldb;
            const char* a3 = a2 + cur.ak; const char* b3 = b2 + (size_t)64 * ldb;
            LDB(B0, vb0, vb1, 0); LDB(B1, vb0, vb1, HTB); SCHED(); LDA(At, SA(0, 0)); STAGE(SA(1, 1), a1, vA, a2s);
            WAIT_V(8); WAIT_L(0); BAR(); MMA(acc[0][0], At, B0); MMA(acc[0][1], At, B1); BAR(); SCHED();
            LDA(At, SA(0, 1)); STAGE(SB(0, 0), b2, vB, b2s); STAGE(SB(0, 1), b2 + 256, vB, b2s); STAGE(SA(0, 0), a2, vA, a2s);
            WAIT_V(8);
            asm volatile("" : "+s"(pn));
            if (pn) { signal(ctl, psig, pn); pn = 0; }
            WAIT_L(0); BAR(); MMA(acc[1][0], At, B0); MMA(acc[1][1], At, B1); BAR(); SCHED();
            LDB(B0, vb0, vb1, 2 * HTB); LDB(B1, vb0, vb1, 3 * HTB); SCHED(); LDA(At, SA(1, 0)); STAGE(SA(0, 1), a2 + ah, vA, a2s);
            WAIT_V(8); WAIT_L(0); BAR(); MMA(acc[0][0], At, B0); MMA(acc[0][1], At, B1); BAR(); SCHED();
            LDA(At, SA(1, 1)); STAGE(SB(1, 0), b3, vB, b2s); STAGE(SB(1, 1), b3 + 256, vB, b2s); STAGE(SA(1, 0), a3, vA, a2s);
            WAIT_V(8); WAIT_L(0); BAR(); MMA(acc[1][0], At, B0); MMA(acc[1][1], At, B1); BAR(); SCHED();
        }
        if (wr == 0) BAR();
        {
            const rsrc_t cr = mkrsrc(cur.C, 0x1000000u);
            const int tz = fresh_tid() & 63;
            if (cur.nsig) {
                const unsigned co = (unsigned)wc * cur.cw + frag_off(tz);
#pragma unroll
                for (int ai = 0; ai < 2; ++ai)
#pragma unroll
                    for (int m = 0; m < 4; ++m)
#pragma unroll
                        for (int bj = 0; bj < 2; ++bj) store_frag(cr, co, gap((unsigned)(ai * 128 + wr * 64 + m * 16) * cur.ldc) + (unsigned)bj * cur.ch, acc[ai][bj][m][0], acc[ai][bj][m][1]);
            } else {
                const unsigned co = (unsigned)(wr * 64 + (tz >> 2)) * cur.ldc + (unsigned)(wc * 64 + (tz & 3) * 16);
#pragma unroll
                for (int ai = 0; ai < 2; ++ai)
#pragma unroll
                    for (int m = 0; m < 4; ++m)
#pragma unroll
                        for (int bj = 0; bj < 2; ++bj)
                            ST16(to_rows(lds + L_SCR + wid * 1024, tz, acc[ai][bj][m][0], acc[ai][bj][m][1]), cr, co, (unsigned)(ai * 128 + m * 16) * cur.ldc + (unsigned)bj * 256u, 0);
            }
        }
        psig = cur.sig; pn = cur.nsig;
        if (!has_next) break;
#pragma unroll
        for (int a = 0; a < 2; ++a)
#pragma unroll
            for (int b = 0; b < 2; ++b)
#pragma unroll
                for (int m = 0; m < 4; ++m)
#pragma unroll
                    for (int n = 0; n < 2; ++n) acc[a][b][m][n] = (f32x4){0.f, 0.f, 0.f, 0.f};
        cur = nxt; ++ui;
        if (wr == 1) BAR();
    }
    WAIT_V(0);
    signal(ctl, psig, pn);
    BAR();
#undef SA
#undef SB
}

DI void gemm_small(ldsp lds, const Unit u, unsigned* ctl) {
    GEMM_LANE_SETUP(u.lda);
    const unsigned vA = va_of(tid, u.lda, u.nt), vB = vb_of(tid, wid, u.ldb);
    const size_t a2s = u.lda ? (size_t)64 * u.lda : (size_t)u.nt * 16384, b2s = (size_t)32 * u.ldb, bk = (size_t)64 * u.ldb;
    f32x4 acc[4][2];
#pragma unroll
    for (int m = 0; m < 4; ++m)
#pragma unroll
        for (int n = 0; n < 2; ++n) acc[m][n] = (f32x4){0.f, 0.f, 0.f, 0.f};
    int nt = u.nt; asm volatile("" : "+s"(nt));
    const unsigned vb0 = (unsigned)(size_t)lds + (unsigned)boff0, vb1 = (unsigned)(size_t)lds + (unsigned)boff1, vb2 = vb0 + 65536u, vb3 = vb1 + 65536u;
#pragma unroll
    for (int t = 0; t < 4; ++t) { STAGE(t * 32768, u.A + (size_t)t * u.ak, vA, a2s); STAGE(t * 32768 + 16384, u.B + t * bk, vB, b2s); }
    bf16x8 A0[4][2], B0[2][2], A1[4][2], B1[2][2];
    WAIT_V(12); BAR();
    LDB(B0, vb0, vb1, 16384); LDA(A0, 0);
#define RDA(dst, m, off) do { dst[m][0] = *(const LAS bf16x8*)(lds + (off) + aoff + (m) * 2048); dst[m][1] = *(const LAS bf16x8*)(lds + (off) + aoff + (m) * 2048 + 1024); } while (0)
#define RDB(dst, nn, v, off) do { s16x4 l0, h0, l1, h1; TRD(l0, v, (off)); TRD(h0, v, (off) + 256); TRD(l1, v, (off) + 8192); TRD(h1, v, (off) + 8448); dst[nn][0] = cat8(l0, h0); dst[nn][1] = cat8(l1, h1); } while (0)
#define MM4(Ac, Bc, m) do { SCHED(); _Pragma("unroll") for (int n = 0; n < 2; ++n) _Pragma("unroll") for (int k = 0; k < 2; ++k) acc[m][n] = mma32(Bc[n][k], Ac[m][k], acc[m][n]); SCHED(); } while (0)
#define KSTEP(i, Ac, Bc, An, Bn, v0, v1, boffn) do { \
        const int tn = min(t0 + (i) + 4, nt - 1); \
        WAIT_L(0); WAIT_V(8); BAR(); \
        MM4(Ac, Bc, 0); RDB(Bn, 0, v0, boffn); RDA(An, 0, (((i) + 1) & 3) * 32768); \
        MM4(Ac, Bc, 1); RDB(Bn, 1, v1, boffn); RDA(An, 1, (((i) + 1) & 3) * 32768); \
        MM4(Ac, Bc, 2); RDA(An, 2, (((i) + 1) & 3) * 32768); STAGE((i) * 32768, u.A + (u.lda ? (unsigned)tn * 128u : gap((unsigned)tn * 2048u)), vA, a2s); \
        MM4(Ac, Bc, 3); RDA(An, 3, (((i) + 1) & 3) * 32768); STAGE((i) * 32768 + 16384, u.B + (size_t)tn * bk, vB, b2s); } while (0)
    for (int t0 = 0; t0 < nt; t0 += 4) {
        KSTEP(0, A0, B0, A1, B1, vb0, vb1, 49152);
        KSTEP(1, A1, B1, A0, B0, vb2, vb3, 16384);
        KSTEP(2, A0, B0, A1, B1, vb2, vb3, 49152);
        KSTEP(3, A1, B1, A0, B0, vb0, vb1, 16384);
    }
    WAIT_L(0); SCHED();
#undef RDA
#undef RDB
#undef MM4
#undef KSTEP
    const rsrc_t cr = mkrsrc(u.C, 256u * u.ldc);
    if (u.nsig) {
#pragma unroll
        for (int m = 0; m < 4; ++m) store_frag(cr, (unsigned)wc * u.cw + frag_off(lane), gap((unsigned)(wr * 64 + m * 16) * u.ldc), acc[m][0], acc[m][1]);
    } else {
#pragma unroll
        for (int m = 0; m < 4; ++m)
            ST16(to_rows(lds + L_SCR + wid * 1024, lane, acc[m][0], acc[m][1]), cr, (unsigned)(wr * 64 + (lane >> 2)) * u.ldc + (unsigned)(wc * 64 + (lane & 3) * 16), (unsigned)(m * 16) * u.ldc, 0);
    }
    WAIT_V(0);
    signal(ctl, u.sig, u.nsig);
    BAR();
}

DI void gemm_wide(ldsp lds, const Unit u, unsigned* ctl) {
    GEMM_LANE_SETUP(u.lda);
    const char *A = u.A, *B = u.B; int nt = u.nt;
    const unsigned vA = va_of(tid, u.lda, nt), vB = vb_of(tid, wid, u.ldb);
    const size_t a2s = u.lda ? (size_t)64 * u.lda : (size_t)nt * 16384, b2s = (size_t)32 * u.ldb, bk = (size_t)64 * u.ldb;
    f32x4 acc[2][4][2];
#pragma unroll
    for (int b = 0; b < 2; ++b)
#pragma unroll
        for (int m = 0; m < 4; ++m)
#pragma unroll
            for (int n = 0; n < 2; ++n) acc[b][m][n] = (f32x4){0.f, 0.f, 0.f, 0.f};
    asm volatile("" : "+s"(nt));
    const unsigned vb0 = (unsigned)(size_t)lds + (unsigned)boff0, vb1 = (unsigned)(size_t)lds + (unsigned)boff1;
#define STG3(slot, t) do { STAGE((slot), A + (t) * u.ak, vA, a2s); STAGE((slot) + 16384, B + (size_t)(t) * bk, vB, b2s); STAGE((slot) + 32768, B + (size_t)(t) * bk + 256, vB, b2s); } while (0)
    STG3(0, 0); STG3(49152, 1); STG3(98304, 2);
    bf16x8 A0[4][2], P0[2][2], Q0[2][2], A1[4][2], P1[2][2], Q1[2][2];
    WAIT_V(12); BAR();
    LDB(P0, vb0, vb1, 16384); LDB(Q0, vb0, vb1, 32768); LDA(A0, 0);
    int sc = 0, sn = 49152;
#define RDA(dst, m) do { dst[m][0] = *(const LAS bf16x8*)(lds + sn + aoff + (m) * 2048); dst[m][1] = *(const LAS bf16x8*)(lds + sn + aoff + (m) * 2048 + 1024); } while (0)
#define RDB(dst, nn, v, off) do { s16x4 l0, h0, l1, h1; TRD(l0, v, (off)); TRD(h0, v, (off) + 256); TRD(l1, v, (off) + 8192); TRD(h1, v, (off) + 8448); dst[nn][0] = cat8(l0, h0); dst[nn][1] = cat8(l1, h1); } while (0)
#define MM4(b, Ac, Bc, m) do { SCHED(); _Pragma("unroll") for (int n = 0; n < 2; ++n) _Pragma("unroll") for (int k = 0; k < 2; ++k) acc[b][m][n] = mma32(Bc[n][k], Ac[m][k], acc[b][m][n]); SCHED(); } while (0)
#define KSTEP(t, Ac, Pc, Qc, An, Pn, Qn) do { \
        const int tn = min((t) + 3, nt - 1); const unsigned u0 = vb0 + (unsigned)sn, u1 = vb1 + (unsigned)sn; \
        WAIT_L(0); WAIT_V(6); BAR(); \
        MM4(0, Ac, Pc, 0); RDB(Pn, 0, u0, 16384); \
        MM4(1, Ac, Qc, 0); RDB(Pn, 1, u1, 16384); \
        MM4(0, Ac, Pc, 1); RDB(Qn, 0, u0, 32768); \
        MM4(1, Ac, Qc, 1); RDB(Qn, 1, u1, 32768); \
        MM4(0, Ac, Pc, 2); RDA(An, 0); STAGE(sc, A + (u.lda ? (unsigned)tn * 128u : gap((unsigned)tn * 2048u)), vA, a2s); \
        MM4(1, Ac, Qc, 2); RDA(An, 1); STAGE(sc + 16384, B + (size_t)tn * bk, vB, b2s); \
        MM4(0, Ac, Pc, 3); RDA(An, 2); STAGE(sc + 32768, B + (size_t)tn * bk + 256, vB, b2s); \
        MM4(1, Ac, Qc, 3); RDA(An, 3); \
        sc = sn; sn = sn == 98304 ? 0 : sn + 49152; } while (0)
    for (int t0 = 0; t0 < nt; t0 += 2) { KSTEP(t0, A0, P0, Q0, A1, P1, Q1); KSTEP(t0 + 1, A1, P1, Q1, A0, P0, Q0); }
    WAIT_L(0); SCHED();
#undef STG3
#undef RDA
#undef RDB
#undef MM4
#undef KSTEP
    const rsrc_t cr = mkrsrc(u.C, 0x1000000u);
    if (u.nsig) {
#pragma unroll
        for (int m = 0; m < 4; ++m)
#pragma unroll
            for (int b = 0; b < 2; ++b) store_frag(cr, (unsigned)wc * u.cw + frag_off(lane), gap((unsigned)(wr * 64 + m * 16) * u.ldc) + (unsigned)b * u.ch, acc[b][m][0], acc[b][m][1]);
    } else {
#pragma unroll
        for (int m = 0; m < 4; ++m)
#pragma unroll
            for (int b = 0; b < 2; ++b)
                ST16(to_rows(lds + L_SCR + wid * 1024, lane, acc[b][m][0], acc[b][m][1]), cr, (unsigned)(wr * 64 + (lane >> 2)) * 4096u + (unsigned)(wc * 64 + (lane & 3) * 16), (unsigned)(m * 65536 + b * 256), 0);
    }
    WAIT_V(0);
    signal(ctl, u.sig, u.nsig);
    BAR();
}

#define win_off(idx) ((unsigned)(idx) * 16384u + ((((unsigned)(idx) >> 7) ^ par) & 1u) * 8192u)
#define km_win(s, h) (win_off(NWIN + (s) * 32 + (h)) ^ 8192u)
#define W_MEM(s, x) ((s) == 0 ? 0 : NWIN + 2048 + 8 * (s) + 2 * (x))
#define W_PART(slot) (192 + (slot) * 64)
#define W_X(slot) (384 + (slot) * 128)
DI bool all_set(unsigned mx) { return __builtin_amdgcn_ballot_w64(mx == SENT) == 0ull; }
DI u32x4 rows_gather(unsigned v) { unsigned a = v, b = v; swap16(a, b); unsigned c = a, d = b; swap32(a, c); swap32(b, d); return (u32x4){a, b, c, d}; }
DI float max3f(float a, float b, float c) { return fmaxf(fmaxf(a, b), c); }
#define KD0 2
#define KD1 6
#define KD2 5
#define NSTR 3
#define KDX 0
#define KPM 4
constexpr int RED_M = 144;
constexpr int L_PX = 32768, L_PM = L_PX + 512, L_ML = L_PM + 512, L_FLAG = L_ML + 256, L_PACE = L_FLAG + 16, L_VM = 34816, L_OWN = L_VM + 2048, L_X = L_OWN + 1536  ;
constexpr unsigned OOB = 0x80000000u;
#define RETRY(i)
#define SEG(i)
#define PST(i)
#define PIN_S(x) asm volatile("" : "+s"(x))
#define PIN_V(x) asm volatile("" : "+v"(x))
DI float ror8(float v) { return __uint_as_float(__builtin_amdgcn_update_dpp(0, __float_as_uint(v), 0x128, 0xf, 0xf, true)); }

template <int ROLE>
DI void chain_loop(const Params& p, const unsigned par, const int rank, const int w, ldsp lds, const bf16x16 (&W)[6][2], const bf16x16 (&WO)[2][2], unsigned long long* stamps) {
    const int lane = fresh_tid() & 63, g = lane >> 4, n = lane & 15;
    const int h = rank >> 2, j = rank & 3;
    unsigned* ctl = (unsigned*)(p.ws + WS_CTL);
    const rsrc_t xr = mkrsrc(p.ws + WS_XCH, (unsigned)XCH_BYTES);
    const rsrc_t qr = mkrsrc(p.ws + WH(WS_QKVX + (size_t)(32 + h) * XH), 33u * XH);
    const int sidx = (n & 2) ? 0xEEEE : 0x4444;
    const unsigned l16 = (unsigned)lane * 16u;
    const int redw = (w * 1024 + (n & 1) * 512 + (n >> 1) * 8 + 2 * (g ^ (n >> 3))) * 4;
    const int redr = (w < 4 ? (n >> 3) * RED_M + (4 * w + g) * 8 + ((n & 7) ^ ((w & 1) * 2)) : 2 * RED_M + (8 * (w & 1) + 2 * g + ((n >> 1) & 1)) * 8 + ((2 * (n >> 2) + (n & 1)) ^ (g & 2))) * 4;
    const bool vown = (g >> 1) == (j & 1);
    const unsigned vg1 = (unsigned)(((j & 2) ^ 2) + (g >> 1)) * 1536u + 1024u + (unsigned)(lane & 31) * 16u;
    const unsigned vg0 = vown ? OOB : (vg1 + (unsigned)(j & 2) * 3072u - 3072u);
    const unsigned pub_off = (g == j ? 0u : (unsigned)(4 * h + g) * 16384u) + (unsigned)j * 1536u + (unsigned)w * 256u + (unsigned)n * 16u;
    const int red = ((j + 1) & 3) * 32 + h;
    const int rho = 4 * (8 * j + w) + 2 * (g >> 1) + ((n >> 2) & 1);
    const unsigned pst_off = n < 8 ? (unsigned)(rho >> 1) * 16384u + (unsigned)(rho & 1) * 4096u + (unsigned)((h ^ rho) & 31) * 128u + (unsigned)((g & 1) * 64 + (n & 3) * 16) : OOB;
    const unsigned mem_off = lane < 32 ? (unsigned)(4 * ((n >> 1) & 1) + 2 * ((n >> 2) & 1) + (n & 1)) * 16u + (unsigned)(lane >> 3) * 32768u : OOB;
    const unsigned xw0 = win_off(W_X(0)), xw1 = win_off(W_X(1));
    unsigned pso = win_off(W_PART(0)), psn = win_off(W_PART(1)), psp = win_off(W_PART(2));
    const unsigned redo = (unsigned)(red >> 1) * 16384u + (unsigned)(red & 1) * 4096u;
    const int vxr = L_X + lane * 16;
    const float csc = 0.08838834764831845f * 1.4426950408889634f;
    float ninf = -__builtin_inff(); PIN_V(ninf);
    u32x4 KX[8], VWX[2];
    unsigned long long kv_ok = 0;
    auto dma_x = [&](int s) {
        if (ROLE == 0 && w < 4) {
            if (!((kv_ok >> (s >> 2)) & 1ull)) {
                kv_ok = __builtin_amdgcn_ballot_w64(__hip_atomic_load(ctl + C_KVC(n, h), RLX_AGENT) + 1u >= 16u);
                if (!((kv_ok >> (s >> 2)) & 1ull)) { wait_count(ctl, C_KVC(s >> 2, h), 16u); kv_ok |= 1ull << (s >> 2); }
                asm volatile("" ::: "memory");
            }
#pragma unroll
            for (int i = 0; i < 2; ++i) {
                const unsigned vo = l16, so = (unsigned)s * 16384u + (unsigned)(w * 2 + i) * 1024u;
                __builtin_amdgcn_raw_ptr_buffer_load_lds(qr, (LAS void*)(lds + L_X + (s & 1) * 16384 + (w * 2 + i) * 1024), 16, vo, so + 32u * XH, 0, 16);
                __builtin_amdgcn_raw_ptr_buffer_load_lds(qr, (LAS void*)(lds + L_X + (s & 1) * 16384 + 8192 + (w * 2 + i) * 1024), 16, vo, so, 0, 16);
            }
        }
    };
    auto load_kx = [&](int s) {
#pragma unroll
        for (int kt = 0; kt < 2; ++kt)
#pragma unroll
            for (int kk = 0; kk < 4; ++kk) KX[kt * 4 + kk] = *(const LAS u32x4*)(lds + vxr + (s & 1) * 16384 + 8192 + kt * 4096 + ((j + kk) & 3) * 1024);
    };
    auto vwx_kt = [&](int s, int kt) {
        bf16x8 VA[4];
#pragma unroll
        for (int kk = 0; kk < 4; ++kk) VA[kk] = *(const LAS bf16x8*)(lds + vxr + (s & 1) * 16384 + kt * 4096 + (kk ^ (j & 2)) * 1024);
#pragma unroll
        for (int ct = 0; ct < 2; ++ct) {
            f32x4 d = {0.f, 0.f, 0.f, 0.f};
#pragma unroll
            for (int kk = 0; kk < 4; ++kk) d = mma32(VA[kk], (kk & 1) ? hi8(WO[ct][kk >> 1]) : lo8(WO[ct][kk >> 1]), d);
            VWX[ct][2 * kt] = pk_bf16(d[0], d[1]); VWX[ct][2 * kt + 1] = pk_bf16(d[2], d[3]);
        }
    };
    auto rmax = [&](float v) { float a = v, b = v; swap16f(a, b); v = max3f(a, b, ninf); a = v; b = v; swap32f(a, b); return max3f(a, b, ninf); };
    auto lo2 = [](f32x4 v) { return (f32x2){v[0], v[1]}; };
    auto hi2 = [](f32x4 v) { return (f32x2){v[2], v[3]}; };
    dma_x(0); dma_x(1);
    asm volatile("s_waitcnt vmcnt(0) lgkmcnt(0)" ::: "memory"); BAR(); asm volatile("" ::: "memory");
    PST(5);
    vwx_kt(0, 0); vwx_kt(0, 1);
    if (ROLE == 2) load_kx(0);
    __builtin_amdgcn_s_setprio(2);

    unsigned iv_t0 = 0, iv_acc = 0;
    unsigned spins = 0, pace_c = 0x7fffffffu, pace_t = 0;
    const u32x4 sv = {SENT, SENT, SENT, SENT};
    for (int s = 0;; ++s) {
        unsigned xw = (s & 1) ? xw1 : xw0;
        unsigned mso = win_off(W_MEM(s, rank >> 5) + (w >> 2)) + (unsigned)(w & 3) * 2048u;
        unsigned pub = pub_off + (g == j ? km_win(s, h) : xw);
        PIN_S(xw); PIN_S(mso); PIN_V(pub);
        {
            if (KD0) __builtin_amdgcn_s_sleep(KD0);
            const u32x2 pv = *(const LAS u32x2*)(lds + L_PACE);
            unsigned target = __builtin_amdgcn_readfirstlane(pv.x); const bool valid = __builtin_amdgcn_readfirstlane(pv.y) + 1u == (unsigned)s && s > 3;
            unsigned now = (unsigned)__builtin_amdgcn_s_memrealtime();
            if (ROLE == 3 && s > 2) pace_c = min(pace_c, now - pace_t);
            if (valid && (int)(target - now) < 100) { while ((int)(target - now) > 0) now = (unsigned)__builtin_amdgcn_s_memrealtime(); }
        }
        SEG(10);
        f32x4 c[6]; u32x4 a1;
        u32x4 a0;
        auto ld_a = [&]() { asm volatile("" ::: "memory"); a0 = ld16_sc1(xr, l16, mso); a1 = ld16_sc1(xr, l16 + 1024, mso); };
        auto ok_a = [&]() { return all_set(max(umax4(a0), umax4(a1))); };
        auto mm_a = [&]() {
#pragma unroll
            for (int i = 0; i < 6; ++i) { c[i] = (f32x4){0.f, 0.f, 0.f, 0.f}; PIN_V(c[i]); }
            SCHED();
            c[0] = smma(a0, W[0][0], c[0], sidx); c[1] = smma(a0, W[1][0], c[1], sidx); c[0] = smma(a1, W[0][1], c[0], sidx); c[1] = smma(a1, W[1][1], c[1], sidx);
            c[2] = smma(a0, W[2][0], c[2], sidx); c[3] = smma(a0, W[3][0], c[3], sidx);
        };
        auto put = [&](int i) { *(LAS f32x2*)(lds + redw + ((i >> 1) * RED_M + (i & 1) * 64) * 4) = lo2(c[i]) + hi2(c[i]); };
        ld_a(); SCHED();
        mm_a();
        if (__builtin_expect(!ok_a(), 0)) { bool out; do { RETRY(0); out = spin_out(spins, ctl); ld_a(); } while (!(ok_a() | out)); mm_a(); }
        SEG(0);
        c[2] = smma(a1, W[2][1], c[2], sidx); c[3] = smma(a1, W[3][1], c[3], sidx);
        put(0); put(1);
        SCHED();
        c[4] = smma(a0, W[4][0], c[4], sidx); c[5] = smma(a0, W[5][0], c[5], sidx); c[4] = smma(a1, W[4][1], c[4], sidx);
        put(2); put(3);
        c[5] = smma(a1, W[5][1], c[5], sidx);
        SCHED();
        put(4); put(5);
        SEG(1);
        LDS_BAR();
        SEG(2);
        if (ROLE <= 1) {
            f32x2 v[8];
#pragma unroll
            for (int ww = 0; ww < 8; ++ww) { const LAS float* q = (const LAS float*)(lds + redr + ww * 4096); v[ww] = (f32x2){q[0], q[512]}; }
            const f32x2 t = ((v[0] + v[1]) + (v[2] + v[3])) + ((v[4] + v[5]) + (v[6] + v[7]));
            const u32x4 o = rows_gather(min(pk_bf16(t.x, t.y), SENT - 1));
            ST16(o, xr, pub, 0, 16);
            *(LAS u32x4*)(lds + L_OWN + w * 256 + n * 16) = o;
        } else {
            if (ROLE == 3 && rank == 0) {
                pace_t = (unsigned)__builtin_amdgcn_s_memrealtime();
                if (lane == 0) { __hip_atomic_store(ctl + C_PROG, (unsigned)s, RLX_AGENT);
                    __hip_atomic_store((unsigned long long*)(ctl + C_PACE + par * 2048), (unsigned long long)(pace_t + pace_c + KPM) | ((unsigned long long)s << 32), RLX_AGENT); }
            }
        }
        SEG(3);
        if (s == NSEG - 1) break;
        LDS_BAR();
        SEG(13);
        unsigned VWM[2];
        auto vwm_pack = [&](const f32x4 (&dv)[2]) {
#pragma unroll
            for (int ct = 0; ct < 2; ++ct) { const f32x2 t = lo2(dv[ct]) + hi2(dv[ct]); VWM[ct] = pk_bf16(t.x, t.y); PIN_V(VWM[ct]); }
        };
        f32x4 dv[2] = {{0.f, 0.f, 0.f, 0.f}, {0.f, 0.f, 0.f, 0.f}}, s0, s1;
        PIN_V(dv[0]); PIN_V(dv[1]);
        u32x4 v0, v1;
        if (ROLE == 0) {
            __builtin_amdgcn_s_sleep(KDX);
            WAIT_V(0);
            if (s + 2 < NSEG - 1) dma_x(s + 2);
        } else {
            unsigned cw = xw + (unsigned)rank * 16384u, c1 = cw + (unsigned)((j + 1) & 3) * 1536u, c2 = cw + (unsigned)((j + 2) & 3) * 1536u, c3 = cw + (unsigned)((j + 3) & 3) * 1536u;
            PIN_S(cw); PIN_S(c1); PIN_S(c2); PIN_S(c3);
            const unsigned xl = ROLE == 2 ? (l16 & ~128u) : l16;
            u32x4 vo;
            f32x4 e0 = {0.f, 0.f, 0.f, 0.f}, e1 = e0;
            if (ROLE == 1) { vo = *(const LAS u32x4*)(lds + L_OWN + 1024 + (lane & 31) * 16); if (!vown) vo = (u32x4){0u, 0u, 0u, 0u}; PIN_V(vo); }
            else {
                const u32x4 X0 = *(const LAS u32x4*)(lds + L_OWN + xl);
                if (ROLE == 2) { e0 = mma32(asbf(KX[0]), asbf(X0), e0); e1 = mma32(asbf(KX[4]), asbf(X0), e1); } else e0 = mma32(asbf(X0), asbf(X0), e0);
                PIN_V(e0); PIN_V(e1);
            }
            if (KD1) __builtin_amdgcn_s_sleep(KD1);
            u32x4 X1, X2, X3;
            auto ld_b = [&]() {
                asm volatile("" ::: "memory");
                if (ROLE >= 2) { X1 = ld16_sc1(xr, xl, c1); X2 = ld16_sc1(xr, xl, c2); X3 = ld16_sc1(xr, xl, c3); }
                else { v0 = ld16_sc1(xr, vg0, cw); v1 = ld16_sc1(xr, vg1, cw); }
            };
            auto ok_b = [&]() { return all_set(ROLE >= 2 ? max(umax4(X1), max(umax4(X2), umax4(X3))) : max(umax4(v0), umax4(v1))); };
            auto mm_b = [&]() {
                if (ROLE == 2) {
                    s0 = mma32(asbf(KX[1]), asbf(X1), e0); s1 = mma32(asbf(KX[5]), asbf(X1), e1);
                    s0 = mma32(asbf(KX[2]), asbf(X2), s0); s1 = mma32(asbf(KX[6]), asbf(X2), s1);
                    s0 = mma32(asbf(KX[3]), asbf(X3), s0); s1 = mma32(asbf(KX[7]), asbf(X3), s1);
                } else if (ROLE == 3) {
                    s0 = mma32(asbf(X1), asbf(X1), e0); s1 = mma32(asbf(X2), asbf(X2), e1); s0 = mma32(asbf(X3), asbf(X3), s0);
                } else {
                    v0 |= vo;
                    *(LAS u32x4*)(lds + L_VM + lane * 16) = v0; *(LAS u32x4*)(lds + L_VM + 1024 + lane * 16) = v1;
                }
            };
            SEG(11);
            ld_b(); SCHED(); mm_b();
            if (__builtin_expect(!ok_b(), 0)) { bool out; do { RETRY(1); out = spin_out(spins, ctl); ld_b(); } while (!(ok_b() | out)); mm_b(); }
            SEG(4);
        }
        float m;
        f32x4 sc;
        if (ROLE >= 2) {
            sc = ROLE == 2 ? (n < 8 ? s0 : s1) : s0 + s1;
            if (ROLE == 3 && g < 2) sc = (f32x4){ninf, ninf, ninf, ninf};
            m = max3f(max3f(sc[0], sc[1], sc[2]), sc[3], ninf);
            if (ROLE == 2) m = max3f(m, ror8(m), ninf);
            m = rmax(m);
            PIN_V(m);
        }
        LDS_BAR();
        if (ROLE == 0) {
            const unsigned ro = w < 3 ? xw + (unsigned)rank * 16384u + (unsigned)w * 2048u : psp + redo + (unsigned)(w - 3) * 2048u;
            ST16(sv, xr, l16, ro, 16); ST16(sv, xr, l16 + 1024, ro, 16);
        }
        if (ROLE != 1) { v0 = *(const LAS u32x4*)(lds + L_VM + lane * 16); v1 = *(const LAS u32x4*)(lds + L_VM + 1024 + lane * 16); }
        if (ROLE >= 2) {
            sc = (sc - m) * csc;
#pragma unroll
            for (int i = 0; i < 4; ++i) sc[i] = __builtin_amdgcn_exp2f(sc[i]);
            *(LAS u32x2*)(lds + (ROLE == 2 ? L_PX + (g * 8 + (n & 7)) * 16 + (n >> 3) * 8 : L_PM + (g < 2 ? 256 : 0) + n * 16 + (g & 1) * 8)) = (u32x2){pk_bf16(sc[0], sc[1]), pk_bf16(sc[2], sc[3])};
            SCHED();
            dv[0] = smma(v0, WO[0][0], dv[0], sidx); dv[1] = smma(v0, WO[1][0], dv[1], sidx);
            float l = (sc[0] + sc[1]) + (sc[2] + sc[3]);
            if (ROLE == 2) l += ror8(l);
            dv[0] = smma(v1, WO[0][1], dv[0], sidx); dv[1] = smma(v1, WO[1][1], dv[1], sidx);
            l = rows_sum(l);
            *(LAS f32x2*)(lds + L_ML + n * 16 + (ROLE == 2 ? 0 : 8)) = (f32x2){m, l};
        } else {
#pragma unroll
            for (int ct = 0; ct < 2; ++ct) { dv[ct] = smma(v0, WO[ct][0], dv[ct], sidx); dv[ct] = smma(v1, WO[ct][1], dv[ct], sidx); }
        }
        vwm_pack(dv);
        SEG(5);
        u32x4 am0 = {VWM[0], 0u, 0u, 0u}, am1 = {VWM[1], 0u, 0u, 0u}, pm = {0u, 0u, 0u, 0u};
        PIN_V(am0); PIN_V(am1); PIN_V(pm);
        LDS_BAR();
        SEG(6);
        {
            const f32x4 ml = *(const LAS f32x4*)(lds + L_ML + n * 16);
            const u32x4 px = *(const LAS u32x4*)(lds + L_PX + (g * 8 + (n & 7)) * 16);
            pm.x = *(const LAS unsigned*)(lds + L_PM + n * 16 + g * 4);
            const f32x4 z = {0.f, 0.f, 0.f, 0.f};
            const f32x4 dx0 = mma32(asbf(VWX[0]), asbf(px), z), dx1 = mma32(asbf(VWX[1]), asbf(px), z), dm0 = mma32(asbf(am0), asbf(pm), z), dm1 = mma32(asbf(am1), asbf(pm), z);
            const float dl = (ml[2] - ml[0]) * csc;
            const float e = __builtin_amdgcn_exp2f(-__builtin_fabsf(dl));
            const float ex = dl > 0.f ? e : 1.f, em = dl > 0.f ? 1.f : e;
            const float inv = __builtin_amdgcn_rcpf(fmaf(ex, ml[1], em * ml[3]));
            const f32x4 d0 = (dx0 * ex + dm0 * em) * inv, d1 = (dx1 * ex + dm1 * em) * inv;
            const u32x4 o = {min(pk_bf16(d0[0], d0[1]), SENT - 1), min(pk_bf16(d0[2], d0[3]), SENT - 1), min(pk_bf16(d1[0], d1[1]), SENT - 1), min(pk_bf16(d1[2], d1[3]), SENT - 1)};
            ST16(o, xr, pst_off, pso, 16);
        }
        SCHED();
        SEG(7);
        __builtin_amdgcn_s_setprio(ROLE == 3 ? 1 : 0);
        unsigned long long pv = 0;
        if (ROLE == 1) { pv = __hip_atomic_load((unsigned long long*)(ctl + C_PACE + par * 2048), RLX_AGENT); SCHED(); }
        vwx_kt(s + 1, 0); vwx_kt(s + 1, 1);
        SCHED();
        if (ROLE == 3) __builtin_amdgcn_s_setprio(2);
        if (ROLE == 3) {
            u32x4 SEL;
            { const unsigned one = (n & 1) ? 0x3f800000u : 0x00003f80u; const int dw = (n & 7) >> 1; SEL = (u32x4){dw == 0 ? one : 0u, dw == 1 ? one : 0u, dw == 2 ? one : 0u, dw == 3 ? one : 0u}; }
            unsigned so = pso + redo, mo = win_off(W_MEM(s + 1, 0) + (red >> 6)) + (unsigned)(red & 63) * 128u;
            PIN_S(so); PIN_S(mo); PIN_V(SEL);
            if (KD2) __builtin_amdgcn_s_sleep(KD2);
            f32x4 ta, tb;
            u32x4 q[4];
            auto ld_c = [&]() {
                asm volatile("" ::: "memory");
#pragma unroll
                for (int i = 0; i < 4; ++i) q[i] = ld16_sc1(xr, l16 + i * 1024, so);
            };
            auto ok_c = [&]() { return all_set(max(max(umax4(q[0]), umax4(q[1])), max(umax4(q[2]), umax4(q[3])))); };
            auto mm_c = [&]() {
                ta = mma32(asbf(SEL), asbf(q[0]), (f32x4){0.f, 0.f, 0.f, 0.f}); tb = mma32(asbf(SEL), asbf(q[1]), (f32x4){0.f, 0.f, 0.f, 0.f});
                ta = mma32(asbf(SEL), asbf(q[2]), ta); tb = mma32(asbf(SEL), asbf(q[3]), tb);
            };
            SEG(12);
            ld_c(); mm_c();
            if (__builtin_expect(!ok_c(), 0)) { bool out; do { RETRY(2); out = spin_out(spins, ctl); ld_c(); } while (!(ok_c() | out)); mm_c(); }
            SEG(8);
            f32x4 t = ta + tb;
#pragma unroll
            for (int i = 0; i < 4; ++i) t[i] += ror8(t[i]);
            unsigned a0 = min(pk_bf16(t[0], t[1]), SENT - 1), a1 = min(pk_bf16(t[2], t[3]), SENT - 1);
            unsigned b0 = a0, b1 = a1; swap16(a0, b0); swap16(a1, b1);
            const u32x4 o = {a0, a1, b0, b1};
            ST16(o, xr, mem_off, mo, 16);
        } else {
            if (ROLE == 2) load_kx(s + 1);
            if (ROLE == 1) *(LAS u32x2*)(lds + L_PACE) = (u32x2){(unsigned)pv, (unsigned)(pv >> 32)};
        }
        { const unsigned t = psp; psp = pso; pso = psn; psn = t; }
        SEG(9);
        if (ROLE != 3) __builtin_amdgcn_s_setprio(2);
        LDS_BAR();
        SEG(14);
    }
    WAIT_V(0);
    __builtin_amdgcn_s_setprio(0);
}

DI void chain_role(const Params& p, unsigned par, int rank, ldsp lds, unsigned long long* stamps) {
    const int tid = fresh_tid(), w = __builtin_amdgcn_readfirstlane(tid >> 6), lane = tid & 63, g = lane >> 4, n = lane & 15;
    const int h = rank >> 2, j = rank & 3;
    const rsrc_t xr = mkrsrc(p.ws + WS_XCH, (unsigned)XCH_BYTES);
    unsigned* ctl = (unsigned*)(p.ws + WS_CTL);
    PST(0);
    const int red = ((j + 1) & 3) * 32 + h;
    bf16x16 W[6][2], WO[2][2];
    {
        ldsp my = lds + w * 16384;
        const int rrow = lane >> 2, rc = (lane & 3) * 8, q4 = n >> 2;
        const int toffo = (8 * g + q4) * 64 + (n & 3) * 8;
        const int toffq = (16 * (g & 1) + 8 * (g >> 1) + 4 * (q4 >> 1) + (q4 & 1)) * 64 + (n & 3) * 8;
#define FRAG(base, st) cat16(cat8(trd((base)), trd((base) + (st))), cat8(trd((base) + 2048), trd((base) + 2048 + (st))))
#pragma unroll
        for (int i = 0; i < 8; ++i) {
            __builtin_amdgcn_global_load_lds((const unsigned*)(p.wq + (size_t)(128 * w + 16 * i + rrow) * 4096 + h * 128 + 32 * j + rc), (LAS unsigned*)(my + i * 1024), 16, 0, 0);
            __builtin_amdgcn_global_load_lds((const unsigned*)(p.wo + (size_t)(h * 128 + 16 * i + rrow) * 1024 + 256 * j + 32 * w + rc), (LAS unsigned*)(my + 8192 + i * 1024), 16, 0, 0);
        }
        {
            const u32x4 sv = {SENT, SENT, SENT, SENT};
            if (w < 6) {
                ST16(sv, xr, (unsigned)lane * 16u, win_off(W_X(0)) + (unsigned)rank * 16384u + (unsigned)w * 1024u, 16);
                ST16(sv, xr, (unsigned)lane * 16u, win_off(W_X(1)) + (unsigned)rank * 16384u + (unsigned)w * 1024u, 16);
            }
            ST16(sv, xr, (unsigned)lane * 16u, win_off(W_PART(w >> 2)) + (unsigned)(red >> 1) * 16384u + (unsigned)(red & 1) * 4096u + (unsigned)(w & 3) * 1024u, 16);
            ST16(sv, xr, (unsigned)lane * 16u, km_win(56 + w, h) + (unsigned)j * 1536u, 16);
            ST16(sv, xr, lane < 32 ? (unsigned)lane * 16u + 1024u : OOB, km_win(56 + w, h) + (unsigned)j * 1536u, 16);
            const int s = 8 * w + (lane >> 3);
#pragma unroll
            for (int x = 0; x < 4; ++x) if (s > 0) ST16(sv, xr, win_off(W_MEM(s, x) + (red >> 6)) + (unsigned)(red & 63) * 128u + (unsigned)(lane & 7) * 16u, 0, 16);
        }
        PST(3);
        WAIT_V(0);
#pragma unroll
        for (int T = 0; T < 2; ++T)
#pragma unroll
            for (int t = 0; t < 2; ++t) { W[T][t] = FRAG(my + t * 4096 + T * 32 + toffq, 128); WO[T][t] = FRAG(my + 8192 + (t ^ (j >> 1)) * 4096 + T * 32 + toffo, 256); }
#pragma unroll
        for (int T = 0; T < 2; ++T) asm volatile("" : "+v"(W[T][0]), "+v"(W[T][1]), "+v"(WO[T][0]), "+v"(WO[T][1]));
        WAIT_L(0);
        PST(1);
#pragma unroll
        for (int i = 0; i < 8; ++i) {
            __builtin_amdgcn_global_load_lds((const unsigned*)(p.wk + (size_t)(128 * w + 16 * i + rrow) * 4096 + h * 128 + 32 * j + rc), (LAS unsigned*)(my + i * 1024), 16, 0, 0);
            __builtin_amdgcn_global_load_lds((const unsigned*)(p.wv + (size_t)(128 * w + 16 * i + rrow) * 4096 + h * 128 + 32 * j + rc), (LAS unsigned*)(my + 8192 + i * 1024), 16, 0, 0);
        }
        WAIT_V(0);
#pragma unroll
        for (int T = 0; T < 4; ++T)
#pragma unroll
            for (int t = 0; t < 2; ++t) W[2 + T][t] = FRAG(my + (T >> 1) * 8192 + t * 4096 + (T & 1) * 32 + toffq, 128);
#pragma unroll
        for (int T = 0; T < 4; ++T) asm volatile("" : "+v"(W[2 + T][0]), "+v"(W[2 + T][1]));
        WAIT_L(0);
#undef FRAG
    }
    PST(2);
    BAR();
    PST(4);
    if (w == 7 && lane < 8) {
        const unsigned* src = (const unsigned*)(p.mem0 + (4 * (red & 1) + 2 * (lane >> 2) + (lane & 1)) * DM + 32 * (red >> 2) + 8 * ((red >> 1) & 1) + 4 * ((lane >> 1) & 1));
        const u32x4 v = {min(src[0], SENT - 1), min(src[1], SENT - 1), min(src[8], SENT - 1), min(src[9], SENT - 1)};
        ST16(v, xr, (unsigned)lane * 16u + (unsigned)(red & 63) * 128u, win_off(W_MEM(0, 0) + (red >> 6)), 16);
    }
    if (w < 5) chain_loop<0>(p, par, rank, w, lds, W, WO, stamps);
    else if (w == 5) chain_loop<1>(p, par, rank, 5, lds, W, WO, stamps);
    else if (w == 6) chain_loop<2>(p, par, rank, 6, lds, W, WO, stamps);
    else chain_loop<3>(p, par, rank, 7, lds, W, WO, stamps);
}

template <bool POLL>
DI void attn_a_item(const Params& p, unsigned par, int s, int h, ldsp my, int lane) {
    const int g = lane >> 4, n = lane & 15;
    const rsrc_t xr = mkrsrc(p.ws + WS_XCH, (unsigned)XCH_BYTES);
    const rsrc_t qr = mkrsrc(p.ws + WH(WS_QKVX + (size_t)h * XH + (size_t)s * 16384), 65u * XH);
#pragma unroll
    for (int i = 0; i < 8; ++i)
        __builtin_amdgcn_raw_ptr_buffer_load_lds(qr, (LAS void*)(my + i * 1024), 16, (unsigned)(g * 256 + (n ^ ((g & 1) * 8)) * 16), 64u * XH + (unsigned)i * 1024u, 0, 16);
    u32x4 Q[2][4], K[2][4], XM[4];
    const unsigned ro = (unsigned)lane * 16u;
#pragma unroll
    for (int t = 0; t < 2; ++t)
#pragma unroll
        for (int k = 0; k < 4; ++k) { Q[t][k] = ld16_sc1(qr, ro + k * 1024, (unsigned)t * 4096u); K[t][k] = ld16_sc1(qr, ro + k * 1024, (unsigned)t * 4096u + 32u * XH); }
    const unsigned qwin = km_win(s, h);
    u32x4 VM[2];
    if (POLL) {
        if (s < 56) wait_count<24>((unsigned*)(p.ws + WS_CTL), C_PROG, (unsigned)s + 2u);
        else { wait_count<4>((unsigned*)(p.ws + WS_CTL), C_PROG, (unsigned)s + 1u); __builtin_amdgcn_s_sleep(16); }
    }
    for (unsigned spins = 0;;) {
        asm volatile("" ::: "memory");
#pragma unroll
        for (int k = 0; k < 4; ++k) XM[k] = ld16_sc1(xr, (unsigned)lane * 16u + (k & 1) * 1536, qwin + (k >> 1) * 3072);
#pragma unroll
        for (int ts = 0; ts < 2; ++ts) VM[ts] = ld16_sc1(xr, (unsigned)(g >> 1) * 1536u + 1024u + (unsigned)(lane & 31) * 16u, qwin + ts * 3072);
        if (!POLL || s < 56) break;
        if (all_set(max(max(max(umax4(XM[0]), umax4(XM[1])), max(umax4(XM[2]), umax4(XM[3]))), max(umax4(VM[0]), umax4(VM[1]))))) break;
        __builtin_amdgcn_s_sleep(4);
        if (spin_out(spins, (unsigned*)(p.ws + WS_CTL))) break;
    }
    SCHED();
#pragma unroll
    for (int ts = 0; ts < 2; ++ts) {
        const u32x4 v = VM[ts];
        const int m = 2 * (n >> 2) + (n & 1), hf = (n >> 1) & 1;
#pragma unroll
        for (int i = 0; i < 4; ++i) { const int e = 64 * ts + 16 * g + 4 * i + 2 * hf;
            *(LAS unsigned*)(my + 8192 + (e >> 3) * 256 + (m ^ (e & 8)) * 16 + (e & 7) * 2) = v[i]; }
    }
    const float csc = 0.08838834764831845f * 1.4426950408889634f, ninf = -__builtin_inff();
    u32x4 PX[2]; u32x2 PM[2];
#pragma unroll
    for (int qt = 0; qt < 2; ++qt) {
        f32x4 sm = {0.f, 0.f, 0.f, 0.f}, s0 = sm, s1 = sm;
#pragma unroll
        for (int k = 0; k < 4; ++k) { sm = mma32(asbf(XM[k]), asbf(Q[qt][k]), sm); s0 = mma32(asbf(K[0][k]), asbf(Q[qt][k]), s0); if (qt == 1) s1 = mma32(asbf(K[1][k]), asbf(Q[qt][k]), s1); }
#pragma unroll
        for (int i = 0; i < 4; ++i) {
            if (g < 2) sm[i] = ninf;
            if (qt == 0) { if (4 * g + i > n) s0[i] = ninf; s1[i] = ninf; }
            else if (4 * g + i > n) s1[i] = ninf;
        }
        float m = fmaxf(fmaxf(fmaxf(s0[0], s0[1]), fmaxf(s0[2], s0[3])), fmaxf(fmaxf(s1[0], s1[1]), fmaxf(s1[2], s1[3])));
        m = fmaxf(m, fmaxf(fmaxf(sm[0], sm[1]), fmaxf(sm[2], sm[3])));
        m = rows_max(m);
        float sum = 0.f;
        s0 = (s0 - m) * csc; s1 = (s1 - m) * csc; sm = (sm - m) * csc;
#pragma unroll
        for (int i = 0; i < 4; ++i) { s0[i] = __builtin_amdgcn_exp2f(s0[i]); s1[i] = __builtin_amdgcn_exp2f(s1[i]); sm[i] = __builtin_amdgcn_exp2f(sm[i]);
            sum += (s0[i] + s1[i]) + sm[i]; }
        sum = rows_sum(sum);
        const float inv = __builtin_amdgcn_rcpf(sum);
        PX[qt] = (u32x4){pk_bf16(s0[0] * inv, s0[1] * inv), pk_bf16(s0[2] * inv, s0[3] * inv), pk_bf16(s1[0] * inv, s1[1] * inv), pk_bf16(s1[2] * inv, s1[3] * inv)};
        PM[qt] = (u32x2){pk_bf16(sm[0] * inv, sm[1] * inv), pk_bf16(sm[2] * inv, sm[3] * inv)};
    }
    asm volatile("s_waitcnt vmcnt(0) lgkmcnt(0)" ::: "memory");
    const int q4 = n >> 2, p4 = n & 3;
    const ldsp vx = my + (p4 >> 1) * 256 + ((4 * g + q4) ^ ((p4 >> 1) * 8)) * 16 + (p4 & 1) * 8, vmm = my + 8192 + (p4 >> 1) * 256 + ((4 * (g & 1) + q4) ^ ((p4 >> 1) * 8)) * 16 + (p4 & 1) * 8;
    const rsrc_t orr = mkrsrc(p.ws + WH(WS_OA + (size_t)s * 524288 + gap((unsigned)h * 4096u)), 524288u);
    const unsigned oo = frag_off(lane);
    bf16x8 VA[8]; s16x4 VMf[8];
#pragma unroll
    for (int et = 0; et < 8; ++et) { VA[et] = cat8(trd(vx + et * 512), trd(vx + 4096 + et * 512)); VMf[et] = trd(vmm + et * 512); }
    SCHED();
#pragma unroll
    for (int ep = 0; ep < 4; ++ep) {
        f32x4 d[2][2];
#pragma unroll
        for (int e2 = 0; e2 < 2; ++e2)
#pragma unroll
            for (int qt = 0; qt < 2; ++qt) d[qt][e2] = mma16(__builtin_bit_cast(u32x2, VMf[2 * ep + e2]), PM[qt], mma32(VA[2 * ep + e2], asbf(PX[qt]), (f32x4){0.f, 0.f, 0.f, 0.f}));
#pragma unroll
        for (int qt = 0; qt < 2; ++qt) store_frag(orr, oo + ep * 1024, (unsigned)qt * 262144u, d[qt][0], d[qt][1]);
    }
    asm volatile("s_waitcnt lgkmcnt(0)" ::: "memory");
}

DI void attn_b_item(const Params& p, unsigned par, int hq, int qb, ldsp lds) {
    const int tid = fresh_tid(), w = __builtin_amdgcn_readfirstlane(tid >> 6), lane = tid & 63, g = lane >> 4, n = lane & 15;
    const int q0 = qb * 128;
    const rsrc_t br = mkrsrc(p.ws + WH(WS_QKVB + (size_t)hq * 4 * BH), 68u * BH);
    const rsrc_t orr = mkrsrc(p.ws + WH(WS_OB + (size_t)qb * 524288), 524288u);
    constexpr int BUF = 57344;
    unsigned to[3];
#pragma unroll
    for (int i = 0; i < 3; ++i) to[i] = gap((unsigned)max(8 * qb - 16 + w * 3 + i, 0) * 1024u);
    const unsigned l16 = (unsigned)lane * 16u, vsw = (unsigned)(g * 256 + (n ^ ((g & 1) * 8)) * 16);
    auto stage = [&](int i) {
        const unsigned so = (unsigned)i * BH; ldsp d = lds + (i & 1) * BUF;
#pragma unroll
        for (int k = 0; k < 3; ++k) {
            __builtin_amdgcn_raw_ptr_buffer_load_lds(br, (LAS void*)(d + (w * 3 + k) * 1024), 16, l16, so + 32u * BH + to[k], 0, 16);
            __builtin_amdgcn_raw_ptr_buffer_load_lds(br, (LAS void*)(d + 24576 + (w * 3 + k) * 1024), 16, vsw, so + 64u * BH + to[k], 0, 16);
        }
        __builtin_amdgcn_raw_ptr_buffer_load_lds(br, (LAS void*)(d + 49152 + w * 1024), 16, l16, so + (unsigned)(qb * 16384 + w * 1024), 0, 16);
    };
    const float ninf = -__builtin_inff(), csc = 0.17677669529663687f * 1.4426950408889634f;
    const int tmin = (256 - q0 - 16 * w) >> 4;
    const int q4 = n >> 2, p4 = n & 3;
    const int kq = w * 1024 + lane * 16;
    const unsigned vb = (unsigned)(size_t)lds + 24576u + (unsigned)(w * 1024 + (p4 >> 1) * 256 + ((4 * g + q4) ^ ((p4 >> 1) * 8)) * 16 + (p4 & 1) * 8);
    stage(0);
#pragma unroll 1
    for (int i = 0; i < 4; ++i) {
        if (i < 3) { stage(i + 1); WAIT_V(7); } else WAIT_V(0);
        BAR(); asm volatile("" ::: "memory");
        const ldsp bp = lds + (i & 1) * BUF;
        const bf16x8 Q = *(const LAS bf16x8*)(bp + 49152 + kq);
        f32x4 sc[17]; bf16x8 KF[17];
#pragma unroll
        for (int t = 0; t < 17; ++t) KF[t] = *(const LAS bf16x8*)(bp + kq + t * 1024);
        SCHED();
#pragma unroll
        for (int t = 0; t < 17; ++t) sc[t] = mma32(KF[t], Q, (f32x4){0.f, 0.f, 0.f, 0.f});
#pragma unroll
        for (int k = 0; k < 4; ++k) { if (4 * g + k <= n) sc[0][k] = ninf; if (4 * g + k > n) sc[16][k] = ninf; }
        if (tmin > 0) {
#pragma unroll
            for (int t = 0; t < 16; ++t) if (t < tmin) sc[t] = (f32x4){ninf, ninf, ninf, ninf};
        }
        float mq[4];
#pragma unroll
        for (int k = 0; k < 4; ++k) {
            mq[k] = max3f(sc[16][k], sc[k][0], sc[k][1]); mq[k] = max3f(mq[k], sc[k][2], sc[k][3]);
#pragma unroll
            for (int t = 4 + k; t < 16; t += 4) mq[k] = max3f(max3f(mq[k], sc[t][0], sc[t][1]), sc[t][2], sc[t][3]);
        }
        const float m = rows_max(max3f(max3f(mq[0], mq[1], mq[2]), mq[3], mq[3]));
        f32x4 sum4 = {0.f, 0.f, 0.f, 0.f};
#pragma unroll
        for (int t = 0; t < 17; ++t) {
            sc[t] = (sc[t] - m) * csc;
#pragma unroll
            for (int k = 0; k < 4; ++k) sc[t][k] = __builtin_amdgcn_exp2f(sc[t][k]);
            sum4 += sc[t];
        }
        const float inv = __builtin_amdgcn_rcpf(rows_sum((sum4[0] + sum4[1]) + (sum4[2] + sum4[3])));
        const unsigned v0 = vb + (unsigned)((i & 1) * BUF);
        s16x4 V0[17], V1[17];
#define VRD(t) TRD(V0[t], v0, (t) * 1024); TRD(V1[t], v0, (t) * 1024 + 512)
        VRD(0); VRD(1); VRD(2); VRD(3); VRD(4); VRD(5); VRD(6); VRD(7); VRD(8); VRD(9); VRD(10); VRD(11); VRD(12); VRD(13); VRD(14); VRD(15); VRD(16);
#undef VRD
        u32x2 P[17];
#pragma unroll
        for (int t = 0; t < 17; ++t) P[t] = (u32x2){pk_bf16(sc[t][0], sc[t][1]), pk_bf16(sc[t][2], sc[t][3])};
        WAIT_L(0); SCHED();
        f32x4 d0 = {0.f, 0.f, 0.f, 0.f}, d1 = d0;
#pragma unroll
        for (int tp = 0; tp < 8; ++tp) {
            const bf16x8 PP = asbf((u32x4){P[2 * tp].x, P[2 * tp].y, P[2 * tp + 1].x, P[2 * tp + 1].y});
            d0 = mma32(cat8(V0[2 * tp], V0[2 * tp + 1]), PP, d0); d1 = mma32(cat8(V1[2 * tp], V1[2 * tp + 1]), PP, d1);
        }
        d0 = mma16(__builtin_bit_cast(u32x2, V0[16]), P[16], d0); d1 = mma16(__builtin_bit_cast(u32x2, V1[16]), P[16], d1);
        BAR();
        store_frag(orr, frag_off(lane), (unsigned)w * 65536u + gap((unsigned)(hq * 4 + i) * 1024u), d0 * inv, d1 * inv);
    }
    WAIT_V(0);
    signal((unsigned*)(p.ws + WS_CTL), C_OB + qb * 32, 1);
}

DI void out_tail(const Params& p, unsigned par, int c, int q, ldsp lds) {
    const int tid = fresh_tid(), w = __builtin_amdgcn_readfirstlane(tid >> 6), lane = tid & 63, g = lane >> 4, n = lane & 15;
    unsigned* ctl = (unsigned*)(p.ws + WS_CTL);
    bf16x8 WT[16][2];
    {
        ldsp my = lds + w * 16384;
        const int toff = (8 * g + (n >> 2)) * 64 + (n & 3) * 8;
#pragma unroll
        for (int r = 0; r < 2; ++r) {
#pragma unroll
            for (int i = 0; i < 16; ++i)
                __builtin_amdgcn_global_load_lds((const unsigned*)(p.wo + (size_t)(512 * w + 256 * r + 16 * i + (lane >> 2)) * 1024 + 32 * c + (lane & 3) * 8), (LAS unsigned*)(my + i * 1024), 16, 0, 0);
            WAIT_V(0);
#pragma unroll
            for (int k = 0; k < 8; ++k)
#pragma unroll
                for (int ct = 0; ct < 2; ++ct) { WT[8 * r + k][ct] = cat8(trd(my + k * 2048 + ct * 32 + toff), trd(my + k * 2048 + ct * 32 + toff + 256)); }
#pragma unroll
            for (int k = 0; k < 8; ++k) asm volatile("" : "+v"(WT[8 * r + k][0]), "+v"(WT[8 * r + k][1]));
            WAIT_L(0);
        }
    }
    BAR();
#pragma unroll 1
    for (int hs = q; hs < 2 * (NSEG - S_TAIL); hs += 2) {
        const int s = S_TAIL + (hs >> 1), rt = hs & 1;
        if (w == 0) wait_count<3>(ctl, C_OS + (s - S_TAIL) * 128 + (c >> 3) * 32, 4u);
        asm volatile("" ::: "memory"); BAR(); asm volatile("" ::: "memory");
        const rsrc_t ar = mkrsrc(p.ws + WH(WS_OA + (size_t)(2 * s + rt) * 262144 + (size_t)w * 32768), 32768u);
        const unsigned ao = (unsigned)lane * 16u;
        u32x4 A[16];
#pragma unroll
        for (int k = 0; k < 16; ++k) A[k] = ld16_sc1(ar, ao + (k & 3) * 1024, gap((unsigned)(k >> 2) * 4096u));
        SCHED();
#pragma unroll
        for (int ct = 0; ct < 2; ++ct) {
            f32x4 acc = {0.f, 0.f, 0.f, 0.f};
#pragma unroll
            for (int k = 0; k < 16; ++k) acc = mma32(WT[k][ct], asbf(A[k]), acc);
            *(LAS f32x4*)(lds + w * 2048 + ct * 1024 + lane * 16) = acc;
        }
        LDS_BAR();
        if (w < 2) {
            f32x4 r[8];
#pragma unroll
            for (int ww = 0; ww < 8; ++ww) r[ww] = *(const LAS f32x4*)(lds + ww * 2048 + w * 1024 + lane * 16);
            SCHED();
            const f32x4 t = ((r[0] + r[1]) + (r[2] + r[3])) + ((r[4] + r[5]) + (r[6] + r[7]));
            *(u32x2*)(p.out + (size_t)(32 * s + 16 * rt + n) * 2048 + 32 * c + 16 * w + 4 * g) = (u32x2){pk_bf16(t[0], t[1]), pk_bf16(t[2], t[3])};
        }
        WAIT_L(0);
    }
    BAR();
}

DI void big_unit(const Params& p, unsigned par, int id, Unit& u) {
    u.lda = DM * 2; u.nt = 16; u.ak = 128; u.ah = 128 * DM * 2;
    if (id < 384) {
        const int pm = id & 7, pn = id >> 3;
        const u16 *w0 = p.wq, *w1 = p.wk, *w2 = p.wv; const u16* W = pn < 16 ? w0 : (pn < 32 ? w1 : w2);
        u.A = (const char*)(p.ax + (size_t)pm * 256 * DM); u.B = (const char*)(W + (pn & 15) * 256); u.ldb = 4096 * 2;
        u.C = p.ws + WH(WS_QKVX + (size_t)(2 * pn) * XH + (size_t)pm * 131072); u.ldc = 256; u.cw = 1024; u.ch = XH;
        if (pn < 16) { u.sig = C_Q + pm * 32; u.nsig = 1; } else { u.sig = C_KVC(2 * pm, 2 * (pn & 15)); u.nsig = 4; }
    } else if (id < 480) {
        const int pm = (id - 384) & 7, pn = (id - 384) >> 3;
        u.A = (const char*)(p.bx + (size_t)pm * 256 * DM); u.B = (const char*)(p.bwqkv + pn * 256); u.ldb = LDB3 * 2;
        u.C = p.ws + WH(WS_QKVB + (size_t)(8 * pn) * BH + (size_t)pm * 32768); u.ldc = 64; u.cw = BH; u.ch = 4 * BH; u.sig = C_B + pm * 32; u.nsig = 1;
    } else {
        const int pm = (id - 480) >> 2, pn = id & 3;
        u.lda = 0; u.ak = 2048; u.ah = 524288u;
        u.A = p.ws + WH(WS_OB + (size_t)pm * 1048576); u.B = (const char*)(p.bwo + pn * 256); u.ldb = DM * 2;
        u.C = (char*)(p.out + (size_t)pm * 256 * 2048 + 1024 + pn * 256); u.ldc = 4096; u.cw = 64; u.ch = 256; u.sig = 0; u.nsig = 0;
    }
}
struct BigSched { const Params& p; unsigned par; int id0, id1, id2; DI bool next(int i, Unit& u) const { const int id = i == 0 ? id0 : (i == 1 ? id1 : (i == 2 ? id2 : -1)); if (id < 0) return false; big_unit(p, par, id, u); return true; } };

__global__ __launch_bounds__(512, 2) void k_fused(Params p) {
    extern __shared__ __attribute__((aligned(16))) unsigned char shm[];
    ldsp lds = (ldsp)shm;
    unsigned* ctl = (unsigned*)(p.ws + WS_CTL);
    const int b = (blockIdx.x & ~7) | ((blockIdx.x + __builtin_amdgcn_readfirstlane(ctl[C_PHASE])) & 7), id = (b & 3) * 32 + (b >> 3);
#define WSTAMP(code)
    const unsigned par = (unsigned)__builtin_amdgcn_readfirstlane(ctl[C_PAR + 1]) < (unsigned)__builtin_amdgcn_readfirstlane(ctl[C_PAR]) ? 1u : 0u;
    WSTAMP(1);
    if ((b & 7) < 4) {
        chain_role(p, par, id, lds, (unsigned long long*)(p.ws + WS_END));
        BAR();
        WSTAMP(2);
    } else {
        {
            const int i = id & 31, kx = id >> 5;
            const u16 *w1 = p.wk, *w2 = p.wv; const u16* Wm = kx < 2 ? w1 : w2;
            Unit u; u.lda = DM * 2; u.ldb = 4096 * 2; u.nt = 16; u.ldc = 256; u.cw = 1024; u.ch = XH; u.ak = 128; u.ah = 0;
            if (i >= 16) {
                const int h = (kx & 1) * 16 + i - 16;
                u.A = (const char*)p.ax; u.B = (const char*)(Wm + h * 128); u.C = p.ws + WH(WS_QKVX + (size_t)(32 + (kx >> 1) * 32 + h) * XH); u.sig = C_KVC(0, h); u.nsig = 1;
                gemm_small(lds, u, ctl);
                WSTAMP(3);
            }
            if (i < 24) {
                const int rb = i < 16 ? 1 + (i & 1) : 3, h = (kx & 1) * 16 + (i < 16 ? i & ~1 : 2 * (i - 16));
                u.A = (const char*)(p.ax + (size_t)rb * 128 * DM); u.B = (const char*)(Wm + h * 128);
                u.C = p.ws + WH(WS_QKVX + (size_t)(32 + (kx >> 1) * 32 + h) * XH + (size_t)rb * 65536); u.sig = C_KVC(rb, h); u.nsig = 2;
                gemm_wide(lds, u, ctl);
                WSTAMP(4);
            }
        }
        if ((id & 31) >= 24) {
            const int t = fresh_tid(), wv = __builtin_amdgcn_readfirstlane(t >> 6), q = (id >> 5) * 8 + (id & 7);
#pragma unroll 1
            for (int i = 0; i < 64; ++i) {
                const char* src = i < 16 ? (const char*)p.ax + (size_t)q * 131072 + i * 8192 : i < 32 ? (const char*)p.bx + (size_t)q * 131072 + (i - 16) * 8192
                                : i < 56 ? (const char*)p.bwqkv + (size_t)q * 196608 + (i - 32) * 8192 : (const char*)p.bwo + (size_t)q * 65536 + (i - 56) * 8192;
                __builtin_amdgcn_global_load_lds((const unsigned*)(src + t * 16), (LAS unsigned*)(lds + L_SCR + wv * 1024), 16, 0, 0);
            }
        }
    }
    volatile LAS unsigned* qw = (volatile LAS unsigned*)(lds + L_QW);
    const int w = __builtin_amdgcn_readfirstlane(threadIdx.x >> 6);
    const int k = b & 3;
    bool fixed = (b & 7) >= 4;
#pragma unroll 1
    for (;;) {
        int kind, x, it = -1, x2 = -1, x3 = -1;
        if (fixed) { const int i = id & 31; kind = 6; x = i < 24 ? (16 + 8 * k + (i & 7)) * 8 + 2 + (i >> 3) : (4 * k + (i & 3)) * 8 + ((i >> 2) & 1); x2 = x + (i < 24 ? 3 : 2); x3 = i < 16 ? 384 + 24 * k + i : -1; fixed = false; }
        else {
        if (threadIdx.x == 0) qw[0] = __hip_atomic_fetch_add(ctl + C_HEAD + k * 32, 1u, RLX_AGENT) + 1u;
        LDS_BAR();
        it = (int)__builtin_amdgcn_readfirstlane(qw[0]) - 8;
        LDS_BAR();
        if (it < 0) { kind = 6; x = 384 + 24 * k + 24 + it; }
        else if (it < 4) { kind = 0; x = it * 4 + k; }
        else if (it < 8) { kind = 6; x = (4 * k + it - 4) * 8 + 4; }
        else if (it < 12) { kind = 2; x = it - 8; }
        else if (it < 16) { kind = 6; x = (4 * k + it - 12) * 8 + 5; }
        else if (it < 20) { kind = 0; x = (it - 12) * 4 + k; }
        else if (it < 52) { kind = 1; x = ((it - 20) >> 1) * 8 + 2 * k + ((it - 20) & 1); }
        else if (it < 52 + NSTR) { kind = 4; x = (it - 52) * 4 + k; }
        else if (it < 60 + NSTR) { const int i = it - 52 - NSTR; kind = 6; x = (4 * k + (i & 3)) * 8 + 6 + (i >> 2); }
        else if (it < 64 + NSTR) { kind = 2; x = 4 + it - 60 - NSTR; }
        else if (it < 72 + NSTR) { kind = 6; x = 480 + (it - 64 - NSTR) * 4 + k; }
        else if (it < 74 + NSTR) { kind = 2; x = 8 + it - 72 - NSTR; }
        else if (it < 76 + NSTR) { kind = 3; x = 80 + 2 * k + it - 74 - NSTR; }
        else if (it < 92 + NSTR) { const int i = it - 76 - NSTR; kind = 5; x = (i >> 3) * 32 + 8 * k + (i & 7); }
        else if (it < 96 + NSTR) { const int i = it - 92 - NSTR; kind = 3; x = (11 + (i >> 1)) * 8 + 2 * k + (i & 1); }
        else break;
        }
        if (kind == 6) {
            if (x >= 480) { const int w0 = C_OB + ((x - 480) >> 2) * 64; wg_wait(ctl, w0, 64u, w0 + 32, 64u); }
            BigSched S{p, par, x, x2, x3};
            gemm_stream(lds, S, ctl);
        } else
        if (kind == 0) {
            const int s0 = (x >> 2) * 4;
            wait_count(ctl, C_Q + (s0 >> 3) * 32, 128u); wait_count<24>(ctl, C_PROG, (unsigned)s0 + 5u);
            asm volatile("" ::: "memory");
#pragma unroll 1
            for (int s = s0; s < s0 + 4; ++s) attn_a_item<false>(p, par, s, (x & 3) * 8 + w, lds + w * 12288, fresh_tid() & 63);
            WAIT_V(0); signal(ctl, C_OA + (x >> 2) * 32, 1);
            BAR();
        } else if (kind == 4) {
#pragma unroll 1
            for (int s = 32 + (x >> 2); s < NSEG; s += NSTR) {
                wait_count(ctl, C_Q + (s >> 3) * 32, 128u); wait_count(ctl, C_KVC(s >> 2, (x & 3) * 8 + w), 16u);
                asm volatile("" ::: "memory");
                attn_a_item<true>(p, par, s, (x & 3) * 8 + w, lds + w * 12288, fresh_tid() & 63);
                WAIT_V(0);
                if (s < S_TAIL) signal(ctl, C_OA + (s >> 2) * 32, 1);
                else { BAR(); const int l = fresh_tid(); if (l < 4) __hip_atomic_fetch_add(ctl + C_OS + (s - S_TAIL) * 128 + l * 32, 1u, RLX_AGENT); }
            }
            BAR();
        } else if (kind == 1) {
            const int qb = x >> 3;
            wait_count(ctl, C_B + (qb >> 1) * 32, 96u); wait_count(ctl, C_B + max((qb >> 1) - 1, 0) * 32, 96u);
            asm volatile("" ::: "memory");
            attn_b_item(p, par, x & 7, qb, lds);
        } else if (kind == 5) {
            out_tail(p, par, x & 31, x >> 5, lds);
        } else {
            const int rb = kind == 2 ? x : x >> 3, col = kind == 2 ? k * 256 : (x & 7) * 128;
            Unit u; u.ldb = DM * 2; u.ldc = 2048 * 2; u.sig = 0; u.nsig = 0; u.lda = 0; u.ak = 2048; u.ah = 0; u.cw = 64; u.ch = 0;
            u.A = p.ws + WH(WS_OA + (size_t)rb * 2097152); u.B = (const char*)(p.wo + col); u.nt = 64; u.C = (char*)(p.out + (size_t)rb * 128 * 2048 + col);
            wg_wait(ctl, C_OA + rb * 32, rb < 8 ? 32u : 128u, C_OA + rb * 32, 32u);
            if (kind == 2) gemm_wide(lds, u, ctl); else gemm_small(lds, u, ctl);
        }
        WSTAMP(16 + (kind == 6 && x >= 480 ? (x < 512 ? 7 : 8) : kind) + ((it & 0xffff) << 8));
    }
}

__global__ __launch_bounds__(512) void k_init(char* ws) {
    const rsrc_t xr = mkrsrc(ws + WS_XCH, NWIN_INIT * 16384u);
    unsigned* ctl = (unsigned*)(ws + WS_CTL);
    const u32x4 sv = {SENT, SENT, SENT, SENT};
    const unsigned xcc = (unsigned)__builtin_amdgcn_s_getreg(20 | (3 << 11)) & 7u, half = blockIdx.x >> 3, win = blockIdx.x & 7;
    if (threadIdx.x < 64 && xcc == 1) {
        unsigned b = ~0u;
        for (int i = 0; i < 5; ++i) {
            const unsigned long long t0 = __builtin_amdgcn_s_memtime();
            __builtin_amdgcn_raw_buffer_store_b32(0u, xr, (unsigned)(2 * 16384 + C_PROBE * 4) + half * 8192u + (unsigned)i * 256u, 0, 16);
            asm volatile("s_waitcnt vmcnt(0)" ::: "memory");
            b = min(b, (unsigned)(__builtin_amdgcn_s_memtime() - t0));
        }
        if (threadIdx.x == 0) ctl[C_PAR + half] = b;
    }
    __syncthreads();
    if (win < 7 && win != 5) ST16(sv, xr, win * 16384u + half * 8192u + threadIdx.x * 16u, 0, 16);
    if (blockIdx.x == 0 && threadIdx.x == 0) ctl[C_PHASE] = xcc;
}

extern "C" void kernel_launch(void* const* d_in, const int* in_sizes, int n_in, void* d_out, int out_size, void* d_ws, size_t ws_size, hipStream_t stream) {
    Params p;
    p.ax = (const u16*)d_in[0]; p.mem0 = (const u16*)d_in[1]; p.wq = (const u16*)d_in[2]; p.wk = (const u16*)d_in[3]; p.wv = (const u16*)d_in[4]; p.wo = (const u16*)d_in[5];
    p.bx = (const u16*)d_in[7]; p.bwqkv = (const u16*)d_in[8]; p.bwo = (const u16*)d_in[9];
    p.out = (u16*)d_out; p.ws = (char*)(((uintptr_t)d_ws + 0x1FFFFFu) & ~(uintptr_t)0x1FFFFFu);
    (void)hipFuncSetAttribute((const void*)k_fused, hipFuncAttributeMaxDynamicSharedMemorySize, LDS_BYTES);
    k_init<<<16, 512, 0, stream>>>(p.ws);
    k_fused<<<256, 512, LDS_BYTES, stream>>>(p);
}
```

```cpp
#include <hip/hip_runtime.h>
#include <stdint.h>

#define LAS __attribute__((address_space(3)))
#define DI __device__ __forceinline__
typedef unsigned short u16;
typedef __attribute__((ext_vector_type(8))) __bf16 bf16x8;
typedef __attribute__((ext_vector_type(16))) __bf16 bf16x16;
typedef __attribute__((ext_vector_type(2))) __bf16 bf16x2;
typedef __attribute__((ext_vector_type(4))) short s16x4;
typedef __attribute__((ext_vector_type(2))) float f32x2;
typedef __attribute__((ext_vector_type(4))) float f32x4;
typedef __attribute__((ext_vector_type(2))) unsigned u32x2;
typedef __attribute__((ext_vector_type(4))) unsigned u32x4;
typedef LAS unsigned char* ldsp;
typedef __amdgpu_buffer_rsrc_t rsrc_t;

constexpr int S_LEN = 2048, DM = 1024, NSEG = 64;
constexpr int LDB3 = 3072, S_TAIL = 52;
constexpr unsigned XH = S_LEN * 512, BH = S_LEN * 128;
constexpr unsigned SENT = 0xFFFFFFFFu;
constexpr size_t WS_XCH = 2u << 20;
constexpr int NWIN = 640, NWIN_INIT = 7;
constexpr size_t XCH_BYTES = (size_t)(NWIN + 2048 + 512) * 16384;
constexpr size_t WS_CTL = WS_XCH + 2 * 16384;
constexpr int C_TMO = 0, C_PAR = 13312  , C_PHASE = 13314, C_HEAD = 3072, C_PROG = 192, C_PACE = 128, C_Q = 256, C_B = C_Q + 8 * 32, C_OA = C_B + 8 * 32, C_OB = C_OA + 16 * 32, C_OS = 16384, C_KV = 4096, C_PROBE = 12288;
constexpr size_t WS_QKVX = 52u << 20;
constexpr size_t WS_QKVB = WS_QKVX + (size_t)96 * XH;
constexpr size_t WS_OA = WS_QKVB + (size_t)96 * BH;
constexpr size_t WS_OB = WS_OA + (size_t)S_LEN * 4096 * 4;
static_assert(WS_OB + (size_t)S_LEN * 1024 * 4 + (2u << 20) <= (256u << 20), "workspace");
#define C_KVC(rb, h) (C_KV + (h) * 32 + (rb))
#define gap(L) (((L) & 8191u) | (((L) >> 13) << 14))
#define WH(off) ((off) + (size_t)((((unsigned)((off) >> 21) ^ par) & 1u) << 13))
constexpr int L_QW = 147456, LDS_BYTES = L_QW + 1024 + 8192;

DI unsigned pk_bf16(float a, float b) { f32x2 v = {a, b}; return __builtin_bit_cast(unsigned, __builtin_convertvector(v, bf16x2)); }
DI s16x4 trd(ldsp p) { return __builtin_amdgcn_ds_read_tr16_b64_v4i16((LAS s16x4*)p); }
DI bf16x8 cat8(s16x4 a, s16x4 b) { return __builtin_bit_cast(bf16x8, __builtin_shufflevector(a, b, 0, 1, 2, 3, 4, 5, 6, 7)); }
DI bf16x16 cat16(bf16x8 a, bf16x8 b) { return __builtin_shufflevector(a, b, 0, 1, 2, 3, 4, 5, 6, 7, 8, 9, 10, 11, 12, 13, 14, 15); }
DI bf16x8 lo8(bf16x16 v) { return __builtin_shufflevector(v, v, 0, 1, 2, 3, 4, 5, 6, 7); }
DI bf16x8 hi8(bf16x16 v) { return __builtin_shufflevector(v, v, 8, 9, 10, 11, 12, 13, 14, 15); }
DI bf16x8 asbf(u32x4 v) { return __builtin_bit_cast(bf16x8, v); }
DI f32x4 mma32(bf16x8 a, bf16x8 b, f32x4 c) { return __builtin_amdgcn_mfma_f32_16x16x32_bf16(a, b, c, 0, 0, 0); }
DI f32x4 mma16(u32x2 a, u32x2 b, f32x4 c) { return mma32(asbf((u32x4){a.x, a.y, 0u, 0u}), asbf((u32x4){b.x, b.y, 0u, 0u}), c); }
DI f32x4 smma(u32x4 a, bf16x16 b, f32x4 c, int idx) { return __builtin_amdgcn_smfmac_f32_16x16x64_bf16(asbf(a), b, c, idx, 0, 0); }
DI rsrc_t mkrsrc(const void* p, unsigned bytes) { return __builtin_amdgcn_make_buffer_rsrc((void*)p, 0, bytes, 0x00020000); }
DI u32x4 ld16_sc1(rsrc_t r, unsigned voff, unsigned soff) { return __builtin_amdgcn_raw_buffer_load_b128(r, voff, soff, 16); }
#define ST16(v, r, voff, soff, aux) do { const u32x4 st_v = (v); __builtin_amdgcn_raw_buffer_store_b128(st_v, r, voff, soff, aux); asm volatile("s_nop 1" :: "v"(st_v)); } while (0)
DI void swap16(unsigned& a, unsigned& b) { auto r = __builtin_amdgcn_permlane16_swap(a, b, false, false); a = r[0]; b = r[1]; }
DI void swap32(unsigned& a, unsigned& b) { auto r = __builtin_amdgcn_permlane32_swap(a, b, false, false); a = r[0]; b = r[1]; }
DI void swap16f(float& a, float& b) { unsigned x = __float_as_uint(a), y = __float_as_uint(b); swap16(x, y); a = __uint_as_float(x); b = __uint_as_float(y); }
DI void swap32f(float& a, float& b) { unsigned x = __float_as_uint(a), y = __float_as_uint(b); swap32(x, y); a = __uint_as_float(x); b = __uint_as_float(y); }
DI float rows_max(float v) { float a = v, b = v; swap16f(a, b); v = fmaxf(a, b); a = v; b = v; swap32f(a, b); return fmaxf(a, b); }
DI float rows_sum(float v) { float a = v, b = v; swap16f(a, b); v = a + b; a = v; b = v; swap32f(a, b); return a + b; }
DI unsigned umax4(u32x4 v) { return max(max(v.x, v.y), max(v.z, v.w)); }
#define WAIT_V(n) asm volatile("s_waitcnt vmcnt(" #n ")" ::: "memory")
#define WAIT_L(n) asm volatile("s_waitcnt lgkmcnt(" #n ")" ::: "memory")
#define BAR() __builtin_amdgcn_s_barrier()
#define SCHED() __builtin_amdgcn_sched_barrier(0)
#define LDS_BAR() do { asm volatile("s_waitcnt lgkmcnt(0)" ::: "memory"); __builtin_amdgcn_s_barrier(); asm volatile("" ::: "memory"); } while (0)

constexpr int HTB = 128 * 64 * 2;
DI int lds_byte(int r, int c) { const int st = (r >> 4) * 2 + (c >> 5), rr = r & 15, cc = c & 31, ob = rr * 64 + cc * 2; return st * 1024 + (ob ^ (((ob >> 9) & 1) << 5)); }
DI void stage_rc(int b, int& R, int& C) { const int st = b / 1024, sb = b % 1024, swz = sb ^ (((sb >> 9) & 1) << 5); R = (st >> 1) * 16 + swz / 64; C = (st & 1) * 32 + (swz % 64) / 2; }

struct Params {
    const u16 *ax, *mem0, *wq, *wk, *wv, *wo, *bx, *bwqkv, *bwo;
    u16* out; char* ws;
};
#define RLX_AGENT __ATOMIC_RELAXED, __HIP_MEMORY_SCOPE_AGENT
DI bool spin_out(unsigned& spins, unsigned* ctl) {
    spins = __builtin_amdgcn_readfirstlane(spins) + 1u;
    if ((spins & 63u) == 0u) {
        if (spins > (1u << 18)) { __hip_atomic_store(ctl + C_TMO, 1u, RLX_AGENT); return true; }
        if (__builtin_amdgcn_readfirstlane(__hip_atomic_load(ctl + C_TMO, RLX_AGENT)) != SENT) return true;
    }
    return false;
}
template <int NAP = 8>
DI void wait_count(unsigned* ctl, int word, unsigned need) {
    unsigned spins = 0;
    while (__builtin_amdgcn_readfirstlane(__hip_atomic_load(ctl + word, RLX_AGENT)) + 1u < need) { __builtin_amdgcn_s_sleep(NAP); if (spin_out(spins, ctl)) break; }
}
DI void wg_wait(unsigned* ctl, int word0, unsigned need0, int word1, unsigned need1) {
    if (threadIdx.x < 64) {
        wait_count(ctl, word0, need0); wait_count(ctl, word1, need1);
        __builtin_amdgcn_fence(__ATOMIC_ACQUIRE, "agent");
        asm volatile("s_waitcnt vmcnt(0)" ::: "memory");
    }
    asm volatile("" ::: "memory"); BAR(); asm volatile("" ::: "memory");
}
struct Unit { const char* A; const char* B; char* C; unsigned lda, ldb, ldc, cw, ch, ak, ah; int nt, sig, nsig; };
DI int fresh_tid() { int t = threadIdx.x; asm volatile("" : "+v"(t)); return t; }
DI void signal(unsigned* ctl, int sig, int nsig) { const int lane = fresh_tid() & 63; if (lane < nsig) __hip_atomic_fetch_add(ctl + sig + (lane & 1) * 32 + (lane >> 1), 1u, RLX_AGENT); }
DI u32x4 pack_pair(f32x4 v0, f32x4 v1) {
    unsigned ax = pk_bf16(v0[0], v0[1]), ay = pk_bf16(v0[2], v0[3]), bx = pk_bf16(v1[0], v1[1]), by = pk_bf16(v1[2], v1[3]);
    swap16(ax, bx); swap16(ay, by);
    return (u32x4){ax, ay, bx, by};
}
DI unsigned frag_off(int lane) { return (unsigned)((((lane >> 4) & 1) * 2 + (lane >> 5)) * 256 + (lane & 15) * 16); }
DI void store_frag(rsrc_t cr, unsigned voff, unsigned soff, f32x4 v0, f32x4 v1) { ST16(pack_pair(v0, v1), cr, voff, soff, 16); }
DI u32x4 to_rows(ldsp sc, int lane, f32x4 v0, f32x4 v1) {
    *(volatile LAS u32x4*)(sc + ((lane & 15) * 4 + ((lane >> 4) & 1) * 2 + (lane >> 5)) * 16) = pack_pair(v0, v1);
    return *(volatile LAS u32x4*)(sc + lane * 16);
}
constexpr int L_SCR = L_QW + 1024;
#define STAGE(bufoff, gbase, voff, step2) do { \
        __builtin_amdgcn_global_load_lds((const unsigned*)((gbase) + (voff)), (LAS unsigned*)(lds + (bufoff) + ldsw), 16, 0, 0); \
        __builtin_amdgcn_global_load_lds((const unsigned*)((gbase) + (step2) + (voff)), (LAS unsigned*)(lds + (bufoff) + ldsw + 8192), 16, 0, 0); } while (0)
#define LDA(dst, off) do { _Pragma("unroll") for (int m = 0; m < 4; ++m) _Pragma("unroll") for (int k = 0; k < 2; ++k) dst[m][k] = *(const LAS bf16x8*)(lds + (off) + aoff + m * 2048 + k * 1024); } while (0)
#define TRD(dst, base, off) asm volatile("ds_read_b64_tr_b16 %0, %1 offset:%2" : "=v"(dst) : "v"(base), "i"(off) : "memory")
#define LDB(dst, v0, v1, off) do { s16x4 l0, h0, l1, h1, l2, h2, l3, h3; \
        TRD(l0, v0, (off)); TRD(h0, v0, (off) + 256); TRD(l1, v0, (off) + 8192); TRD(h1, v0, (off) + 8448); \
        TRD(l2, v1, (off)); TRD(h2, v1, (off) + 256); TRD(l3, v1, (off) + 8192); TRD(h3, v1, (off) + 8448); \
        dst[0][0] = cat8(l0, h0); dst[0][1] = cat8(l1, h1); dst[1][0] = cat8(l2, h2); dst[1][1] = cat8(l3, h3); } while (0)
#define MMA(accv, At, Bt) do { SCHED(); __builtin_amdgcn_s_setprio(1); _Pragma("unroll") for (int m = 0; m < 4; ++m) _Pragma("unroll") for (int n = 0; n < 2; ++n) _Pragma("unroll") for (int k = 0; k < 2; ++k) \
        accv[m][n] = mma32(Bt[n][k], At[m][k], accv[m][n]); __builtin_amdgcn_s_setprio(0); } while (0)
DI unsigned va_of(int tid, unsigned lda, int nt) { int R, C; stage_rc(tid * 16, R, C); return lda ? (unsigned)R * lda + (unsigned)C * 2u : (unsigned)((tid >> 7) * nt * 4096 + (tid & 127) * 16); }
DI unsigned vb_of(int tid, int wid, unsigned ldb) { return (unsigned)(8 * (wid >> 1) + ((tid >> 2) & 7)) * ldb + (unsigned)((2 * (wid & 1) + ((tid >> 5) & 1)) * 64 + (((tid & 3) ^ (((wid >> 1) & 1) << 1)) * 16)); }
#define GEMM_LANE_SETUP(lda0) \
    const int tid = fresh_tid(); \
    const int wid = __builtin_amdgcn_readfirstlane(tid >> 6), lane = tid & 63, wr = wid >> 2, wc = wid & 3, fr = lane & 15, fq = lane >> 4; \
    const unsigned ldsw = (unsigned)wid * 1024u; \
    const int aoff = (lda0) ? lds_byte(wr * 64 + fr, fq * 8) : wr * 8192 + lane * 16; \
    const int boff0 = fq * 2048 + wc * 512 + (fr >> 2) * 64 + (fq & 1) * 32 + (fr & 3) * 8, boff1 = boff0 ^ 32;

template <class Sched>
DI void gemm_stream(ldsp lds, const Sched& S, unsigned* ctl) {
    Unit cur, nxt; int ui = 0;
    if (!S.next(0, cur)) return;
    GEMM_LANE_SETUP(cur.lda);
#define SA(b, h) (((b) * 2 + (h)) * HTB)
#define SB(b, h) ((4 + (b) * 2 + (h)) * HTB)
    const unsigned vb0 = (unsigned)(size_t)lds + 65536u + (unsigned)boff0, vb1 = (unsigned)(size_t)lds + 65536u + (unsigned)boff1;
    f32x4 acc[2][2][4][2];
#pragma unroll
    for (int a = 0; a < 2; ++a)
#pragma unroll
        for (int b = 0; b < 2; ++b)
#pragma unroll
            for (int m = 0; m < 4; ++m)
#pragma unroll
                for (int n = 0; n < 2; ++n) acc[a][b][m][n] = (f32x4){0.f, 0.f, 0.f, 0.f};
    bf16x8 At[4][2], B0[2][2], B1[2][2];
    const unsigned vA = va_of(tid, cur.lda, cur.nt); unsigned vB = vb_of(tid, wid, cur.ldb);
    const size_t a2s = cur.lda ? (size_t)64 * cur.lda : (size_t)cur.nt * 16384;
    {
        const size_t ah = cur.ah, b2s = (size_t)32 * cur.ldb, bk = (size_t)64 * cur.ldb;
        STAGE(SB(0, 0), cur.B, vB, b2s); STAGE(SB(0, 1), cur.B + 256, vB, b2s); STAGE(SA(0, 0), cur.A, vA, a2s); STAGE(SA(0, 1), cur.A + ah, vA, a2s);
        if (wr == 1) BAR();
        WAIT_V(2); BAR();
        STAGE(SB(1, 0), cur.B + bk, vB, b2s); STAGE(SA(1, 0), cur.A + cur.ak, vA, a2s); STAGE(SB(1, 1), cur.B + bk + 256, vB, b2s);
        WAIT_V(6); BAR();
    }
    int psig = 0, pn = 0;
    for (;;) {
        const bool has_next = S.next(ui + 1, nxt);
        if (!has_next) nxt = cur;
        int nt = cur.nt; asm volatile("" : "+s"(nt));
        for (int t = 0; t < nt; t += 2) {
            const bool last = (t == nt - 2);
            const char* a1 = cur.A + (cur.lda ? (unsigned)(t + 1) * 128u : gap((unsigned)(t + 1) * 2048u)) + cur.ah;
            if (last) vB = vb_of(fresh_tid(), wid, nxt.ldb);
            const unsigned ldb = last ? nxt.ldb : cur.ldb;
            const char* a2 = last ? nxt.A : cur.A + (cur.lda ? (unsigned)(t + 2) * 128u : gap((unsigned)(t + 2) * 2048u));
            const char* b2 = last ? nxt.B : cur.B + (size_t)(t + 2) * 64 * cur.ldb;
            const size_t ah = cur.ah, b2s = (size_t)32 * ldb;
            const char* a3 = a2 + cur.ak; const char* b3 = b2 + (size_t)64 * ldb;
            LDB(B0, vb0, vb1, 0); LDB(B1, vb0, vb1, HTB); SCHED(); LDA(At, SA(0, 0)); STAGE(SA(1, 1), a1, vA, a2s);
            WAIT_V(8); WAIT_L(0); BAR(); MMA(acc[0][0], At, B0); MMA(acc[0][1], At, B1); BAR(); SCHED();
            LDA(At, SA(0, 1)); STAGE(SB(0, 0), b2, vB, b2s); STAGE(SB(0, 1), b2 + 256, vB, b2s); STAGE(SA(0, 0), a2, vA, a2s);
            WAIT_V(8);
            asm volatile("" : "+s"(pn));
            if (pn) { signal(ctl, psig, pn); pn = 0; }
            WAIT_L(0); BAR(); MMA(acc[1][0], At, B0); MMA(acc[1][1], At, B1); BAR(); SCHED();
            LDB(B0, vb0, vb1, 2 * HTB); LDB(B1, vb0, vb1, 3 * HTB); SCHED(); LDA(At, SA(1, 0)); STAGE(SA(0, 1), a2 + ah, vA, a2s);
            WAIT_V(8); WAIT_L(0); BAR(); MMA(acc[0][0], At, B0); MMA(acc[0][1], At, B1); BAR(); SCHED();
            LDA(At, SA(1, 1)); STAGE(SB(1, 0), b3, vB, b2s); STAGE(SB(1, 1), b3 + 256, vB, b2s); STAGE(SA(1, 0), a3, vA, a2s);
            WAIT_V(8); WAIT_L(0); BAR(); MMA(acc[1][0], At, B0); MMA(acc[1][1], At, B1); BAR(); SCHED();
        }
        if (wr == 0) BAR();
        {
            const rsrc_t cr = mkrsrc(cur.C, 0x1000000u);
            const int tz = fresh_tid() & 63;
            if (cur.nsig) {
                const unsigned co = (unsigned)wc * cur.cw + frag_off(tz);
#pragma unroll
                for (int ai = 0; ai < 2; ++ai)
#pragma unroll
                    for (int m = 0; m < 4; ++m)
#pragma unroll
                        for (int bj = 0; bj < 2; ++bj) store_frag(cr, co, gap((unsigned)(ai * 128 + wr * 64 + m * 16) * cur.ldc) + (unsigned)bj * cur.ch, acc[ai][bj][m][0], acc[ai][bj][m][1]);
            } else {
                const unsigned co = (unsigned)(wr * 64 + (tz >> 2)) * cur.ldc + (unsigned)(wc * 64 + (tz & 3) * 16);
#pragma unroll
                for (int ai = 0; ai < 2; ++ai)
#pragma unroll
                    for (int m = 0; m < 4; ++m)
#pragma unroll
                        for (int bj = 0; bj < 2; ++bj)
                            ST16(to_rows(lds + L_SCR + wid * 1024, tz, acc[ai][bj][m][0], acc[ai][bj][m][1]), cr, co, (unsigned)(ai * 128 + m * 16) * cur.ldc + (unsigned)bj * 256u, 0);
            }
        }
        psig = cur.sig; pn = cur.nsig;
        if (!has_next) break;
#pragma unroll
        for (int a = 0; a < 2; ++a)
#pragma unroll
            for (int b = 0; b < 2; ++b)
#pragma unroll
                for (int m = 0; m < 4; ++m)
#pragma unroll
                    for (int n = 0; n < 2; ++n) acc[a][b][m][n] = (f32x4){0.f, 0.f, 0.f, 0.f};
        cur = nxt; ++ui;
        if (wr == 1) BAR();
    }
    WAIT_V(0);
    signal(ctl, psig, pn);
    BAR();
#undef SA
#undef SB
}

DI void gemm_small(ldsp lds, const Unit u, unsigned* ctl) {
    GEMM_LANE_SETUP(u.lda);
    const unsigned vA = va_of(tid, u.lda, u.nt), vB = vb_of(tid, wid, u.ldb);
    const size_t a2s = u.lda ? (size_t)64 * u.lda : (size_t)u.nt * 16384, b2s = (size_t)32 * u.ldb, bk = (size_t)64 * u.ldb;
    f32x4 acc[4][2];
#pragma unroll
    for (int m = 0; m < 4; ++m)
#pragma unroll
        for (int n = 0; n < 2; ++n) acc[m][n] = (f32x4){0.f, 0.f, 0.f, 0.f};
    int nt = u.nt; asm volatile("" : "+s"(nt));
    const unsigned vb0 = (unsigned)(size_t)lds + (unsigned)boff0, vb1 = (unsigned)(size_t)lds + (unsigned)boff1, vb2 = vb0 + 65536u, vb3 = vb1 + 65536u;
#pragma unroll
    for (int t = 0; t < 4; ++t) { STAGE(t * 32768, u.A + (size_t)t * u.ak, vA, a2s); STAGE(t * 32768 + 16384, u.B + t * bk, vB, b2s); }
    bf16x8 A0[4][2], B0[2][2], A1[4][2], B1[2][2];
    WAIT_V(12); BAR();
    LDB(B0, vb0, vb1, 16384); LDA(A0, 0);
#define RDA(dst, m, off) do { dst[m][0] = *(const LAS bf16x8*)(lds + (off) + aoff + (m) * 2048); dst[m][1] = *(const LAS bf16x8*)(lds + (off) + aoff + (m) * 2048 + 1024); } while (0)
#define RDB(dst, nn, v, off) do { s16x4 l0, h0, l1, h1; TRD(l0, v, (off)); TRD(h0, v, (off) + 256); TRD(l1, v, (off) + 8192); TRD(h1, v, (off) + 8448); dst[nn][0] = cat8(l0, h0); dst[nn][1] = cat8(l1, h1); } while (0)
#define MM4(Ac, Bc, m) do { SCHED(); _Pragma("unroll") for (int n = 0; n < 2; ++n) _Pragma("unroll") for (int k = 0; k < 2; ++k) acc[m][n] = mma32(Bc[n][k], Ac[m][k], acc[m][n]); SCHED(); } while (0)
#define KSTEP(i, Ac, Bc, An, Bn, v0, v1, boffn) do { \
        const int tn = min(t0 + (i) + 4, nt - 1); \
        WAIT_L(0); WAIT_V(8); BAR(); \
        MM4(Ac, Bc, 0); RDB(Bn, 0, v0, boffn); RDA(An, 0, (((i) + 1) & 3) * 32768); \
        MM4(Ac, Bc, 1); RDB(Bn, 1, v1, boffn); RDA(An, 1, (((i) + 1) & 3) * 32768); \
        MM4(Ac, Bc, 2); RDA(An, 2, (((i) + 1) & 3) * 32768); STAGE((i) * 32768, u.A + (u.lda ? (unsigned)tn * 128u : gap((unsigned)tn * 2048u)), vA, a2s); \
        MM4(Ac, Bc, 3); RDA(An, 3, (((i) + 1) & 3) * 32768); STAGE((i) * 32768 + 16384, u.B + (size_t)tn * bk, vB, b2s); } while (0)
    for (int t0 = 0; t0 < nt; t0 += 4) {
        KSTEP(0, A0, B0, A1, B1, vb0, vb1, 49152);
        KSTEP(1, A1, B1, A0, B0, vb2, vb3, 16384);
        KSTEP(2, A0, B0, A1, B1, vb2, vb3, 49152);
        KSTEP(3, A1, B1, A0, B0, vb0, vb1, 16384);
    }
    WAIT_L(0); SCHED();
#undef RDA
#undef RDB
#undef MM4
#undef KSTEP
    const rsrc_t cr = mkrsrc(u.C, 256u * u.ldc);
    if (u.nsig) {
#pragma unroll
        for (int m = 0; m < 4; ++m) store_frag(cr, (unsigned)wc * u.cw + frag_off(lane), gap((unsigned)(wr * 64 + m * 16) * u.ldc), acc[m][0], acc[m][1]);
    } else {
#pragma unroll
        for (int m = 0; m < 4; ++m)
            ST16(to_rows(lds + L_SCR + wid * 1024, lane, acc[m][0], acc[m][1]), cr, (unsigned)(wr * 64 + (lane >> 2)) * u.ldc + (unsigned)(wc * 64 + (lane & 3) * 16), (unsigned)(m * 16) * u.ldc, 0);
    }
    WAIT_V(0);
    signal(ctl, u.sig, u.nsig);
    BAR();
}

DI void gemm_wide(ldsp lds, const Unit u, unsigned* ctl) {
    GEMM_LANE_SETUP(u.lda);
    const char *A = u.A, *B = u.B; int nt = u.nt;
    const unsigned vA = va_of(tid, u.lda, nt), vB = vb_of(tid, wid, u.ldb);
    const size_t a2s = u.lda ? (size_t)64 * u.lda : (size_t)nt * 16384, b2s = (size_t)32 * u.ldb, bk = (size_t)64 * u.ldb;
    f32x4 acc[2][4][2];
#pragma unroll
    for (int b = 0; b < 2; ++b)
#pragma unroll
        for (int m = 0; m < 4; ++m)
#pragma unroll
            for (int n = 0; n < 2; ++n) acc[b][m][n] = (f32x4){0.f, 0.f, 0.f, 0.f};
    asm volatile("" : "+s"(nt));
    const unsigned vb0 = (unsigned)(size_t)lds + (unsigned)boff0, vb1 = (unsigned)(size_t)lds + (unsigned)boff1;
#define STG3(slot, t) do { STAGE((slot), A + (t) * u.ak, vA, a2s); STAGE((slot) + 16384, B + (size_t)(t) * bk, vB, b2s); STAGE((slot) + 32768, B + (size_t)(t) * bk + 256, vB, b2s); } while (0)
    STG3(0, 0); STG3(49152, 1); STG3(98304, 2);
    bf16x8 A0[4][2], P0[2][2], Q0[2][2], A1[4][2], P1[2][2], Q1[2][2];
    WAIT_V(12); BAR();
    LDB(P0, vb0, vb1, 16384); LDB(Q0, vb0, vb1, 32768); LDA(A0, 0);
    int sc = 0, sn = 49152;
#define RDA(dst, m) do { dst[m][0] = *(const LAS bf16x8*)(lds + sn + aoff + (m) * 2048); dst[m][1] = *(const LAS bf16x8*)(lds + sn + aoff + (m) * 2048 + 1024); } while (0)
#define RDB(dst, nn, v, off) do { s16x4 l0, h0, l1, h1; TRD(l0, v, (off)); TRD(h0, v, (off) + 256); TRD(l1, v, (off) + 8192); TRD(h1, v, (off) + 8448); dst[nn][0] = cat8(l0, h0); dst[nn][1] = cat8(l1, h1); } while (0)
#define MM4(b, Ac, Bc, m) do { SCHED(); _Pragma("unroll") for (int n = 0; n < 2; ++n) _Pragma("unroll") for (int k = 0; k < 2; ++k) acc[b][m][n] = mma32(Bc[n][k], Ac[m][k], acc[b][m][n]); SCHED(); } while (0)
#define KSTEP(t, Ac, Pc, Qc, An, Pn, Qn) do { \
        const int tn = min((t) + 3, nt - 1); const unsigned u0 = vb0 + (unsigned)sn, u1 = vb1 + (unsigned)sn; \
        WAIT_L(0); WAIT_V(6); BAR(); \
        MM4(0, Ac, Pc, 0); RDB(Pn, 0, u0, 16384); \
        MM4(1, Ac, Qc, 0); RDB(Pn, 1, u1, 16384); \
        MM4(0, Ac, Pc, 1); RDB(Qn, 0, u0, 32768); \
        MM4(1, Ac, Qc, 1); RDB(Qn, 1, u1, 32768); \
        MM4(0, Ac, Pc, 2); RDA(An, 0); STAGE(sc, A + (u.lda ? (unsigned)tn * 128u : gap((unsigned)tn * 2048u)), vA, a2s); \
        MM4(1, Ac, Qc, 2); RDA(An, 1); STAGE(sc + 16384, B + (size_t)tn * bk, vB, b2s); \
        MM4(0, Ac, Pc, 3); RDA(An, 2); STAGE(sc + 32768, B + (size_t)tn * bk + 256, vB, b2s); \
        MM4(1, Ac, Qc, 3); RDA(An, 3); \
        sc = sn; sn = sn == 98304 ? 0 : sn + 49152; } while (0)
    for (int t0 = 0; t0 < nt; t0 += 2) { KSTEP(t0, A0, P0, Q0, A1, P1, Q1); KSTEP(t0 + 1, A1, P1, Q1, A0, P0, Q0); }
    WAIT_L(0); SCHED();
#undef STG3
#undef RDA
#undef RDB
#undef MM4
#undef KSTEP
    const rsrc_t cr = mkrsrc(u.C, 0x1000000u);
    if (u.nsig) {
#pragma unroll
        for (int m = 0; m < 4; ++m)
#pragma unroll
            for (int b = 0; b < 2; ++b) store_frag(cr, (unsigned)wc * u.cw + frag_off(lane), gap((unsigned)(wr * 64 + m * 16) * u.ldc) + (unsigned)b * u.ch, acc[b][m][0], acc[b][m][1]);
    } else {
#pragma unroll
        for (int m = 0; m < 4; ++m)
#pragma unroll
            for (int b = 0; b < 2; ++b)
                ST16(to_rows(lds + L_SCR + wid * 1024, lane, acc[b][m][0], acc[b][m][1]), cr, (unsigned)(wr * 64 + (lane >> 2)) * 4096u + (unsigned)(wc * 64 + (lane & 3) * 16), (unsigned)(m * 65536 + b * 256), 0);
    }
    WAIT_V(0);
    signal(ctl, u.sig, u.nsig);
    BAR();
}

#define win_off(idx) ((unsigned)(idx) * 16384u + ((((unsigned)(idx) >> 7) ^ par) & 1u) * 8192u)
#define km_win(s, h) (win_off(NWIN + (s) * 32 + (h)) ^ 8192u)
#define W_MEM(s, x) ((s) == 0 ? 0 : NWIN + 2048 + 8 * (s) + 2 * (x))
#define W_PART(slot) (192 + (slot) * 64)
#define W_X(slot) (384 + (slot) * 128)
DI bool all_set(unsigned mx) { return __builtin_amdgcn_ballot_w64(mx == SENT) == 0ull; }
DI u32x4 rows_gather(unsigned v) { unsigned a = v, b = v; swap16(a, b); unsigned c = a, d = b; swap32(a, c); swap32(b, d); return (u32x4){a, b, c, d}; }
DI float max3f(float a, float b, float c) { return fmaxf(fmaxf(a, b), c); }
constexpr int KD0 = 2, KD1 = 6, KD2 = 5, KPM = 4;
constexpr int NSTR = 3;
constexpr int RED_M = 144;
constexpr int L_PX = 32768, L_PM = L_PX + 512, L_ML = L_PM + 512, L_PACE = L_ML + 256, L_VM = 34816, L_OWN = L_VM + 2048, L_X = L_OWN + 1536  ;
constexpr unsigned OOB = 0x80000000u;
#define PIN_S(x) asm volatile("" : "+s"(x))
#define PIN_V(x) asm volatile("" : "+v"(x))
DI float ror8(float v) { return __uint_as_float(__builtin_amdgcn_update_dpp(0, __float_as_uint(v), 0x128, 0xf, 0xf, true)); }

template <int ROLE>
DI void chain_loop(const Params& p, const unsigned par, const int rank, const int w, ldsp lds, const bf16x16 (&W)[6][2], const bf16x16 (&WO)[2][2]) {
    const int lane = fresh_tid() & 63, g = lane >> 4, n = lane & 15;
    const int h = rank >> 2, j = rank & 3;
    unsigned* ctl = (unsigned*)(p.ws + WS_CTL);
    const rsrc_t xr = mkrsrc(p.ws + WS_XCH, (unsigned)XCH_BYTES);
    const rsrc_t qr = mkrsrc(p.ws + WH(WS_QKVX + (size_t)(32 + h) * XH), 33u * XH);
    const int sidx = (n & 2) ? 0xEEEE : 0x4444;
    const unsigned l16 = (unsigned)lane * 16u;
    const int redw = (w * 1024 + (n & 1) * 512 + (n >> 1) * 8 + 2 * (g ^ (n >> 3))) * 4;
    const int redr = (w < 4 ? (n >> 3) * RED_M + (4 * w + g) * 8 + ((n & 7) ^ ((w & 1) * 2)) : 2 * RED_M + (8 * (w & 1) + 2 * g + ((n >> 1) & 1)) * 8 + ((2 * (n >> 2) + (n & 1)) ^ (g & 2))) * 4;
    const bool vown = (g >> 1) == (j & 1);
    const unsigned vg1 = (unsigned)(((j & 2) ^ 2) + (g >> 1)) * 1536u + 1024u + (unsigned)(lane & 31) * 16u;
    const unsigned vg0 = vown ? OOB : (vg1 + (unsigned)(j & 2) * 3072u - 3072u);
    const unsigned pub_off = (g == j ? 0u : (unsigned)(4 * h + g) * 16384u) + (unsigned)j * 1536u + (unsigned)w * 256u + (unsigned)n * 16u;
    const int red = ((j + 1) & 3) * 32 + h;
    const int rho = 4 * (8 * j + w) + 2 * (g >> 1) + ((n >> 2) & 1);
    const unsigned pst_off = n < 8 ? (unsigned)(rho >> 1) * 16384u + (unsigned)(rho & 1) * 4096u + (unsigned)((h ^ rho) & 31) * 128u + (unsigned)((g & 1) * 64 + (n & 3) * 16) : OOB;
    const unsigned mem_off = lane < 32 ? (unsigned)(4 * ((n >> 1) & 1) + 2 * ((n >> 2) & 1) + (n & 1)) * 16u + (unsigned)(lane >> 3) * 32768u : OOB;
    const unsigned xw0 = win_off(W_X(0)), xw1 = win_off(W_X(1));
    unsigned pso = win_off(W_PART(0)), psn = win_off(W_PART(1)), psp = win_off(W_PART(2));
    const unsigned redo = (unsigned)(red >> 1) * 16384u + (unsigned)(red & 1) * 4096u;
    const int vxr = L_X + lane * 16;
    const float csc = 0.08838834764831845f * 1.4426950408889634f;
    float ninf = -__builtin_inff(); PIN_V(ninf);
    u32x4 KX[8], VWX[2];
    unsigned long long kv_ok = 0;
    auto dma_x = [&](int s) {
        if (ROLE == 0 && w < 4) {
            if (!((kv_ok >> (s >> 2)) & 1ull)) {
                kv_ok = __builtin_amdgcn_ballot_w64(__hip_atomic_load(ctl + C_KVC(n, h), RLX_AGENT) + 1u >= 16u);
                if (!((kv_ok >> (s >> 2)) & 1ull)) { wait_count(ctl, C_KVC(s >> 2, h), 16u); kv_ok |= 1ull << (s >> 2); }
                asm volatile("" ::: "memory");
            }
#pragma unroll
            for (int i = 0; i < 2; ++i) {
                const unsigned vo = l16, so = (unsigned)s * 16384u + (unsigned)(w * 2 + i) * 1024u;
                __builtin_amdgcn_raw_ptr_buffer_load_lds(qr, (LAS void*)(lds + L_X + (s & 1) * 16384 + (w * 2 + i) * 1024), 16, vo, so + 32u * XH, 0, 16);
                __builtin_amdgcn_raw_ptr_buffer_load_lds(qr, (LAS void*)(lds + L_X + (s & 1) * 16384 + 8192 + (w * 2 + i) * 1024), 16, vo, so, 0, 16);
            }
        }
    };
    auto load_kx = [&](int s) {
#pragma unroll
        for (int kt = 0; kt < 2; ++kt)
#pragma unroll
            for (int kk = 0; kk < 4; ++kk) KX[kt * 4 + kk] = *(const LAS u32x4*)(lds + vxr + (s & 1) * 16384 + 8192 + kt * 4096 + ((j + kk) & 3) * 1024);
    };
    auto vwx_kt = [&](int s, int kt) {
        bf16x8 VA[4];
#pragma unroll
        for (int kk = 0; kk < 4; ++kk) VA[kk] = *(const LAS bf16x8*)(lds + vxr + (s & 1) * 16384 + kt * 4096 + (kk ^ (j & 2)) * 1024);
#pragma unroll
        for (int ct = 0; ct < 2; ++ct) {
            f32x4 d = {0.f, 0.f, 0.f, 0.f};
#pragma unroll
            for (int kk = 0; kk < 4; ++kk) d = mma32(VA[kk], (kk & 1) ? hi8(WO[ct][kk >> 1]) : lo8(WO[ct][kk >> 1]), d);
            VWX[ct][2 * kt] = pk_bf16(d[0], d[1]); VWX[ct][2 * kt + 1] = pk_bf16(d[2], d[3]);
        }
    };
    auto rmax = [&](float v) { float a = v, b = v; swap16f(a, b); v = max3f(a, b, ninf); a = v; b = v; swap32f(a, b); return max3f(a, b, ninf); };
    auto lo2 = [](f32x4 v) { return (f32x2){v[0], v[1]}; };
    auto hi2 = [](f32x4 v) { return (f32x2){v[2], v[3]}; };
    dma_x(0); dma_x(1);
    asm volatile("s_waitcnt vmcnt(0) lgkmcnt(0)" ::: "memory"); BAR(); asm volatile("" ::: "memory");
    vwx_kt(0, 0); vwx_kt(0, 1);
    if (ROLE == 2) load_kx(0);
    __builtin_amdgcn_s_setprio(2);

    unsigned spins = 0, pace_c = 0x7fffffffu, pace_t = 0;
    const u32x4 sv = {SENT, SENT, SENT, SENT};
    for (int s = 0;; ++s) {
        unsigned xw = (s & 1) ? xw1 : xw0;
        unsigned mso = win_off(W_MEM(s, rank >> 5) + (w >> 2)) + (unsigned)(w & 3) * 2048u;
        unsigned pub = pub_off + (g == j ? km_win(s, h) : xw);
        PIN_S(xw); PIN_S(mso); PIN_V(pub);
        {
            __builtin_amdgcn_s_sleep(KD0);
            const u32x2 pv = *(const LAS u32x2*)(lds + L_PACE);
            unsigned target = __builtin_amdgcn_readfirstlane(pv.x); const bool valid = __builtin_amdgcn_readfirstlane(pv.y) + 1u == (unsigned)s && s > 3;
            unsigned now = (unsigned)__builtin_amdgcn_s_memrealtime();
            if (ROLE == 3 && s > 2) pace_c = min(pace_c, now - pace_t);
            if (valid && (int)(target - now) < 100) { while ((int)(target - now) > 0) now = (unsigned)__builtin_amdgcn_s_memrealtime(); }
        }
        f32x4 c[6]; u32x4 a1;
        u32x4 a0;
        auto ld_a = [&]() { asm volatile("" ::: "memory"); a0 = ld16_sc1(xr, l16, mso); a1 = ld16_sc1(xr, l16 + 1024, mso); };
        auto ok_a = [&]() { return all_set(max(umax4(a0), umax4(a1))); };
        auto mm_a = [&]() {
#pragma unroll
            for (int i = 0; i < 6; ++i) { c[i] = (f32x4){0.f, 0.f, 0.f, 0.f}; PIN_V(c[i]); }
            SCHED();
            c[0] = smma(a0, W[0][0], c[0], sidx); c[1] = smma(a0, W[1][0], c[1], sidx); c[0] = smma(a1, W[0][1], c[0], sidx); c[1] = smma(a1, W[1][1], c[1], sidx);
            c[2] = smma(a0, W[2][0], c[2], sidx); c[3] = smma(a0, W[3][0], c[3], sidx);
        };
        auto put = [&](int i) { *(LAS f32x2*)(lds + redw + ((i >> 1) * RED_M + (i & 1) * 64) * 4) = lo2(c[i]) + hi2(c[i]); };
        ld_a(); SCHED();
        mm_a();
        if (__builtin_expect(!ok_a(), 0)) { bool out; do { out = spin_out(spins, ctl); ld_a(); } while (!(ok_a() | out)); mm_a(); }
        c[2] = smma(a1, W[2][1], c[2], sidx); c[3] = smma(a1, W[3][1], c[3], sidx);
        put(0); put(1);
        SCHED();
        c[4] = smma(a0, W[4][0], c[4], sidx); c[5] = smma(a0, W[5][0], c[5], sidx); c[4] = smma(a1, W[4][1], c[4], sidx);
        put(2); put(3);
        c[5] = smma(a1, W[5][1], c[5], sidx);
        SCHED();
        put(4); put(5);
        LDS_BAR();
        if (ROLE <= 1) {
            f32x2 v[8];
#pragma unroll
            for (int ww = 0; ww < 8; ++ww) { const LAS float* q = (const LAS float*)(lds + redr + ww * 4096); v[ww] = (f32x2){q[0], q[512]}; }
            const f32x2 t = ((v[0] + v[1]) + (v[2] + v[3])) + ((v[4] + v[5]) + (v[6] + v[7]));
            const u32x4 o = rows_gather(min(pk_bf16(t.x, t.y), SENT - 1));
            ST16(o, xr, pub, 0, 16);
            *(LAS u32x4*)(lds + L_OWN + w * 256 + n * 16) = o;
        } else {
            if (ROLE == 3 && rank == 0) {
                pace_t = (unsigned)__builtin_amdgcn_s_memrealtime();
                if (lane == 0) { __hip_atomic_store(ctl + C_PROG, (unsigned)s, RLX_AGENT);
                    __hip_atomic_store((unsigned long long*)(ctl + C_PACE + par * 2048), (unsigned long long)(pace_t + pace_c + KPM) | ((unsigned long long)s << 32), RLX_AGENT); }
            }
        }
        if (s == NSEG - 1) break;
        LDS_BAR();
        unsigned VWM[2];
        auto vwm_pack = [&](const f32x4 (&dv)[2]) {
#pragma unroll
            for (int ct = 0; ct < 2; ++ct) { const f32x2 t = lo2(dv[ct]) + hi2(dv[ct]); VWM[ct] = pk_bf16(t.x, t.y); PIN_V(VWM[ct]); }
        };
        f32x4 dv[2] = {{0.f, 0.f, 0.f, 0.f}, {0.f, 0.f, 0.f, 0.f}}, s0, s1;
        PIN_V(dv[0]); PIN_V(dv[1]);
        u32x4 v0, v1;
        if (ROLE == 0) {
            WAIT_V(0);
            if (s + 2 < NSEG - 1) dma_x(s + 2);
        } else {
            unsigned cw = xw + (unsigned)rank * 16384u, c1 = cw + (unsigned)((j + 1) & 3) * 1536u, c2 = cw + (unsigned)((j + 2) & 3) * 1536u, c3 = cw + (unsigned)((j + 3) & 3) * 1536u;
            PIN_S(cw); PIN_S(c1); PIN_S(c2); PIN_S(c3);
            const unsigned xl = ROLE == 2 ? (l16 & ~128u) : l16;
            u32x4 vo;
            f32x4 e0 = {0.f, 0.f, 0.f, 0.f}, e1 = e0;
            if (ROLE == 1) { vo = *(const LAS u32x4*)(lds + L_OWN + 1024 + (lane & 31) * 16); if (!vown) vo = (u32x4){0u, 0u, 0u, 0u}; PIN_V(vo); }
            else {
                const u32x4 X0 = *(const LAS u32x4*)(lds + L_OWN + xl);
                if (ROLE == 2) { e0 = mma32(asbf(KX[0]), asbf(X0), e0); e1 = mma32(asbf(KX[4]), asbf(X0), e1); } else e0 = mma32(asbf(X0), asbf(X0), e0);
                PIN_V(e0); PIN_V(e1);
            }
            __builtin_amdgcn_s_sleep(KD1);
            u32x4 X1, X2, X3;
            auto ld_b = [&]() {
                asm volatile("" ::: "memory");
                if (ROLE >= 2) { X1 = ld16_sc1(xr, xl, c1); X2 = ld16_sc1(xr, xl, c2); X3 = ld16_sc1(xr, xl, c3); }
                else { v0 = ld16_sc1(xr, vg0, cw); v1 = ld16_sc1(xr, vg1, cw); }
            };
            auto ok_b = [&]() { return all_set(ROLE >= 2 ? max(umax4(X1), max(umax4(X2), umax4(X3))) : max(umax4(v0), umax4(v1))); };
            auto mm_b = [&]() {
                if (ROLE == 2) {
                    s0 = mma32(asbf(KX[1]), asbf(X1), e0); s1 = mma32(asbf(KX[5]), asbf(X1), e1);
                    s0 = mma32(asbf(KX[2]), asbf(X2), s0); s1 = mma32(asbf(KX[6]), asbf(X2), s1);
                    s0 = mma32(asbf(KX[3]), asbf(X3), s0); s1 = mma32(asbf(KX[7]), asbf(X3), s1);
                } else if (ROLE == 3) {
                    s0 = mma32(asbf(X1), asbf(X1), e0); s1 = mma32(asbf(X2), asbf(X2), e1); s0 = mma32(asbf(X3), asbf(X3), s0);
                } else {
                    v0 |= vo;
                    *(LAS u32x4*)(lds + L_VM + lane * 16) = v0; *(LAS u32x4*)(lds + L_VM + 1024 + lane * 16) = v1;
                }
            };
            ld_b(); SCHED(); mm_b();
            if (__builtin_expect(!ok_b(), 0)) { bool out; do { out = spin_out(spins, ctl); ld_b(); } while (!(ok_b() | out)); mm_b(); }
        }
        float m;
        f32x4 sc;
        if (ROLE >= 2) {
            sc = ROLE == 2 ? (n < 8 ? s0 : s1) : s0 + s1;
            if (ROLE == 3 && g < 2) sc = (f32x4){ninf, ninf, ninf, ninf};
            m = max3f(max3f(sc[0], sc[1], sc[2]), sc[3], ninf);
            if (ROLE == 2) m = max3f(m, ror8(m), ninf);
            m = rmax(m);
            PIN_V(m);
        }
        LDS_BAR();
        if (ROLE == 0) {
            const unsigned ro = w < 3 ? xw + (unsigned)rank * 16384u + (unsigned)w * 2048u : psp + redo + (unsigned)(w - 3) * 2048u;
            ST16(sv, xr, l16, ro, 16); ST16(sv, xr, l16 + 1024, ro, 16);
        }
        if (ROLE != 1) { v0 = *(const LAS u32x4*)(lds + L_VM + lane * 16); v1 = *(const LAS u32x4*)(lds + L_VM + 1024 + lane * 16); }
        if (ROLE >= 2) {
            sc = (sc - m) * csc;
#pragma unroll
            for (int i = 0; i < 4; ++i) sc[i] = __builtin_amdgcn_exp2f(sc[i]);
            *(LAS u32x2*)(lds + (ROLE == 2 ? L_PX + (g * 8 + (n & 7)) * 16 + (n >> 3) * 8 : L_PM + (g < 2 ? 256 : 0) + n * 16 + (g & 1) * 8)) = (u32x2){pk_bf16(sc[0], sc[1]), pk_bf16(sc[2], sc[3])};
            SCHED();
            dv[0] = smma(v0, WO[0][0], dv[0], sidx); dv[1] = smma(v0, WO[1][0], dv[1], sidx);
            float l = (sc[0] + sc[1]) + (sc[2] + sc[3]);
            if (ROLE == 2) l += ror8(l);
            dv[0] = smma(v1, WO[0][1], dv[0], sidx); dv[1] = smma(v1, WO[1][1], dv[1], sidx);
            l = rows_sum(l);
            *(LAS f32x2*)(lds + L_ML + n * 16 + (ROLE == 2 ? 0 : 8)) = (f32x2){m, l};
        } else {
#pragma unroll
            for (int ct = 0; ct < 2; ++ct) { dv[ct] = smma(v0, WO[ct][0], dv[ct], sidx); dv[ct] = smma(v1, WO[ct][1], dv[ct], sidx); }
        }
        vwm_pack(dv);
        u32x4 am0 = {VWM[0], 0u, 0u, 0u}, am1 = {VWM[1], 0u, 0u, 0u}, pm = {0u, 0u, 0u, 0u};
        PIN_V(am0); PIN_V(am1); PIN_V(pm);
        LDS_BAR();
        {
            const f32x4 ml = *(const LAS f32x4*)(lds + L_ML + n * 16);
            const u32x4 px = *(const LAS u32x4*)(lds + L_PX + (g * 8 + (n & 7)) * 16);
            pm.x = *(const LAS unsigned*)(lds + L_PM + n * 16 + g * 4);
            const f32x4 z = {0.f, 0.f, 0.f, 0.f};
            const f32x4 dx0 = mma32(asbf(VWX[0]), asbf(px), z), dx1 = mma32(asbf(VWX[1]), asbf(px), z), dm0 = mma32(asbf(am0), asbf(pm), z), dm1 = mma32(asbf(am1), asbf(pm), z);
            const float dl = (ml[2] - ml[0]) * csc;
            const float e = __builtin_amdgcn_exp2f(-__builtin_fabsf(dl));
            const float ex = dl > 0.f ? e : 1.f, em = dl > 0.f ? 1.f : e;
            const float inv = __builtin_amdgcn_rcpf(fmaf(ex, ml[1], em * ml[3]));
            const f32x4 d0 = (dx0 * ex + dm0 * em) * inv, d1 = (dx1 * ex + dm1 * em) * inv;
            const u32x4 o = {min(pk_bf16(d0[0], d0[1]), SENT - 1), min(pk_bf16(d0[2], d0[3]), SENT - 1), min(pk_bf16(d1[0], d1[1]), SENT - 1), min(pk_bf16(d1[2], d1[3]), SENT - 1)};
            ST16(o, xr, pst_off, pso, 16);
        }
        SCHED();
        __builtin_amdgcn_s_setprio(ROLE == 3 ? 1 : 0);
        unsigned long long pv = 0;
        if (ROLE == 1) { pv = __hip_atomic_load((unsigned long long*)(ctl + C_PACE + par * 2048), RLX_AGENT); SCHED(); }
        vwx_kt(s + 1, 0); vwx_kt(s + 1, 1);
        SCHED();
        if (ROLE == 3) __builtin_amdgcn_s_setprio(2);
        if (ROLE == 3) {
            u32x4 SEL;
            { const unsigned one = (n & 1) ? 0x3f800000u : 0x00003f80u; const int dw = (n & 7) >> 1; SEL = (u32x4){dw == 0 ? one : 0u, dw == 1 ? one : 0u, dw == 2 ? one : 0u, dw == 3 ? one : 0u}; }
            unsigned so = pso + redo, mo = win_off(W_MEM(s + 1, 0) + (red >> 6)) + (unsigned)(red & 63) * 128u;
            PIN_S(so); PIN_S(mo); PIN_V(SEL);
            __builtin_amdgcn_s_sleep(KD2);
            f32x4 ta, tb;
            u32x4 q[4];
            auto ld_c = [&]() {
                asm volatile("" ::: "memory");
#pragma unroll
                for (int i = 0; i < 4; ++i) q[i] = ld16_sc1(xr, l16 + i * 1024, so);
            };
            auto ok_c = [&]() { return all_set(max(max(umax4(q[0]), umax4(q[1])), max(umax4(q[2]), umax4(q[3])))); };
            auto mm_c = [&]() {
                ta = mma32(asbf(SEL), asbf(q[0]), (f32x4){0.f, 0.f, 0.f, 0.f}); tb = mma32(asbf(SEL), asbf(q[1]), (f32x4){0.f, 0.f, 0.f, 0.f});
                ta = mma32(asbf(SEL), asbf(q[2]), ta); tb = mma32(asbf(SEL), asbf(q[3]), tb);
            };
            ld_c(); mm_c();
            if (__builtin_expect(!ok_c(), 0)) { bool out; do { out = spin_out(spins, ctl); ld_c(); } while (!(ok_c() | out)); mm_c(); }
            f32x4 t = ta + tb;
#pragma unroll
            for (int i = 0; i < 4; ++i) t[i] += ror8(t[i]);
            unsigned a0 = min(pk_bf16(t[0], t[1]), SENT - 1), a1 = min(pk_bf16(t[2], t[3]), SENT - 1);
            unsigned b0 = a0, b1 = a1; swap16(a0, b0); swap16(a1, b1);
            const u32x4 o = {a0, a1, b0, b1};
            ST16(o, xr, mem_off, mo, 16);
        } else {
            if (ROLE == 2) load_kx(s + 1);
            if (ROLE == 1) *(LAS u32x2*)(lds + L_PACE) = (u32x2){(unsigned)pv, (unsigned)(pv >> 32)};
        }
        { const unsigned t = psp; psp = pso; pso = psn; psn = t; }
        if (ROLE != 3) __builtin_amdgcn_s_setprio(2);
        LDS_BAR();
    }
    WAIT_V(0);
    __builtin_amdgcn_s_setprio(0);
}

DI void chain_role(const Params& p, unsigned par, int rank, ldsp lds) {
    const int tid = fresh_tid(), w = __builtin_amdgcn_readfirstlane(tid >> 6), lane = tid & 63, g = lane >> 4, n = lane & 15;
    const int h = rank >> 2, j = rank & 3;
    const rsrc_t xr = mkrsrc(p.ws + WS_XCH, (unsigned)XCH_BYTES);
    const int red = ((j + 1) & 3) * 32 + h;
    bf16x16 W[6][2], WO[2][2];
    {
        ldsp my = lds + w * 16384;
        const int rrow = lane >> 2, rc = (lane & 3) * 8, q4 = n >> 2;
        const int toffo = (8 * g + q4) * 64 + (n & 3) * 8;
        const int toffq = (16 * (g & 1) + 8 * (g >> 1) + 4 * (q4 >> 1) + (q4 & 1)) * 64 + (n & 3) * 8;
#define FRAG(base, st) cat16(cat8(trd((base)), trd((base) + (st))), cat8(trd((base) + 2048), trd((base) + 2048 + (st))))
#pragma unroll
        for (int i = 0; i < 8; ++i) {
            __builtin_amdgcn_global_load_lds((const unsigned*)(p.wq + (size_t)(128 * w + 16 * i + rrow) * 4096 + h * 128 + 32 * j + rc), (LAS unsigned*)(my + i * 1024), 16, 0, 0);
            __builtin_amdgcn_global_load_lds((const unsigned*)(p.wo + (size_t)(h * 128 + 16 * i + rrow) * 1024 + 256 * j + 32 * w + rc), (LAS unsigned*)(my + 8192 + i * 1024), 16, 0, 0);
        }
        {
            const u32x4 sv = {SENT, SENT, SENT, SENT};
            if (w < 6) {
                ST16(sv, xr, (unsigned)lane * 16u, win_off(W_X(0)) + (unsigned)rank * 16384u + (unsigned)w * 1024u, 16);
                ST16(sv, xr, (unsigned)lane * 16u, win_off(W_X(1)) + (unsigned)rank * 16384u + (unsigned)w * 1024u, 16);
            }
            ST16(sv, xr, (unsigned)lane * 16u, win_off(W_PART(w >> 2)) + (unsigned)(red >> 1) * 16384u + (unsigned)(red & 1) * 4096u + (unsigned)(w & 3) * 1024u, 16);
            ST16(sv, xr, (unsigned)lane * 16u, km_win(56 + w, h) + (unsigned)j * 1536u, 16);
            ST16(sv, xr, lane < 32 ? (unsigned)lane * 16u + 1024u : OOB, km_win(56 + w, h) + (unsigned)j * 1536u, 16);
            const int s = 8 * w + (lane >> 3);
#pragma unroll
            for (int x = 0; x < 4; ++x) if (s > 0) ST16(sv, xr, win_off(W_MEM(s, x) + (red >> 6)) + (unsigned)(red & 63) * 128u + (unsigned)(lane & 7) * 16u, 0, 16);
        }
        WAIT_V(0);
#pragma unroll
        for (int T = 0; T < 2; ++T)
#pragma unroll
            for (int t = 0; t < 2; ++t) { W[T][t] = FRAG(my + t * 4096 + T * 32 + toffq, 128); WO[T][t] = FRAG(my + 8192 + (t ^ (j >> 1)) * 4096 + T * 32 + toffo, 256); }
#pragma unroll
        for (int T = 0; T < 2; ++T) asm volatile("" : "+v"(W[T][0]), "+v"(W[T][1]), "+v"(WO[T][0]), "+v"(WO[T][1]));
        WAIT_L(0);
#pragma unroll
        for (int i = 0; i < 8; ++i) {
            __builtin_amdgcn_global_load_lds((const unsigned*)(p.wk + (size_t)(128 * w + 16 * i + rrow) * 4096 + h * 128 + 32 * j + rc), (LAS unsigned*)(my + i * 1024), 16, 0, 0);
            __builtin_amdgcn_global_load_lds((const unsigned*)(p.wv + (size_t)(128 * w + 16 * i + rrow) * 4096 + h * 128 + 32 * j + rc), (LAS unsigned*)(my + 8192 + i * 1024), 16, 0, 0);
        }
        WAIT_V(0);
#pragma unroll
        for (int T = 0; T < 4; ++T)
#pragma unroll
            for (int t = 0; t < 2; ++t) W[2 + T][t] = FRAG(my + (T >> 1) * 8192 + t * 4096 + (T & 1) * 32 + toffq, 128);
#pragma unroll
        for (int T = 0; T < 4; ++T) asm volatile("" : "+v"(W[2 + T][0]), "+v"(W[2 + T][1]));
        WAIT_L(0);
#undef FRAG
    }
    BAR();
    if (w == 7 && lane < 8) {
        const unsigned* src = (const unsigned*)(p.mem0 + (4 * (red & 1) + 2 * (lane >> 2) + (lane & 1)) * DM + 32 * (red >> 2) + 8 * ((red >> 1) & 1) + 4 * ((lane >> 1) & 1));
        const u32x4 v = {min(src[0], SENT - 1), min(src[1], SENT - 1), min(src[8], SENT - 1), min(src[9], SENT - 1)};
        ST16(v, xr, (unsigned)lane * 16u + (unsigned)(red & 63) * 128u, win_off(W_MEM(0, 0) + (red >> 6)), 16);
    }
    if (w < 5) chain_loop<0>(p, par, rank, w, lds, W, WO);
    else if (w == 5) chain_loop<1>(p, par, rank, 5, lds, W, WO);
    else if (w == 6) chain_loop<2>(p, par, rank, 6, lds, W, WO);
    else chain_loop<3>(p, par, rank, 7, lds, W, WO);
}

template <bool POLL>
DI void attn_a_item(const Params& p, unsigned par, int s, int h, ldsp my, int lane) {
    const int g = lane >> 4, n = lane & 15;
    const rsrc_t xr = mkrsrc(p.ws + WS_XCH, (unsigned)XCH_BYTES);
    const rsrc_t qr = mkrsrc(p.ws + WH(WS_QKVX + (size_t)h * XH + (size_t)s * 16384), 65u * XH);
#pragma unroll
    for (int i = 0; i < 8; ++i)
        __builtin_amdgcn_raw_ptr_buffer_load_lds(qr, (LAS void*)(my + i * 1024), 16, (unsigned)(g * 256 + (n ^ ((g & 1) * 8)) * 16), 64u * XH + (unsigned)i * 1024u, 0, 16);
    u32x4 Q[2][4], K[2][4], XM[4];
    const unsigned ro = (unsigned)lane * 16u;
#pragma unroll
    for (int t = 0; t < 2; ++t)
#pragma unroll
        for (int k = 0; k < 4; ++k) { Q[t][k] = ld16_sc1(qr, ro + k * 1024, (unsigned)t * 4096u); K[t][k] = ld16_sc1(qr, ro + k * 1024, (unsigned)t * 4096u + 32u * XH); }
    const unsigned qwin = km_win(s, h);
    u32x4 VM[2];
    if (POLL) {
        if (s < 56) wait_count<24>((unsigned*)(p.ws + WS_CTL), C_PROG, (unsigned)s + 2u);
        else { wait_count<4>((unsigned*)(p.ws + WS_CTL), C_PROG, (unsigned)s + 1u); __builtin_amdgcn_s_sleep(16); }
    }
    for (unsigned spins = 0;;) {
        asm volatile("" ::: "memory");
#pragma unroll
        for (int k = 0; k < 4; ++k) XM[k] = ld16_sc1(xr, (unsigned)lane * 16u + (k & 1) * 1536, qwin + (k >> 1) * 3072);
#pragma unroll
        for (int ts = 0; ts < 2; ++ts) VM[ts] = ld16_sc1(xr, (unsigned)(g >> 1) * 1536u + 1024u + (unsigned)(lane & 31) * 16u, qwin + ts * 3072);
        if (!POLL || s < 56) break;
        if (all_set(max(max(max(umax4(XM[0]), umax4(XM[1])), max(umax4(XM[2]), umax4(XM[3]))), max(umax4(VM[0]), umax4(VM[1]))))) break;
        __builtin_amdgcn_s_sleep(4);
        if (spin_out(spins, (unsigned*)(p.ws + WS_CTL))) break;
    }
    SCHED();
#pragma unroll
    for (int ts = 0; ts < 2; ++ts) {
        const u32x4 v = VM[ts];
        const int m = 2 * (n >> 2) + (n & 1), hf = (n >> 1) & 1;
#pragma unroll
        for (int i = 0; i < 4; ++i) { const int e = 64 * ts + 16 * g + 4 * i + 2 * hf;
            *(LAS unsigned*)(my + 8192 + (e >> 3) * 256 + (m ^ (e & 8)) * 16 + (e & 7) * 2) = v[i]; }
    }
    const float csc = 0.08838834764831845f * 1.4426950408889634f, ninf = -__builtin_inff();
    u32x4 PX[2]; u32x2 PM[2];
#pragma unroll
    for (int qt = 0; qt < 2; ++qt) {
        f32x4 sm = {0.f, 0.f, 0.f, 0.f}, s0 = sm, s1 = sm;
#pragma unroll
        for (int k = 0; k < 4; ++k) { sm = mma32(asbf(XM[k]), asbf(Q[qt][k]), sm); s0 = mma32(asbf(K[0][k]), asbf(Q[qt][k]), s0); if (qt == 1) s1 = mma32(asbf(K[1][k]), asbf(Q[qt][k]), s1); }
#pragma unroll
        for (int i = 0; i < 4; ++i) {
            if (g < 2) sm[i] = ninf;
            if (qt == 0) { if (4 * g + i > n) s0[i] = ninf; s1[i] = ninf; }
            else if (4 * g + i > n) s1[i] = ninf;
        }
        float m = fmaxf(fmaxf(fmaxf(s0[0], s0[1]), fmaxf(s0[2], s0[3])), fmaxf(fmaxf(s1[0], s1[1]), fmaxf(s1[2], s1[3])));
        m = fmaxf(m, fmaxf(fmaxf(sm[0], sm[1]), fmaxf(sm[2], sm[3])));
        m = rows_max(m);
        float sum = 0.f;
        s0 = (s0 - m) * csc; s1 = (s1 - m) * csc; sm = (sm - m) * csc;
#pragma unroll
        for (int i = 0; i < 4; ++i) { s0[i] = __builtin_amdgcn_exp2f(s0[i]); s1[i] = __builtin_amdgcn_exp2f(s1[i]); sm[i] = __builtin_amdgcn_exp2f(sm[i]);
            sum += (s0[i] + s1[i]) + sm[i]; }
        sum = rows_sum(sum);
        const float inv = __builtin_amdgcn_rcpf(sum);
        PX[qt] = (u32x4){pk_bf16(s0[0] * inv, s0[1] * inv), pk_bf16(s0[2] * inv, s0[3] * inv), pk_bf16(s1[0] * inv, s1[1] * inv), pk_bf16(s1[2] * inv, s1[3] * inv)};
        PM[qt] = (u32x2){pk_bf16(sm[0] * inv, sm[1] * inv), pk_bf16(sm[2] * inv, sm[3] * inv)};
    }
    asm volatile("s_waitcnt vmcnt(0) lgkmcnt(0)" ::: "memory");
    const int q4 = n >> 2, p4 = n & 3;
    const ldsp vx = my + (p4 >> 1) * 256 + ((4 * g + q4) ^ ((p4 >> 1) * 8)) * 16 + (p4 & 1) * 8, vmm = my + 8192 + (p4 >> 1) * 256 + ((4 * (g & 1) + q4) ^ ((p4 >> 1) * 8)) * 16 + (p4 & 1) * 8;
    const rsrc_t orr = mkrsrc(p.ws + WH(WS_OA + (size_t)s * 524288 + gap((unsigned)h * 4096u)), 524288u);
    const unsigned oo = frag_off(lane);
    bf16x8 VA[8]; s16x4 VMf[8];
#pragma unroll
    for (int et = 0; et < 8; ++et) { VA[et] = cat8(trd(vx + et * 512), trd(vx + 4096 + et * 512)); VMf[et] = trd(vmm + et * 512); }
    SCHED();
#pragma unroll
    for (int ep = 0; ep < 4; ++ep) {
        f32x4 d[2][2];
#pragma unroll
        for (int e2 = 0; e2 < 2; ++e2)
#pragma unroll
            for (int qt = 0; qt < 2; ++qt) d[qt][e2] = mma16(__builtin_bit_cast(u32x2, VMf[2 * ep + e2]), PM[qt], mma32(VA[2 * ep + e2], asbf(PX[qt]), (f32x4){0.f, 0.f, 0.f, 0.f}));
#pragma unroll
        for (int qt = 0; qt < 2; ++qt) store_frag(orr, oo + ep * 1024, (unsigned)qt * 262144u, d[qt][0], d[qt][1]);
    }
    asm volatile("s_waitcnt lgkmcnt(0)" ::: "memory");
}

DI void attn_b_item(const Params& p, unsigned par, int hq, int qb, ldsp lds) {
    const int tid = fresh_tid(), w = __builtin_amdgcn_readfirstlane(tid >> 6), lane = tid & 63, g = lane >> 4, n = lane & 15;
    const int q0 = qb * 128;
    const rsrc_t br = mkrsrc(p.ws + WH(WS_QKVB + (size_t)hq * 4 * BH), 68u * BH);
    const rsrc_t orr = mkrsrc(p.ws + WH(WS_OB + (size_t)qb * 524288), 524288u);
    constexpr int BUF = 57344;
    unsigned to[3];
#pragma unroll
    for (int i = 0; i < 3; ++i) to[i] = gap((unsigned)max(8 * qb - 16 + w * 3 + i, 0) * 1024u);
    const unsigned l16 = (unsigned)lane * 16u, vsw = (unsigned)(g * 256 + (n ^ ((g & 1) * 8)) * 16);
    auto stage = [&](int i) {
        const unsigned so = (unsigned)i * BH; ldsp d = lds + (i & 1) * BUF;
#pragma unroll
        for (int k = 0; k < 3; ++k) {
            __builtin_amdgcn_raw_ptr_buffer_load_lds(br, (LAS void*)(d + (w * 3 + k) * 1024), 16, l16, so + 32u * BH + to[k], 0, 16);
            __builtin_amdgcn_raw_ptr_buffer_load_lds(br, (LAS void*)(d + 24576 + (w * 3 + k) * 1024), 16, vsw, so + 64u * BH + to[k], 0, 16);
        }
        __builtin_amdgcn_raw_ptr_buffer_load_lds(br, (LAS void*)(d + 49152 + w * 1024), 16, l16, so + (unsigned)(qb * 16384 + w * 1024), 0, 16);
    };
    const float ninf = -__builtin_inff(), csc = 0.17677669529663687f * 1.4426950408889634f;
    const int tmin = (256 - q0 - 16 * w) >> 4;
    const int q4 = n >> 2, p4 = n & 3;
    const int kq = w * 1024 + lane * 16;
    const unsigned vb = (unsigned)(size_t)lds + 24576u + (unsigned)(w * 1024 + (p4 >> 1) * 256 + ((4 * g + q4) ^ ((p4 >> 1) * 8)) * 16 + (p4 & 1) * 8);
    stage(0);
#pragma unroll 1
    for (int i = 0; i < 4; ++i) {
        if (i < 3) { stage(i + 1); WAIT_V(7); } else WAIT_V(0);
        BAR(); asm volatile("" ::: "memory");
        const ldsp bp = lds + (i & 1) * BUF;
        const bf16x8 Q = *(const LAS bf16x8*)(bp + 49152 + kq);
        f32x4 sc[17]; bf16x8 KF[17];
#pragma unroll
        for (int t = 0; t < 17; ++t) KF[t] = *(const LAS bf16x8*)(bp + kq + t * 1024);
        SCHED();
#pragma unroll
        for (int t = 0; t < 17; ++t) sc[t] = mma32(KF[t], Q, (f32x4){0.f, 0.f, 0.f, 0.f});
#pragma unroll
        for (int k = 0; k < 4; ++k) { if (4 * g + k <= n) sc[0][k] = ninf; if (4 * g + k > n) sc[16][k] = ninf; }
        if (tmin > 0) {
#pragma unroll
            for (int t = 0; t < 16; ++t) if (t < tmin) sc[t] = (f32x4){ninf, ninf, ninf, ninf};
        }
        float mq[4];
#pragma unroll
        for (int k = 0; k < 4; ++k) {
            mq[k] = max3f(sc[16][k], sc[k][0], sc[k][1]); mq[k] = max3f(mq[k], sc[k][2], sc[k][3]);
#pragma unroll
            for (int t = 4 + k; t < 16; t += 4) mq[k] = max3f(max3f(mq[k], sc[t][0], sc[t][1]), sc[t][2], sc[t][3]);
        }
        const float m = rows_max(max3f(max3f(mq[0], mq[1], mq[2]), mq[3], mq[3]));
        f32x4 sum4 = {0.f, 0.f, 0.f, 0.f};
#pragma unroll
        for (int t = 0; t < 17; ++t) {
            sc[t] = (sc[t] - m) * csc;
#pragma unroll
            for (int k = 0; k < 4; ++k) sc[t][k] = __builtin_amdgcn_exp2f(sc[t][k]);
            sum4 += sc[t];
        }
        const float inv = __builtin_amdgcn_rcpf(rows_sum((sum4[0] + sum4[1]) + (sum4[2] + sum4[3])));
        const unsigned v0 = vb + (unsigned)((i & 1) * BUF);
        s16x4 V0[17], V1[17];
#define VRD(t) TRD(V0[t], v0, (t) * 1024); TRD(V1[t], v0, (t) * 1024 + 512)
        VRD(0); VRD(1); VRD(2); VRD(3); VRD(4); VRD(5); VRD(6); VRD(7); VRD(8); VRD(9); VRD(10); VRD(11); VRD(12); VRD(13); VRD(14); VRD(15); VRD(16);
#undef VRD
        u32x2 P[17];
#pragma unroll
        for (int t = 0; t < 17; ++t) P[t] = (u32x2){pk_bf16(sc[t][0], sc[t][1]), pk_bf16(sc[t][2], sc[t][3])};
        WAIT_L(0); SCHED();
        f32x4 d0 = {0.f, 0.f, 0.f, 0.f}, d1 = d0;
#pragma unroll
        for (int tp = 0; tp < 8; ++tp) {
            const bf16x8 PP = asbf((u32x4){P[2 * tp].x, P[2 * tp].y, P[2 * tp + 1].x, P[2 * tp + 1].y});
            d0 = mma32(cat8(V0[2 * tp], V0[2 * tp + 1]), PP, d0); d1 = mma32(cat8(V1[2 * tp], V1[2 * tp + 1]), PP, d1);
        }
        d0 = mma16(__builtin_bit_cast(u32x2, V0[16]), P[16], d0); d1 = mma16(__builtin_bit_cast(u32x2, V1[16]), P[16], d1);
        BAR();
        store_frag(orr, frag_off(lane), (unsigned)w * 65536u + gap((unsigned)(hq * 4 + i) * 1024u), d0 * inv, d1 * inv);
    }
    WAIT_V(0);
    signal((unsigned*)(p.ws + WS_CTL), C_OB + qb * 32, 1);
}

DI void out_tail(const Params& p, unsigned par, int c, int q, ldsp lds) {
    const int tid = fresh_tid(), w = __builtin_amdgcn_readfirstlane(tid >> 6), lane = tid & 63, g = lane >> 4, n = lane & 15;
    unsigned* ctl = (unsigned*)(p.ws + WS_CTL);
    bf16x8 WT[16][2];
    {
        ldsp my = lds + w * 16384;
        const int toff = (8 * g + (n >> 2)) * 64 + (n & 3) * 8;
#pragma unroll
        for (int r = 0; r < 2; ++r) {
#pragma unroll
            for (int i = 0; i < 16; ++i)
                __builtin_amdgcn_global_load_lds((const unsigned*)(p.wo + (size_t)(512 * w + 256 * r + 16 * i + (lane >> 2)) * 1024 + 32 * c + (lane & 3) * 8), (LAS unsigned*)(my + i * 1024), 16, 0, 0);
            WAIT_V(0);
#pragma unroll
            for (int k = 0; k < 8; ++k)
#pragma unroll
                for (int ct = 0; ct < 2; ++ct) { WT[8 * r + k][ct] = cat8(trd(my + k * 2048 + ct * 32 + toff), trd(my + k * 2048 + ct * 32 + toff + 256)); }
#pragma unroll
            for (int k = 0; k < 8; ++k) asm volatile("" : "+v"(WT[8 * r + k][0]), "+v"(WT[8 * r + k][1]));
            WAIT_L(0);
        }
    }
    BAR();
#pragma unroll 1
    for (int hs = q; hs < 2 * (NSEG - S_TAIL); hs += 2) {
        const int s = S_TAIL + (hs >> 1), rt = hs & 1;
        if (w == 0) wait_count<3>(ctl, C_OS + (s - S_TAIL) * 128 + (c >> 3) * 32, 4u);
        asm volatile("" ::: "memory"); BAR(); asm volatile("" ::: "memory");
        const rsrc_t ar = mkrsrc(p.ws + WH(WS_OA + (size_t)(2 * s + rt) * 262144 + (size_t)w * 32768), 32768u);
        const unsigned ao = (unsigned)lane * 16u;
        u32x4 A[16];
#pragma unroll
        for (int k = 0; k < 16; ++k) A[k] = ld16_sc1(ar, ao + (k & 3) * 1024, gap((unsigned)(k >> 2) * 4096u));
        SCHED();
#pragma unroll
        for (int ct = 0; ct < 2; ++ct) {
            f32x4 acc = {0.f, 0.f, 0.f, 0.f};
#pragma unroll
            for (int k = 0; k < 16; ++k) acc = mma32(WT[k][ct], asbf(A[k]), acc);
            *(LAS f32x4*)(lds + w * 2048 + ct * 1024 + lane * 16) = acc;
        }
        LDS_BAR();
        if (w < 2) {
            f32x4 r[8];
#pragma unroll
            for (int ww = 0; ww < 8; ++ww) r[ww] = *(const LAS f32x4*)(lds + ww * 2048 + w * 1024 + lane * 16);
            SCHED();
            const f32x4 t = ((r[0] + r[1]) + (r[2] + r[3])) + ((r[4] + r[5]) + (r[6] + r[7]));
            *(u32x2*)(p.out + (size_t)(32 * s + 16 * rt + n) * 2048 + 32 * c + 16 * w + 4 * g) = (u32x2){pk_bf16(t[0], t[1]), pk_bf16(t[2], t[3])};
        }
        WAIT_L(0);
    }
    BAR();
}

DI void big_unit(const Params& p, unsigned par, int id, Unit& u) {
    u.lda = DM * 2; u.nt = 16; u.ak = 128; u.ah = 128 * DM * 2;
    if (id < 384) {
        const int pm = id & 7, pn = id >> 3;
        const u16 *w0 = p.wq, *w1 = p.wk, *w2 = p.wv; const u16* W = pn < 16 ? w0 : (pn < 32 ? w1 : w2);
        u.A = (const char*)(p.ax + (size_t)pm * 256 * DM); u.B = (const char*)(W + (pn & 15) * 256); u.ldb = 4096 * 2;
        u.C = p.ws + WH(WS_QKVX + (size_t)(2 * pn) * XH + (size_t)pm * 131072); u.ldc = 256; u.cw = 1024; u.ch = XH;
        if (pn < 16) { u.sig = C_Q + pm * 32; u.nsig = 1; } else { u.sig = C_KVC(2 * pm, 2 * (pn & 15)); u.nsig = 4; }
    } else if (id < 480) {
        const int pm = (id - 384) & 7, pn = (id - 384) >> 3;
        u.A = (const char*)(p.bx + (size_t)pm * 256 * DM); u.B = (const char*)(p.bwqkv + pn * 256); u.ldb = LDB3 * 2;
        u.C = p.ws + WH(WS_QKVB + (size_t)(8 * pn) * BH + (size_t)pm * 32768); u.ldc = 64; u.cw = BH; u.ch = 4 * BH; u.sig = C_B + pm * 32; u.nsig = 1;
    } else {
        const int pm = (id - 480) >> 2, pn = id & 3;
        u.lda = 0; u.ak = 2048; u.ah = 524288u;
        u.A = p.ws + WH(WS_OB + (size_t)pm * 1048576); u.B = (const char*)(p.bwo + pn * 256); u.ldb = DM * 2;
        u.C = (char*)(p.out + (size_t)pm * 256 * 2048 + 1024 + pn * 256); u.ldc = 4096; u.cw = 64; u.ch = 256; u.sig = 0; u.nsig = 0;
    }
}
struct BigSched { const Params& p; unsigned par; int id0, id1, id2; DI bool next(int i, Unit& u) const { const int id = i == 0 ? id0 : (i == 1 ? id1 : (i == 2 ? id2 : -1)); if (id < 0) return false; big_unit(p, par, id, u); return true; } };

__global__ __launch_bounds__(512, 2) void k_fused(Params p) {
    extern __shared__ __attribute__((aligned(16))) unsigned char shm[];
    ldsp lds = (ldsp)shm;
    unsigned* ctl = (unsigned*)(p.ws + WS_CTL);
    const int b = (blockIdx.x & ~7) | ((blockIdx.x + __builtin_amdgcn_readfirstlane(ctl[C_PHASE])) & 7), id = (b & 3) * 32 + (b >> 3);
    const unsigned par = (unsigned)__builtin_amdgcn_readfirstlane(ctl[C_PAR + 1]) < (unsigned)__builtin_amdgcn_readfirstlane(ctl[C_PAR]) ? 1u : 0u;
    if ((b & 7) < 4) {
        chain_role(p, par, id, lds);
        BAR();
    } else {
        {
            const int i = id & 31, kx = id >> 5;
            const u16 *w1 = p.wk, *w2 = p.wv; const u16* Wm = kx < 2 ? w1 : w2;
            Unit u; u.lda = DM * 2; u.ldb = 4096 * 2; u.nt = 16; u.ldc = 256; u.cw = 1024; u.ch = XH; u.ak = 128; u.ah = 0;
            if (i >= 16) {
                const int h = (kx & 1) * 16 + i - 16;
                u.A = (const char*)p.ax; u.B = (const char*)(Wm + h * 128); u.C = p.ws + WH(WS_QKVX + (size_t)(32 + (kx >> 1) * 32 + h) * XH); u.sig = C_KVC(0, h); u.nsig = 1;
                gemm_small(lds, u, ctl);
            }
            if (i < 24) {
                const int rb = i < 16 ? 1 + (i & 1) : 3, h = (kx & 1) * 16 + (i < 16 ? i & ~1 : 2 * (i - 16));
                u.A = (const char*)(p.ax + (size_t)rb * 128 * DM); u.B = (const char*)(Wm + h * 128);
                u.C = p.ws + WH(WS_QKVX + (size_t)(32 + (kx >> 1) * 32 + h) * XH + (size_t)rb * 65536); u.sig = C_KVC(rb, h); u.nsig = 2;
                gemm_wide(lds, u, ctl);
            }
        }
        if ((id & 31) >= 24) {
            const int t = fresh_tid(), wv = __builtin_amdgcn_readfirstlane(t >> 6), q = (id >> 5) * 8 + (id & 7);
#pragma unroll 1
            for (int i = 0; i < 64; ++i) {
                const char* src = i < 16 ? (const char*)p.ax + (size_t)q * 131072 + i * 8192 : i < 32 ? (const char*)p.bx + (size_t)q * 131072 + (i - 16) * 8192
                                : i < 56 ? (const char*)p.bwqkv + (size_t)q * 196608 + (i - 32) * 8192 : (const char*)p.bwo + (size_t)q * 65536 + (i - 56) * 8192;
                __builtin_amdgcn_global_load_lds((const unsigned*)(src + t * 16), (LAS unsigned*)(lds + L_SCR + wv * 1024), 16, 0, 0);
            }
        }
    }
    volatile LAS unsigned* qw = (volatile LAS unsigned*)(lds + L_QW);
    const int w = __builtin_amdgcn_readfirstlane(threadIdx.x >> 6);
    const int k = b & 3;
    bool fixed = (b & 7) >= 4;
#pragma unroll 1
    for (;;) {
        int kind, x, x2 = -1, x3 = -1;
        if (fixed) { const int i = id & 31; kind = 6; x = i < 24 ? (16 + 8 * k + (i & 7)) * 8 + 2 + (i >> 3) : (4 * k + (i & 3)) * 8 + ((i >> 2) & 1); x2 = x + (i < 24 ? 3 : 2); x3 = i < 16 ? 384 + 24 * k + i : -1; fixed = false; }
        else {
        if (threadIdx.x == 0) qw[0] = __hip_atomic_fetch_add(ctl + C_HEAD + k * 32, 1u, RLX_AGENT) + 1u;
        LDS_BAR();
        const int it = (int)__builtin_amdgcn_readfirstlane(qw[0]) - 8;
        LDS_BAR();
        if (it < 0) { kind = 6; x = 384 + 24 * k + 24 + it; }
        else if (it < 4) { kind = 0; x = it * 4 + k; }
        else if (it < 8) { kind = 6; x = (4 * k + it - 4) * 8 + 4; }
        else if (it < 12) { kind = 2; x = it - 8; }
        else if (it < 16) { kind = 6; x = (4 * k + it - 12) * 8 + 5; }
        else if (it < 20) { kind = 0; x = (it - 12) * 4 + k; }
        else if (it < 52) { kind = 1; x = ((it - 20) >> 1) * 8 + 2 * k + ((it - 20) & 1); }
        else if (it < 52 + NSTR) { kind = 4; x = (it - 52) * 4 + k; }
        else if (it < 60 + NSTR) { const int i = it - 52 - NSTR; kind = 6; x = (4 * k + (i & 3)) * 8 + 6 + (i >> 2); }
        else if (it < 64 + NSTR) { kind = 2; x = 4 + it - 60 - NSTR; }
        else if (it < 72 + NSTR) { kind = 6; x = 480 + (it - 64 - NSTR) * 4 + k; }
        else if (it < 74 + NSTR) { kind = 2; x = 8 + it - 72 - NSTR; }
        else if (it < 76 + NSTR) { kind = 3; x = 80 + 2 * k + it - 74 - NSTR; }
        else if (it < 92 + NSTR) { const int i = it - 76 - NSTR; kind = 5; x = (i >> 3) * 32 + 8 * k + (i & 7); }
        else if (it < 96 + NSTR) { const int i = it - 92 - NSTR; kind = 3; x = (11 + (i >> 1)) * 8 + 2 * k + (i & 1); }
        else break;
        }
        if (kind == 6) {
            if (x >= 480) { const int w0 = C_OB + ((x - 480) >> 2) * 64; wg_wait(ctl, w0, 64u, w0 + 32, 64u); }
            BigSched S{p, par, x, x2, x3};
            gemm_stream(lds, S, ctl);
        } else
        if (kind == 0) {
            const int s0 = (x >> 2) * 4;
            wait_count(ctl, C_Q + (s0 >> 3) * 32, 128u); wait_count<24>(ctl, C_PROG, (unsigned)s0 + 5u);
            asm volatile("" ::: "memory");
#pragma unroll 1
            for (int s = s0; s < s0 + 4; ++s) attn_a_item<false>(p, par, s, (x & 3) * 8 + w, lds + w * 12288, fresh_tid() & 63);
            WAIT_V(0); signal(ctl, C_OA + (x >> 2) * 32, 1);
            BAR();
        } else if (kind == 4) {
#pragma unroll 1
            for (int s = 32 + (x >> 2); s < NSEG; s += NSTR) {
                wait_count(ctl, C_Q + (s >> 3) * 32, 128u); wait_count(ctl, C_KVC(s >> 2, (x & 3) * 8 + w), 16u);
                asm volatile("" ::: "memory");
                attn_a_item<true>(p, par, s, (x & 3) * 8 + w, lds + w * 12288, fresh_tid() & 63);
                WAIT_V(0);
                if (s < S_TAIL) signal(ctl, C_OA + (s >> 2) * 32, 1);
                else { BAR(); const int l = fresh_tid(); if (l < 4) __hip_atomic_fetch_add(ctl + C_OS + (s - S_TAIL) * 128 + l * 32, 1u, RLX_AGENT); }
            }
            BAR();
        } else if (kind == 1) {
            const int qb = x >> 3;
            wait_count(ctl, C_B + (qb >> 1) * 32, 96u); wait_count(ctl, C_B + max((qb >> 1) - 1, 0) * 32, 96u);
            asm volatile("" ::: "memory");
            attn_b_item(p, par, x & 7, qb, lds);
        } else if (kind == 5) {
            out_tail(p, par, x & 31, x >> 5, lds);
        } else {
            const int rb = kind == 2 ? x : x >> 3, col = kind == 2 ? k * 256 : (x & 7) * 128;
            Unit u; u.ldb = DM * 2; u.ldc = 2048 * 2; u.sig = 0; u.nsig = 0; u.lda = 0; u.ak = 2048; u.ah = 0; u.cw = 64; u.ch = 0;
            u.A = p.ws + WH(WS_OA + (size_t)rb * 2097152); u.B = (const char*)(p.wo + col); u.nt = 64; u.C = (char*)(p.out + (size_t)rb * 128 * 2048 + col);
            wg_wait(ctl, C_OA + rb * 32, rb < 8 ? 32u : 128u, C_OA + rb * 32, 32u);
            if (kind == 2) gemm_wide(lds, u, ctl); else gemm_small(lds, u, ctl);
        }
    }
}

__global__ __launch_bounds__(512) void k_init(char* ws) {
    const rsrc_t xr = mkrsrc(ws + WS_XCH, NWIN_INIT * 16384u);
    unsigned* ctl = (unsigned*)(ws + WS_CTL);
    const u32x4 sv = {SENT, SENT, SENT, SENT};
    const unsigned xcc = (unsigned)__builtin_amdgcn_s_getreg(20 | (3 << 11)) & 7u, half = blockIdx.x >> 3, win = blockIdx.x & 7;
    if (threadIdx.x < 64 && xcc == 1) {
        unsigned b = ~0u;
        for (int i = 0; i < 5; ++i) {
            const unsigned long long t0 = __builtin_amdgcn_s_memtime();
            __builtin_amdgcn_raw_buffer_store_b32(0u, xr, (unsigned)(2 * 16384 + C_PROBE * 4) + half * 8192u + (unsigned)i * 256u, 0, 16);
            asm volatile("s_waitcnt vmcnt(0)" ::: "memory");
            b = min(b, (unsigned)(__builtin_amdgcn_s_memtime() - t0));
        }
        if (threadIdx.x == 0) ctl[C_PAR + half] = b;
    }
    __syncthreads();
    if (win < 7 && win != 5) ST16(sv, xr, win * 16384u + half * 8192u + threadIdx.x * 16u, 0, 16);
    if (blockIdx.x == 0 && threadIdx.x == 0) ctl[C_PHASE] = xcc;
}

extern "C" void kernel_launch(void* const* d_in, const int* in_sizes, int n_in, void* d_out, int out_size, void* d_ws, size_t ws_size, hipStream_t stream) {
    Params p;
    p.ax = (const u16*)d_in[0]; p.mem0 = (const u16*)d_in[1]; p.wq = (const u16*)d_in[2]; p.wk = (const u16*)d_in[3]; p.wv = (const u16*)d_in[4]; p.wo = (const u16*)d_in[5];
    p.bx = (const u16*)d_in[7]; p.bwqkv = (const u16*)d_in[8]; p.bwo = (const u16*)d_in[9];
    p.out = (u16*)d_out; p.ws = (char*)(((uintptr_t)d_ws + 0x1FFFFFu) & ~(uintptr_t)0x1FFFFFu);
    (void)hipFuncSetAttribute((const void*)k_fused, hipFuncAttributeMaxDynamicSharedMemorySize, LDS_BYTES);
    (void)hipFuncSetAttribute((const void*)k_init, hipFuncAttributeMaxDynamicSharedMemorySize, 98304);
    k_init<<<16, 512, 98304, stream>>>(p.ws);
    k_fused<<<256, 512, LDS_BYTES, stream>>>(p);
}
```
